```python
import torch

def _softmax(x, axis=-1):
    return torch.softmax(x, dim=axis)
def _gelu(x):
    return torch.nn.functional.gelu(x, approximate="tanh")
def _silu(x):
    return torch.nn.functional.silu(x)
def _sigmoid(x):
    return torch.sigmoid(x)
def _layernorm(x, axis=-1, eps=1e-5):
    m = x.mean(dim=axis, keepdim=True)
    v = x.var(dim=axis, keepdim=True, unbiased=False)
    return (x - m) / torch.sqrt(v + eps)
def _scatter_add(tgt, idx, src, axis=0):
    return tgt.clone().index_add_(axis, idx, src)
def _segment_sum(src, seg, n):
    out = torch.zeros((n,) + src.shape[1:], dtype=src.dtype)
    return out.index_add_(0, seg, src)
def _pad(x, pw):
    flat = tuple(v for pair in reversed(pw) for v in pair)
    return torch.nn.functional.pad(x, flat)

kernel_name = 'sequential_preLN__sliding_block__dense_activation_squared_relu__prompt'

def setup_inputs():

    _w = (lambda a: (lambda t: t.to(torch.bfloat16) if t.is_floating_point() else t)(torch.as_tensor(a)))
    def _A_setup():
        B, S, D = 1, 2048, 448
        return {
            "x": _w((lambda *s: torch.randn(*s, dtype=torch.bfloat16) * 0.05)(B, S, D)),
            "wqkv": _w((lambda *s: torch.randn(*s, dtype=torch.bfloat16) * 0.05)(D, 3 * D)),
            "wo": _w((lambda *s: torch.randn(*s, dtype=torch.bfloat16) * 0.05)(D, D)),
        }
    _A_in = _A_setup()

    def _B_setup():
        B, S, D, F = 1, 2048, 448, 1792
        return {
            "x": _w((lambda *s: torch.randn(*s, dtype=torch.bfloat16) * 0.05)(B, S, D)),
            "w1": _w((lambda *s: torch.randn(*s, dtype=torch.bfloat16) * 0.05)(D, F)),
            "b1": _w((lambda *s: torch.randn(*s, dtype=torch.bfloat16) * 0.4)(F)),
            "w2": _w((lambda *s: torch.randn(*s, dtype=torch.bfloat16) * 0.05)(F, D)),
            "b2": _w((lambda *s: torch.randn(*s, dtype=torch.bfloat16) * 1.6)(D)),
        }
    _B_in = _B_setup()

    return {
        "A_x": _A_in["x"],
        "A_wqkv": _A_in["wqkv"],
        "A_wo": _A_in["wo"],
        "B_x": _B_in["x"],
        "B_w1": _B_in["w1"],
        "B_b1": _B_in["b1"],
        "B_w2": _B_in["w2"],
        "B_b2": _B_in["b2"],
    }

def reference(A_x, A_wqkv, A_wo, B_x, B_w1, B_b1, B_w2, B_b2):
    def _A_ref(x, wqkv, wo):
        B, S, D = x.shape
        H = 16; Dh = D // H; W = 256
        qkv = (lambda x, *d: x.permute(*d))((x @ wqkv).reshape(B, S, 3, H, Dh), 2, 0, 3, 1, 4)
        q, k, v = qkv[0], qkv[1], qkv[2]
        scores = torch.einsum("bhqd,bhkd->bhqk", q, k) * (Dh ** -0.5)
        idx = torch.arange(S)
        mask = (idx[:, None] - idx[None, :] >= 0) & (idx[:, None] - idx[None, :] < W)
        scores = torch.where(mask[None, None], scores, -1e9)
        p = _softmax(scores, axis=-1)
        o = torch.einsum("bhqk,bhkd->bhqd", p, v)
        return (lambda x, *d: x.permute(*d))(o, 0, 2, 1, 3).reshape(B, S, D) @ wo

    def _B_ref(x, w1, b1, w2, b2):
        h = torch.einsum("bsd,df->bsf", x, w1) + b1
        zero = torch.zeros_like(h)
        r = torch.where(h > zero, h, zero)
        h = r * r
        out = torch.einsum("bsf,fd->bsd", h, w2) + b2
        return out

    x = A_x
    h = x + _A_ref(_layernorm(x, axis=-1), A_wqkv, A_wo)
    h2 = h + _B_ref(_layernorm(h, axis=-1), B_w1, B_b1, B_w2, B_b2)
    return h2
```

```cpp
#include <hip/hip_runtime.h>
#include <hip/hip_bf16.h>
typedef __hip_bfloat16 bf16;
__device__ __forceinline__ float b2f(bf16 x){ return __bfloat162float(x); }
__device__ __forceinline__ bf16 f2b(float x){ return __float2bfloat16(x); }
constexpr int S=2048, D=448, H=16, DH=28, W=256, F=1792;

__global__ void ln_kernel(const bf16* x, bf16* y){
  int row = blockIdx.x; int lane = threadIdx.x;
  float v[7]; float s=0;
  for(int i=0;i<7;i++){ v[i]=b2f(x[row*D+lane+64*i]); s+=v[i]; }
  for(int m=32;m>=1;m>>=1) s+=__shfl_xor(s,m);
  float mean=s/D; float q=0;
  for(int i=0;i<7;i++){ float d=v[i]-mean; q+=d*d; }
  for(int m=32;m>=1;m>>=1) q+=__shfl_xor(q,m);
  float rstd = 1.0f/sqrtf(q/D+1e-5f);
  for(int i=0;i<7;i++) y[row*D+lane+64*i]=f2b((v[i]-mean)*rstd);
}
__global__ void gemm_kernel(const bf16* A, const bf16* B, bf16* C, const bf16* bias, const bf16* res, int N, int K, int mode){
  __shared__ float sA[16][17], sB[16][17];
  int tx=threadIdx.x&15, ty=threadIdx.x>>4;
  int row=blockIdx.y*16+ty, col=blockIdx.x*16+tx;
  float acc=0;
  for(int k0=0;k0<K;k0+=16){
    sA[ty][tx]=b2f(A[row*K+k0+tx]); sB[ty][tx]=b2f(B[(k0+ty)*N+col]);
    __syncthreads();
    for(int k=0;k<16;k++) acc+=sA[ty][k]*sB[k][tx];
    __syncthreads();
  }
  if(mode==1) acc = b2f(res[row*N+col]) + b2f(f2b(acc));
  if(mode==2){ acc += b2f(bias[col]); acc = acc>0?acc:0; acc=acc*acc; }
  if(mode==3){ acc = b2f(res[row*N+col]) + acc + b2f(bias[col]); }
  C[row*N+col]=f2b(acc);
}
__global__ void attn_kernel(const bf16* qkv, bf16* o){
  int t = blockIdx.x*blockDim.x+threadIdx.x; int h = t % H; int i = t / H;
  float q[DH], acc[DH];
  for(int d=0;d<DH;d++){ q[d]=b2f(qkv[i*3*D + h*DH+d])*0.18898223650461362f; acc[d]=0; }
  float m=-1e30f, l=0;
  int j0 = i-W+1; if(j0<0) j0=0;
  for(int j=j0;j<=i;j++){
    float s=0; for(int d=0;d<DH;d++) s+=q[d]*b2f(qkv[j*3*D + D + h*DH+d]);
    float mn=fmaxf(m,s); float a=__expf(m-mn); float p=__expf(s-mn);
    l=l*a+p; for(int d=0;d<DH;d++) acc[d]=acc[d]*a+p*b2f(qkv[j*3*D+2*D+h*DH+d]);
    m=mn;
  }
  for(int d=0;d<DH;d++) o[i*D+h*DH+d]=f2b(acc[d]/l);
}
extern "C" void kernel_launch(void* const* d_in, const int* in_sizes, int n_in, void* d_out, int out_size, void* d_ws, size_t ws_size, hipStream_t stream){
  const bf16* x=(const bf16*)d_in[0]; const bf16* wqkv=(const bf16*)d_in[1]; const bf16* wo=(const bf16*)d_in[2];
  const bf16* w1=(const bf16*)d_in[4]; const bf16* b1=(const bf16*)d_in[5]; const bf16* w2=(const bf16*)d_in[6]; const bf16* b2=(const bf16*)d_in[7];
  bf16* ws=(bf16*)d_ws; bf16* ln=ws; bf16* qkv=ln+S*D; bf16* o=qkv+S*3*D; bf16* h=o+S*D; bf16* h1=h+S*D;
  ln_kernel<<<S,64,0,stream>>>(x,ln);
  gemm_kernel<<<dim3(3*D/16,S/16),256,0,stream>>>(ln,wqkv,qkv,nullptr,nullptr,3*D,D,0);
  attn_kernel<<<S*H/64,64,0,stream>>>(qkv,o);
  gemm_kernel<<<dim3(D/16,S/16),256,0,stream>>>(o,wo,h,nullptr,x,D,D,1);
  ln_kernel<<<S,64,0,stream>>>(h,ln);
  gemm_kernel<<<dim3(F/16,S/16),256,0,stream>>>(ln,w1,h1,b1,nullptr,F,D,2);
  gemm_kernel<<<dim3(D/16,S/16),256,0,stream>>>(h1,w2,(bf16*)d_out,b2,h,D,F,3);
}
```

```cpp
#include <hip/hip_runtime.h>
#include <hip/hip_bf16.h>
#include <stdint.h>

#define LAS __attribute__((address_space(3)))
#define GAS __attribute__((address_space(1)))
typedef short bf16x8 __attribute__((ext_vector_type(8)));
typedef short s16x4 __attribute__((ext_vector_type(4)));
typedef short v4i16_t __attribute__((ext_vector_type(4)));
typedef float f32x4 __attribute__((ext_vector_type(4)));
typedef float f32x2 __attribute__((ext_vector_type(2)));
typedef unsigned u32x4 __attribute__((ext_vector_type(4)));
typedef unsigned u32x2 __attribute__((ext_vector_type(2)));
typedef __bf16 bf16x2_t __attribute__((ext_vector_type(2)));
typedef LAS char* ldsp;

constexpr int SEQ = 2048, DM = 448, FF = 1792;
constexpr size_t WS_CTL = 0;
constexpr size_t WS_KV = 65536;
constexpr size_t WS_O = WS_KV + 256 * 16384;
constexpr size_t WS_H = WS_O + 256 * 8192;
constexpr size_t WS_H1 = WS_H + (size_t)SEQ * 896;
constexpr size_t WS_DBG = WS_H1 + (size_t)SEQ * 3584;
constexpr int LDS_BYTES = 160 * 1024;
constexpr int MISC = 147456;
constexpr unsigned SPIN_CAP = 1u << 16;

#define FLAG_IDX(ph, c) (64 + ((ph) * 16 + (c)) * 32)

#define WAITV(n) asm volatile("s_waitcnt vmcnt(" #n ")" ::: "memory")
#define BAR() do { asm volatile("s_waitcnt lgkmcnt(0)" ::: "memory"); __builtin_amdgcn_s_barrier(); asm volatile("" ::: "memory"); } while (0)
#define MFMA(a, b, c) __builtin_amdgcn_mfma_f32_16x16x32_bf16((a), (b), (c), 0, 0, 0)

__device__ __forceinline__ unsigned cvtpk(float lo, float hi) { f32x2 v = {lo, hi}; bf16x2_t b = __builtin_convertvector(v, bf16x2_t); return __builtin_bit_cast(unsigned, b); }
__device__ __forceinline__ float dot2(unsigned a, unsigned b, float c) { return __builtin_amdgcn_fdot2_f32_bf16(__builtin_bit_cast(bf16x2_t, a), __builtin_bit_cast(bf16x2_t, b), c, false); }
__device__ __forceinline__ float bflo(unsigned u) { return __uint_as_float(u << 16); }
__device__ __forceinline__ float bfhi(unsigned u) { return __uint_as_float(u & 0xffff0000u); }
__device__ __forceinline__ bf16x8 ld128(ldsp p) { return *(const LAS bf16x8*)p; }
__device__ __forceinline__ s16x4 ldtr(ldsp p) { return __builtin_bit_cast(s16x4, __builtin_amdgcn_ds_read_tr16_b64_v4i16((LAS v4i16_t*)p)); }
__device__ __forceinline__ bf16x8 cat(s16x4 lo, s16x4 hi) { return __builtin_shufflevector(lo, hi, 0, 1, 2, 3, 4, 5, 6, 7); }
__device__ __forceinline__ int rfl(int x) { return __builtin_amdgcn_readfirstlane(x); }
__device__ __forceinline__ void dma16(unsigned voff, const void* sbase, unsigned lds) {
  unsigned keep;
  asm volatile("s_mov_b32 %0, m0\n\ts_mov_b32 m0, %3\n\ts_nop 0\n\tglobal_load_lds_dwordx4 %1, %2\n\ts_mov_b32 m0, %0" : "=&s"(keep) : "v"(voff), "s"(sbase), "s"(lds) : "memory");
}
__device__ __forceinline__ void dma16_sc1(unsigned voff, const void* sbase, unsigned lds) {
  unsigned keep;
  asm volatile("s_mov_b32 %0, m0\n\ts_mov_b32 m0, %3\n\ts_nop 0\n\tglobal_load_lds_dwordx4 %1, %2 sc1\n\ts_mov_b32 m0, %0" : "=&s"(keep) : "v"(voff), "s"(sbase), "s"(lds) : "memory");
}
__device__ __forceinline__ void st128_sc1(unsigned voff, const void* sbase, u32x4 v) {
  asm volatile("global_store_dwordx4 %0, %1, %2 sc1\n\ts_nop 1" ::"v"(voff), "v"(v), "s"(sbase) : "memory");
}
__device__ __forceinline__ u32x2 ld64_sc1(unsigned voff, const void* sbase) {
  u32x2 r;
  asm volatile("global_load_dwordx2 %0, %1, %2 sc1\n\ts_waitcnt vmcnt(0)" : "=&v"(r) : "v"(voff), "s"(sbase) : "memory");
  return r;
}
__device__ __forceinline__ float red_g_sum(float v) {
  auto a = __builtin_amdgcn_permlane16_swap(__float_as_uint(v), __float_as_uint(v), false, false);
  v = __uint_as_float(a[0]) + __uint_as_float(a[1]);
  auto b = __builtin_amdgcn_permlane32_swap(__float_as_uint(v), __float_as_uint(v), false, false);
  return __uint_as_float(b[0]) + __uint_as_float(b[1]);
}
__device__ __forceinline__ float red_g_max(float v) {
  auto a = __builtin_amdgcn_permlane16_swap(__float_as_uint(v), __float_as_uint(v), false, false);
  v = fmaxf(__uint_as_float(a[0]), __uint_as_float(a[1]));
  auto b = __builtin_amdgcn_permlane32_swap(__float_as_uint(v), __float_as_uint(v), false, false);
  return fmaxf(__uint_as_float(b[0]), __uint_as_float(b[1]));
}
__device__ __forceinline__ void poll_flags(const unsigned* flags, int n, unsigned epoch, int lane) {
  for (unsigned spins = 0; spins < SPIN_CAP; ++spins) {
    unsigned v = epoch;
    if (lane < n) v = __hip_atomic_load((const GAS unsigned*)flags + lane, __ATOMIC_RELAXED, __HIP_MEMORY_SCOPE_AGENT);
    if (__all(v == epoch)) break;
    __builtin_amdgcn_s_sleep(1);
  }
  asm volatile("" ::: "memory");
}

struct Params {
  const char* x; const char* wqkv; const char* wo; const char* w1; const char* b1; const char* w2; const char* b2;
  char* out; char* ws;
};

#define STAMP(i) do {} while (0)

__global__ void __launch_bounds__(512, 2) fused_block(Params p) {
  extern __shared__ __attribute__((aligned(16))) char smem[];
  ldsp lds = (ldsp)smem;
  const unsigned lds0 = (unsigned)(uintptr_t)lds;
  const int tid = threadIdx.x, lane = tid & 63, w = rfl(tid >> 6);
  const int l15 = lane & 15, g = lane >> 4, q4 = l15 >> 2, p4 = l15 & 3;
  const int b = blockIdx.x, r_ = b >> 3;
  const int c = 2 * (b & 7) + (r_ >> 4), j = r_ & 15;
  unsigned* ctl = (unsigned*)(p.ws + WS_CTL);
  unsigned epoch = 0;
  STAMP(0);
  if (w == 0) epoch = (unsigned)rfl((int)__hip_atomic_load((const GAS unsigned*)ctl, __ATOMIC_RELAXED, __HIP_MEMORY_SCOPE_AGENT)) + 1u;
  const unsigned ONES = 0x3f803f80u;

  constexpr int KIMG = 0, VIMG = 24576, QIMG = 49152;
  {
    const int h = j;
    constexpr int SLOT = 28672, WOFF = 16384;
    unsigned voff0, voff1 = 0, voff2 = 0;
    const char* gbase; unsigned ldst; unsigned kstep; unsigned istep = 0;
    if (w < 4) {
      voff0 = (unsigned)((32 * w + (lane >> 3)) * 896 + (((lane & 7) ^ ((lane >> 3) & 7)) << 4));
      gbase = p.x + (size_t)c * 128 * 896; ldst = w * 4096; kstep = 128; istep = 8 * 896;
    } else {
      unsigned vo[3];
#pragma unroll
      for (int ii = 0; ii < 3; ++ii) {
        const int u = 192 * (w - 4) + 64 * ii + lane, row = u / 12, rem = u - 12 * row;
        const int piece = rem >> 2, c16 = rem & 3, hb = (c16 >> 1) ^ ((row >> 3) & 1);
        vo[ii] = (unsigned)(row * 2688 + piece * 896 + hb * 32 + (c16 & 1) * 16);
      }
      voff0 = vo[0]; voff1 = vo[1]; voff2 = vo[2];
      gbase = p.wqkv + 56 * h - 8 * (h & 1); ldst = WOFF + (w - 4) * 3072; kstep = 64 * 2688;
    }
    auto issue = [&](int kc, int slot) {
      const char* gb = gbase + (size_t)kc * kstep; const unsigned ld = lds0 + slot * SLOT + ldst;
      if (w < 4) {
#pragma unroll
        for (int ii = 0; ii < 4; ++ii) dma16(voff0, gb + ii * istep, ld + ii * 1024);
      } else { dma16(voff0, gb, ld); dma16(voff1, gb, ld + 1024); dma16(voff2, gb, ld + 2048); }
    };
#pragma unroll
    for (int s = 0; s < 5; ++s) issue(s, s);

    const int tg = w >> 1, ng = w & 1;
    f32x4 acc[3][2];
#pragma unroll
    for (int e = 0; e < 3; ++e) { acc[e][0] = f32x4{0, 0, 0, 0}; acc[e][1] = f32x4{0, 0, 0, 0}; }
    float ssum = 0.f, ssq = 0.f, cs[3] = {0.f, 0.f, 0.f};
    int xb[2], ab[3];
    xb[0] = (32 * tg + 16 * ng + l15) * 128 + ((g ^ (l15 & 7)) << 4);
    xb[1] = (32 * tg + 16 * (ng ^ 1) + l15) * 128 + ((g ^ (l15 & 7)) << 4);
#pragma unroll
    for (int e = 0; e < 3; ++e) ab[e] = WOFF + (8 * g + q4) * 192 + ((((3 * ng + e) * 32) ^ ((g & 1) << 5))) + 8 * p4;

#pragma unroll
    for (int s = 0; s < 7; ++s) {
      if (w < 4) { if (s == 0) WAITV(16); else if (s <= 3) WAITV(12); else if (s == 4) WAITV(8); else if (s == 5) WAITV(4); else WAITV(0); }
      else { if (s == 0) WAITV(12); else if (s <= 3) WAITV(9); else if (s == 4) WAITV(6); else if (s == 5) WAITV(3); else WAITV(0); }
      BAR();
      if (s >= 1 && s <= 2) issue(s + 4, s - 1);
      ldsp sl = lds + (s % 5) * SLOT;
#pragma unroll
      for (int ks = 0; ks < 2; ++ks) {
        bf16x8 xf[2], af[3];
        xf[0] = ld128(sl + (xb[0] ^ (ks << 6))); xf[1] = ld128(sl + (xb[1] ^ (ks << 6)));
#pragma unroll
        for (int e = 0; e < 3; ++e) af[e] = cat(ldtr(sl + ab[e] + (32 * ks) * 192), ldtr(sl + ab[e] + (32 * ks + 4) * 192));
#pragma unroll
        for (int e = 0; e < 3; ++e) { acc[e][0] = MFMA(af[e], xf[0], acc[e][0]); acc[e][1] = MFMA(af[e], xf[1], acc[e][1]); }
        const u32x4 xu = __builtin_bit_cast(u32x4, xf[0]);
#pragma unroll
        for (int i = 0; i < 4; ++i) { ssum = dot2(xu[i], ONES, ssum); ssq = dot2(xu[i], xu[i], ssq); }
        if (((2 * s + ks) & 3) == tg) {
#pragma unroll
          for (int e = 0; e < 3; ++e) { const u32x4 au = __builtin_bit_cast(u32x4, af[e]);
#pragma unroll
            for (int i = 0; i < 4; ++i) cs[e] = dot2(au[i], ONES, cs[e]); }
        }
      }
    }
    STAMP(1);
    LAS f32x2* stats = (LAS f32x2*)(lds + MISC);
    LAS float* csb = (LAS float*)(lds + MISC + 1024);
    ssum = red_g_sum(ssum); ssq = red_g_sum(ssq);
    { const float mean = ssum * (1.f / 448.f), var = fmaxf(ssq * (1.f / 448.f) - mean * mean, 0.f);
      if (g == 0) stats[32 * tg + 16 * ng + l15] = f32x2{mean, rsqrtf(var + 1e-5f)}; }
#pragma unroll
    for (int e = 0; e < 3; ++e) { const float v = red_g_sum(cs[e]); if (g == 0) csb[tg * 96 + 16 * (3 * ng + e) + l15] = v; }
    BAR();
    f32x2 st[2]; st[0] = stats[32 * tg + 16 * ng + l15]; st[1] = stats[32 * tg + 16 * (ng ^ 1) + l15];
#pragma unroll
    for (int e = 0; e < 3; ++e) {
      const int nt = 3 * ng + e, which = nt >> 1, par = nt & 1;
      f32x4 cv = *(LAS f32x4*)(csb + 16 * nt + 4 * g);
      cv += *(LAS f32x4*)(csb + 96 + 16 * nt + 4 * g); cv += *(LAS f32x4*)(csb + 192 + 16 * nt + 4 * g); cv += *(LAS f32x4*)(csb + 288 + 16 * nt + 4 * g);
      const bool pad = (h & 1) ? (par == 0 && g == 0) : (par == 1 && g == 3);
      const float sc = (which == 0) ? 0.27264657f : 1.f;
#pragma unroll
      for (int tt = 0; tt < 2; ++tt) {
        const int row = 32 * tg + 16 * (tt ^ ng) + l15;
        f32x4 v = (acc[e][tt] - st[tt].x * cv) * (st[tt].y * sc);
        u32x2 o; o.x = cvtpk(v[0], v[1]); o.y = cvtpk(v[2], v[3]);
        if (pad) { o.x = (which == 2) ? 0x00003f80u : 0u; o.y = 0u; }
        int a;
        if (which == 2) a = VIMG + (256 + row) * 64 + (((2 * par + (g >> 1)) ^ ((row >> 1) & 3)) << 4) + (g & 1) * 8;
        else a = (which == 0 ? QIMG + row * 64 : KIMG + (256 + row) * 64) + ((g ^ ((row >> 1) & 3)) << 4) + par * 8;
        *(LAS u32x2*)(lds + a) = o;
      }
    }
    BAR();
    if (c < 15) {
      const char* blob = p.ws + WS_KV + (size_t)(c * 16 + h) * 16384;
      const u32x4 v0 = *(LAS u32x4*)(lds + KIMG + 16384 + tid * 16), v1 = *(LAS u32x4*)(lds + VIMG + 16384 + tid * 16);
      st128_sc1(tid * 16, blob, v0); st128_sc1(8192 + tid * 16, blob, v1);
    }
    WAITV(0);
    BAR();
    if (tid == 0) __hip_atomic_store((GAS unsigned*)ctl + FLAG_IDX(0, c) + h, epoch, __ATOMIC_RELAXED, __HIP_MEMORY_SCOPE_AGENT);
    STAMP(2);
  }
  {
    const int h = j;
    if (w == 0) {
      if (c >= 1) poll_flags(ctl + FLAG_IDX(0, c - 1) + h, 1, epoch, lane);
      if (c >= 2) poll_flags(ctl + FLAG_IDX(0, c - 2) + h, 1, epoch, lane);
    }
    BAR();
    STAMP(3);
    {
      const int tl = w >> 2, src = c - 2 + tl, pc = (w & 3) * 2048;
      if (src >= 0) {
        const char* blob = p.ws + WS_KV + (size_t)(src * 16 + h) * 16384;
        dma16_sc1(lane * 16, blob + pc, lds0 + KIMG + tl * 8192 + pc); dma16_sc1(lane * 16, blob + pc + 1024, lds0 + KIMG + tl * 8192 + pc + 1024);
        dma16_sc1(lane * 16, blob + 8192 + pc, lds0 + VIMG + tl * 8192 + pc); dma16_sc1(lane * 16, blob + 8192 + pc + 1024, lds0 + VIMG + tl * 8192 + pc + 1024);
      } else {
        const u32x4 z = {0, 0, 0, 0};
        *(LAS u32x4*)(lds + VIMG + tl * 8192 + pc + lane * 16) = z; *(LAS u32x4*)(lds + VIMG + tl * 8192 + pc + 1024 + lane * 16) = z;
      }
    }
    const int sw = (l15 >> 1) & 3;
    const bf16x8 qf = ld128(lds + QIMG + (16 * w + l15) * 64 + ((g ^ sw) << 4));
    WAITV(0);
    BAR();
    STAMP(4);
    const int tmin = (c >= 2) ? 0 : (2 - c) * 8;
    f32x4 sc[17];
    const float NEG = -__builtin_inff();
    ldsp kb = lds + KIMG + (16 * w + l15) * 64 + ((g ^ sw) << 4);
#pragma unroll
    for (int u = 0; u < 17; ++u) {
      if (w + u >= tmin) sc[u] = MFMA(ld128(kb + u * 1024), qf, (f32x4{0, 0, 0, 0}));
      else sc[u] = f32x4{NEG, NEG, NEG, NEG};
    }
#pragma unroll
    for (int r = 0; r < 4; ++r) { if (4 * g + r <= l15) sc[0][r] = NEG; if (4 * g + r > l15) sc[16][r] = NEG; }
    float m = fmaxf(fmaxf(sc[0][0], sc[0][1]), fmaxf(sc[0][2], sc[0][3]));
#pragma unroll
    for (int u = 1; u < 17; ++u) m = fmaxf(fmaxf(m, fmaxf(sc[u][0], sc[u][1])), fmaxf(sc[u][2], sc[u][3]));
    m = red_g_max(m);
    unsigned pk[17][2];
#pragma unroll
    for (int u = 0; u < 17; ++u) {
      pk[u][0] = cvtpk(__builtin_amdgcn_exp2f(sc[u][0] - m), __builtin_amdgcn_exp2f(sc[u][1] - m));
      pk[u][1] = cvtpk(__builtin_amdgcn_exp2f(sc[u][2] - m), __builtin_amdgcn_exp2f(sc[u][3] - m));
    }
    f32x4 oa[2] = {f32x4{0, 0, 0, 0}, f32x4{0, 0, 0, 0}};
    const int vsw = (2 * g + (q4 >> 1)) & 3;
    ldsp vb0 = lds + VIMG + (16 * w + 4 * g + q4) * 64 + (((p4 >> 1) ^ vsw) << 4) + (p4 & 1) * 8;
    ldsp vb1 = lds + VIMG + (16 * w + 4 * g + q4) * 64 + (((2 + (p4 >> 1)) ^ vsw) << 4) + (p4 & 1) * 8;
#pragma unroll
    for (int uu = 0; uu < 9; ++uu) {
      const int ua = 2 * uu, ub = (uu < 8) ? 2 * uu + 1 : 16;
      u32x4 pu; pu.x = pk[ua][0]; pu.y = pk[ua][1];
      if (uu < 8) { pu.z = pk[ub][0]; pu.w = pk[ub][1]; } else { pu.z = 0; pu.w = 0; }
      const bf16x8 pf = __builtin_bit_cast(bf16x8, pu);
      oa[0] = MFMA(cat(ldtr(vb0 + ua * 1024), ldtr(vb0 + ub * 1024)), pf, oa[0]);
      oa[1] = MFMA(cat(ldtr(vb1 + ua * 1024), ldtr(vb1 + ub * 1024)), pf, oa[1]);
    }
    float l = (h & 1) ? oa[0][0] : oa[1][0];
    l = __shfl(l, (h & 1) ? l15 : 48 + l15);
    const float inv = 1.f / l;
    u32x4 ov;
    ov.x = cvtpk(oa[0][0] * inv, oa[0][1] * inv); ov.y = cvtpk(oa[0][2] * inv, oa[0][3] * inv);
    ov.z = cvtpk(oa[1][0] * inv, oa[1][1] * inv); ov.w = cvtpk(oa[1][2] * inv, oa[1][3] * inv);
    if (h & 1) { if (g == 0) { ov.x = 0; ov.y = 0; } } else { if (g == 3) { ov.z = 0; ov.w = 0; } }
    st128_sc1((unsigned)((16 * w + l15) * 64 + ((g ^ sw) << 4)), p.ws + WS_O + (size_t)(c * 16 + h) * 8192, ov);
    WAITV(0);
    BAR();
    if (tid == 0) __hip_atomic_store((GAS unsigned*)ctl + FLAG_IDX(1, c) + h, epoch, __ATOMIC_RELAXED, __HIP_MEMORY_SCOPE_AGENT);
    STAMP(5);
  }
  const int ns = j & 3, col4 = (ns < 2) ? 128 * ns : 256 + 96 * (ns - 2), ngr = (ns < 2) ? 4 : 3;
  auto stage_w256 = [&](const char* wbase, unsigned rowstride, unsigned dst) {
    const int rin = lane >> 4, pos = lane & 15;
    unsigned vo[2];
#pragma unroll
    for (int par = 0; par < 2; ++par) { int blk = (pos >> 1) ^ (4 * par + rin); if (blk >= 2 * ngr) blk -= 2; vo[par] = (unsigned)(rin * rowstride + blk * 32 + (pos & 1) * 16); }
#pragma unroll
    for (int i = 0; i < 14; ++i) { const int ins = 14 * w + i; dma16((i & 1) ? vo[1] : vo[0], wbase + (size_t)ins * 4 * rowstride, lds0 + dst + ins * 1024); }
  };
  {
    constexpr int OT = 0, WI = 32768;
    const int tq = j >> 2, pr = w & 3, tt = w >> 2;
    stage_w256(p.wo + col4 * 2, 896, WI);
    const int tok = 128 * c + 32 * tq + 16 * tt + l15;
    u32x2 xr0 = {0, 0}, xr1 = {0, 0};
    if (pr < ngr) { const GAS char* xp = (const GAS char*)p.x + (size_t)tok * 896 + (col4 + 32 * pr + 4 * g) * 2; xr0 = *(const GAS u32x2*)xp; xr1 = *(const GAS u32x2*)(xp + 32); }
    if (w == 0) poll_flags(ctl + FLAG_IDX(1, c), 16, epoch, lane);
    BAR();
    STAMP(6);
#pragma unroll
    for (int i = 0; i < 4; ++i) { const int pc = 4 * w + i, hd = pc >> 1;
      dma16_sc1(lane * 16, p.ws + WS_O + (size_t)(c * 16 + hd) * 8192 + tq * 2048 + (pc & 1) * 1024, lds0 + OT + pc * 1024); }
    WAITV(0);
    BAR();
    STAMP(7);
    if (pr < ngr) {
      f32x4 a0 = {0, 0, 0, 0}, a1 = {0, 0, 0, 0};
      const int rx = 4 * (g & 1) + q4;
      ldsp ab0 = lds + WI + (4 * g + q4) * 256 + (((2 * pr) ^ rx) << 5) + 8 * p4;
      ldsp ab1 = lds + WI + (4 * g + q4) * 256 + (((2 * pr + 1) ^ rx) << 5) + 8 * p4;
      ldsp ob = lds + OT + (16 * tt + l15) * 64 + ((g ^ ((l15 >> 1) & 3)) << 4);
#pragma unroll
      for (int s = 0; s < 16; ++s) {
        const int rb = (28 * s - 4 * (s & 1)) * 256;
        const bf16x8 of = ld128(ob + s * 2048);
        a0 = MFMA(cat(ldtr(ab0 + rb), ldtr(ab0 + rb + 4096)), of, a0);
        a1 = MFMA(cat(ldtr(ab1 + rb), ldtr(ab1 + rb + 4096)), of, a1);
      }
      u32x4 hv;
      hv.x = cvtpk(a0[0] + bflo(xr0.x), a0[1] + bfhi(xr0.x)); hv.y = cvtpk(a0[2] + bflo(xr0.y), a0[3] + bfhi(xr0.y));
      hv.z = cvtpk(a1[0] + bflo(xr1.x), a1[1] + bfhi(xr1.x)); hv.w = cvtpk(a1[2] + bflo(xr1.y), a1[3] + bfhi(xr1.y));
      st128_sc1((unsigned)(tok * 896 + (col4 / 32 + pr) * 64 + g * 16), p.ws + WS_H, hv);
    }
    WAITV(0);
    BAR();
    if (tid == 0) __hip_atomic_store((GAS unsigned*)ctl + FLAG_IDX(2, c) + j, epoch, __ATOMIC_RELAXED, __HIP_MEMORY_SCOPE_AGENT);
    STAMP(8);
  }
  {
    const int f0 = 448 * (j >> 2) + col4, pr = w & 3, th = w >> 2;
    stage_w256(p.w1 + f0 * 2, 3584, 0);
    u32x2 bb0 = {0, 0}, bb1 = {0, 0};
    if (pr < ngr) { const GAS char* bp = (const GAS char*)p.b1 + (f0 + 32 * pr + 4 * g) * 2; bb0 = *(const GAS u32x2*)bp; bb1 = *(const GAS u32x2*)(bp + 32); }
    WAITV(0);
    BAR();
    bf16x8 af[2][14];
    float cs0 = 0.f, cs1 = 0.f;
    {
      const int rx = 4 * (g & 1) + q4;
      ldsp ab0 = lds + (4 * g + q4) * 256 + (((2 * pr) ^ rx) << 5) + 8 * p4;
      ldsp ab1 = lds + (4 * g + q4) * 256 + (((2 * pr + 1) ^ rx) << 5) + 8 * p4;
#pragma unroll
      for (int s = 0; s < 14; ++s) {
        af[0][s] = cat(ldtr(ab0 + s * 8192), ldtr(ab0 + s * 8192 + 4096));
        af[1][s] = cat(ldtr(ab1 + s * 8192), ldtr(ab1 + s * 8192 + 4096));
        const u32x4 u0 = __builtin_bit_cast(u32x4, af[0][s]), u1 = __builtin_bit_cast(u32x4, af[1][s]);
#pragma unroll
        for (int i = 0; i < 4; ++i) { cs0 = dot2(u0[i], ONES, cs0); cs1 = dot2(u1[i], ONES, cs1); }
      }
    }
    LAS float* csw = (LAS float*)(lds + MISC + 2048) + w * 32;
    cs0 = red_g_sum(cs0); cs1 = red_g_sum(cs1);
    if (g == 0) { csw[l15] = cs0; csw[16 + l15] = cs1; }
    if (w == 0) poll_flags(ctl + FLAG_IDX(2, c), 16, epoch, lane);
    BAR();
    STAMP(9);
    {
      const unsigned vo = (unsigned)((16 * w + (lane >> 3)) * 896 + (((lane & 7) ^ ((lane >> 3) & 7)) << 4));
      const char* hb = p.ws + WS_H + (size_t)c * 128 * 896;
#pragma unroll
      for (int kc = 0; kc < 7; ++kc) { dma16_sc1(vo, hb + kc * 128, lds0 + kc * 16384 + w * 2048); dma16_sc1(vo, hb + kc * 128 + 8 * 896, lds0 + kc * 16384 + w * 2048 + 1024); }
    }
    f32x4 acc[2][4];
#pragma unroll
    for (int t = 0; t < 4; ++t) { acc[0][t] = f32x4{0, 0, 0, 0}; acc[1][t] = f32x4{0, 0, 0, 0}; }
    float ssum = 0.f, ssq = 0.f;
    int xb[4];
#pragma unroll
    for (int t = 0; t < 4; ++t) xb[t] = (64 * th + 16 * ((t + pr) & 3) + l15) * 128 + ((g ^ (l15 & 7)) << 4);
#pragma unroll
    for (int kc = 0; kc < 7; ++kc) {
      if (kc == 0) WAITV(12); else if (kc == 1) WAITV(10); else if (kc == 2) WAITV(8); else if (kc == 3) WAITV(6); else if (kc == 4) WAITV(4); else if (kc == 5) WAITV(2); else WAITV(0);
      BAR();
      if (kc == 0) STAMP(10);
#pragma unroll
      for (int ks = 0; ks < 2; ++ks) {
        bf16x8 xf[4];
#pragma unroll
        for (int t = 0; t < 4; ++t) xf[t] = ld128(lds + kc * 16384 + (xb[t] ^ (ks << 6)));
#pragma unroll
        for (int t = 0; t < 4; ++t) { acc[0][t] = MFMA(af[0][2 * kc + ks], xf[t], acc[0][t]); acc[1][t] = MFMA(af[1][2 * kc + ks], xf[t], acc[1][t]); }
        const u32x4 xu = __builtin_bit_cast(u32x4, xf[0]);
#pragma unroll
        for (int i = 0; i < 4; ++i) { ssum = dot2(xu[i], ONES, ssum); ssq = dot2(xu[i], xu[i], ssq); }
      }
    }
    STAMP(11);
    LAS f32x2* stats = (LAS f32x2*)(lds + MISC);
    ssum = red_g_sum(ssum); ssq = red_g_sum(ssq);
    { const float mean = ssum * (1.f / 448.f), var = fmaxf(ssq * (1.f / 448.f) - mean * mean, 0.f);
      if (g == 0) stats[64 * th + 16 * pr + l15] = f32x2{mean, rsqrtf(var + 1e-5f)}; }
    BAR();
    if (pr < ngr) {
      const f32x4 c0 = *(LAS f32x4*)(csw + 4 * g), c1 = *(LAS f32x4*)(csw + 16 + 4 * g);
      const f32x4 b0 = {bflo(bb0.x), bfhi(bb0.x), bflo(bb0.y), bfhi(bb0.y)}, b1v = {bflo(bb1.x), bfhi(bb1.x), bflo(bb1.y), bfhi(bb1.y)};
#pragma unroll
      for (int t = 0; t < 4; ++t) {
        const int row = 64 * th + 16 * ((t + pr) & 3) + l15;
        const f32x2 st = stats[row];
        f32x4 z0 = (acc[0][t] - st.x * c0) * st.y + b0, z1 = (acc[1][t] - st.x * c1) * st.y + b1v;
#pragma unroll
        for (int i = 0; i < 4; ++i) { z0[i] = fmaxf(z0[i], 0.f); z0[i] *= z0[i]; z1[i] = fmaxf(z1[i], 0.f); z1[i] *= z1[i]; }
        u32x4 hv; hv.x = cvtpk(z0[0], z0[1]); hv.y = cvtpk(z0[2], z0[3]); hv.z = cvtpk(z1[0], z1[1]); hv.w = cvtpk(z1[2], z1[3]);
        st128_sc1((unsigned)((128 * c + row) * 3584 + (f0 / 32 + pr) * 64 + g * 16), p.ws + WS_H1, hv);
      }
    }
    WAITV(0);
    BAR();
    if (tid == 0) __hip_atomic_store((GAS unsigned*)ctl + FLAG_IDX(3, c) + j, epoch, __ATOMIC_RELAXED, __HIP_MEMORY_SCOPE_AGENT);
    STAMP(12);
  }
  {
    const int th = j >> 3, cs = j & 7, col0 = (cs < 4) ? 64 * cs : 256 + 48 * (cs - 4), nblk = (cs < 4) ? 4 : 3;
    const int ng = w & 1, kq = w >> 1;
    bf16x8 af[2][14];
    {
      const int rin = lane >> 3, pos = lane & 7;
      int blk = (pos >> 1) ^ ((rin >> 1) & 3); if (blk >= nblk) blk = nblk - 1;
      const unsigned vo = (unsigned)(rin * 896 + blk * 32 + (pos & 1) * 16);
      const int rx = (2 * g + (q4 >> 1)) & 3;
      ldsp ab0 = lds + (kq * 224 + 4 * g + q4) * 128 + (((2 * ng) ^ rx) << 5) + 8 * p4;
      ldsp ab1 = lds + (kq * 224 + 4 * g + q4) * 128 + (((2 * ng + 1) ^ rx) << 5) + 8 * p4;
#pragma unroll
      for (int rd = 0; rd < 2; ++rd) {
#pragma unroll
        for (int i = 0; i < 14; ++i) {
          const int R = 112 * w + 8 * i, kk = w >> 1, fr = 448 * kk + 224 * rd + (R - 224 * kk);
          dma16(vo, p.w2 + (size_t)fr * 896 + col0 * 2, lds0 + R * 128);
        }
        WAITV(0);
        BAR();
#pragma unroll
        for (int s = 0; s < 7; ++s) {
          af[0][7 * rd + s] = cat(ldtr(ab0 + s * 4096), ldtr(ab0 + s * 4096 + 2048));
          af[1][7 * rd + s] = cat(ldtr(ab1 + s * 4096), ldtr(ab1 + s * 4096 + 2048));
        }
        BAR();
      }
    }
    const int tokf = 128 * c + 64 * th + 16 * kq + l15;
    u32x2 bb0, bb1;
    { const int t0 = col0 / 16 + 2 * ng, t1 = (2 * ng + 1 < nblk) ? t0 + 1 : t0;
      bb0 = *(const GAS u32x2*)((const GAS char*)p.b2 + (16 * t0 + 4 * g) * 2); bb1 = *(const GAS u32x2*)((const GAS char*)p.b2 + (16 * t1 + 4 * g) * 2); }
    u32x2 hr0, hr1;
    { const int t0 = col0 / 16 + 2 * ng, t1 = (2 * ng + 1 < nblk) ? t0 + 1 : t0;
      hr0 = ld64_sc1((unsigned)(tokf * 896 + (t0 >> 1) * 64 + g * 16 + (t0 & 1) * 8), p.ws + WS_H);
      hr1 = ld64_sc1((unsigned)(tokf * 896 + (t1 >> 1) * 64 + g * 16 + (t1 & 1) * 8), p.ws + WS_H); }
    if (w == 0) poll_flags(ctl + FLAG_IDX(3, c), 16, epoch, lane);
    if (b == 0 && w == 1) {
      unsigned ep = (unsigned)rfl((int)__hip_atomic_load((const GAS unsigned*)ctl, __ATOMIC_RELAXED, __HIP_MEMORY_SCOPE_AGENT)) + 1u;
      for (unsigned spins = 0; spins < SPIN_CAP; ++spins) {
        bool ok = true;
#pragma unroll
        for (int i = 0; i < 4; ++i) { const int cc = 4 * i + (lane >> 4); ok &= (__hip_atomic_load((const GAS unsigned*)ctl + FLAG_IDX(0, cc) + l15, __ATOMIC_RELAXED, __HIP_MEMORY_SCOPE_AGENT) == ep); }
        if (__all(ok)) break;
        __builtin_amdgcn_s_sleep(1);
      }
      if (lane == 0) __hip_atomic_store((GAS unsigned*)ctl, ep, __ATOMIC_RELAXED, __HIP_MEMORY_SCOPE_AGENT);
    }
    BAR();
    STAMP(13);
    const unsigned vo = (unsigned)((32 * ng + (lane >> 3)) * 3584 + (((lane & 7) ^ ((lane >> 3) & 7)) << 4));
    const char* hb = p.ws + WS_H1 + (size_t)(128 * c + 64 * th) * 3584 + kq * 896;
    auto issue = [&](int i) {
      const unsigned ld = lds0 + (i & 3) * 32768 + kq * 8192 + ng * 4096;
#pragma unroll
      for (int ii = 0; ii < 4; ++ii) dma16_sc1(vo, hb + i * 128 + ii * 8 * 3584, ld + ii * 1024);
    };
#pragma unroll
    for (int i = 0; i < 4; ++i) issue(i);
    f32x4 acc[2][4];
#pragma unroll
    for (int t = 0; t < 4; ++t) { acc[0][t] = f32x4{0, 0, 0, 0}; acc[1][t] = f32x4{0, 0, 0, 0}; }
    int xb[4];
#pragma unroll
    for (int t = 0; t < 4; ++t) xb[t] = kq * 8192 + (16 * ((t + kq) & 3) + l15) * 128 + ((g ^ (l15 & 7)) << 4);
#pragma unroll
    for (int i = 0; i < 7; ++i) {
      if (i == 0) WAITV(12); else if (i <= 4) WAITV(8); else if (i == 5) WAITV(4); else WAITV(0);
      BAR();
      if (i == 0) STAMP(14);
      if (i >= 1 && i <= 3) issue(i + 3);
#pragma unroll
      for (int ks = 0; ks < 2; ++ks) {
        bf16x8 xf[4];
#pragma unroll
        for (int t = 0; t < 4; ++t) xf[t] = ld128(lds + (i & 3) * 32768 + (xb[t] ^ (ks << 6)));
#pragma unroll
        for (int t = 0; t < 4; ++t) { acc[0][t] = MFMA(af[0][2 * i + ks], xf[t], acc[0][t]); acc[1][t] = MFMA(af[1][2 * i + ks], xf[t], acc[1][t]); }
      }
    }
    BAR();
#pragma unroll
    for (int t = 1; t < 4; ++t) {
      ldsp d = lds + ((((t + kq) & 3) * 3 + (t - 1)) * 2 + ng) * 2048 + lane * 16;
      *(LAS f32x4*)d = acc[0][t]; *(LAS f32x4*)(d + 1024) = acc[1][t];
    }
    BAR();
    f32x4 r0 = acc[0][0], r1 = acc[1][0];
#pragma unroll
    for (int t = 1; t < 4; ++t) {
      ldsp d = lds + ((kq * 3 + (t - 1)) * 2 + ng) * 2048 + lane * 16;
      r0 += *(LAS f32x4*)d; r1 += *(LAS f32x4*)(d + 1024);
    }
    u32x2 o0, o1;
    o0.x = cvtpk(r0[0] + bflo(bb0.x) + bflo(hr0.x), r0[1] + bfhi(bb0.x) + bfhi(hr0.x)); o0.y = cvtpk(r0[2] + bflo(bb0.y) + bflo(hr0.y), r0[3] + bfhi(bb0.y) + bfhi(hr0.y));
    o1.x = cvtpk(r1[0] + bflo(bb1.x) + bflo(hr1.x), r1[1] + bfhi(bb1.x) + bfhi(hr1.x)); o1.y = cvtpk(r1[2] + bflo(bb1.y) + bflo(hr1.y), r1[3] + bfhi(bb1.y) + bfhi(hr1.y));
    GAS char* op = (GAS char*)p.out + (size_t)tokf * 896 + (col0 + 32 * ng + 4 * g) * 2;
    *(GAS u32x2*)op = o0;
    if (2 * ng + 1 < nblk) *(GAS u32x2*)(op + 32) = o1;
    STAMP(15);
  }
}

extern "C" void kernel_launch(void* const* d_in, const int* in_sizes, int n_in, void* d_out, int out_size, void* d_ws, size_t ws_size, hipStream_t stream) {
  Params p;
  p.x = (const char*)d_in[0]; p.wqkv = (const char*)d_in[1]; p.wo = (const char*)d_in[2];
  p.w1 = (const char*)d_in[4]; p.b1 = (const char*)d_in[5]; p.w2 = (const char*)d_in[6]; p.b2 = (const char*)d_in[7];
  p.out = (char*)d_out; p.ws = (char*)d_ws;
  (void)hipFuncSetAttribute((const void*)fused_block, hipFuncAttributeMaxDynamicSharedMemorySize, LDS_BYTES);
  hipLaunchKernelGGL(fused_block, dim3(256), dim3(512), LDS_BYTES, stream, p);
}
```

```cpp
#include <hip/hip_runtime.h>
#include <hip/hip_bf16.h>
#include <stdint.h>

#define LAS __attribute__((address_space(3)))
#define GAS __attribute__((address_space(1)))
typedef short bf16x8 __attribute__((ext_vector_type(8)));
typedef short s16x4 __attribute__((ext_vector_type(4)));
typedef short v4i16_t __attribute__((ext_vector_type(4)));
typedef float f32x4 __attribute__((ext_vector_type(4)));
typedef float f32x2 __attribute__((ext_vector_type(2)));
typedef unsigned u32x4 __attribute__((ext_vector_type(4)));
typedef unsigned u32x2 __attribute__((ext_vector_type(2)));
typedef __bf16 bf16x2_t __attribute__((ext_vector_type(2)));
typedef LAS char* ldsp;

constexpr int SEQ = 2048, DM = 448, FF = 1792;
constexpr size_t WS_CTL = 0;
constexpr size_t WS_KV = 65536;
constexpr size_t WS_O = WS_KV + 256 * 16384;
constexpr size_t WS_H = WS_O + 256 * 8192;
constexpr size_t WS_H1 = WS_H + (size_t)SEQ * 896;
constexpr size_t WS_ST = WS_H1 + (size_t)SEQ * 3584;
constexpr size_t WS_DBG = WS_ST + (size_t)SEQ * 32;
constexpr int LDS_BYTES = 160 * 1024;
constexpr int MISC = 147456;
constexpr int M_STATS = MISC, M_CSB = MISC + 1024, M_PART = MISC + 1536, M_PF = MISC + 2560, M_TILE = MISC + 3072;
constexpr unsigned SPIN_CAP = 1u << 16;

#define FLAG_IDX(ph, c) (64 + ((ph) * 16 + (c)) * 32)

#define WAITV(n) asm volatile("s_waitcnt vmcnt(" #n ")" ::: "memory")
#define BAR() do { asm volatile("s_waitcnt lgkmcnt(0)" ::: "memory"); __builtin_amdgcn_s_barrier(); asm volatile("" ::: "memory"); } while (0)
#define SCHED() __builtin_amdgcn_sched_barrier(0)
#define RAWBAR() __builtin_amdgcn_s_barrier()
#define MFMA(a, b, c) __builtin_amdgcn_mfma_f32_16x16x32_bf16((a), (b), (c), 0, 0, 0)

__device__ __forceinline__ unsigned cvtpk(float lo, float hi) { f32x2 v = {lo, hi}; bf16x2_t b = __builtin_convertvector(v, bf16x2_t); return __builtin_bit_cast(unsigned, b); }
__device__ __forceinline__ float dot2(unsigned a, unsigned b, float c) { return __builtin_amdgcn_fdot2_f32_bf16(__builtin_bit_cast(bf16x2_t, a), __builtin_bit_cast(bf16x2_t, b), c, false); }
__device__ __forceinline__ float bflo(unsigned u) { return __uint_as_float(u << 16); }
__device__ __forceinline__ float bfhi(unsigned u) { return __uint_as_float(u & 0xffff0000u); }
__device__ __forceinline__ bf16x8 ld128(ldsp p) { return *(const LAS bf16x8*)p; }
__device__ __forceinline__ s16x4 ldtr(ldsp p) { return __builtin_bit_cast(s16x4, __builtin_amdgcn_ds_read_tr16_b64_v4i16((LAS v4i16_t*)p)); }
__device__ __forceinline__ bf16x8 cat(s16x4 lo, s16x4 hi) { return __builtin_shufflevector(lo, hi, 0, 1, 2, 3, 4, 5, 6, 7); }
__device__ __forceinline__ int rfl(int x) { return __builtin_amdgcn_readfirstlane(x); }
__device__ __forceinline__ void dma16(unsigned voff, const void* sbase, unsigned lds) {
  unsigned keep;
  asm volatile("s_mov_b32 %0, m0\n\ts_mov_b32 m0, %3\n\ts_nop 0\n\tglobal_load_lds_dwordx4 %1, %2\n\ts_mov_b32 m0, %0" : "=&s"(keep) : "v"(voff), "s"(sbase), "s"(lds) : "memory");
}
__device__ __forceinline__ void dma16_sc1(unsigned voff, const void* sbase, unsigned lds) {
  unsigned keep;
  asm volatile("s_mov_b32 %0, m0\n\ts_mov_b32 m0, %3\n\ts_nop 0\n\tglobal_load_lds_dwordx4 %1, %2 sc1\n\ts_mov_b32 m0, %0" : "=&s"(keep) : "v"(voff), "s"(sbase), "s"(lds) : "memory");
}
__device__ __forceinline__ void pf4(unsigned voff, const void* sbase, unsigned lds) {
  unsigned keep;
  asm volatile("s_mov_b32 %0, m0\n\ts_mov_b32 m0, %3\n\ts_nop 0\n\tglobal_load_lds_dword %1, %2\n\ts_mov_b32 m0, %0" : "=&s"(keep) : "v"(voff), "s"(sbase), "s"(lds) : "memory");
}
__device__ __forceinline__ void st128_sc1(unsigned voff, const void* sbase, u32x4 v) {
  asm volatile("global_store_dwordx4 %0, %1, %2 sc1\n\ts_nop 1" ::"v"(voff), "v"(v), "s"(sbase) : "memory");
}
__device__ __forceinline__ void st64_sc1(unsigned voff, const void* sbase, u32x2 v) {
  asm volatile("global_store_dwordx2 %0, %1, %2 sc1\n\ts_nop 1" ::"v"(voff), "v"(v), "s"(sbase) : "memory");
}
__device__ __forceinline__ void st32_sc1(const void* sbase, unsigned v) {
  asm volatile("global_store_dword %0, %1, %2 sc1\n\ts_nop 1" ::"v"(0u), "v"(v), "s"(sbase) : "memory");
}
__device__ __forceinline__ float red_g_sum(float v) {
  auto a = __builtin_amdgcn_permlane16_swap(__float_as_uint(v), __float_as_uint(v), false, false);
  v = __uint_as_float(a[0]) + __uint_as_float(a[1]);
  auto b = __builtin_amdgcn_permlane32_swap(__float_as_uint(v), __float_as_uint(v), false, false);
  return __uint_as_float(b[0]) + __uint_as_float(b[1]);
}
__device__ __forceinline__ float red_g_max(float v) {
  auto a = __builtin_amdgcn_permlane16_swap(__float_as_uint(v), __float_as_uint(v), false, false);
  v = fmaxf(__uint_as_float(a[0]), __uint_as_float(a[1]));
  auto b = __builtin_amdgcn_permlane32_swap(__float_as_uint(v), __float_as_uint(v), false, false);
  return fmaxf(__uint_as_float(b[0]), __uint_as_float(b[1]));
}
__device__ __forceinline__ void poll_flags(const unsigned* base, unsigned voff, unsigned epoch) {
#pragma unroll 1
  for (unsigned spins = 0; spins < SPIN_CAP; ++spins) {
    unsigned v;
    asm volatile("global_load_dword %0, %1, %2 sc1\n\ts_waitcnt vmcnt(0)" : "=&v"(v) : "v"(voff), "s"(base) : "memory");
    if (__all(v == epoch)) break;
    __builtin_amdgcn_s_sleep(1);
  }
}

struct Params {
  const char* x; const char* wqkv; const char* wo; const char* w1; const char* b1; const char* w2; const char* b2;
  char* out; char* ws;
};

#define STAMP(i) do {} while (0)

__global__ void __launch_bounds__(512, 2) fused_block(Params p) {
  extern __shared__ __attribute__((aligned(16))) char smem[];
  ldsp lds = (ldsp)smem;
  const unsigned lds0 = (unsigned)(uintptr_t)lds;
  const int tid = threadIdx.x, lane = tid & 63, w = rfl(tid >> 6);
  const int l15 = lane & 15, g = lane >> 4, q4 = l15 >> 2, p4 = l15 & 3;
  const int b = blockIdx.x, r_ = b >> 3;
  const int c = 2 * (b & 7) + (r_ >> 4), j = r_ & 15;
  const unsigned* ctl = (const unsigned*)(p.ws + WS_CTL);
  unsigned epoch = 0;
  STAMP(0);
  if (w == 0 || w == 7) epoch = (unsigned)rfl((int)__hip_atomic_load((const GAS unsigned*)ctl, __ATOMIC_RELAXED, __HIP_MEMORY_SCOPE_AGENT)) + 1u;
  const unsigned ONES = 0x3f803f80u;
  const bf16x8 onesf = __builtin_bit_cast(bf16x8, (u32x4{ONES, ONES, ONES, ONES}));

  const int ns = j & 3, col4 = (ns < 2) ? 128 * ns : 256 + 96 * (ns - 2), ngr = (ns < 2) ? 4 : 3;
  const int pr = w & 3, wh = w >> 2;
  const int prc = (pr < ngr) ? pr : 0;
  const int th5 = j >> 3, cs5 = j & 7, col0 = (cs5 < 4) ? 64 * cs5 : 256 + 48 * (cs5 - 4), nblk = (cs5 < 4) ? 4 : 3;
  const int ng5 = w & 1, kq = w >> 1;
  u32x2 xr0, xr1, bb0, bb1, b20, b21;
  {
    const int tok = 128 * c + 32 * (j >> 2) + 16 * wh + l15;
    const GAS char* xp = (const GAS char*)p.x + (size_t)tok * 896 + (col4 + 32 * prc + 4 * g) * 2; xr0 = *(const GAS u32x2*)xp; xr1 = *(const GAS u32x2*)(xp + 32);
    const GAS char* bp = (const GAS char*)p.b1 + (448 * (j >> 2) + col4 + 32 * prc + 4 * g) * 2; bb0 = *(const GAS u32x2*)bp; bb1 = *(const GAS u32x2*)(bp + 32);
    const int t0 = col0 / 16 + 2 * ng5, t1 = (2 * ng5 + 1 < nblk) ? t0 + 1 : t0;
    b20 = *(const GAS u32x2*)((const GAS char*)p.b2 + (16 * t0 + 4 * g) * 2); b21 = *(const GAS u32x2*)((const GAS char*)p.b2 + (16 * t1 + 4 * g) * 2);
  }

  constexpr int KIMG = 0, VIMG = 24576, QIMG = 49152, WI = 32768;
  unsigned wvo[2];
  auto w256_setup = [&](unsigned rowstride) {
    const int rin = lane >> 4, pos = lane & 15;
#pragma unroll
    for (int par = 0; par < 2; ++par) { int blk = (pos >> 1) ^ (4 * par + rin); if (blk >= 2 * ngr) blk -= 2; wvo[par] = (unsigned)(rin * rowstride + blk * 32 + (pos & 1) * 16); }
  };
  {
    const int h = j;
    constexpr int SLOT = 28672, WOFF = 16384;
    unsigned voff0, voff1 = 0, voff2 = 0;
    const char* gbase; unsigned ldst; unsigned kstep; unsigned istep = 0;
    if (w < 4) {
      voff0 = (unsigned)((32 * w + (lane >> 3)) * 896 + (((lane & 7) ^ ((lane >> 3) & 7)) << 4));
      gbase = p.x + (size_t)c * 128 * 896; ldst = w * 4096; kstep = 128; istep = 8 * 896;
    } else {
      unsigned vo[3];
#pragma unroll
      for (int ii = 0; ii < 3; ++ii) {
        const int u = 192 * (w - 4) + 64 * ii + lane, row = u / 12, rem = u - 12 * row;
        const int piece = rem >> 2, c16 = rem & 3, hb = (c16 >> 1) ^ ((row >> 3) & 1);
        vo[ii] = (unsigned)(row * 2688 + piece * 896 + hb * 32 + (c16 & 1) * 16);
      }
      voff0 = vo[0]; voff1 = vo[1]; voff2 = vo[2];
      gbase = p.wqkv + 56 * h - 8 * (h & 1); ldst = WOFF + (w - 4) * 3072; kstep = 64 * 2688;
    }
    auto issue = [&](int kc, int slot) {
      const char* gb = gbase + (size_t)kc * kstep; const unsigned ld = lds0 + slot * SLOT + ldst;
      if (w < 4) {
#pragma unroll
        for (int ii = 0; ii < 4; ++ii) dma16(voff0, gb + ii * istep, ld + ii * 1024);
      } else { dma16(voff0, gb, ld); dma16(voff1, gb, ld + 1024); dma16(voff2, gb, ld + 2048); }
    };
#pragma unroll
    for (int s = 0; s < 3; ++s) { issue(s, s); RAWBAR(); }
    {
      const int ln = (b * 8 + w) * 14 + (lane < 14 ? lane : 13);
      const char* pb = p.wo; unsigned o = (unsigned)ln * 128u;
      if (ln >= 3136) { pb = p.w1; o = (unsigned)(ln - 3136) * 128u; }
      if (ln >= 3136 + 12544) { pb = p.w2; o = (unsigned)(ln - 3136 - 12544) * 128u; if (ln >= 3136 + 2 * 12544) o = 0; }
      const unsigned long long a = (unsigned long long)(uintptr_t)pb + o;
      unsigned keep;
      asm volatile("s_mov_b32 %0, m0\n\ts_mov_b32 m0, %2\n\ts_nop 0\n\tglobal_load_lds_dword %1, off\n\ts_mov_b32 m0, %0" : "=&s"(keep) : "v"(a), "s"(lds0 + M_PF) : "memory");
    }

    const int tg = w >> 1, ng = w & 1;
    const int e0 = (tg == 3) ? 0 : tg;
    f32x4 acc[3][2], asum = {0, 0, 0, 0}, asq = {0, 0, 0, 0}, acs = {0, 0, 0, 0};
#pragma unroll
    for (int e = 0; e < 3; ++e) { acc[e][0] = f32x4{0, 0, 0, 0}; acc[e][1] = f32x4{0, 0, 0, 0}; }
    int xb[2], ab[3], nts[3];
    xb[0] = (32 * tg + 16 * ng + l15) * 128 + ((g ^ (l15 & 7)) << 4);
    xb[1] = (32 * tg + 16 * (ng ^ 1) + l15) * 128 + ((g ^ (l15 & 7)) << 4);
#pragma unroll
    for (int e = 0; e < 3; ++e) { int t = e0 + e; if (t >= 3) t -= 3; nts[e] = 3 * ng + t; ab[e] = WOFF + (8 * g + q4) * 192 + (((nts[e] * 32) ^ ((g & 1) << 5))) + 8 * p4; }
    bf16x8 xf[2][2], af[2][3];
    auto rdf = [&](int st, int buf) {
      ldsp sl = lds + ((st >> 1) % 5) * SLOT; const int ks = st & 1;
      xf[buf][0] = ld128(sl + (xb[0] ^ (ks << 6))); xf[buf][1] = ld128(sl + (xb[1] ^ (ks << 6)));
#pragma unroll
      for (int e = 0; e < 3; ++e) af[buf][e] = cat(ldtr(sl + ab[e] + (32 * ks) * 192), ldtr(sl + ab[e] + (32 * ks + 4) * 192));
    };
    if (w < 4) WAITV(9); else WAITV(7);
    BAR();
    rdf(0, 0);
#pragma unroll
    for (int st = 0; st < 14; ++st) {
      const int kc = st >> 1;
      if ((st & 1) && st < 13) {
        if (w < 4) { if (kc + 1 <= 2) WAITV(9); else if (kc + 3 <= 6) WAITV(8); else if (kc + 2 <= 6) WAITV(4); else WAITV(0); }
        else { if (kc + 1 <= 2) WAITV(4); else if (kc + 2 <= 6) WAITV(3); else WAITV(0); }
        BAR();
      }
      if (st < 13) rdf(st + 1, (st + 1) & 1);
      if (!(st & 1) && kc + 3 <= 6 && w < 4) issue(kc + 3, (kc + 3) % 5);
      SCHED();
      const int cb = st & 1;
#pragma unroll
      for (int e = 0; e < 3; ++e) { acc[e][0] = MFMA(af[cb][e], xf[cb][0], acc[e][0]); acc[e][1] = MFMA(af[cb][e], xf[cb][1], acc[e][1]); }
      asum = MFMA(onesf, xf[cb][0], asum); asq = MFMA(xf[cb][0], xf[cb][0], asq); acs = MFMA(af[cb][0], onesf, acs);
      SCHED();
      if ((st & 1) && kc + 3 <= 6 && w >= 4) issue(kc + 3, (kc + 3) % 5);
    }
    asm volatile("" : "+v"(xr0), "+v"(xr1), "+v"(bb0), "+v"(bb1), "+v"(b20), "+v"(b21));
    STAMP(1);
    LAS f32x2* stats = (LAS f32x2*)(lds + M_STATS);
    LAS float* csb = (LAS float*)(lds + M_CSB);
    {
      const int r = l15 & 3;
      const float dq = (r == 0) ? asq[0] : (r == 1) ? asq[1] : (r == 2) ? asq[2] : asq[3];
      const float mean = asum[0] * (1.f / 448.f), var = fmaxf(dq * (1.f / 448.f) - mean * mean, 0.f);
      if (g == q4) stats[32 * tg + 16 * ng + l15] = f32x2{mean, rsqrtf(var + 1e-5f)};
      if (tg < 3 && l15 == 0) *(LAS f32x4*)(csb + 16 * nts[0] + 4 * g) = acs;
    }
    BAR();
    f32x2 st[2]; st[0] = stats[32 * tg + 16 * ng + l15]; st[1] = stats[32 * tg + 16 * (ng ^ 1) + l15];
#pragma unroll
    for (int e = 0; e < 3; ++e) {
      const int nt = nts[e], which = nt >> 1, par = nt & 1;
      const f32x4 cv = *(LAS f32x4*)(csb + 16 * nt + 4 * g);
      const bool pad = (h & 1) ? (par == 0 && g == 0) : (par == 1 && g == 3);
      const float sc = (which == 0) ? 0.27264657f : 1.f;
      const int ibase = (which == 0) ? QIMG : (which == 1) ? KIMG + 16384 : VIMG + 16384;
      const int cch = (which == 2) ? 2 * par + (g >> 1) : g, sub = (which == 2) ? (g & 1) * 8 : par * 8;
      const unsigned padx = (which == 2) ? 0x00003f80u : 0u;
#pragma unroll
      for (int tt = 0; tt < 2; ++tt) {
        const int row = 32 * tg + 16 * (tt ^ ng) + l15;
        f32x4 v = (acc[e][tt] - st[tt].x * cv) * (st[tt].y * sc);
        u32x2 o; o.x = cvtpk(v[0], v[1]); o.y = cvtpk(v[2], v[3]);
        o.x = pad ? padx : o.x; o.y = pad ? 0u : o.y;
        *(LAS u32x2*)(lds + ibase + row * 64 + ((cch ^ ((row >> 1) & 3)) << 4) + sub) = o;
      }
    }
    BAR();
    if (c < 15) {
      const char* blob = p.ws + WS_KV + (size_t)(c * 16 + h) * 16384;
      const u32x4 v0 = *(LAS u32x4*)(lds + KIMG + 16384 + tid * 16), v1 = *(LAS u32x4*)(lds + VIMG + 16384 + tid * 16);
      st128_sc1(tid * 16, blob, v0); st128_sc1(8192 + tid * 16, blob, v1);
    }
    if (c < 2) {
      const u32x4 z = {0, 0, 0, 0};
#pragma unroll
      for (int i = 0; i < 2; ++i) { *(LAS u32x4*)(lds + KIMG + tid * 16 + i * 8192) = z; *(LAS u32x4*)(lds + VIMG + tid * 16 + i * 8192) = z; }
    }
    WAITV(0);
    BAR();
    if (tid == 0) st32_sc1(ctl + FLAG_IDX(0, c) + h, epoch);
    STAMP(2);
    w256_setup(896);
    if (w < 7) {
#pragma unroll
      for (int i = 0; i < 13; ++i) { const int ins = 24 + w + 7 * i; if (ins < 112) dma16((ins & 1) ? wvo[1] : wvo[0], p.wo + col4 * 2 + (size_t)ins * 4 * 896, lds0 + WI + ins * 1024); }
    }
  }
  {
    const int h = j;
    if (w == 7 && c >= 1) poll_flags(ctl, (unsigned)((FLAG_IDX(0, (c >= 2 && (lane & 1)) ? c - 2 : c - 1) + h) * 4), epoch);
    BAR();
    STAMP(3);
    {
      const int tl = w >> 2, src = c - 2 + tl, pc = (w & 3) * 2048;
      if (src >= 0) {
        const char* blob = p.ws + WS_KV + (size_t)(src * 16 + h) * 16384;
        dma16_sc1(lane * 16, blob + pc, lds0 + KIMG + tl * 8192 + pc); dma16_sc1(lane * 16, blob + pc + 1024, lds0 + KIMG + tl * 8192 + pc + 1024);
        dma16_sc1(lane * 16, blob + 8192 + pc, lds0 + VIMG + tl * 8192 + pc); dma16_sc1(lane * 16, blob + 8192 + pc + 1024, lds0 + VIMG + tl * 8192 + pc + 1024);
      }
    }
    const int sw = (l15 >> 1) & 3;
    const bf16x8 qf = ld128(lds + QIMG + (16 * w + l15) * 64 + ((g ^ sw) << 4));
    WAITV(0);
    BAR();
    STAMP(4);
    const int tmin = (c >= 2) ? 0 : (2 - c) * 8;
    f32x4 sc[17];
    const float NEG = -__builtin_inff();
    ldsp kb = lds + KIMG + (16 * w + l15) * 64 + ((g ^ sw) << 4);
    {
      bf16x8 kf[17];
#pragma unroll
      for (int u = 0; u < 17; ++u) kf[u] = ld128(kb + u * 1024);
      SCHED();
#pragma unroll
      for (int u = 0; u < 17; ++u) { const float ci = (w + u >= tmin) ? 0.f : NEG; sc[u] = MFMA(kf[u], qf, (f32x4{ci, ci, ci, ci})); }
      SCHED();
    }
    const int vsw = (2 * g + (q4 >> 1)) & 3;
    ldsp vb0 = lds + VIMG + (16 * w + 4 * g + q4) * 64 + (((p4 >> 1) ^ vsw) << 4) + (p4 & 1) * 8;
    ldsp vb1 = lds + VIMG + (16 * w + 4 * g + q4) * 64 + (((2 + (p4 >> 1)) ^ vsw) << 4) + (p4 & 1) * 8;
    bf16x8 vf[9][2];
#pragma unroll
    for (int uu = 0; uu < 9; ++uu) {
      const int ua = 2 * uu, ub = (uu < 8) ? 2 * uu + 1 : 16;
      vf[uu][0] = cat(ldtr(vb0 + ua * 1024), ldtr(vb0 + ub * 1024)); vf[uu][1] = cat(ldtr(vb1 + ua * 1024), ldtr(vb1 + ub * 1024));
    }
    SCHED();
#pragma unroll
    for (int r = 0; r < 4; ++r) { sc[0][r] = (4 * g + r <= l15) ? NEG : sc[0][r]; sc[16][r] = (4 * g + r > l15) ? NEG : sc[16][r]; }
    float m = fmaxf(fmaxf(sc[0][0], sc[0][1]), fmaxf(sc[0][2], sc[0][3]));
#pragma unroll
    for (int u = 1; u < 17; ++u) m = fmaxf(fmaxf(m, fmaxf(sc[u][0], sc[u][1])), fmaxf(sc[u][2], sc[u][3]));
    m = red_g_max(m);
    unsigned pk[17][2];
#pragma unroll
    for (int u = 0; u < 17; ++u) {
      pk[u][0] = cvtpk(__builtin_amdgcn_exp2f(sc[u][0] - m), __builtin_amdgcn_exp2f(sc[u][1] - m));
      pk[u][1] = cvtpk(__builtin_amdgcn_exp2f(sc[u][2] - m), __builtin_amdgcn_exp2f(sc[u][3] - m));
    }
    f32x4 oa[2] = {f32x4{0, 0, 0, 0}, f32x4{0, 0, 0, 0}};
#pragma unroll
    for (int uu = 0; uu < 9; ++uu) {
      const int ua = 2 * uu, ub = (uu < 8) ? 2 * uu + 1 : 16;
      u32x4 pu; pu.x = pk[ua][0]; pu.y = pk[ua][1];
      if (uu < 8) { pu.z = pk[ub][0]; pu.w = pk[ub][1]; } else { pu.z = 0; pu.w = 0; }
      const bf16x8 pf = __builtin_bit_cast(bf16x8, pu);
      oa[0] = MFMA(vf[uu][0], pf, oa[0]); oa[1] = MFMA(vf[uu][1], pf, oa[1]);
    }
    float l = (h & 1) ? oa[0][0] : oa[1][0];
    l = __shfl(l, (h & 1) ? l15 : 48 + l15);
    const float inv = __builtin_amdgcn_rcpf(l);
    u32x4 ov;
    ov.x = cvtpk(oa[0][0] * inv, oa[0][1] * inv); ov.y = cvtpk(oa[0][2] * inv, oa[0][3] * inv);
    ov.z = cvtpk(oa[1][0] * inv, oa[1][1] * inv); ov.w = cvtpk(oa[1][2] * inv, oa[1][3] * inv);
    { const bool z0 = (h & 1) && g == 0, z1 = !(h & 1) && g == 3; ov.x = z0 ? 0u : ov.x; ov.y = z0 ? 0u : ov.y; ov.z = z1 ? 0u : ov.z; ov.w = z1 ? 0u : ov.w; }
    st128_sc1((unsigned)((16 * w + l15) * 64 + ((g ^ sw) << 4)), p.ws + WS_O + (size_t)(c * 16 + h) * 8192, ov);
    BAR();
#pragma unroll
    for (int i = 0; i < 3; ++i) { const int ins = 3 * w + i; dma16((ins & 1) ? wvo[1] : wvo[0], p.wo + col4 * 2 + (size_t)ins * 4 * 896, lds0 + WI + ins * 1024); }
    WAITV(3);
    BAR();
    if (tid == 0) st32_sc1(ctl + FLAG_IDX(1, c) + h, epoch);
    STAMP(5);
  }
  {
    constexpr int OT = 0;
    const int tq = j >> 2, tt = wh;
    const int tok = 128 * c + 32 * tq + 16 * tt + l15;
    if (w == 7) poll_flags(ctl, (unsigned)((FLAG_IDX(1, c) + l15) * 4), epoch);
    BAR();
    STAMP(6);
#pragma unroll
    for (int i = 0; i < 4; ++i) { const int pc = 4 * w + i, hd = pc >> 1;
      dma16_sc1(lane * 16, p.ws + WS_O + (size_t)(c * 16 + hd) * 8192 + tq * 2048 + (pc & 1) * 1024, lds0 + OT + pc * 1024); }
    WAITV(0);
    BAR();
    STAMP(7);
    {
      f32x4 a0 = {0, 0, 0, 0}, a1 = {0, 0, 0, 0};
      const int rx = 4 * (g & 1) + q4;
      ldsp ab0 = lds + WI + (4 * g + q4) * 256 + (((2 * prc) ^ rx) << 5) + 8 * p4;
      ldsp ab1 = lds + WI + (4 * g + q4) * 256 + (((2 * prc + 1) ^ rx) << 5) + 8 * p4;
      ldsp ab0h = ab0 + 65536, ab1h = ab1 + 65536;
      ldsp ob = lds + OT + (16 * tt + l15) * 64 + ((g ^ ((l15 >> 1) & 3)) << 4);
      bf16x8 of[4], f0[4], f1[4];
      auto rd = [&](int s, int buf) {
        const int rb = (28 * s - 4 * (s & 1)) * 256;
        of[buf] = ld128(ob + s * 2048);
        if (rb + 4096 < 65536) { f0[buf] = cat(ldtr(ab0 + rb), ldtr(ab0 + rb + 4096)); f1[buf] = cat(ldtr(ab1 + rb), ldtr(ab1 + rb + 4096)); }
        else { f0[buf] = cat(ldtr(ab0h + (rb - 65536)), ldtr(ab0h + (rb - 65536 + 4096))); f1[buf] = cat(ldtr(ab1h + (rb - 65536)), ldtr(ab1h + (rb - 65536 + 4096))); }
      };
      rd(0, 0); rd(1, 1); rd(2, 2);
#pragma unroll
      for (int s = 0; s < 16; ++s) {
        if (s + 3 < 16) rd(s + 3, (s + 3) & 3);
        SCHED();
        a0 = MFMA(f0[s & 3], of[s & 3], a0); a1 = MFMA(f1[s & 3], of[s & 3], a1);
        SCHED();
      }
      u32x4 hv;
      hv.x = cvtpk(a0[0] + bflo(xr0.x), a0[1] + bfhi(xr0.x)); hv.y = cvtpk(a0[2] + bflo(xr0.y), a0[3] + bfhi(xr0.y));
      hv.z = cvtpk(a1[0] + bflo(xr1.x), a1[1] + bfhi(xr1.x)); hv.w = cvtpk(a1[2] + bflo(xr1.y), a1[3] + bfhi(xr1.y));
      if (pr < ngr) st128_sc1((unsigned)(tok * 896 + (col4 / 32 + pr) * 64 + g * 16), p.ws + WS_H, hv);
      float s1 = 0.f, s2 = 0.f;
#pragma unroll
      for (int i = 0; i < 4; ++i) { s1 = dot2(hv[i], ONES, s1); s2 = dot2(hv[i], hv[i], s2); }
      s1 = red_g_sum(s1); s2 = red_g_sum(s2);
      if (pr >= ngr) { s1 = 0.f; s2 = 0.f; }
      if (g == 0) ((LAS f32x2*)(lds + M_PART))[(16 * tt + l15) * 4 + pr] = f32x2{s1, s2};
    }
    BAR();
    if (tid < 32) {
      const f32x4 u0 = *(LAS f32x4*)(lds + M_PART + tid * 32), u1 = *(LAS f32x4*)(lds + M_PART + tid * 32 + 16);
      st64_sc1((unsigned)((128 * c + 32 * tq + tid) * 32 + ns * 8), p.ws + WS_ST,
               u32x2{__float_as_uint((u0[0] + u0[2]) + (u1[0] + u1[2])), __float_as_uint((u0[1] + u0[3]) + (u1[1] + u1[3]))});
    }
    const char* w1b = p.w1 + (448 * (j >> 2) + col4) * 2;
    w256_setup(3584);
    if (w < 7) {
#pragma unroll
      for (int i = 0; i < 4; ++i) { const int ins = w + 7 * i; dma16((ins & 1) ? wvo[1] : wvo[0], w1b + (size_t)ins * 4 * 3584, lds0 + ins * 1024); }
      WAITV(4);
    } else WAITV(0);
    BAR();
    if (tid == 0) st32_sc1(ctl + FLAG_IDX(2, c) + j, epoch);
    STAMP(8);
    if (w < 7) {
#pragma unroll
      for (int i = 4; i < 16; ++i) { const int ins = w + 7 * i; dma16((ins & 1) ? wvo[1] : wvo[0], w1b + (size_t)ins * 4 * 3584, lds0 + ins * 1024); }
    }
  }
  {
    const int f0 = 448 * (j >> 2) + col4, th = wh;
    if (w == 7) poll_flags(ctl, (unsigned)((FLAG_IDX(2, c) + l15) * 4), epoch);
    WAITV(0);
    BAR();
    STAMP(9);
    const unsigned vo = (unsigned)((16 * w + (lane >> 3)) * 896 + (((lane & 7) ^ ((lane >> 3) & 7)) << 4));
    const char* hb = p.ws + WS_H + (size_t)c * 128 * 896;
    auto caddr = [&](int kc) { return (kc < 2) ? 114688 + kc * 16384 : (kc - 2) * 16384; };
    auto issue4 = [&](int kc) { dma16_sc1(vo, hb + kc * 128, lds0 + caddr(kc) + w * 2048); dma16_sc1(vo, hb + kc * 128 + 8 * 896, lds0 + caddr(kc) + w * 2048 + 1024); };
    dma16_sc1((unsigned)((lane & 31) * 16), p.ws + WS_ST + (size_t)(128 * c + 16 * w) * 32, lds0 + M_TILE + w * 1024);
    issue4(0); RAWBAR(); issue4(1);
    bf16x8 af[2][14];
    f32x4 c0 = {0, 0, 0, 0}, c1 = {0, 0, 0, 0};
    {
      const int rx = 4 * (g & 1) + q4;
      ldsp ab0 = lds + (4 * g + q4) * 256 + (((2 * prc) ^ rx) << 5) + 8 * p4;
      ldsp ab1 = lds + (4 * g + q4) * 256 + (((2 * prc + 1) ^ rx) << 5) + 8 * p4;
      ldsp ab0h = ab0 + 65536, ab1h = ab1 + 65536;
#pragma unroll
      for (int s = 0; s < 14; ++s) {
        if (s < 8) { af[0][s] = cat(ldtr(ab0 + s * 8192), ldtr(ab0 + s * 8192 + 4096)); af[1][s] = cat(ldtr(ab1 + s * 8192), ldtr(ab1 + s * 8192 + 4096)); }
        else { af[0][s] = cat(ldtr(ab0h + (s - 8) * 8192), ldtr(ab0h + (s - 8) * 8192 + 4096)); af[1][s] = cat(ldtr(ab1h + (s - 8) * 8192), ldtr(ab1h + (s - 8) * 8192 + 4096)); }
      }
#pragma unroll
      for (int s = 0; s < 14; ++s) { c0 = MFMA(af[0][s], onesf, c0); c1 = MFMA(af[1][s], onesf, c1); }
    }
    BAR();
    issue4(2); RAWBAR(); issue4(3);
    f32x4 acc[2][4];
#pragma unroll
    for (int t = 0; t < 4; ++t) { acc[0][t] = f32x4{0, 0, 0, 0}; acc[1][t] = f32x4{0, 0, 0, 0}; }
    int xb[4];
#pragma unroll
    for (int t = 0; t < 4; ++t) xb[t] = (64 * th + 16 * t + l15) * 128 + ((g ^ (l15 & 7)) << 4);
    bf16x8 xf[2][4];
    auto rdf = [&](int st, int buf) {
      const int ca = caddr(st >> 1);
#pragma unroll
      for (int t = 0; t < 4; ++t) xf[buf][t] = ld128(lds + ca + (xb[t] ^ ((st & 1) << 6)));
    };
    WAITV(6); BAR();
    STAMP(10);
    rdf(0, 0);
#pragma unroll
    for (int st = 0; st < 14; ++st) {
      const int kc = st >> 1;
      if ((st & 1) && st < 13) {
        if (w < 4) { if (kc + 4 <= 6) WAITV(6); else if (kc + 3 <= 6) WAITV(4); else if (kc + 2 <= 6) WAITV(2); else WAITV(0); }
        else { if (kc + 3 <= 6) WAITV(4); else if (kc + 2 <= 6) WAITV(2); else WAITV(0); }
        BAR();
      }
      if (st < 13) rdf(st + 1, (st + 1) & 1);
      if (!(st & 1) && kc + 4 <= 6 && w < 4) issue4(kc + 4);
      SCHED();
#pragma unroll
      for (int t = 0; t < 4; ++t) { acc[0][t] = MFMA(af[0][st], xf[st & 1][t], acc[0][t]); acc[1][t] = MFMA(af[1][st], xf[st & 1][t], acc[1][t]); }
      SCHED();
      if ((st & 1) && kc + 4 <= 6 && w >= 4) issue4(kc + 4);
    }
    STAMP(11);
    {
      const f32x4 b0 = {bflo(bb0.x), bfhi(bb0.x), bflo(bb0.y), bfhi(bb0.y)}, b1v = {bflo(bb1.x), bfhi(bb1.x), bflo(bb1.y), bfhi(bb1.y)};
#pragma unroll
      for (int t = 0; t < 4; ++t) {
        const int row = 64 * th + 16 * t + l15;
        ldsp sp = lds + M_TILE + (row >> 4) * 1024 + (row & 15) * 32;
        const f32x4 u0 = *(LAS f32x4*)sp, u1 = *(LAS f32x4*)(sp + 16);
        const float mean = ((u0[0] + u0[2]) + (u1[0] + u1[2])) * (1.f / 448.f);
        const float var = fmaxf(((u0[1] + u0[3]) + (u1[1] + u1[3])) * (1.f / 448.f) - mean * mean, 0.f);
        const float rstd = rsqrtf(var + 1e-5f);
        f32x4 z0 = (acc[0][t] - mean * c0) * rstd + b0, z1 = (acc[1][t] - mean * c1) * rstd + b1v;
#pragma unroll
        for (int i = 0; i < 4; ++i) { z0[i] = fmaxf(z0[i], 0.f); z0[i] *= z0[i]; z1[i] = fmaxf(z1[i], 0.f); z1[i] *= z1[i]; }
        u32x4 hv; hv.x = cvtpk(z0[0], z0[1]); hv.y = cvtpk(z0[2], z0[3]); hv.z = cvtpk(z1[0], z1[1]); hv.w = cvtpk(z1[2], z1[3]);
        if (pr < ngr) st128_sc1((unsigned)((128 * c + row) * 3584 + (f0 / 32 + pr) * 64 + g * 16), p.ws + WS_H1, hv);
      }
    }
    BAR();
  }
  {
    bf16x8 af[2][14];
    const int rin = lane >> 3, pos = lane & 7;
    int blk = (pos >> 1) ^ ((rin >> 1) & 3); if (blk >= nblk) blk = nblk - 1;
    const unsigned wv = (unsigned)(rin * 896 + blk * 32 + (pos & 1) * 16);
    auto stage = [&](int rd, int i0, int i1) {
#pragma unroll
      for (int i = i0; i < i1; ++i) { const int ins = w + 7 * i, kk = ins / 28, fr = 448 * kk + 224 * rd + 8 * (ins - 28 * kk);
        dma16(wv, p.w2 + (size_t)fr * 896 + col0 * 2, lds0 + ins * 1024); }
    };
    if (w < 7) { stage(0, 0, 4); WAITV(4); } else WAITV(0);
    BAR();
    if (tid == 0) st32_sc1(ctl + FLAG_IDX(3, c) + j, epoch);
    STAMP(12);
    if (w < 7) stage(0, 4, 16);
    const int rx = (2 * g + (q4 >> 1)) & 3;
    ldsp ab0 = lds + (kq * 224 + 4 * g + q4) * 128 + (((2 * ng5) ^ rx) << 5) + 8 * p4;
    ldsp ab1 = lds + (kq * 224 + 4 * g + q4) * 128 + (((2 * ng5 + 1) ^ rx) << 5) + 8 * p4;
    {
      const int m0 = col0 >> 5; int u = w * 96 + lane;
      const int r0 = u / 12, c0 = u - 12 * r0; int cc0 = m0 * 4 + c0; if (cc0 > 55) cc0 = 55;
      dma16_sc1((unsigned)(r0 * 896 + cc0 * 16), p.ws + WS_H + (size_t)(128 * c + 64 * th5) * 896, lds0 + M_TILE + w * 1536);
      if (lane < 32) { u += 64; const int r1 = u / 12, c1 = u - 12 * r1; int cc1 = m0 * 4 + c1; if (cc1 > 55) cc1 = 55;
        dma16_sc1((unsigned)(r1 * 896 + cc1 * 16), p.ws + WS_H + (size_t)(128 * c + 64 * th5) * 896, lds0 + M_TILE + w * 1536 + 1024); }
    }
    WAITV(0);
    BAR();
#pragma unroll
    for (int s = 0; s < 7; ++s) { af[0][s] = cat(ldtr(ab0 + s * 4096), ldtr(ab0 + s * 4096 + 2048)); af[1][s] = cat(ldtr(ab1 + s * 4096), ldtr(ab1 + s * 4096 + 2048)); }
    BAR();
    if (w < 7) stage(1, 0, 16);
    if (w == 7) {
      poll_flags(ctl, (unsigned)((FLAG_IDX(3, c) + l15) * 4), epoch);
      if (b == 0) {
#pragma unroll
        for (int i = 0; i < 4; ++i) poll_flags(ctl, (unsigned)((FLAG_IDX(0, 4 * i + g) + l15) * 4), epoch);
        if (lane == 0) st32_sc1(ctl, epoch);
      }
    }
    WAITV(0);
    BAR();
    STAMP(13);
    const unsigned vo = (unsigned)((32 * ng5 + (lane >> 3)) * 3584 + (((lane & 7) ^ ((lane >> 3) & 7)) << 4));
    const char* hb = p.ws + WS_H1 + (size_t)(128 * c + 64 * th5) * 3584 + kq * 896;
    auto saddr = [&](int i) { return ((i & 3) == 0) ? 114688 : ((i & 3) - 1) * 32768; };
    auto issue = [&](int i) {
      const unsigned ld = lds0 + saddr(i) + kq * 8192 + ng5 * 4096;
#pragma unroll
      for (int ii = 0; ii < 4; ++ii) dma16_sc1(vo, hb + i * 128 + ii * 8 * 3584, ld + ii * 1024);
    };
    issue(0);
#pragma unroll
    for (int s = 0; s < 7; ++s) { af[0][7 + s] = cat(ldtr(ab0 + s * 4096), ldtr(ab0 + s * 4096 + 2048)); af[1][7 + s] = cat(ldtr(ab1 + s * 4096), ldtr(ab1 + s * 4096 + 2048)); }
    BAR();
    issue(1); RAWBAR(); issue(2);
    f32x4 acc[2][4];
#pragma unroll
    for (int t = 0; t < 4; ++t) { acc[0][t] = f32x4{0, 0, 0, 0}; acc[1][t] = f32x4{0, 0, 0, 0}; }
    int xb[4];
#pragma unroll
    for (int t = 0; t < 4; ++t) xb[t] = kq * 8192 + (16 * ((t + kq) & 3) + l15) * 128 + ((g ^ (l15 & 7)) << 4);
    bf16x8 xf[2][4];
    auto rdf = [&](int st, int buf) {
      const int sa = saddr(st >> 1);
#pragma unroll
      for (int t = 0; t < 4; ++t) xf[buf][t] = ld128(lds + sa + (xb[t] ^ ((st & 1) << 6)));
    };
    WAITV(8); BAR();
    STAMP(14);
    rdf(0, 0);
#pragma unroll
    for (int st = 0; st < 14; ++st) {
      const int i = st >> 1;
      if ((st & 1) && st < 13) {
        if (w < 4) { if (i + 3 <= 6) WAITV(8); else if (i + 2 <= 6) WAITV(4); else WAITV(0); }
        else { if (i + 2 <= 6) WAITV(4); else WAITV(0); }
        BAR();
      }
      if (st < 13) rdf(st + 1, (st + 1) & 1);
      if (!(st & 1) && i + 3 <= 6 && w < 4) issue(i + 3);
      SCHED();
#pragma unroll
      for (int t = 0; t < 4; ++t) { acc[0][t] = MFMA(af[0][st], xf[st & 1][t], acc[0][t]); acc[1][t] = MFMA(af[1][st], xf[st & 1][t], acc[1][t]); }
      SCHED();
      if ((st & 1) && i + 3 <= 6 && w >= 4) issue(i + 3);
    }
    BAR();
#pragma unroll
    for (int t = 1; t < 4; ++t) {
      ldsp d = lds + ((((t + kq) & 3) * 3 + (t - 1)) * 2 + ng5) * 2048 + lane * 16;
      *(LAS f32x4*)d = acc[0][t]; *(LAS f32x4*)(d + 1024) = acc[1][t];
    }
    BAR();
    f32x4 r0 = acc[0][0], r1 = acc[1][0];
#pragma unroll
    for (int t = 1; t < 4; ++t) {
      ldsp d = lds + ((kq * 3 + (t - 1)) * 2 + ng5) * 2048 + lane * 16;
      r0 += *(LAS f32x4*)d; r1 += *(LAS f32x4*)(d + 1024);
    }
    const int tokl = 16 * kq + l15;
    const int t0 = col0 / 16 + 2 * ng5, t1 = (2 * ng5 + 1 < nblk) ? t0 + 1 : t0, m0 = col0 >> 5;
    const u32x2 hr0 = *(LAS u32x2*)(lds + M_TILE + tokl * 192 + ((t0 >> 1) - m0) * 64 + g * 16 + (t0 & 1) * 8);
    const u32x2 hr1 = *(LAS u32x2*)(lds + M_TILE + tokl * 192 + ((t1 >> 1) - m0) * 64 + g * 16 + (t1 & 1) * 8);
    u32x2 o0, o1;
    o0.x = cvtpk(r0[0] + bflo(b20.x) + bflo(hr0.x), r0[1] + bfhi(b20.x) + bfhi(hr0.x)); o0.y = cvtpk(r0[2] + bflo(b20.y) + bflo(hr0.y), r0[3] + bfhi(b20.y) + bfhi(hr0.y));
    o1.x = cvtpk(r1[0] + bflo(b21.x) + bflo(hr1.x), r1[1] + bfhi(b21.x) + bfhi(hr1.x)); o1.y = cvtpk(r1[2] + bflo(b21.y) + bflo(hr1.y), r1[3] + bfhi(b21.y) + bfhi(hr1.y));
    GAS char* op = (GAS char*)p.out + (size_t)(128 * c + 64 * th5 + tokl) * 896 + (col0 + 32 * ng5 + 4 * g) * 2;
    *(GAS u32x2*)op = o0;
    if (2 * ng5 + 1 < nblk) *(GAS u32x2*)(op + 32) = o1;
    STAMP(15);
  }
}

extern "C" void kernel_launch(void* const* d_in, const int* in_sizes, int n_in, void* d_out, int out_size, void* d_ws, size_t ws_size, hipStream_t stream) {
  Params p;
  p.x = (const char*)d_in[0]; p.wqkv = (const char*)d_in[1]; p.wo = (const char*)d_in[2];
  p.w1 = (const char*)d_in[4]; p.b1 = (const char*)d_in[5]; p.w2 = (const char*)d_in[6]; p.b2 = (const char*)d_in[7];
  p.out = (char*)d_out; p.ws = (char*)d_ws;
  (void)hipFuncSetAttribute((const void*)fused_block, hipFuncAttributeMaxDynamicSharedMemorySize, LDS_BYTES);
  hipLaunchKernelGGL(fused_block, dim3(256), dim3(512), LDS_BYTES, stream, p);
}
```

```cpp
#include <hip/hip_runtime.h>
#include <hip/hip_bf16.h>
#include <stdint.h>

#define LAS __attribute__((address_space(3)))
#define GAS __attribute__((address_space(1)))
typedef short bf16x8 __attribute__((ext_vector_type(8)));
typedef short s16x4 __attribute__((ext_vector_type(4)));
typedef short v4i16_t __attribute__((ext_vector_type(4)));
typedef float f32x4 __attribute__((ext_vector_type(4)));
typedef float f32x2 __attribute__((ext_vector_type(2)));
typedef unsigned u32x4 __attribute__((ext_vector_type(4)));
typedef unsigned u32x2 __attribute__((ext_vector_type(2)));
typedef __bf16 bf16x2_t __attribute__((ext_vector_type(2)));
typedef LAS char* ldsp;

constexpr int SEQ = 2048, DM = 448, FF = 1792;
constexpr size_t WS_CTL = 0;
constexpr size_t WS_KV = 65536;
constexpr size_t WS_O = WS_KV + 256 * 16384;
constexpr size_t WS_H = WS_O + 256 * 8192;
constexpr size_t WS_H1 = WS_H + (size_t)SEQ * 896;
constexpr size_t WS_ST = WS_H1 + (size_t)SEQ * 3584;
constexpr size_t WS_DBG = WS_ST + (size_t)SEQ * 32;
constexpr int LDS_BYTES = 160 * 1024;
constexpr int MISC = 147456;
constexpr int M_STATS = MISC, M_CSB = MISC + 1024, M_PART = MISC + 1536, M_PF = MISC + 2560, M_TILE = MISC + 3072;
constexpr unsigned SPIN_CAP = 1u << 16;

#define FLAG_IDX(ph, c) (64 + ((ph) * 16 + (c)) * 32)

#define WAITV(n) asm volatile("s_waitcnt vmcnt(" #n ")" ::: "memory")
#define BAR() do { asm volatile("s_waitcnt lgkmcnt(0)" ::: "memory"); __builtin_amdgcn_s_barrier(); asm volatile("" ::: "memory"); } while (0)
#define SCHED() __builtin_amdgcn_sched_barrier(0)
#define RAWBAR() __builtin_amdgcn_s_barrier()
#define MFMA(a, b, c) __builtin_amdgcn_mfma_f32_16x16x32_bf16((a), (b), (c), 0, 0, 0)

__device__ __forceinline__ unsigned cvtpk(float lo, float hi) { f32x2 v = {lo, hi}; bf16x2_t b = __builtin_convertvector(v, bf16x2_t); return __builtin_bit_cast(unsigned, b); }
__device__ __forceinline__ float dot2(unsigned a, unsigned b, float c) { return __builtin_amdgcn_fdot2_f32_bf16(__builtin_bit_cast(bf16x2_t, a), __builtin_bit_cast(bf16x2_t, b), c, false); }
__device__ __forceinline__ float bflo(unsigned u) { return __uint_as_float(u << 16); }
__device__ __forceinline__ float bfhi(unsigned u) { return __uint_as_float(u & 0xffff0000u); }
__device__ __forceinline__ bf16x8 ld128(ldsp p) { return *(const LAS bf16x8*)p; }
__device__ __forceinline__ s16x4 ldtr(ldsp p) { return __builtin_bit_cast(s16x4, __builtin_amdgcn_ds_read_tr16_b64_v4i16((LAS v4i16_t*)p)); }
__device__ __forceinline__ bf16x8 cat(s16x4 lo, s16x4 hi) { return __builtin_shufflevector(lo, hi, 0, 1, 2, 3, 4, 5, 6, 7); }
__device__ __forceinline__ int rfl(int x) { return __builtin_amdgcn_readfirstlane(x); }
__device__ __forceinline__ void dma16(unsigned voff, const void* sbase, unsigned lds) {
  unsigned keep;
  asm volatile("s_mov_b32 %0, m0\n\ts_mov_b32 m0, %3\n\ts_nop 0\n\tglobal_load_lds_dwordx4 %1, %2\n\ts_mov_b32 m0, %0" : "=&s"(keep) : "v"(voff), "s"(sbase), "s"(lds) : "memory");
}
__device__ __forceinline__ void dma16_sc1(unsigned voff, const void* sbase, unsigned lds) {
  unsigned keep;
  asm volatile("s_mov_b32 %0, m0\n\ts_mov_b32 m0, %3\n\ts_nop 0\n\tglobal_load_lds_dwordx4 %1, %2 sc1\n\ts_mov_b32 m0, %0" : "=&s"(keep) : "v"(voff), "s"(sbase), "s"(lds) : "memory");
}
__device__ __forceinline__ void pf4(unsigned voff, const void* sbase, unsigned lds) {
  unsigned keep;
  asm volatile("s_mov_b32 %0, m0\n\ts_mov_b32 m0, %3\n\ts_nop 0\n\tglobal_load_lds_dword %1, %2\n\ts_mov_b32 m0, %0" : "=&s"(keep) : "v"(voff), "s"(sbase), "s"(lds) : "memory");
}
__device__ __forceinline__ void st128_sc1(unsigned voff, const void* sbase, u32x4 v) {
  asm volatile("global_store_dwordx4 %0, %1, %2 sc1\n\ts_nop 1" ::"v"(voff), "v"(v), "s"(sbase) : "memory");
}
__device__ __forceinline__ void st64_sc1(unsigned voff, const void* sbase, u32x2 v) {
  asm volatile("global_store_dwordx2 %0, %1, %2 sc1\n\ts_nop 1" ::"v"(voff), "v"(v), "s"(sbase) : "memory");
}
__device__ __forceinline__ void st32_sc1(const void* sbase, unsigned v) {
  asm volatile("global_store_dword %0, %1, %2 sc1\n\ts_nop 1" ::"v"(0u), "v"(v), "s"(sbase) : "memory");
}
__device__ __forceinline__ float red_g_sum(float v) {
  auto a = __builtin_amdgcn_permlane16_swap(__float_as_uint(v), __float_as_uint(v), false, false);
  v = __uint_as_float(a[0]) + __uint_as_float(a[1]);
  auto b = __builtin_amdgcn_permlane32_swap(__float_as_uint(v), __float_as_uint(v), false, false);
  return __uint_as_float(b[0]) + __uint_as_float(b[1]);
}
__device__ __forceinline__ float red_g_max(float v) {
  auto a = __builtin_amdgcn_permlane16_swap(__float_as_uint(v), __float_as_uint(v), false, false);
  v = fmaxf(__uint_as_float(a[0]), __uint_as_float(a[1]));
  auto b = __builtin_amdgcn_permlane32_swap(__float_as_uint(v), __float_as_uint(v), false, false);
  return fmaxf(__uint_as_float(b[0]), __uint_as_float(b[1]));
}
__device__ __forceinline__ void poll_flags(const unsigned* base, unsigned voff, unsigned epoch) {
#pragma unroll 1
  for (unsigned spins = 0; spins < SPIN_CAP; ++spins) {
    unsigned v;
    asm volatile("global_load_dword %0, %1, %2 sc1\n\ts_waitcnt vmcnt(0)" : "=&v"(v) : "v"(voff), "s"(base) : "memory");
    if (__all(v == epoch)) break;
    __builtin_amdgcn_s_sleep(1);
  }
}

struct Params {
  const char* x; const char* wqkv; const char* wo; const char* w1; const char* b1; const char* w2; const char* b2;
  char* out; char* ws;
};

#define STAMP(i) do {} while (0)

__global__ void __launch_bounds__(512, 2) fused_block(Params p) {
  extern __shared__ __attribute__((aligned(16))) char smem[];
  ldsp lds = (ldsp)smem;
  const unsigned lds0 = (unsigned)(uintptr_t)lds;
  const int tid = threadIdx.x, lane = tid & 63, w = rfl(tid >> 6);
  const int l15 = lane & 15, g = lane >> 4, q4 = l15 >> 2, p4 = l15 & 3;
  const int b = blockIdx.x, r_ = b >> 3;
  const int c = 2 * (b & 7) + (r_ >> 4), j = r_ & 15;
  const unsigned* ctl = (const unsigned*)(p.ws + WS_CTL);
  unsigned epoch = 0;
  STAMP(0);
  if (w == 0 || w == 7) epoch = (unsigned)rfl((int)__hip_atomic_load((const GAS unsigned*)ctl, __ATOMIC_RELAXED, __HIP_MEMORY_SCOPE_AGENT)) + 1u;
  const unsigned ONES = 0x3f803f80u;
  const bf16x8 onesf = __builtin_bit_cast(bf16x8, (u32x4{ONES, ONES, ONES, ONES}));

  const int ns = j & 3, col4 = (ns < 2) ? 128 * ns : 256 + 96 * (ns - 2), ngr = (ns < 2) ? 4 : 3;
  const int pr = w & 3, wh = w >> 2;
  const int prc = (pr < ngr) ? pr : 0;
  const int th5 = j >> 3, cs5 = j & 7, col0 = (cs5 < 4) ? 64 * cs5 : 256 + 48 * (cs5 - 4), nblk = (cs5 < 4) ? 4 : 3;
  const int ng5 = w & 1, kq = w >> 1;
  u32x2 xr0, xr1, bb0, bb1, b20, b21;
  {
    const int tok = 128 * c + 32 * (j >> 2) + 16 * wh + l15;
    const GAS char* xp = (const GAS char*)p.x + (size_t)tok * 896 + (col4 + 32 * prc + 4 * g) * 2; xr0 = *(const GAS u32x2*)xp; xr1 = *(const GAS u32x2*)(xp + 32);
    const GAS char* bp = (const GAS char*)p.b1 + (448 * (j >> 2) + col4 + 32 * prc + 4 * g) * 2; bb0 = *(const GAS u32x2*)bp; bb1 = *(const GAS u32x2*)(bp + 32);
    const int t0 = col0 / 16 + 2 * ng5, t1 = (2 * ng5 + 1 < nblk) ? t0 + 1 : t0;
    b20 = *(const GAS u32x2*)((const GAS char*)p.b2 + (16 * t0 + 4 * g) * 2); b21 = *(const GAS u32x2*)((const GAS char*)p.b2 + (16 * t1 + 4 * g) * 2);
  }

  constexpr int KIMG = 0, VIMG = 24576, QIMG = 49152, WI = 32768;
  unsigned wvo[2];
  auto w256_setup = [&](unsigned rowstride) {
    const int rin = lane >> 4, pos = lane & 15;
#pragma unroll
    for (int par = 0; par < 2; ++par) { int blk = (pos >> 1) ^ (4 * par + rin); if (blk >= 2 * ngr) blk -= 2; wvo[par] = (unsigned)(rin * rowstride + blk * 32 + (pos & 1) * 16); }
  };
  {
    const int h = j;
    constexpr int SLOT = 28672, WOFF = 16384;
    unsigned voff0, voff1 = 0, voff2 = 0;
    const char* gbase; unsigned ldst; unsigned kstep; unsigned istep = 0;
    if (w < 4) {
      voff0 = (unsigned)((32 * w + (lane >> 3)) * 896 + (((lane & 7) ^ ((lane >> 3) & 7)) << 4));
      gbase = p.x + (size_t)c * 128 * 896; ldst = w * 4096; kstep = 128; istep = 8 * 896;
    } else {
      unsigned vo[3];
#pragma unroll
      for (int ii = 0; ii < 3; ++ii) {
        const int u = 192 * (w - 4) + 64 * ii + lane, row = u / 12, rem = u - 12 * row;
        const int piece = rem >> 2, c16 = rem & 3, hb = (c16 >> 1) ^ ((row >> 3) & 1);
        vo[ii] = (unsigned)(row * 2688 + piece * 896 + hb * 32 + (c16 & 1) * 16);
      }
      voff0 = vo[0]; voff1 = vo[1]; voff2 = vo[2];
      gbase = p.wqkv + 56 * h - 8 * (h & 1); ldst = WOFF + (w - 4) * 3072; kstep = 64 * 2688;
    }
    auto issue = [&](int kc, int slot) {
      const char* gb = gbase + (size_t)kc * kstep; const unsigned ld = lds0 + slot * SLOT + ldst;
      if (w < 4) {
#pragma unroll
        for (int ii = 0; ii < 4; ++ii) dma16(voff0, gb + ii * istep, ld + ii * 1024);
      } else { dma16(voff0, gb, ld); dma16(voff1, gb, ld + 1024); dma16(voff2, gb, ld + 2048); }
    };
#pragma unroll
    for (int s = 0; s < 3; ++s) { issue(s, s); RAWBAR(); }
    {
      const int ln = (b * 8 + w) * 14 + (lane < 14 ? lane : 13);
      const char* pb = p.wo; unsigned o = (unsigned)ln * 128u;
      if (ln >= 3136) { pb = p.w1; o = (unsigned)(ln - 3136) * 128u; }
      if (ln >= 3136 + 12544) { pb = p.w2; o = (unsigned)(ln - 3136 - 12544) * 128u; if (ln >= 3136 + 2 * 12544) o = 0; }
      const unsigned long long a = (unsigned long long)(uintptr_t)pb + o;
      unsigned keep;
      unsigned long long a2 = a;
      asm volatile("s_mov_b32 %0, m0\n\ts_mov_b32 m0, %2\n\ts_nop 0\n\tglobal_load_lds_dword %1, off\n\ts_mov_b32 m0, %0" : "=&s"(keep) : "v"(a), "s"(lds0 + M_PF) : "memory");
      asm volatile("s_mov_b32 %0, m0\n\ts_mov_b32 m0, %2\n\ts_nop 0\n\tglobal_load_lds_dword %1, off\n\ts_mov_b32 m0, %0" : "=&s"(keep) : "v"(a2), "s"(lds0 + M_PF) : "memory");
    }

    const int tg = w >> 1, ng = w & 1;
    const int e0 = (tg == 3) ? 0 : tg;
    f32x4 acc[3][2], asum = {0, 0, 0, 0}, asq = {0, 0, 0, 0}, acs = {0, 0, 0, 0};
#pragma unroll
    for (int e = 0; e < 3; ++e) { acc[e][0] = f32x4{0, 0, 0, 0}; acc[e][1] = f32x4{0, 0, 0, 0}; }
    int xb[2], ab[3], nts[3];
    xb[0] = (32 * tg + 16 * ng + l15) * 128 + ((g ^ (l15 & 7)) << 4);
    xb[1] = (32 * tg + 16 * (ng ^ 1) + l15) * 128 + ((g ^ (l15 & 7)) << 4);
#pragma unroll
    for (int e = 0; e < 3; ++e) { int t = e0 + e; if (t >= 3) t -= 3; nts[e] = 3 * ng + t; ab[e] = WOFF + (8 * g + q4) * 192 + (((nts[e] * 32) ^ ((g & 1) << 5))) + 8 * p4; }
    bf16x8 xf[2][2], af[2][3];
    auto rdf = [&](int st, int buf) {
      ldsp sl = lds + ((st >> 1) % 5) * SLOT; const int ks = st & 1;
      xf[buf][0] = ld128(sl + (xb[0] ^ (ks << 6))); xf[buf][1] = ld128(sl + (xb[1] ^ (ks << 6)));
#pragma unroll
      for (int e = 0; e < 3; ++e) af[buf][e] = cat(ldtr(sl + ab[e] + (32 * ks) * 192), ldtr(sl + ab[e] + (32 * ks + 4) * 192));
    };
    if (w < 4) WAITV(10); else WAITV(8);
    BAR();
    rdf(0, 0);
#pragma unroll
    for (int st = 0; st < 14; ++st) {
      const int kc = st >> 1;
      if ((st & 1) && st < 13) {
        if (w < 4) { if (kc + 1 <= 2) WAITV(10); else if (kc + 3 <= 6) WAITV(8); else if (kc + 2 <= 6) WAITV(4); else WAITV(0); }
        else { if (kc + 1 <= 2) WAITV(5); else if (kc + 2 <= 6) WAITV(3); else WAITV(0); }
        BAR();
      }
      if (st < 13) rdf(st + 1, (st + 1) & 1);
      if (!(st & 1) && kc + 3 <= 6 && w < 4) issue(kc + 3, (kc + 3) % 5);
      SCHED();
      const int cb = st & 1;
#pragma unroll
      for (int e = 0; e < 3; ++e) { acc[e][0] = MFMA(af[cb][e], xf[cb][0], acc[e][0]); acc[e][1] = MFMA(af[cb][e], xf[cb][1], acc[e][1]); }
      asum = MFMA(onesf, xf[cb][0], asum); asq = MFMA(xf[cb][0], xf[cb][0], asq); acs = MFMA(af[cb][0], onesf, acs);
      SCHED();
      if ((st & 1) && kc + 3 <= 6 && w >= 4) issue(kc + 3, (kc + 3) % 5);
    }
    asm volatile("" : "+v"(xr0), "+v"(xr1), "+v"(bb0), "+v"(bb1), "+v"(b20), "+v"(b21));
    STAMP(1);
    LAS f32x2* stats = (LAS f32x2*)(lds + M_STATS);
    LAS float* csb = (LAS float*)(lds + M_CSB);
    {
      const int r = l15 & 3;
      const float dq = (r == 0) ? asq[0] : (r == 1) ? asq[1] : (r == 2) ? asq[2] : asq[3];
      const float mean = asum[0] * (1.f / 448.f), var = fmaxf(dq * (1.f / 448.f) - mean * mean, 0.f);
      if (g == q4) stats[32 * tg + 16 * ng + l15] = f32x2{mean, rsqrtf(var + 1e-5f)};
      if (tg < 3 && l15 == 0) *(LAS f32x4*)(csb + 16 * nts[0] + 4 * g) = acs;
    }
    BAR();
    f32x2 st[2]; st[0] = stats[32 * tg + 16 * ng + l15]; st[1] = stats[32 * tg + 16 * (ng ^ 1) + l15];
#pragma unroll
    for (int e = 0; e < 3; ++e) {
      const int nt = nts[e], which = nt >> 1, par = nt & 1;
      const f32x4 cv = *(LAS f32x4*)(csb + 16 * nt + 4 * g);
      const bool pad = (h & 1) ? (par == 0 && g == 0) : (par == 1 && g == 3);
      const float sc = (which == 0) ? 0.27264657f : 1.f;
      const int ibase = (which == 0) ? QIMG : (which == 1) ? KIMG + 16384 : VIMG + 16384;
      const int cch = (which == 2) ? 2 * par + (g >> 1) : g, sub = (which == 2) ? (g & 1) * 8 : par * 8;
      const unsigned padx = (which == 2) ? 0x00003f80u : 0u;
#pragma unroll
      for (int tt = 0; tt < 2; ++tt) {
        const int row = 32 * tg + 16 * (tt ^ ng) + l15;
        f32x4 v = (acc[e][tt] - st[tt].x * cv) * (st[tt].y * sc);
        u32x2 o; o.x = cvtpk(v[0], v[1]); o.y = cvtpk(v[2], v[3]);
        o.x = pad ? padx : o.x; o.y = pad ? 0u : o.y;
        *(LAS u32x2*)(lds + ibase + row * 64 + ((cch ^ ((row >> 1) & 3)) << 4) + sub) = o;
      }
    }
    BAR();
    if (c < 15) {
      const char* blob = p.ws + WS_KV + (size_t)(c * 16 + h) * 16384;
      const u32x4 v0 = *(LAS u32x4*)(lds + KIMG + 16384 + tid * 16), v1 = *(LAS u32x4*)(lds + VIMG + 16384 + tid * 16);
      st128_sc1(tid * 16, blob, v0); st128_sc1(8192 + tid * 16, blob, v1);
    }
    if (c < 2) {
      const u32x4 z = {0, 0, 0, 0};
#pragma unroll
      for (int i = 0; i < 2; ++i) { *(LAS u32x4*)(lds + KIMG + tid * 16 + i * 8192) = z; *(LAS u32x4*)(lds + VIMG + tid * 16 + i * 8192) = z; }
    }
    WAITV(0);
    BAR();
    if (tid == 0) st32_sc1(ctl + FLAG_IDX(0, c) + h, epoch);
    STAMP(2);
    w256_setup(896);
    if (w < 7) {
#pragma unroll
      for (int i = 0; i < 13; ++i) { const int ins = 24 + w + 7 * i; if (ins < 112) dma16((ins & 1) ? wvo[1] : wvo[0], p.wo + col4 * 2 + (size_t)ins * 4 * 896, lds0 + WI + ins * 1024); }
    }
  }
  {
    const int h = j;
    if (w == 7 && c >= 1) poll_flags(ctl, (unsigned)((FLAG_IDX(0, (c >= 2 && (lane & 1)) ? c - 2 : c - 1) + h) * 4), epoch);
    BAR();
    STAMP(3);
    {
      const int tl = w >> 2, src = c - 2 + tl, pc = (w & 3) * 2048;
      if (src >= 0) {
        const char* blob = p.ws + WS_KV + (size_t)(src * 16 + h) * 16384;
        dma16_sc1(lane * 16, blob + pc, lds0 + KIMG + tl * 8192 + pc); dma16_sc1(lane * 16, blob + pc + 1024, lds0 + KIMG + tl * 8192 + pc + 1024);
        dma16_sc1(lane * 16, blob + 8192 + pc, lds0 + VIMG + tl * 8192 + pc); dma16_sc1(lane * 16, blob + 8192 + pc + 1024, lds0 + VIMG + tl * 8192 + pc + 1024);
      }
    }
    const int sw = (l15 >> 1) & 3;
    const bf16x8 qf = ld128(lds + QIMG + (16 * w + l15) * 64 + ((g ^ sw) << 4));
    WAITV(0);
    BAR();
    STAMP(4);
    const int tmin = (c >= 2) ? 0 : (2 - c) * 8;
    f32x4 sc[17];
    const float NEG = -__builtin_inff();
    ldsp kb = lds + KIMG + (16 * w + l15) * 64 + ((g ^ sw) << 4);
    {
      bf16x8 kf[17];
#pragma unroll
      for (int u = 0; u < 17; ++u) kf[u] = ld128(kb + u * 1024);
      SCHED();
#pragma unroll
      for (int u = 0; u < 17; ++u) { const float ci = (w + u >= tmin) ? 0.f : NEG; sc[u] = MFMA(kf[u], qf, (f32x4{ci, ci, ci, ci})); }
      SCHED();
    }
    const int vsw = (2 * g + (q4 >> 1)) & 3;
    ldsp vb0 = lds + VIMG + (16 * w + 4 * g + q4) * 64 + (((p4 >> 1) ^ vsw) << 4) + (p4 & 1) * 8;
    ldsp vb1 = lds + VIMG + (16 * w + 4 * g + q4) * 64 + (((2 + (p4 >> 1)) ^ vsw) << 4) + (p4 & 1) * 8;
    bf16x8 vf[9][2];
#pragma unroll
    for (int uu = 0; uu < 9; ++uu) {
      const int ua = 2 * uu, ub = (uu < 8) ? 2 * uu + 1 : 16;
      vf[uu][0] = cat(ldtr(vb0 + ua * 1024), ldtr(vb0 + ub * 1024)); vf[uu][1] = cat(ldtr(vb1 + ua * 1024), ldtr(vb1 + ub * 1024));
    }
    SCHED();
#pragma unroll
    for (int r = 0; r < 4; ++r) { sc[0][r] = (4 * g + r <= l15) ? NEG : sc[0][r]; sc[16][r] = (4 * g + r > l15) ? NEG : sc[16][r]; }
    float m = fmaxf(fmaxf(sc[0][0], sc[0][1]), fmaxf(sc[0][2], sc[0][3]));
#pragma unroll
    for (int u = 1; u < 17; ++u) m = fmaxf(fmaxf(m, fmaxf(sc[u][0], sc[u][1])), fmaxf(sc[u][2], sc[u][3]));
    m = red_g_max(m);
    unsigned pk[17][2];
#pragma unroll
    for (int u = 0; u < 17; ++u) {
      pk[u][0] = cvtpk(__builtin_amdgcn_exp2f(sc[u][0] - m), __builtin_amdgcn_exp2f(sc[u][1] - m));
      pk[u][1] = cvtpk(__builtin_amdgcn_exp2f(sc[u][2] - m), __builtin_amdgcn_exp2f(sc[u][3] - m));
    }
    f32x4 oa[2] = {f32x4{0, 0, 0, 0}, f32x4{0, 0, 0, 0}};
#pragma unroll
    for (int uu = 0; uu < 9; ++uu) {
      const int ua = 2 * uu, ub = (uu < 8) ? 2 * uu + 1 : 16;
      u32x4 pu; pu.x = pk[ua][0]; pu.y = pk[ua][1];
      if (uu < 8) { pu.z = pk[ub][0]; pu.w = pk[ub][1]; } else { pu.z = 0; pu.w = 0; }
      const bf16x8 pf = __builtin_bit_cast(bf16x8, pu);
      oa[0] = MFMA(vf[uu][0], pf, oa[0]); oa[1] = MFMA(vf[uu][1], pf, oa[1]);
    }
    float l = (h & 1) ? oa[0][0] : oa[1][0];
    l = __shfl(l, (h & 1) ? l15 : 48 + l15);
    const float inv = __builtin_amdgcn_rcpf(l);
    u32x4 ov;
    ov.x = cvtpk(oa[0][0] * inv, oa[0][1] * inv); ov.y = cvtpk(oa[0][2] * inv, oa[0][3] * inv);
    ov.z = cvtpk(oa[1][0] * inv, oa[1][1] * inv); ov.w = cvtpk(oa[1][2] * inv, oa[1][3] * inv);
    { const bool z0 = (h & 1) && g == 0, z1 = !(h & 1) && g == 3; ov.x = z0 ? 0u : ov.x; ov.y = z0 ? 0u : ov.y; ov.z = z1 ? 0u : ov.z; ov.w = z1 ? 0u : ov.w; }
    st128_sc1((unsigned)((16 * w + l15) * 64 + ((g ^ sw) << 4)), p.ws + WS_O + (size_t)(c * 16 + h) * 8192, ov);
    BAR();
#pragma unroll
    for (int i = 0; i < 3; ++i) { const int ins = 3 * w + i; dma16((ins & 1) ? wvo[1] : wvo[0], p.wo + col4 * 2 + (size_t)ins * 4 * 896, lds0 + WI + ins * 1024); }
    WAITV(3);
    BAR();
    if (tid == 0) st32_sc1(ctl + FLAG_IDX(1, c) + h, epoch);
    STAMP(5);
  }
  {
    constexpr int OT = 0;
    const int tq = j >> 2, tt = wh;
    const int tok = 128 * c + 32 * tq + 16 * tt + l15;
    if (w == 7) poll_flags(ctl, (unsigned)((FLAG_IDX(1, c) + l15) * 4), epoch);
    BAR();
    STAMP(6);
#pragma unroll
    for (int i = 0; i < 4; ++i) { const int pc = 4 * w + i, hd = pc >> 1;
      dma16_sc1(lane * 16, p.ws + WS_O + (size_t)(c * 16 + hd) * 8192 + tq * 2048 + (pc & 1) * 1024, lds0 + OT + pc * 1024); }
    WAITV(0);
    BAR();
    STAMP(7);
    {
      f32x4 a0 = {0, 0, 0, 0}, a1 = {0, 0, 0, 0};
      const int rx = 4 * (g & 1) + q4;
      ldsp ab0 = lds + WI + (4 * g + q4) * 256 + (((2 * prc) ^ rx) << 5) + 8 * p4;
      ldsp ab1 = lds + WI + (4 * g + q4) * 256 + (((2 * prc + 1) ^ rx) << 5) + 8 * p4;
      ldsp ab0h = ab0 + 65536, ab1h = ab1 + 65536;
      ldsp ob = lds + OT + (16 * tt + l15) * 64 + ((g ^ ((l15 >> 1) & 3)) << 4);
      bf16x8 of[4], f0[4], f1[4];
      auto rd = [&](int s, int buf) {
        const int rb = (28 * s - 4 * (s & 1)) * 256;
        of[buf] = ld128(ob + s * 2048);
        if (rb + 4096 < 65536) { f0[buf] = cat(ldtr(ab0 + rb), ldtr(ab0 + rb + 4096)); f1[buf] = cat(ldtr(ab1 + rb), ldtr(ab1 + rb + 4096)); }
        else { f0[buf] = cat(ldtr(ab0h + (rb - 65536)), ldtr(ab0h + (rb - 65536 + 4096))); f1[buf] = cat(ldtr(ab1h + (rb - 65536)), ldtr(ab1h + (rb - 65536 + 4096))); }
      };
      rd(0, 0); rd(1, 1); rd(2, 2);
#pragma unroll
      for (int s = 0; s < 16; ++s) {
        if (s + 3 < 16) rd(s + 3, (s + 3) & 3);
        SCHED();
        a0 = MFMA(f0[s & 3], of[s & 3], a0); a1 = MFMA(f1[s & 3], of[s & 3], a1);
        SCHED();
      }
      u32x4 hv;
      hv.x = cvtpk(a0[0] + bflo(xr0.x), a0[1] + bfhi(xr0.x)); hv.y = cvtpk(a0[2] + bflo(xr0.y), a0[3] + bfhi(xr0.y));
      hv.z = cvtpk(a1[0] + bflo(xr1.x), a1[1] + bfhi(xr1.x)); hv.w = cvtpk(a1[2] + bflo(xr1.y), a1[3] + bfhi(xr1.y));
      if (pr < ngr) st128_sc1((unsigned)(tok * 896 + (col4 / 32 + pr) * 64 + g * 16), p.ws + WS_H, hv);
      float s1 = 0.f, s2 = 0.f;
#pragma unroll
      for (int i = 0; i < 4; ++i) { s1 = dot2(hv[i], ONES, s1); s2 = dot2(hv[i], hv[i], s2); }
      s1 = red_g_sum(s1); s2 = red_g_sum(s2);
      if (pr >= ngr) { s1 = 0.f; s2 = 0.f; }
      if (g == 0) ((LAS f32x2*)(lds + M_PART))[(16 * tt + l15) * 4 + pr] = f32x2{s1, s2};
    }
    BAR();
    if (tid < 32) {
      const f32x4 u0 = *(LAS f32x4*)(lds + M_PART + tid * 32), u1 = *(LAS f32x4*)(lds + M_PART + tid * 32 + 16);
      st64_sc1((unsigned)((128 * c + 32 * tq + tid) * 32 + ns * 8), p.ws + WS_ST,
               u32x2{__float_as_uint((u0[0] + u0[2]) + (u1[0] + u1[2])), __float_as_uint((u0[1] + u0[3]) + (u1[1] + u1[3]))});
    }
    const char* w1b = p.w1 + (448 * (j >> 2) + col4) * 2;
    w256_setup(3584);
    if (w < 7) {
#pragma unroll
      for (int i = 0; i < 4; ++i) { const int ins = w + 7 * i; dma16((ins & 1) ? wvo[1] : wvo[0], w1b + (size_t)ins * 4 * 3584, lds0 + ins * 1024); }
      WAITV(4);
    } else WAITV(0);
    BAR();
    if (tid == 0) st32_sc1(ctl + FLAG_IDX(2, c) + j, epoch);
    STAMP(8);
    if (w < 7) {
#pragma unroll
      for (int i = 4; i < 16; ++i) { const int ins = w + 7 * i; dma16((ins & 1) ? wvo[1] : wvo[0], w1b + (size_t)ins * 4 * 3584, lds0 + ins * 1024); }
    }
  }
  {
    const int f0 = 448 * (j >> 2) + col4, th = wh;
    if (w == 7) poll_flags(ctl, (unsigned)((FLAG_IDX(2, c) + l15) * 4), epoch);
    WAITV(0);
    BAR();
    STAMP(9);
    const unsigned vo = (unsigned)((16 * w + (lane >> 3)) * 896 + (((lane & 7) ^ ((lane >> 3) & 7)) << 4));
    const char* hb = p.ws + WS_H + (size_t)c * 128 * 896;
    auto caddr = [&](int kc) { return (kc < 2) ? 114688 + kc * 16384 : (kc - 2) * 16384; };
    auto issue4 = [&](int kc) { dma16_sc1(vo, hb + kc * 128, lds0 + caddr(kc) + w * 2048); dma16_sc1(vo, hb + kc * 128 + 8 * 896, lds0 + caddr(kc) + w * 2048 + 1024); };
    dma16_sc1((unsigned)((lane & 31) * 16), p.ws + WS_ST + (size_t)(128 * c + 16 * w) * 32, lds0 + M_TILE + w * 1024);
    issue4(0); RAWBAR(); issue4(1);
    bf16x8 af[2][14];
    f32x4 c0 = {0, 0, 0, 0}, c1 = {0, 0, 0, 0};
    {
      const int rx = 4 * (g & 1) + q4;
      ldsp ab0 = lds + (4 * g + q4) * 256 + (((2 * prc) ^ rx) << 5) + 8 * p4;
      ldsp ab1 = lds + (4 * g + q4) * 256 + (((2 * prc + 1) ^ rx) << 5) + 8 * p4;
      ldsp ab0h = ab0 + 65536, ab1h = ab1 + 65536;
#pragma unroll
      for (int s = 0; s < 14; ++s) {
        if (s < 8) { af[0][s] = cat(ldtr(ab0 + s * 8192), ldtr(ab0 + s * 8192 + 4096)); af[1][s] = cat(ldtr(ab1 + s * 8192), ldtr(ab1 + s * 8192 + 4096)); }
        else { af[0][s] = cat(ldtr(ab0h + (s - 8) * 8192), ldtr(ab0h + (s - 8) * 8192 + 4096)); af[1][s] = cat(ldtr(ab1h + (s - 8) * 8192), ldtr(ab1h + (s - 8) * 8192 + 4096)); }
      }
#pragma unroll
      for (int s = 0; s < 14; ++s) { c0 = MFMA(af[0][s], onesf, c0); c1 = MFMA(af[1][s], onesf, c1); }
    }
    BAR();
    issue4(2); RAWBAR(); issue4(3);
    f32x4 acc[2][4];
#pragma unroll
    for (int t = 0; t < 4; ++t) { acc[0][t] = f32x4{0, 0, 0, 0}; acc[1][t] = f32x4{0, 0, 0, 0}; }
    int xb[4];
#pragma unroll
    for (int t = 0; t < 4; ++t) xb[t] = (64 * th + 16 * t + l15) * 128 + ((g ^ (l15 & 7)) << 4);
    bf16x8 xf[2][4];
    auto rdf = [&](int st, int buf) {
      const int ca = caddr(st >> 1);
#pragma unroll
      for (int t = 0; t < 4; ++t) xf[buf][t] = ld128(lds + ca + (xb[t] ^ ((st & 1) << 6)));
    };
    WAITV(6); BAR();
    STAMP(10);
    rdf(0, 0);
#pragma unroll
    for (int st = 0; st < 14; ++st) {
      const int kc = st >> 1;
      if ((st & 1) && st < 13) {
        if (w < 4) { if (kc + 4 <= 6) WAITV(6); else if (kc + 3 <= 6) WAITV(4); else if (kc + 2 <= 6) WAITV(2); else WAITV(0); }
        else { if (kc + 3 <= 6) WAITV(4); else if (kc + 2 <= 6) WAITV(2); else WAITV(0); }
        BAR();
      }
      if (st < 13) rdf(st + 1, (st + 1) & 1);
      if (!(st & 1) && kc + 4 <= 6 && w < 4) issue4(kc + 4);
      SCHED();
#pragma unroll
      for (int t = 0; t < 4; ++t) { acc[0][t] = MFMA(af[0][st], xf[st & 1][t], acc[0][t]); acc[1][t] = MFMA(af[1][st], xf[st & 1][t], acc[1][t]); }
      SCHED();
      if ((st & 1) && kc + 4 <= 6 && w >= 4) issue4(kc + 4);
    }
    STAMP(11);
    {
      const f32x4 b0 = {bflo(bb0.x), bfhi(bb0.x), bflo(bb0.y), bfhi(bb0.y)}, b1v = {bflo(bb1.x), bfhi(bb1.x), bflo(bb1.y), bfhi(bb1.y)};
#pragma unroll
      for (int t = 0; t < 4; ++t) {
        const int row = 64 * th + 16 * t + l15;
        ldsp sp = lds + M_TILE + (row >> 4) * 1024 + (row & 15) * 32;
        const f32x4 u0 = *(LAS f32x4*)sp, u1 = *(LAS f32x4*)(sp + 16);
        const float mean = ((u0[0] + u0[2]) + (u1[0] + u1[2])) * (1.f / 448.f);
        const float var = fmaxf(((u0[1] + u0[3]) + (u1[1] + u1[3])) * (1.f / 448.f) - mean * mean, 0.f);
        const float rstd = rsqrtf(var + 1e-5f);
        f32x4 z0 = (acc[0][t] - mean * c0) * rstd + b0, z1 = (acc[1][t] - mean * c1) * rstd + b1v;
#pragma unroll
        for (int i = 0; i < 4; ++i) { z0[i] = fmaxf(z0[i], 0.f); z0[i] *= z0[i]; z1[i] = fmaxf(z1[i], 0.f); z1[i] *= z1[i]; }
        u32x4 hv; hv.x = cvtpk(z0[0], z0[1]); hv.y = cvtpk(z0[2], z0[3]); hv.z = cvtpk(z1[0], z1[1]); hv.w = cvtpk(z1[2], z1[3]);
        if (pr < ngr) st128_sc1((unsigned)((128 * c + row) * 3584 + (f0 / 32 + pr) * 64 + g * 16), p.ws + WS_H1, hv);
      }
    }
    BAR();
  }
  {
    bf16x8 af[2][14];
    const int rin = lane >> 3, pos = lane & 7;
    int blk = (pos >> 1) ^ ((rin >> 1) & 3); if (blk >= nblk) blk = nblk - 1;
    const unsigned wv = (unsigned)(rin * 896 + blk * 32 + (pos & 1) * 16);
    auto stage = [&](int rd, int i0, int i1) {
#pragma unroll
      for (int i = i0; i < i1; ++i) { const int ins = w + 7 * i, kk = ins / 28, fr = 448 * kk + 224 * rd + 8 * (ins - 28 * kk);
        dma16(wv, p.w2 + (size_t)fr * 896 + col0 * 2, lds0 + ins * 1024); }
    };
    if (w < 7) { stage(0, 0, 4); WAITV(4); } else WAITV(0);
    BAR();
    if (tid == 0) st32_sc1(ctl + FLAG_IDX(3, c) + j, epoch);
    STAMP(12);
    if (w < 7) stage(0, 4, 16);
    const int rx = (2 * g + (q4 >> 1)) & 3;
    ldsp ab0 = lds + (kq * 224 + 4 * g + q4) * 128 + (((2 * ng5) ^ rx) << 5) + 8 * p4;
    ldsp ab1 = lds + (kq * 224 + 4 * g + q4) * 128 + (((2 * ng5 + 1) ^ rx) << 5) + 8 * p4;
    {
      const int m0 = col0 >> 5; int u = w * 96 + lane;
      const int r0 = u / 12, c0 = u - 12 * r0; int cc0 = m0 * 4 + c0; if (cc0 > 55) cc0 = 55;
      dma16_sc1((unsigned)(r0 * 896 + cc0 * 16), p.ws + WS_H + (size_t)(128 * c + 64 * th5) * 896, lds0 + M_TILE + w * 1536);
      if (lane < 32) { u += 64; const int r1 = u / 12, c1 = u - 12 * r1; int cc1 = m0 * 4 + c1; if (cc1 > 55) cc1 = 55;
        dma16_sc1((unsigned)(r1 * 896 + cc1 * 16), p.ws + WS_H + (size_t)(128 * c + 64 * th5) * 896, lds0 + M_TILE + w * 1536 + 1024); }
    }
    WAITV(0);
    BAR();
#pragma unroll
    for (int s = 0; s < 7; ++s) { af[0][s] = cat(ldtr(ab0 + s * 4096), ldtr(ab0 + s * 4096 + 2048)); af[1][s] = cat(ldtr(ab1 + s * 4096), ldtr(ab1 + s * 4096 + 2048)); }
    BAR();
    if (w < 7) stage(1, 0, 16);
    if (w == 7) {
      poll_flags(ctl, (unsigned)((FLAG_IDX(3, c) + l15) * 4), epoch);
      if (b == 0) {
#pragma unroll
        for (int i = 0; i < 4; ++i) poll_flags(ctl, (unsigned)((FLAG_IDX(0, 4 * i + g) + l15) * 4), epoch);
        if (lane == 0) st32_sc1(ctl, epoch);
      }
    }
    WAITV(0);
    BAR();
    STAMP(13);
    const unsigned vo = (unsigned)((32 * ng5 + (lane >> 3)) * 3584 + (((lane & 7) ^ ((lane >> 3) & 7)) << 4));
    const char* hb = p.ws + WS_H1 + (size_t)(128 * c + 64 * th5) * 3584 + kq * 896;
    auto saddr = [&](int i) { return ((i & 3) == 0) ? 114688 : ((i & 3) - 1) * 32768; };
    auto issue = [&](int i) {
      const unsigned ld = lds0 + saddr(i) + kq * 8192 + ng5 * 4096;
#pragma unroll
      for (int ii = 0; ii < 4; ++ii) dma16_sc1(vo, hb + i * 128 + ii * 8 * 3584, ld + ii * 1024);
    };
    issue(0);
#pragma unroll
    for (int s = 0; s < 7; ++s) { af[0][7 + s] = cat(ldtr(ab0 + s * 4096), ldtr(ab0 + s * 4096 + 2048)); af[1][7 + s] = cat(ldtr(ab1 + s * 4096), ldtr(ab1 + s * 4096 + 2048)); }
    BAR();
    issue(1); RAWBAR(); issue(2);
    f32x4 acc[2][4];
#pragma unroll
    for (int t = 0; t < 4; ++t) { acc[0][t] = f32x4{0, 0, 0, 0}; acc[1][t] = f32x4{0, 0, 0, 0}; }
    int xb[4];
#pragma unroll
    for (int t = 0; t < 4; ++t) xb[t] = kq * 8192 + (16 * ((t + kq) & 3) + l15) * 128 + ((g ^ (l15 & 7)) << 4);
    bf16x8 xf[2][4];
    auto rdf = [&](int st, int buf) {
      const int sa = saddr(st >> 1);
#pragma unroll
      for (int t = 0; t < 4; ++t) xf[buf][t] = ld128(lds + sa + (xb[t] ^ ((st & 1) << 6)));
    };
    WAITV(8); BAR();
    STAMP(14);
    rdf(0, 0);
#pragma unroll
    for (int st = 0; st < 14; ++st) {
      const int i = st >> 1;
      if ((st & 1) && st < 13) {
        if (w < 4) { if (i + 3 <= 6) WAITV(8); else if (i + 2 <= 6) WAITV(4); else WAITV(0); }
        else { if (i + 2 <= 6) WAITV(4); else WAITV(0); }
        BAR();
      }
      if (st < 13) rdf(st + 1, (st + 1) & 1);
      if (!(st & 1) && i + 3 <= 6 && w < 4) issue(i + 3);
      SCHED();
#pragma unroll
      for (int t = 0; t < 4; ++t) { acc[0][t] = MFMA(af[0][st], xf[st & 1][t], acc[0][t]); acc[1][t] = MFMA(af[1][st], xf[st & 1][t], acc[1][t]); }
      SCHED();
      if ((st & 1) && i + 3 <= 6 && w >= 4) issue(i + 3);
    }
    BAR();
#pragma unroll
    for (int t = 1; t < 4; ++t) {
      ldsp d = lds + ((((t + kq) & 3) * 3 + (t - 1)) * 2 + ng5) * 2048 + lane * 16;
      *(LAS f32x4*)d = acc[0][t]; *(LAS f32x4*)(d + 1024) = acc[1][t];
    }
    BAR();
    f32x4 r0 = acc[0][0], r1 = acc[1][0];
#pragma unroll
    for (int t = 1; t < 4; ++t) {
      ldsp d = lds + ((kq * 3 + (t - 1)) * 2 + ng5) * 2048 + lane * 16;
      r0 += *(LAS f32x4*)d; r1 += *(LAS f32x4*)(d + 1024);
    }
    const int tokl = 16 * kq + l15;
    const int t0 = col0 / 16 + 2 * ng5, t1 = (2 * ng5 + 1 < nblk) ? t0 + 1 : t0, m0 = col0 >> 5;
    const u32x2 hr0 = *(LAS u32x2*)(lds + M_TILE + tokl * 192 + ((t0 >> 1) - m0) * 64 + g * 16 + (t0 & 1) * 8);
    const u32x2 hr1 = *(LAS u32x2*)(lds + M_TILE + tokl * 192 + ((t1 >> 1) - m0) * 64 + g * 16 + (t1 & 1) * 8);
    u32x2 o0, o1;
    o0.x = cvtpk(r0[0] + bflo(b20.x) + bflo(hr0.x), r0[1] + bfhi(b20.x) + bfhi(hr0.x)); o0.y = cvtpk(r0[2] + bflo(b20.y) + bflo(hr0.y), r0[3] + bfhi(b20.y) + bfhi(hr0.y));
    o1.x = cvtpk(r1[0] + bflo(b21.x) + bflo(hr1.x), r1[1] + bfhi(b21.x) + bfhi(hr1.x)); o1.y = cvtpk(r1[2] + bflo(b21.y) + bflo(hr1.y), r1[3] + bfhi(b21.y) + bfhi(hr1.y));
    GAS char* op = (GAS char*)p.out + (size_t)(128 * c + 64 * th5 + tokl) * 896 + (col0 + 32 * ng5 + 4 * g) * 2;
    *(GAS u32x2*)op = o0;
    if (2 * ng5 + 1 < nblk) *(GAS u32x2*)(op + 32) = o1;
    STAMP(15);
  }
}

extern "C" void kernel_launch(void* const* d_in, const int* in_sizes, int n_in, void* d_out, int out_size, void* d_ws, size_t ws_size, hipStream_t stream) {
  Params p;
  p.x = (const char*)d_in[0]; p.wqkv = (const char*)d_in[1]; p.wo = (const char*)d_in[2];
  p.w1 = (const char*)d_in[4]; p.b1 = (const char*)d_in[5]; p.w2 = (const char*)d_in[6]; p.b2 = (const char*)d_in[7];
  p.out = (char*)d_out; p.ws = (char*)d_ws;
  (void)hipFuncSetAttribute((const void*)fused_block, hipFuncAttributeMaxDynamicSharedMemorySize, LDS_BYTES);
  hipLaunchKernelGGL(fused_block, dim3(256), dim3(512), LDS_BYTES, stream, p);
}
```

```cpp
#include <hip/hip_runtime.h>
#include <hip/hip_bf16.h>
#include <stdint.h>
#include <type_traits>

#define LAS __attribute__((address_space(3)))
#define GAS __attribute__((address_space(1)))
typedef short bf16x8 __attribute__((ext_vector_type(8)));
typedef short s16x4 __attribute__((ext_vector_type(4)));
typedef short v4i16_t __attribute__((ext_vector_type(4)));
typedef float f32x4 __attribute__((ext_vector_type(4)));
typedef float f32x2 __attribute__((ext_vector_type(2)));
typedef unsigned u32x4 __attribute__((ext_vector_type(4)));
typedef unsigned u32x2 __attribute__((ext_vector_type(2)));
typedef __bf16 bf16x2_t __attribute__((ext_vector_type(2)));
typedef LAS char* ldsp;

constexpr int SEQ = 2048;
constexpr size_t WS_CTL = 0;
constexpr size_t WS_KV = 262144;
constexpr size_t WS_O = WS_KV + 256 * 16384;
constexpr size_t WS_H = WS_O + 256 * 8192;
constexpr size_t WS_H1 = WS_H + (size_t)SEQ * 896;
constexpr size_t WS_ST = WS_H1 + (size_t)SEQ * 3584;
constexpr size_t WS_WT = WS_ST + (size_t)SEQ * 32;
constexpr unsigned WT_WO = 0, WT_W1 = 448 * 1024, WT_W2 = WT_W1 + 1568 * 1024, WT_BYTES = WT_W2 + 1568 * 1024;
constexpr size_t WS_DBG = WS_WT + WT_BYTES;
constexpr int LDS_BYTES = 160 * 1024;
constexpr int M_PT = 143360;
constexpr int M_XT = 147456;
constexpr int M_TILE = 131072;
constexpr int MISC = 157696;
constexpr int M_LN = MISC, M_B1 = MISC + 1024, M_B2 = MISC + 1280, M_PF = MISC + 1536, M_PART = MISC + 2048, M_FILL = MISC + 3072;
constexpr unsigned SPIN_CAP = 1u << 16;

#define FLAG_IDX(ph, c, j) (64 + (((ph) * 16 + (c)) * 16 + (j)) * 32)

#define WAITV(n) asm volatile("s_waitcnt vmcnt(" #n ")" ::: "memory")
#define BAR() do { asm volatile("s_waitcnt lgkmcnt(0)" ::: "memory"); __builtin_amdgcn_s_barrier(); asm volatile("" ::: "memory"); } while (0)
#define BARN() do { asm volatile("" ::: "memory"); __builtin_amdgcn_s_barrier(); asm volatile("" ::: "memory"); } while (0)
#define SCHED() __builtin_amdgcn_sched_barrier(0)
#define RAWBAR() __builtin_amdgcn_s_barrier()
#define ILV(n) do { _Pragma("unroll") for (int i_ = 0; i_ < (n); ++i_) { __builtin_amdgcn_sched_group_barrier(0x008, 1, 0); __builtin_amdgcn_sched_group_barrier(0x100, 1, 0); } } while (0)
#define MFMA(a, b, c) __builtin_amdgcn_mfma_f32_16x16x32_bf16((a), (b), (c), 0, 0, 0)
#define IC(x) std::integral_constant<int, (x)>{}

template <int I, int N, class F> __device__ __forceinline__ void sfor(F&& f) { if constexpr (I < N) { f(std::integral_constant<int, I>{}); sfor<I + 1, N>(f); } }

__device__ __forceinline__ unsigned cvtpk(float lo, float hi) { f32x2 v = {lo, hi}; bf16x2_t b = __builtin_convertvector(v, bf16x2_t); return __builtin_bit_cast(unsigned, b); }
__device__ __forceinline__ unsigned cvtpk(f32x2 v) { bf16x2_t b = __builtin_convertvector(v, bf16x2_t); return __builtin_bit_cast(unsigned, b); }
__device__ __forceinline__ float dot2(unsigned a, unsigned b, float c) { return __builtin_amdgcn_fdot2_f32_bf16(__builtin_bit_cast(bf16x2_t, a), __builtin_bit_cast(bf16x2_t, b), c, false); }
__device__ __forceinline__ float bflo(unsigned u) { return __uint_as_float(u << 16); }
__device__ __forceinline__ float bfhi(unsigned u) { return __uint_as_float(u & 0xffff0000u); }
__device__ __forceinline__ f32x2 bf2(unsigned u) { return f32x2{bflo(u), bfhi(u)}; }
__device__ __forceinline__ f32x2 lo2(f32x4 v) { return f32x2{v[0], v[1]}; }
__device__ __forceinline__ f32x2 hi2(f32x4 v) { return f32x2{v[2], v[3]}; }
__device__ __forceinline__ bf16x8 ld128(ldsp p) { return *(const LAS bf16x8*)p; }
__device__ __forceinline__ s16x4 ldtr(ldsp p) { return __builtin_bit_cast(s16x4, __builtin_amdgcn_ds_read_tr16_b64_v4i16((LAS v4i16_t*)p)); }
__device__ __forceinline__ bf16x8 cat(s16x4 lo, s16x4 hi) { return __builtin_shufflevector(lo, hi, 0, 1, 2, 3, 4, 5, 6, 7); }
__device__ __forceinline__ int rfl(int x) { return __builtin_amdgcn_readfirstlane(x); }
template <int LADD, int VADD, bool SC1>
__device__ __forceinline__ void dma(unsigned voff, const void* sbase, unsigned lbase) {
  if constexpr (VADD == 0) {
    if constexpr (SC1) asm volatile("s_add_i32 m0, %2, %3\n\ts_nop 0\n\tglobal_load_lds_dwordx4 %0, %1 sc1" ::"v"(voff), "s"(sbase), "s"(lbase), "i"(LADD) : "memory", "scc");
    else asm volatile("s_add_i32 m0, %2, %3\n\ts_nop 0\n\tglobal_load_lds_dwordx4 %0, %1" ::"v"(voff), "s"(sbase), "s"(lbase), "i"(LADD) : "memory", "scc");
  } else {
    unsigned tmp;
    if constexpr (SC1) asm volatile("s_add_i32 m0, %3, %4\n\tv_add_u32 %0, %5, %1\n\tglobal_load_lds_dwordx4 %0, %2 sc1" : "=&v"(tmp) : "v"(voff), "s"(sbase), "s"(lbase), "i"(LADD), "i"(VADD) : "memory", "scc");
    else asm volatile("s_add_i32 m0, %3, %4\n\tv_add_u32 %0, %5, %1\n\tglobal_load_lds_dwordx4 %0, %2" : "=&v"(tmp) : "v"(voff), "s"(sbase), "s"(lbase), "i"(LADD), "i"(VADD) : "memory", "scc");
  }
}
template <int LADD>
__device__ __forceinline__ void dma2c_sc1(unsigned voff, const void* sbase, unsigned lbase) {
  asm volatile("s_add_i32 m0, %2, %3\n\ts_nop 0\n\tglobal_load_lds_dwordx4 %0, %1 sc1\n\tglobal_load_lds_dwordx4 %0, %1 offset:1024 sc1" ::"v"(voff), "s"(sbase), "s"(lbase), "i"(LADD) : "memory", "scc");
}
template <int OFF>
__device__ __forceinline__ void ldfrag1(bf16x8& f, unsigned voff, const void* sbase) {
  asm volatile("global_load_dwordx4 %0, %1, %2 offset:%3 sc1" : "=v"(f) : "v"(voff), "s"(sbase), "i"(OFF) : "memory");
}
__device__ __forceinline__ void pin1(bf16x8& f) { asm volatile("" : "+v"(f)); }
template <int N>
__device__ __forceinline__ void ldfrags(bf16x8* f, unsigned voff, const void* sbase) {
  sfor<0, N>([&](auto I) { constexpr int i = decltype(I)::value; ldfrag1<(i & 3) * 1024>(f[i], voff + (unsigned)((i >> 2) * 4096), sbase); });
}
template <int N>
__device__ __forceinline__ void pin(bf16x8* f) { sfor<0, N>([&](auto I) { pin1(f[decltype(I)::value]); }); }
__device__ __forceinline__ void st128_sc1(unsigned voff, const void* sbase, u32x4 v) {
  asm volatile("global_store_dwordx4 %0, %1, %2 sc1\n\ts_nop 1" ::"v"(voff), "v"(v), "s"(sbase) : "memory");
}
__device__ __forceinline__ void st64_sc1(unsigned voff, const void* sbase, u32x2 v) {
  asm volatile("global_store_dwordx2 %0, %1, %2 sc1\n\ts_nop 1" ::"v"(voff), "v"(v), "s"(sbase) : "memory");
}
__device__ __forceinline__ void st32_sc1(const void* sbase, unsigned v) {
  asm volatile("global_store_dword %0, %1, %2 sc1\n\ts_nop 1" ::"v"(0u), "v"(v), "s"(sbase) : "memory");
}
__device__ __forceinline__ float max3(float a, float b, float c) { return __builtin_elementwise_maximum(__builtin_elementwise_maximum(a, b), c); }
__device__ __forceinline__ float red_g_sum(float v) {
  auto a = __builtin_amdgcn_permlane16_swap(__float_as_uint(v), __float_as_uint(v), false, false);
  v = __uint_as_float(a[0]) + __uint_as_float(a[1]);
  auto b = __builtin_amdgcn_permlane32_swap(__float_as_uint(v), __float_as_uint(v), false, false);
  return __uint_as_float(b[0]) + __uint_as_float(b[1]);
}
__device__ __forceinline__ float red_g_max(float v) {
  auto a = __builtin_amdgcn_permlane16_swap(__float_as_uint(v), __float_as_uint(v), false, false);
  v = __builtin_elementwise_maximum(__uint_as_float(a[0]), __uint_as_float(a[1]));
  auto b = __builtin_amdgcn_permlane32_swap(__float_as_uint(v), __float_as_uint(v), false, false);
  return __builtin_elementwise_maximum(__uint_as_float(b[0]), __uint_as_float(b[1]));
}
__device__ __forceinline__ void poll_flags(const unsigned* base, unsigned voff, unsigned epoch) {
#pragma unroll 1
  for (unsigned spins = 0; spins < SPIN_CAP; ++spins) {
    unsigned v;
    asm volatile("global_load_dword %0, %1, %2 sc1\n\ts_waitcnt vmcnt(0)" : "=&v"(v) : "v"(voff), "s"(base) : "memory");
    if (__all(v == epoch)) break;
    __builtin_amdgcn_s_sleep(1);
  }
}

struct Params {
  const char* x; const char* wqkv; const char* wo; const char* w1; const char* b1; const char* w2; const char* b2;
  char* out; char* ws;
};

#define FS(i) do {} while (0)
#define STAMP(i) do {} while (0)

#define LANEVARS() int tz_ = tid; asm volatile("" : "+v"(tz_)); const int lane = tz_ & 63, l15 = lane & 15, g = lane >> 4, q4 = l15 >> 2, p4 = l15 & 3; \
  const unsigned lane16 = (unsigned)lane * 16u, x8 = (unsigned)(((lane & 7) ^ ((lane >> 3) & 7)) << 4); (void)q4; (void)p4; (void)g; (void)x8; (void)lane16

__global__ void __launch_bounds__(512, 2) fused_block(Params p) {
  extern __shared__ __attribute__((aligned(16))) char smem[];
  ldsp lds = (ldsp)smem;
  const int tid = threadIdx.x, lane = tid & 63, w = rfl(tid >> 6);
  const int l15 = lane & 15, g = lane >> 4, q4 = l15 >> 2, p4 = l15 & 3;
  const int b = blockIdx.x, r_ = b >> 3;
  const int c = 2 * (b & 7) + (r_ >> 4), j = r_ & 15;
  const unsigned* ctl = (const unsigned*)(p.ws + WS_CTL);
  unsigned epoch = 0;
  FS(0);
  STAMP(0);
  const unsigned ONES = 0x3f803f80u;
  const bf16x8 onesf = __builtin_bit_cast(bf16x8, (u32x4{ONES, ONES, ONES, ONES}));
  const unsigned lane16 = (unsigned)lane * 16u;
  const unsigned x8 = (unsigned)(((lane & 7) ^ ((lane >> 3) & 7)) << 4);

  const int ns = j & 3, col4 = 128 * ns - (((ns >> 1) & ns) << 5), ngr = 4 - (ns >> 1);
  const int pr = w & 3, wh = w >> 2;
  const int prc = (pr < ngr) ? pr : 0;
  const int th5 = j >> 3, cs5 = j & 7, col0 = 64 * cs5 - 16 * ((cs5 >> 2) * (cs5 & 3)), nblk = 4 - (cs5 >> 2);
  const int ng5 = w & 1, kq = w >> 1;
  const char* wt = p.ws + WS_WT;
  bf16x8 wr[32];
  const unsigned wo_ = WT_WO + (unsigned)(col4 / 16 + 2 * prc) * 16u * 1024u;
  const int T1 = (448 * (j >> 2) + col4) / 16;
  const unsigned w1o = WT_W1 + (unsigned)(T1 + 2 * prc) * 14u * 1024u;
  const unsigned wro = lane16 + ((w < 4) ? wo_ : w1o);
  constexpr int KIMG = 0, VIMG = 24576, QIMG = 49152;
  {
    const int h = j;
    constexpr int SLOT = 28672, WOFF = 16384;
    const unsigned iw = (unsigned)(w >> 2);
    unsigned voff[4];
    {
      const unsigned xo = (unsigned)((32 * w + (lane >> 3)) * 896) + x8;
#pragma unroll
      for (int ii = 0; ii < 4; ++ii) {
        const unsigned u = (unsigned)(192 * (w & 3) + 64 * ii + lane), row = (u * 43691u) >> 19, rem = u - 12u * row;
        const unsigned piece = rem >> 2, c16 = rem & 3, hb = (c16 >> 1) ^ ((row >> 3) & 1);
        const unsigned wo_ = (ii < 3) ? row * 2688u + piece * 896u + hb * 32u + (c16 & 1) * 16u : 0u;
        voff[ii] = iw ? wo_ : xo + ii * 7168u;
      }
    }
    const unsigned long long mW = 0ull - (unsigned long long)iw;
    const unsigned long long bx_ = (unsigned long long)(uintptr_t)p.x + (unsigned)(c * 128 * 896), bw_ = (unsigned long long)(uintptr_t)p.wqkv + (unsigned)(56 * h - 8 * (h & 1));
    const char* gb = (const char*)(uintptr_t)(bx_ + ((bw_ - bx_) & mW));
    const unsigned kstep = 128 + iw * (64 * 2688 - 128), ldst = w * 4096 + iw * (WOFF + (w - 4) * 3072 - w * 4096);
    const unsigned smask = iw - 1u, l3 = iw ? (unsigned)M_FILL : ldst + 3072u;
    auto issue1 = [&](auto SL, auto II) {
      constexpr int sl = decltype(SL)::value, ii = decltype(II)::value;
      if constexpr (ii < 3) dma<sl * SLOT + ii * 1024, 0, false>(voff[ii], gb, ldst);
      else { dma<0, 0, false>(voff[3], gb, l3 + (smask & (unsigned)(sl * SLOT))); gb += kstep; }
    };
    auto issue = [&](auto SL) { issue1(SL, IC(0)); issue1(SL, IC(1)); issue1(SL, IC(2)); issue1(SL, IC(3)); };
    const int ptm = (b >= 144) ? 2 : (b >= 32) ? 1 : 0;
    const int ptq = b - ((ptm == 2) ? 144 : (ptm == 1) ? 32 : 0);
    const int pts = (ptm == 1) ? ptq >> 3 : ptq >> 1, ptG = (ptm == 1) ? ptq & 7 : ptq & 1;
    {
      const unsigned stride = (ptm == 1) ? 3584u : 896u;
      const unsigned long long m0_ = (unsigned long long)(uintptr_t)p.wo, m1_ = (unsigned long long)(uintptr_t)p.w1, m2_ = (unsigned long long)(uintptr_t)p.w2;
      const unsigned r0 = (ptm == 0) ? (unsigned)(28 * pts - 4 * (pts & 1)) : (unsigned)(32 * pts);
      const char* src = (const char*)(uintptr_t)(((ptm == 0) ? m0_ : (ptm == 1) ? m1_ : m2_) + (unsigned long long)(r0 * stride + ptG * 448));
      const int wq = (w < 7) ? w : 6;
#pragma unroll
      for (int i = 0; i < 2; ++i) { const unsigned u = (unsigned)((2 * wq + i) * 64 + lane), row = (u * 9363u) >> 18, c16 = u - 28u * row;
        dma<M_PT, 0, false>(row * stride + c16 * 16u, src, (unsigned)((2 * wq + i) * 1024)); }
      if (lane == 63) asm volatile("s_mov_b32 m0, %2\n\ts_nop 0\n\tglobal_load_lds_dword %0, %1 sc1" ::"v"(0u), "s"(p.ws + WS_CTL), "i"(M_PF) : "memory");
    }

    FS(1);
    issue(IC(0)); RAWBAR(); issue(IC(1)); RAWBAR(); issue(IC(2));
    FS(2);
    const int tg = w >> 1, ng = w & 1;
    f32x4 acc[3][2], asum = {0, 0, 0, 0}, asq = {0, 0, 0, 0}, acs[3];
#pragma unroll
    for (int e = 0; e < 3; ++e) { acc[e][0] = f32x4{0, 0, 0, 0}; acc[e][1] = f32x4{0, 0, 0, 0}; acs[e] = f32x4{0, 0, 0, 0}; }
    const int xb = (32 * tg + 16 * ng + l15) * 128 + ((g ^ (l15 & 7)) << 4);
    int ab[3];
#pragma unroll
    for (int e = 0; e < 3; ++e) ab[e] = WOFF + (8 * g + q4) * 192 + ((((3 * ng + e) * 32) ^ ((g & 1) << 5))) + 8 * p4;
    bf16x8 xf[2][2], af[2][3];
    auto rdp = [&](int st, int buf, int part) {
      ldsp sl = lds + ((st >> 1) % 5) * SLOT; const int ks = st & 1;
      if (part < 2) xf[buf][part] = ld128(sl + (xb ^ (ks << 6) ^ (part * 2048)));
      af[buf][part] = cat(ldtr(sl + ab[part] + (32 * ks) * 192), ldtr(sl + ab[part] + (32 * ks + 4) * 192));
    };
    auto rdf = [&](int st, int buf) { rdp(st, buf, 0); rdp(st, buf, 1); rdp(st, buf, 2); };
    unsigned tfl[4] = {0, 0, 0, 0};
    const unsigned tfo = (unsigned)(FLAG_IDX(4, 4 * g, l15) * 4);
    WAITV(8);
    BAR();
    FS(3);
    rdf(0, 0);
    sfor<0, 14>([&](auto ST) {
      constexpr int st = decltype(ST)::value, kc = st >> 1, cb = st & 1;
      if constexpr ((st & 1) && st < 13) {
        if constexpr (kc <= 1) WAITV(8); else if constexpr (kc <= 3) WAITV(6); else if constexpr (kc == 4) WAITV(4); else WAITV(0);
        BARN();
        if constexpr (st == 5) { if (tid == 0) st32_sc1(ctl + FLAG_IDX(4, b >> 4, b & 15), (unsigned)rfl((int)*(LAS unsigned*)(lds + M_PF + 252)) + 1u); }
        if constexpr (st == 11) {
          if (w == 7) {
#pragma unroll
            for (int i = 0; i < 4; ++i) asm volatile("global_load_dword %0, %1, %2 sc1" : "=v"(tfl[i]) : "v"(tfo + (unsigned)(i * 2048)), "s"(ctl) : "memory");
          }
        }
      }
      if constexpr (st == 0) {
        const unsigned ptb = (ptm == 0) ? WT_WO : (ptm == 1) ? WT_W1 : WT_W2, ptn = (ptm == 0) ? 16u : (ptm == 1) ? 14u : 56u;
        ldsp src = lds + M_PT + (4 * g + q4) * 448 + 8 * p4;
#pragma unroll
        for (int i = 0; i < 2; ++i) {
          const int t = (w + 8 * i < 14) ? w + 8 * i : w;
          const bf16x8 f = cat(ldtr(src + t * 32), ldtr(src + 16 * 448 + t * 32));
          st128_sc1(lane16 + ptb + ((unsigned)(14 * ptG + t) * ptn + (unsigned)pts) * 1024u, p.ws + WS_WT, __builtin_bit_cast(u32x4, f));
        }
      }
      SCHED();
      if constexpr (st < 13) rdp(st + 1, (st + 1) & 1, 0);
      acc[0][0] = MFMA(af[cb][0], xf[cb][0], acc[0][0]); acc[0][1] = MFMA(af[cb][0], xf[cb][1], acc[0][1]); asum = MFMA(onesf, xf[cb][0], asum); acs[0] = MFMA(af[cb][0], onesf, acs[0]);
      ILV(3);
      SCHED();
      if constexpr (kc + 3 <= 6) issue1(IC((kc + 3) % 5), IC(2 * (st & 1)));
      SCHED();
      if constexpr (st < 13) rdp(st + 1, (st + 1) & 1, 1);
      acc[1][0] = MFMA(af[cb][1], xf[cb][0], acc[1][0]); acc[1][1] = MFMA(af[cb][1], xf[cb][1], acc[1][1]); asq = MFMA(xf[cb][0], xf[cb][0], asq); acs[1] = MFMA(af[cb][1], onesf, acs[1]);
      ILV(3);
      SCHED();
      if constexpr (kc + 3 <= 6) issue1(IC((kc + 3) % 5), IC(2 * (st & 1) + 1));
      SCHED();
      if constexpr (st < 13) rdp(st + 1, (st + 1) & 1, 2);
      acc[2][0] = MFMA(af[cb][2], xf[cb][0], acc[2][0]); acc[2][1] = MFMA(af[cb][2], xf[cb][1], acc[2][1]); acs[2] = MFMA(af[cb][2], onesf, acs[2]);
      ILV(2);
      SCHED();
    });
    FS(4);
    STAMP(1);
    epoch = (unsigned)rfl((int)*(LAS unsigned*)(lds + M_PF + 252)) + 1u;
    if (w == 7) {
      asm volatile("s_waitcnt vmcnt(0)" : "+v"(tfl[0]), "+v"(tfl[1]), "+v"(tfl[2]), "+v"(tfl[3])::"memory");
#pragma unroll 1
      for (unsigned spins = 0; spins < SPIN_CAP && !__all(tfl[0] == epoch && tfl[1] == epoch && tfl[2] == epoch && tfl[3] == epoch); ++spins) {
        asm volatile("global_load_dword %0, %4, %5 sc1\n\tglobal_load_dword %1, %4, %5 offset:2048 sc1\n\tglobal_load_dword %2, %6, %5 sc1\n\tglobal_load_dword %3, %6, %5 offset:2048 sc1\n\ts_waitcnt vmcnt(0)"
                     : "=&v"(tfl[0]), "=&v"(tfl[1]), "=&v"(tfl[2]), "=&v"(tfl[3]) : "v"(tfo), "s"(ctl), "v"(tfo + 4096u) : "memory");
      }
    }
    {
      const float d01 = (p4 & 1) ? asq[1] : asq[0], d23 = (p4 & 1) ? asq[3] : asq[2], dq = (p4 & 2) ? d23 : d01;
      const float mean = asum[0] * (1.f / 448.f);
      if (g == q4) *(LAS f32x2*)(lds + M_LN + (32 * tg + 16 * ng + l15) * 8) = f32x2{mean, rsqrtf(fmaxf(dq * (1.f / 448.f) - mean * mean, 0.f) + 1e-5f)};
    }
    BAR();
    FS(5);
    float mu[2], rs[2];
#pragma unroll
    for (int t = 0; t < 2; ++t) { const f32x2 st_ = *(LAS f32x2*)(lds + M_LN + (32 * tg + 16 * (t ^ ng) + l15) * 8); mu[t] = st_[0]; rs[t] = st_[1]; }
    const bool pad0 = (h & 1) && g == 0, pad1 = !(h & 1) && g == 3;
    const int rsw = ((l15 >> 1) & 3) << 4, rowb = (32 * tg + l15) * 64;
    auto epi = [&](auto NG) {
      constexpr int ngc = decltype(NG)::value;
      sfor<0, 3>([&](auto E) {
        constexpr int e = decltype(E)::value, nt = 3 * ngc + e, which = nt >> 1, par = nt & 1;
        constexpr int ibase = (which == 0) ? QIMG : (which == 1) ? KIMG + 16384 : VIMG + 16384;
        const int off = (which == 2) ? ((((2 * par + (g >> 1)) << 4) ^ rsw) + (g & 1) * 8) : (((g << 4) ^ rsw) + par * 8);
        const bool pad = par ? pad1 : pad0;
#pragma unroll
        for (int t = 0; t < 2; ++t) {
          const float sc = (which == 0) ? rs[t] * 0.27264657f : rs[t], nm = -mu[t] * sc;
          const f32x2 v0 = lo2(acc[e][t]) * sc + lo2(acs[e]) * nm, v1 = hi2(acc[e][t]) * sc + hi2(acs[e]) * nm;
          u32x2 o; o.x = cvtpk(v0); o.y = cvtpk(v1);
          o.x = pad ? ((which == 2) ? 0x00003f80u : 0u) : o.x; o.y = pad ? 0u : o.y;
          *(LAS u32x2*)(lds + ibase + (t ^ ngc) * 1024 + rowb + off) = o;
        }
      });
    };
    if (ng) epi(IC(1)); else epi(IC(0));
    if (c < 2) {
      const u32x4 z = {0, 0, 0, 0};
#pragma unroll
      for (int i = 0; i < 2; ++i) { *(LAS u32x4*)(lds + KIMG + tid * 16 + i * 8192) = z; *(LAS u32x4*)(lds + VIMG + tid * 16 + i * 8192) = z; }
    }
    BAR();
    FS(6);
    {
      const char* blob = p.ws + WS_KV + (size_t)(c * 16 + h) * 16384;
      const u32x4 v0 = *(LAS u32x4*)(lds + KIMG + 16384 + tid * 16), v1 = *(LAS u32x4*)(lds + VIMG + 16384 + tid * 16);
      st128_sc1(tid * 16, blob, v0); st128_sc1(8192 + tid * 16, blob, v1);
    }
    ldfrags<4>(wr, wro, wt);
    FS(7);
    WAITV(4);
    BAR();
    if (tid == 0) st32_sc1(ctl + FLAG_IDX(0, c, h), epoch);
    FS(8);
    STAMP(2);
    if (w != 7) ldfrags<12>(wr + 4, wro + 4 * 1024, wt);
  }
  {
    int pos = lane & 15; if (pos >= 4 * ngr) pos = 4 * ngr - 1;
    int xch = (lane & 15) ^ (((lane >> 4) + 4 * w) & 15); if (xch >= 4 * ngr) xch = 0;
    dma<M_XT, 0, false>((unsigned)(((lane >> 4) + 4 * w) * 896 + xch * 16), p.x + (size_t)(128 * c + 32 * (j >> 2)) * 896 + col4 * 2, (unsigned)(w * 1024));
    if (w == 5 && lane < 16) dma<M_B1, 0, false>((unsigned)(pos * 16), p.b1 + (448 * (j >> 2) + col4) * 2, 0u);
    if (w == 4 && lane < 8) dma<M_B2, 0, false>((unsigned)((lane < 2 * nblk ? lane : 0) * 16), p.b2 + col0 * 2, 0u);
  }
  {
    const int h = j;
    FS(9);
    const int sw = (l15 >> 1) & 3;
    if (w == 7 && c >= 1) poll_flags(ctl, (unsigned)(FLAG_IDX(0, (c >= 2 && (lane & 1)) ? c - 2 : c - 1, h) * 4), epoch);
    BAR();
    FS(10);
    {
      const int tl = w >> 2, src = c - 2 + tl, pc = (w & 3) * 2048;
      if (src >= 0) {
        const char* blob = p.ws + WS_KV + (size_t)(src * 16 + h) * 16384 + pc;
        dma2c_sc1<KIMG>(lane16, blob, (unsigned)(tl * 8192 + pc)); dma2c_sc1<VIMG>(lane16, blob + 8192, (unsigned)(tl * 8192 + pc));
      }
    }
    const bf16x8 qf = ld128(lds + QIMG + (16 * w + l15) * 64 + ((g ^ sw) << 4));
    WAITV(0);
    BAR();
    FS(11);
    STAMP(3);
    STAMP(4);
    if (w == 7) ldfrags<12>(wr + 4, wro + 4 * 1024, wt);
    f32x4 sc[17];
    const float NEG = -__builtin_inff();
    ldsp kb = lds + KIMG + (16 * w + l15) * 64 + ((g ^ sw) << 4);
    {
      bf16x8 kf[2][6];
#pragma unroll
      for (int u = 0; u < 6; ++u) kf[0][u] = ld128(kb + u * 1024);
#pragma unroll
      for (int bt = 0; bt < 3; ++bt) {
        if (bt < 2) {
#pragma unroll
          for (int u = 0; u < 6; ++u) if (6 * (bt + 1) + u < 17) kf[(bt + 1) & 1][u] = ld128(kb + (6 * (bt + 1) + u) * 1024);
        }
        SCHED();
#pragma unroll
        for (int u = 0; u < 6; ++u) if (6 * bt + u < 17) sc[6 * bt + u] = MFMA(kf[bt & 1][u], qf, (f32x4{0, 0, 0, 0}));
        SCHED();
      }
    }
    FS(12);
    ldfrags<6>(wr + 16, wro + 16 * 1024, wt);
    const int vsw = (2 * g + (q4 >> 1)) & 3;
    ldsp vb0 = lds + VIMG + (16 * w + 4 * g + q4) * 64 + (((p4 >> 1) ^ vsw) << 4) + (p4 & 1) * 8;
    ldsp vb1 = lds + VIMG + (16 * w + 4 * g + q4) * 64 + (((2 + (p4 >> 1)) ^ vsw) << 4) + (p4 & 1) * 8;
    bf16x8 vf[2][3][2];
    auto rdv = [&](int bt) {
#pragma unroll
      for (int i = 0; i < 3; ++i) { const int uu = 3 * bt + i, ua = 2 * uu, ub = (uu < 8) ? 2 * uu + 1 : 15;
        vf[bt & 1][i][0] = cat(ldtr(vb0 + ua * 1024), ldtr(vb0 + ub * 1024)); vf[bt & 1][i][1] = cat(ldtr(vb1 + ua * 1024), ldtr(vb1 + ub * 1024)); }
    };
    rdv(0);
    SCHED();
    if (c < 2) {
      const int tmin = (2 - c) * 8;
#pragma unroll
      for (int u = 0; u < 16; ++u) if (w + u < tmin) sc[u] = f32x4{NEG, NEG, NEG, NEG};
    }
#pragma unroll
    for (int r = 0; r < 4; ++r) { sc[0][r] = (4 * g + r <= l15) ? NEG : sc[0][r]; sc[16][r] = (4 * g + r > l15) ? NEG : sc[16][r]; }
    float m = max3(sc[0][0], sc[0][1], sc[0][2]), m2 = max3(sc[0][3], sc[1][0], sc[1][1]);
    m = max3(m, sc[1][2], sc[1][3]);
#pragma unroll
    for (int u = 2; u < 17; ++u) { if (u & 1) m = max3(max3(m, sc[u][0], sc[u][1]), sc[u][2], sc[u][3]); else m2 = max3(max3(m2, sc[u][0], sc[u][1]), sc[u][2], sc[u][3]); }
    m = max3(m, m2, m2);
    m = red_g_max(m);
    FS(13);
    SCHED();
    ldfrags<6>(wr + 22, wro + 22 * 1024, wt);
    unsigned pk[17][2];
    const f32x2 mm = {m, m};
    f32x4 oa[2] = {f32x4{0, 0, 0, 0}, f32x4{0, 0, 0, 0}};
#pragma unroll
    for (int bt = 0; bt < 3; ++bt) {
#pragma unroll
      for (int u = 6 * bt; u < 6 * bt + 6 && u < 17; ++u) {
        const f32x2 d0 = lo2(sc[u]) - mm, d1 = hi2(sc[u]) - mm;
        pk[u][0] = cvtpk(__builtin_amdgcn_exp2f(d0[0]), __builtin_amdgcn_exp2f(d0[1]));
        pk[u][1] = cvtpk(__builtin_amdgcn_exp2f(d1[0]), __builtin_amdgcn_exp2f(d1[1]));
      }
      if (bt < 2) rdv(bt + 1);
      if (bt == 0 && w < 4) ldfrags<4>(wr + 28, wro + 28 * 1024, wt);
      SCHED();
#pragma unroll
      for (int i = 0; i < 3; ++i) {
        const int uu = 3 * bt + i;
        u32x4 pu; pu.x = pk[2 * uu][0]; pu.y = pk[2 * uu][1];
        if (uu < 8) { pu.z = pk[2 * uu + 1][0]; pu.w = pk[2 * uu + 1][1]; } else { pu.z = 0; pu.w = 0; }
        const bf16x8 pf = __builtin_bit_cast(bf16x8, pu);
        oa[0] = MFMA(vf[bt & 1][i][0], pf, oa[0]); oa[1] = MFMA(vf[bt & 1][i][1], pf, oa[1]);
      }
      SCHED();
    }
    FS(15);
    int tz2 = tid; asm volatile("" : "+v"(tz2));
    const int l15b = tz2 & 15, gb = (tz2 & 63) >> 4, swb = (l15b >> 1) & 3;
    const float lsel = (h & 1) ? oa[0][0] : oa[1][0];
    const float l = __uint_as_float((unsigned)__builtin_amdgcn_ds_bpermute((((h & 1) ? 0 : 48) + l15b) * 4, (int)__float_as_uint(lsel)));
    const float inv = __builtin_amdgcn_rcpf(l);
    u32x4 ov;
    ov.x = cvtpk(lo2(oa[0]) * inv); ov.y = cvtpk(hi2(oa[0]) * inv); ov.z = cvtpk(lo2(oa[1]) * inv); ov.w = cvtpk(hi2(oa[1]) * inv);
    { const bool z0 = (h & 1) && gb == 0, z1 = !(h & 1) && gb == 3; ov.x = z0 ? 0u : ov.x; ov.y = z0 ? 0u : ov.y; ov.z = z1 ? 0u : ov.z; ov.w = z1 ? 0u : ov.w; }
    st128_sc1((unsigned)((16 * w + l15b) * 64 + ((gb ^ swb) << 4)), p.ws + WS_O + (size_t)(c * 16 + h) * 8192, ov);
    FS(16);
    WAITV(0);
    BAR();
    if (tid == 0) st32_sc1(ctl + FLAG_IDX(1, c, h), epoch);
    FS(18);
    STAMP(5);
  }
  {
    LANEVARS();
    constexpr int OT = 0;
    const int tq = j >> 2;
    if (w >= 4) {
      pin<28>(wr);
#pragma unroll
      for (int i = 0; i < 28; ++i) *(LAS bf16x8*)(lds + 32768 + (w - 4) * 28672 + i * 1024 + lane * 16) = wr[i];
      __builtin_amdgcn_s_setprio(3);
      poll_flags(ctl, (unsigned)(FLAG_IDX(1, c, l15) * 4), epoch);
      const char* ob_ = p.ws + WS_O + (size_t)(c * 16 + 4 * (w - 4)) * 8192 + tq * 2048;
      const unsigned lb = (unsigned)(OT + (w - 4) * 8192);
      dma2c_sc1<0>(lane16, ob_, lb); dma2c_sc1<2048>(lane16 + 8192, ob_, lb); dma2c_sc1<4096>(lane16 + 16384, ob_, lb); dma2c_sc1<6144>(lane16 + 24576, ob_, lb);
      __builtin_amdgcn_s_setprio(0);
    }
    FS(19);
    WAITV(0);
    BAR();
    FS(20);
    STAMP(6);
    STAMP(7);
    if (w < 4) {
      pin<32>(wr);
      f32x4 a0[2], a1[2];
#pragma unroll
      for (int t = 0; t < 2; ++t) { a0[t] = f32x4{0, 0, 0, 0}; a1[t] = f32x4{0, 0, 0, 0}; }
      ldsp ob = lds + OT + l15 * 64 + ((g ^ ((l15 >> 1) & 3)) << 4);
      u32x2 xr[2][2];
#pragma unroll
      for (int t = 0; t < 2; ++t) { ldsp xp = lds + M_XT + (16 * t + l15) * 256 + (g & 1) * 8;
        xr[t][0] = *(LAS u32x2*)(xp + (((4 * prc + (g >> 1)) ^ l15) << 4)); xr[t][1] = *(LAS u32x2*)(xp + (((4 * prc + 2 + (g >> 1)) ^ l15) << 4)); }
      bf16x8 of[4][2];
#pragma unroll
      for (int s = 0; s < 3; ++s) { of[s][0] = ld128(ob + s * 2048); of[s][1] = ld128(ob + s * 2048 + 1024); }
#pragma unroll
      for (int s = 0; s < 16; ++s) {
        if (s + 3 < 16) { of[(s + 3) & 3][0] = ld128(ob + (s + 3) * 2048); of[(s + 3) & 3][1] = ld128(ob + (s + 3) * 2048 + 1024); }
        SCHED();
#pragma unroll
        for (int t = 0; t < 2; ++t) { a0[t] = MFMA(wr[s], of[s & 3][t], a0[t]); a1[t] = MFMA(wr[16 + s], of[s & 3][t], a1[t]); }
        SCHED();
      }
      FS(21);
      const unsigned ho = (unsigned)((128 * c + 32 * tq + l15) * 896 + (col4 / 32 + pr) * 64 + g * 16);
#pragma unroll
      for (int t = 0; t < 2; ++t) {
        u32x4 hv;
        hv.x = cvtpk(lo2(a0[t]) + bf2(xr[t][0].x)); hv.y = cvtpk(hi2(a0[t]) + bf2(xr[t][0].y));
        hv.z = cvtpk(lo2(a1[t]) + bf2(xr[t][1].x)); hv.w = cvtpk(hi2(a1[t]) + bf2(xr[t][1].y));
        if (pr < ngr) st128_sc1(ho + t * 16 * 896, p.ws + WS_H, hv);
        float s1 = 0.f, s2 = 0.f;
#pragma unroll
        for (int i = 0; i < 4; ++i) { s1 = dot2(hv[i], ONES, s1); s2 = dot2(hv[i], hv[i], s2); }
        s1 = red_g_sum(s1); s2 = red_g_sum(s2);
        if (pr >= ngr) { s1 = 0.f; s2 = 0.f; }
        if (g == 0) ((LAS f32x2*)(lds + M_PART))[(16 * t + l15) * 4 + pr] = f32x2{s1, s2};
      }
    }
    FS(22);
    BAR();
    if (tid < 32) {
      const f32x4 u0 = *(LAS f32x4*)(lds + M_PART + tid * 32), u1 = *(LAS f32x4*)(lds + M_PART + tid * 32 + 16);
      st64_sc1((unsigned)((128 * c + 32 * tq + tid) * 32 + ns * 8), p.ws + WS_ST,
               u32x2{__float_as_uint((u0[0] + u0[2]) + (u1[0] + u1[2])), __float_as_uint((u0[1] + u0[3]) + (u1[1] + u1[3]))});
    }
    if (w < 4) {
#pragma unroll
      for (int i = 0; i < 28; ++i) wr[i] = ld128(lds + 32768 + w * 28672 + i * 1024 + lane * 16);
    }
    FS(23);
    if (w < 4) WAITV(0);
    BAR();
    if (tid == 0) st32_sc1(ctl + FLAG_IDX(2, c, j), epoch);
    FS(25);
    STAMP(8);
  }
  {
    LANEVARS();
    const int f0_ = 448 * (j >> 2) + col4, th = wh;
    FS(26);
    f32x4 c0 = {0, 0, 0, 0}, c1 = {0, 0, 0, 0};
    pin<28>(wr);
#pragma unroll
    for (int s = 0; s < 14; ++s) { c0 = MFMA(wr[s], onesf, c0); c1 = MFMA(wr[14 + s], onesf, c1); }
    asm volatile("" : "+v"(c0), "+v"(c1));
    SCHED();
    if (w == 7) poll_flags(ctl, (unsigned)(FLAG_IDX(2, c, l15) * 4), epoch);
    FS(29);
    BAR();
    const unsigned vo = (unsigned)((16 * w + (lane >> 3)) * 896) + x8, wl2k = (unsigned)(w * 2048);
    const char* hb = p.ws + WS_H + (size_t)c * 128 * 896;
    auto issue4 = [&](auto KC) { constexpr int kc = decltype(KC)::value; dma<kc * 16384, kc * 128, true>(vo, hb, wl2k); dma<kc * 16384 + 1024, kc * 128 + 8 * 896, true>(vo, hb, wl2k); };
    if (lane < 32) dma<M_TILE, 0, true>(lane16, p.ws + WS_ST + (size_t)(128 * c + 16 * w) * 32, (unsigned)(w * 512));
    issue4(IC(0)); RAWBAR(); issue4(IC(1)); RAWBAR(); issue4(IC(2)); RAWBAR(); issue4(IC(3));
    FS(31);
    f32x4 acc[2][4];
#pragma unroll
    for (int t = 0; t < 4; ++t) { acc[0][t] = f32x4{0, 0, 0, 0}; acc[1][t] = f32x4{0, 0, 0, 0}; }
    const int xb = (64 * th + l15) * 128 + ((g ^ (l15 & 7)) << 4);
    bf16x8 xf[2][4];
    auto rdh = [&](int st, int buf, int half) {
      ldsp r = lds + (st >> 1) * 16384 + (xb ^ ((st & 1) << 6));
      xf[buf][2 * half] = ld128(r + 2 * half * 2048); xf[buf][2 * half + 1] = ld128(r + (2 * half + 1) * 2048);
    };
    auto rdf = [&](int st, int buf) { rdh(st, buf, 0); rdh(st, buf, 1); };
    bf16x8 bf[2][14];
    const unsigned w2o0 = WT_W2 + ((unsigned)(col0 / 16 + 2 * ng5) * 56u + 14u * kq) * 1024u, w2o1 = (2 * ng5 + 1 < nblk) ? w2o0 + 56u * 1024u : w2o0;
    WAITV(6); BAR();
    FS(32);
    STAMP(9);
    STAMP(10);
    rdf(0, 0);
    sfor<0, 14>([&](auto ST) {
      constexpr int st = decltype(ST)::value;
      if constexpr (st == 1) WAITV(4); if constexpr (st == 3) WAITV(8); if constexpr (st == 5) WAITV(12); if constexpr (st == 7) WAITV(16); if constexpr (st == 9) WAITV(14); if constexpr (st == 11) WAITV(12);
      if constexpr ((st & 1) && st < 13) BARN();
      if constexpr (st == 2) {
        if (tid < 128) {
          const f32x4 u0 = *(LAS f32x4*)(lds + M_TILE + tid * 32), u1 = *(LAS f32x4*)(lds + M_TILE + tid * 32 + 16);
          const float mean = ((u0[0] + u0[2]) + (u1[0] + u1[2])) * (1.f / 448.f);
          const float rstd = rsqrtf(fmaxf(((u0[1] + u0[3]) + (u1[1] + u1[3])) * (1.f / 448.f) - mean * mean, 0.f) + 1e-5f);
          *(LAS f32x2*)(lds + M_LN + tid * 8) = f32x2{rstd, -mean * rstd};
        }
      }
      SCHED();
      if constexpr (st < 13) rdh(st + 1, (st + 1) & 1, 0);
#pragma unroll
      for (int t = 0; t < 2; ++t) { acc[0][t] = MFMA(wr[st], xf[st & 1][t], acc[0][t]); acc[1][t] = MFMA(wr[14 + st], xf[st & 1][t], acc[1][t]); }
      ILV(2);
      SCHED();
      if constexpr (st == 1 || st == 3 || st == 5) issue4(IC((st + 7) / 2));
      if constexpr (st >= 1 && st <= 10) { constexpr int s = st - 1; ldfrags<1>(&bf[0][s], lane16 + w2o0 + s * 1024, wt); ldfrags<1>(&bf[1][s], lane16 + w2o1 + s * 1024, wt); }
      SCHED();
      if constexpr (st < 13) rdh(st + 1, (st + 1) & 1, 1);
#pragma unroll
      for (int t = 2; t < 4; ++t) { acc[0][t] = MFMA(wr[st], xf[st & 1][t], acc[0][t]); acc[1][t] = MFMA(wr[14 + st], xf[st & 1][t], acc[1][t]); }
      ILV(2);
      SCHED();
    });
    FS(33);
    STAMP(11);
    {
      const u32x2 bb0 = *(LAS u32x2*)(lds + M_B1 + (32 * prc + 4 * g) * 2), bb1 = *(LAS u32x2*)(lds + M_B1 + (32 * prc + 4 * g) * 2 + 32);
      const f32x2 b00 = bf2(bb0.x), b01 = bf2(bb0.y), b10 = bf2(bb1.x), b11 = bf2(bb1.y);
      const unsigned ho = (unsigned)((128 * c + 64 * th + l15) * 3584 + (f0_ / 32 + pr) * 64 + g * 16);
      f32x2 ln[4];
#pragma unroll
      for (int t = 0; t < 4; ++t) ln[t] = *(LAS f32x2*)(lds + M_LN + (64 * th + 16 * t + l15) * 8);
#pragma unroll
      for (int t = 0; t < 4; ++t) {
        const float a = ln[t][0], nm = ln[t][1];
        f32x2 z[4] = {lo2(acc[0][t]) * a + (lo2(c0) * nm + b00), hi2(acc[0][t]) * a + (hi2(c0) * nm + b01), lo2(acc[1][t]) * a + (lo2(c1) * nm + b10), hi2(acc[1][t]) * a + (hi2(c1) * nm + b11)};
        u32x4 hv;
#pragma unroll
        for (int i = 0; i < 4; ++i) {
          f32x2 r = {__builtin_elementwise_maximum(z[i][0], 0.f), __builtin_elementwise_maximum(z[i][1], 0.f)};
          hv[i] = cvtpk(r * r);
        }
        if (pr < ngr) st128_sc1(ho + t * 16 * 3584, p.ws + WS_H1, hv);
      }
    }
    FS(34);
    WAITV(0);
    BAR();
    if (tid == 0) st32_sc1(ctl + FLAG_IDX(3, c, j), epoch);
    FS(38);
    STAMP(12);
    if (w != 7) {
      ldfrags<4>(&bf[0][10], lane16 + w2o0 + 10 * 1024, wt); ldfrags<4>(&bf[1][10], lane16 + w2o1 + 10 * 1024, wt);
    }
    if (w >= 5 && w < 7) {
      const int m0_ = col0 >> 5;
      const char* hr = p.ws + WS_H + (size_t)(128 * c + 64 * th5) * 896;
      sfor<0, 6>([&](auto I) { constexpr int i = decltype(I)::value;
        const unsigned u = (unsigned)((w - 5) * 384 + i * 64 + lane), r0 = (u * 43691u) >> 19, c0_ = u - 12u * r0; unsigned cc0 = m0_ * 4 + c0_; if (cc0 > 55) cc0 = 55;
        dma<M_TILE + i * 1024, 0, true>(r0 * 896 + cc0 * 16, hr, (unsigned)((w - 5) * 6144)); });
    }
    const unsigned vo5 = (unsigned)((32 * ng5 + (lane >> 3)) * 3584 + kq * 896) + x8, wl4k = (unsigned)(w * 4096);
    const char* hb5 = p.ws + WS_H1 + (size_t)(128 * c + 64 * th5) * 3584;
    auto issue5 = [&](auto I) {
      constexpr int i = decltype(I)::value, sa = (i & 3) * 32768;
      sfor<0, 4>([&](auto II) { constexpr int ii = decltype(II)::value; dma<sa + ii * 1024, i * 128 + ii * 8 * 3584, true>(vo5, hb5, wl4k); });
    };
    FS(39);
    if (w == 7) {
      poll_flags(ctl, (unsigned)(FLAG_IDX(3, c, l15) * 4), epoch);
      if (b == 0) {
#pragma unroll 1
        for (int i = 0; i < 4; ++i) poll_flags(ctl, (unsigned)(FLAG_IDX(0, 4 * i + g, l15) * 4), epoch);
        if (lane == 0) st32_sc1(ctl, epoch);
      }
    }
    FS(40);
    BAR();
    FS(41);
    STAMP(13);
    issue5(IC(0)); RAWBAR(); issue5(IC(1));
    if (w == 7) {
      ldfrags<4>(&bf[0][10], lane16 + w2o0 + 10 * 1024, wt); ldfrags<4>(&bf[1][10], lane16 + w2o1 + 10 * 1024, wt);
    }
    FS(42);
    f32x4 ac5[2][4];
#pragma unroll
    for (int t = 0; t < 4; ++t) { ac5[0][t] = f32x4{0, 0, 0, 0}; ac5[1][t] = f32x4{0, 0, 0, 0}; }
    int yb[4];
#pragma unroll
    for (int t = 0; t < 4; ++t) yb[t] = kq * 8192 + (16 * ((t + kq) & 3) + l15) * 128 + ((g ^ (l15 & 7)) << 4);
    bf16x8 yf[2][4];
    auto rdz = [&](int st, int buf, int half) {
      const int sa = ((st >> 1) & 3) * 32768;
      yf[buf][2 * half] = ld128(lds + sa + (yb[2 * half] ^ ((st & 1) << 6))); yf[buf][2 * half + 1] = ld128(lds + sa + (yb[2 * half + 1] ^ ((st & 1) << 6)));
    };
    auto rdy = [&](int st, int buf) { rdz(st, buf, 0); rdz(st, buf, 1); };
    if (w == 7) WAITV(0); else WAITV(4);
    pin<14>(bf[0]); pin<14>(bf[1]);
    BAR();
    FS(43);
    STAMP(14);
    rdy(0, 0);
    sfor<0, 14>([&](auto ST) {
      constexpr int st = decltype(ST)::value;
      if constexpr ((st & 1) && st < 13) {
        if constexpr (st < 11) WAITV(4); else WAITV(0);
        BARN();
      }
      SCHED();
      if constexpr (st < 13) rdz(st + 1, (st + 1) & 1, 0);
#pragma unroll
      for (int t = 0; t < 2; ++t) { ac5[0][t] = MFMA(bf[0][st], yf[st & 1][t], ac5[0][t]); ac5[1][t] = MFMA(bf[1][st], yf[st & 1][t], ac5[1][t]); }
      ILV(2);
      SCHED();
      if constexpr (st == 0) issue5(IC(2));
      if constexpr ((st & 1) && st <= 7) issue5(IC((st + 5) / 2));
      SCHED();
      if constexpr (st < 13) rdz(st + 1, (st + 1) & 1, 1);
#pragma unroll
      for (int t = 2; t < 4; ++t) { ac5[0][t] = MFMA(bf[0][st], yf[st & 1][t], ac5[0][t]); ac5[1][t] = MFMA(bf[1][st], yf[st & 1][t], ac5[1][t]); }
      ILV(2);
      SCHED();
    });
    FS(44);
    BAR();
#pragma unroll
    for (int t = 1; t < 4; ++t) {
      ldsp d = lds + ((((t + kq) & 3) * 3 + (t - 1)) * 2 + ng5) * 2048 + lane * 16;
      *(LAS f32x4*)d = ac5[0][t]; *(LAS f32x4*)(d + 1024) = ac5[1][t];
    }
    const int tokl = 16 * kq + l15;
    const int t0 = col0 / 16 + 2 * ng5, t1 = (2 * ng5 + 1 < nblk) ? t0 + 1 : t0, m0_ = col0 >> 5;
    const u32x2 hr0 = *(LAS u32x2*)(lds + M_TILE + tokl * 192 + ((t0 >> 1) - m0_) * 64 + g * 16 + (t0 & 1) * 8);
    const u32x2 hr1 = *(LAS u32x2*)(lds + M_TILE + tokl * 192 + ((t1 >> 1) - m0_) * 64 + g * 16 + (t1 & 1) * 8);
    const u32x2 b20 = *(LAS u32x2*)(lds + M_B2 + (32 * ng5 + 4 * g) * 2), b21 = *(LAS u32x2*)(lds + M_B2 + ((2 * ng5 + 1 < nblk) ? (32 * ng5 + 16) : 32 * ng5) * 2 + 8 * g);
    f32x2 r00 = lo2(ac5[0][0]) + (bf2(b20.x) + bf2(hr0.x)), r01 = hi2(ac5[0][0]) + (bf2(b20.y) + bf2(hr0.y));
    f32x2 r10 = lo2(ac5[1][0]) + (bf2(b21.x) + bf2(hr1.x)), r11 = hi2(ac5[1][0]) + (bf2(b21.y) + bf2(hr1.y));
    BAR();
    FS(45);
#pragma unroll
    for (int t = 1; t < 4; ++t) {
      ldsp d = lds + ((kq * 3 + (t - 1)) * 2 + ng5) * 2048 + lane * 16;
      const f32x4 u0 = *(LAS f32x4*)d, u1 = *(LAS f32x4*)(d + 1024);
      r00 += lo2(u0); r01 += hi2(u0); r10 += lo2(u1); r11 += hi2(u1);
    }
    GAS char* op = (GAS char*)p.out + (size_t)(128 * c + 64 * th5 + tokl) * 896 + (col0 + 32 * ng5 + 4 * g) * 2;
    *(GAS u32x2*)op = u32x2{cvtpk(r00), cvtpk(r01)};
    if (2 * ng5 + 1 < nblk) *(GAS u32x2*)(op + 32) = u32x2{cvtpk(r10), cvtpk(r11)};
    STAMP(15);
    FS(63);
  }
}

extern "C" void kernel_launch(void* const* d_in, const int* in_sizes, int n_in, void* d_out, int out_size, void* d_ws, size_t ws_size, hipStream_t stream) {
  Params p;
  p.x = (const char*)d_in[0]; p.wqkv = (const char*)d_in[1]; p.wo = (const char*)d_in[2];
  p.w1 = (const char*)d_in[4]; p.b1 = (const char*)d_in[5]; p.w2 = (const char*)d_in[6]; p.b2 = (const char*)d_in[7];
  p.out = (char*)d_out; p.ws = (char*)d_ws;
  (void)hipFuncSetAttribute((const void*)fused_block, hipFuncAttributeMaxDynamicSharedMemorySize, LDS_BYTES);
  hipLaunchKernelGGL(fused_block, dim3(256), dim3(512), LDS_BYTES, stream, p);
}
```

```cpp
#include <hip/hip_runtime.h>
#include <hip/hip_bf16.h>
#include <stdint.h>
#include <type_traits>

#define LAS __attribute__((address_space(3)))
#define GAS __attribute__((address_space(1)))
typedef short bf16x8 __attribute__((ext_vector_type(8)));
typedef short s16x4 __attribute__((ext_vector_type(4)));
typedef short v4i16_t __attribute__((ext_vector_type(4)));
typedef float f32x4 __attribute__((ext_vector_type(4)));
typedef float f32x2 __attribute__((ext_vector_type(2)));
typedef unsigned u32x4 __attribute__((ext_vector_type(4)));
typedef unsigned u32x2 __attribute__((ext_vector_type(2)));
typedef __bf16 bf16x2_t __attribute__((ext_vector_type(2)));
typedef LAS char* ldsp;

constexpr int SEQ = 2048;
constexpr size_t WS_CTL = 0;
constexpr size_t WS_KV = 262144;
constexpr size_t WS_O = WS_KV + 256 * 16384;
constexpr size_t WS_H = WS_O + 256 * 8192;
constexpr size_t WS_H1 = WS_H + (size_t)SEQ * 896;
constexpr size_t WS_ST = WS_H1 + (size_t)SEQ * 3584;
constexpr size_t WS_WT = WS_ST + (size_t)SEQ * 32;
constexpr unsigned WT_W1 = 0, WT_W2 = WT_W1 + 1568 * 1024, WT_BYTES = WT_W2 + 1568 * 1024;
constexpr size_t WS_DBG = WS_WT + WT_BYTES;
constexpr int LDS_BYTES = 160 * 1024;
constexpr int M_PT = 143360;
constexpr int M_XT = 147456;
constexpr int M_TILE = 131072;
constexpr int MISC = 157696;
constexpr int M_LN = MISC, M_B1 = MISC + 1024, M_B2 = MISC + 1280, M_PF = MISC + 1536, M_PART = MISC + 2048, M_FILL = MISC + 3072;
constexpr unsigned SPIN_CAP = 1u << 16;

#define FLAG_IDX(ph, c, j) (64 + (((ph) * 16 + (c)) * 16 + (j)) * 32)

#define WAITV(n) asm volatile("s_waitcnt vmcnt(" #n ")" ::: "memory")
#define BAR() do { asm volatile("s_waitcnt lgkmcnt(0)" ::: "memory"); __builtin_amdgcn_s_barrier(); asm volatile("" ::: "memory"); } while (0)
#define BARN() do { asm volatile("" ::: "memory"); __builtin_amdgcn_s_barrier(); asm volatile("" ::: "memory"); } while (0)
#define SCHED() __builtin_amdgcn_sched_barrier(0)
#define RAWBAR() __builtin_amdgcn_s_barrier()
#define ILV(n) do { _Pragma("unroll") for (int i_ = 0; i_ < (n); ++i_) { __builtin_amdgcn_sched_group_barrier(0x008, 1, 0); __builtin_amdgcn_sched_group_barrier(0x100, 1, 0); } } while (0)
#define MFMA(a, b, c) __builtin_amdgcn_mfma_f32_16x16x32_bf16((a), (b), (c), 0, 0, 0)
#define IC(x) std::integral_constant<int, (x)>{}

template <int I, int N, class F> __device__ __forceinline__ void sfor(F&& f) { if constexpr (I < N) { f(std::integral_constant<int, I>{}); sfor<I + 1, N>(f); } }

__device__ __forceinline__ unsigned cvtpk(float lo, float hi) { f32x2 v = {lo, hi}; bf16x2_t b = __builtin_convertvector(v, bf16x2_t); return __builtin_bit_cast(unsigned, b); }
__device__ __forceinline__ unsigned cvtpk(f32x2 v) { bf16x2_t b = __builtin_convertvector(v, bf16x2_t); return __builtin_bit_cast(unsigned, b); }
__device__ __forceinline__ float dot2(unsigned a, unsigned b, float c) { return __builtin_amdgcn_fdot2_f32_bf16(__builtin_bit_cast(bf16x2_t, a), __builtin_bit_cast(bf16x2_t, b), c, false); }
__device__ __forceinline__ float bflo(unsigned u) { return __uint_as_float(u << 16); }
__device__ __forceinline__ float bfhi(unsigned u) { return __uint_as_float(u & 0xffff0000u); }
__device__ __forceinline__ f32x2 bf2(unsigned u) { return f32x2{bflo(u), bfhi(u)}; }
__device__ __forceinline__ f32x2 lo2(f32x4 v) { return f32x2{v[0], v[1]}; }
__device__ __forceinline__ f32x2 hi2(f32x4 v) { return f32x2{v[2], v[3]}; }
__device__ __forceinline__ bf16x8 ld128(ldsp p) { return *(const LAS bf16x8*)p; }
__device__ __forceinline__ s16x4 ldtr(ldsp p) { return __builtin_bit_cast(s16x4, __builtin_amdgcn_ds_read_tr16_b64_v4i16((LAS v4i16_t*)p)); }
__device__ __forceinline__ bf16x8 cat(s16x4 lo, s16x4 hi) { return __builtin_shufflevector(lo, hi, 0, 1, 2, 3, 4, 5, 6, 7); }
__device__ __forceinline__ int rfl(int x) { return __builtin_amdgcn_readfirstlane(x); }
template <int LADD, int VADD, bool SC1>
__device__ __forceinline__ void dma(unsigned voff, const void* sbase, unsigned lbase) {
  if constexpr (VADD == 0) {
    if constexpr (SC1) asm volatile("s_add_i32 m0, %2, %3\n\ts_nop 0\n\tglobal_load_lds_dwordx4 %0, %1 sc1" ::"v"(voff), "s"(sbase), "s"(lbase), "i"(LADD) : "memory", "scc");
    else asm volatile("s_add_i32 m0, %2, %3\n\ts_nop 0\n\tglobal_load_lds_dwordx4 %0, %1" ::"v"(voff), "s"(sbase), "s"(lbase), "i"(LADD) : "memory", "scc");
  } else {
    unsigned tmp;
    if constexpr (SC1) asm volatile("s_add_i32 m0, %3, %4\n\tv_add_u32 %0, %5, %1\n\tglobal_load_lds_dwordx4 %0, %2 sc1" : "=&v"(tmp) : "v"(voff), "s"(sbase), "s"(lbase), "i"(LADD), "i"(VADD) : "memory", "scc");
    else asm volatile("s_add_i32 m0, %3, %4\n\tv_add_u32 %0, %5, %1\n\tglobal_load_lds_dwordx4 %0, %2" : "=&v"(tmp) : "v"(voff), "s"(sbase), "s"(lbase), "i"(LADD), "i"(VADD) : "memory", "scc");
  }
}
template <int LADD>
__device__ __forceinline__ void dma2c_sc1(unsigned voff, const void* sbase, unsigned lbase) {
  asm volatile("s_add_i32 m0, %2, %3\n\ts_nop 0\n\tglobal_load_lds_dwordx4 %0, %1 sc1\n\tglobal_load_lds_dwordx4 %0, %1 offset:1024 sc1" ::"v"(voff), "s"(sbase), "s"(lbase), "i"(LADD) : "memory", "scc");
}
template <int OFF>
__device__ __forceinline__ void ldfrag1(bf16x8& f, unsigned voff, const void* sbase) {
  asm volatile("global_load_dwordx4 %0, %1, %2 offset:%3 sc1" : "=v"(f) : "v"(voff), "s"(sbase), "i"(OFF) : "memory");
}
__device__ __forceinline__ void pin1(bf16x8& f) { asm volatile("" : "+v"(f)); }
template <int N>
__device__ __forceinline__ void ldfrags(bf16x8* f, unsigned voff, const void* sbase) {
  sfor<0, N>([&](auto I) { constexpr int i = decltype(I)::value; ldfrag1<(i & 3) * 1024>(f[i], voff + (unsigned)((i >> 2) * 4096), sbase); });
}
template <int N>
__device__ __forceinline__ void pin(bf16x8* f) { sfor<0, N>([&](auto I) { pin1(f[decltype(I)::value]); }); }
__device__ __forceinline__ void st128_sc1(unsigned voff, const void* sbase, u32x4 v) {
  asm volatile("global_store_dwordx4 %0, %1, %2 sc1\n\ts_nop 1" ::"v"(voff), "v"(v), "s"(sbase) : "memory");
}
__device__ __forceinline__ void st64_sc1(unsigned voff, const void* sbase, u32x2 v) {
  asm volatile("global_store_dwordx2 %0, %1, %2 sc1\n\ts_nop 1" ::"v"(voff), "v"(v), "s"(sbase) : "memory");
}
__device__ __forceinline__ void st32_sc1(const void* sbase, unsigned v) {
  asm volatile("global_store_dword %0, %1, %2 sc1\n\ts_nop 1" ::"v"(0u), "v"(v), "s"(sbase) : "memory");
}
__device__ __forceinline__ float max3(float a, float b, float c) { return __builtin_elementwise_maximum(__builtin_elementwise_maximum(a, b), c); }
__device__ __forceinline__ float red_g_sum(float v) {
  auto a = __builtin_amdgcn_permlane16_swap(__float_as_uint(v), __float_as_uint(v), false, false);
  v = __uint_as_float(a[0]) + __uint_as_float(a[1]);
  auto b = __builtin_amdgcn_permlane32_swap(__float_as_uint(v), __float_as_uint(v), false, false);
  return __uint_as_float(b[0]) + __uint_as_float(b[1]);
}
__device__ __forceinline__ float red_g_max(float v) {
  auto a = __builtin_amdgcn_permlane16_swap(__float_as_uint(v), __float_as_uint(v), false, false);
  v = __builtin_elementwise_maximum(__uint_as_float(a[0]), __uint_as_float(a[1]));
  auto b = __builtin_amdgcn_permlane32_swap(__float_as_uint(v), __float_as_uint(v), false, false);
  return __builtin_elementwise_maximum(__uint_as_float(b[0]), __uint_as_float(b[1]));
}
__device__ __forceinline__ void poll_flags(const unsigned* base, unsigned voff, unsigned epoch) {
#pragma unroll 1
  for (unsigned spins = 0; spins < SPIN_CAP; ++spins) {
    unsigned v;
    asm volatile("global_load_dword %0, %1, %2 sc1\n\ts_waitcnt vmcnt(0)" : "=&v"(v) : "v"(voff), "s"(base) : "memory");
    if (__all(v == epoch)) break;
    __builtin_amdgcn_s_sleep(1);
  }
}

struct Params {
  const char* x; const char* wqkv; const char* wo; const char* w1; const char* b1; const char* w2; const char* b2;
  char* out; char* ws;
};

#define FS(i) do {} while (0)
#define STAMP(i) do {} while (0)

#define LANEVARS() int tz_ = tid; asm volatile("" : "+v"(tz_)); const int lane = tz_ & 63, l15 = lane & 15, g = lane >> 4, q4 = l15 >> 2, p4 = l15 & 3; \
  const unsigned lane16 = (unsigned)lane * 16u, x8 = (unsigned)(((lane & 7) ^ ((lane >> 3) & 7)) << 4); (void)q4; (void)p4; (void)g; (void)x8; (void)lane16

__global__ void __launch_bounds__(512, 2) fused_block(Params p) {
  extern __shared__ __attribute__((aligned(16))) char smem[];
  ldsp lds = (ldsp)smem;
  const int tid = threadIdx.x, lane = tid & 63, w = rfl(tid >> 6);
  const int l15 = lane & 15, g = lane >> 4, q4 = l15 >> 2, p4 = l15 & 3;
  const int b = blockIdx.x, r_ = b >> 3;
  const int c = 2 * (b & 7) + (r_ >> 4), j = r_ & 15;
  const unsigned* ctl = (const unsigned*)(p.ws + WS_CTL);
  unsigned epoch = 0;
  FS(0);
  STAMP(0);
  const unsigned ONES = 0x3f803f80u;
  const bf16x8 onesf = __builtin_bit_cast(bf16x8, (u32x4{ONES, ONES, ONES, ONES}));
  const unsigned lane16 = (unsigned)lane * 16u;
  const unsigned x8 = (unsigned)(((lane & 7) ^ ((lane >> 3) & 7)) << 4);

  constexpr int KIMG = 0, VIMG = 24576, QIMG = 49152;
  {
    const int h = j;
    constexpr int SLOT = 28672, WOFF = 16384;
    const unsigned iw = (unsigned)(w >> 2);
    unsigned voff[4];
    {
      const unsigned xo = (unsigned)((32 * w + (lane >> 3)) * 896) + x8;
#pragma unroll
      for (int ii = 0; ii < 4; ++ii) {
        const unsigned u = (unsigned)(192 * (w & 3) + 64 * ii + lane), row = (u * 43691u) >> 19, rem = u - 12u * row;
        const unsigned piece = rem >> 2, c16 = rem & 3, hb = (c16 >> 1) ^ ((row >> 3) & 1);
        const unsigned wo_ = (ii < 3) ? row * 2688u + piece * 896u + hb * 32u + (c16 & 1) * 16u : 0u;
        voff[ii] = iw ? wo_ : xo + ii * 7168u;
      }
    }
    const unsigned long long mW = 0ull - (unsigned long long)iw;
    const unsigned long long bx_ = (unsigned long long)(uintptr_t)p.x + (unsigned)(c * 128 * 896), bw_ = (unsigned long long)(uintptr_t)p.wqkv + (unsigned)(56 * h - 8 * (h & 1));
    const char* gb = (const char*)(uintptr_t)(bx_ + ((bw_ - bx_) & mW));
    const unsigned kstep = 128 + iw * (64 * 2688 - 128), ldst = w * 4096 + iw * (WOFF + (w - 4) * 3072 - w * 4096);
    const unsigned smask = iw - 1u, l3 = iw ? (unsigned)M_FILL : ldst + 3072u;
    auto issue1 = [&](auto SL, auto II) {
      constexpr int sl = decltype(SL)::value, ii = decltype(II)::value;
      if constexpr (ii < 3) dma<sl * SLOT + ii * 1024, 0, false>(voff[ii], gb, ldst);
      else { dma<0, 0, false>(voff[3], gb, l3 + (smask & (unsigned)(sl * SLOT))); gb += kstep; }
    };
    auto issue = [&](auto SL) { issue1(SL, IC(0)); issue1(SL, IC(1)); issue1(SL, IC(2)); issue1(SL, IC(3)); };
    FS(1);
    issue(IC(0)); RAWBAR(); issue(IC(1));
    FS(2);
    const int ptq0 = (b < 224) ? b : 223, ptm = (ptq0 >= 112) ? 2 : 1, ptq = ptq0 - ((ptm == 2) ? 112 : 0);
    const int pts = (ptm == 1) ? ptq >> 3 : ptq >> 1, ptG = (ptm == 1) ? ptq & 7 : ptq & 1;
    auto pt_issue = [&]() {
      const unsigned stride = (ptm == 1) ? 3584u : 896u;
      const char* src = ((ptm == 1) ? p.w1 : p.w2) + (size_t)(32 * pts * stride + ptG * 448);
      const int wq = (w < 7) ? w : 6;
#pragma unroll
      for (int i = 0; i < 2; ++i) { const unsigned u = (unsigned)((2 * wq + i) * 64 + lane), row = (u * 9363u) >> 18, c16 = u - 28u * row;
        dma<M_PT, 0, false>(row * stride + c16 * 16u, src, (unsigned)((2 * wq + i) * 1024)); }
      if (lane == 63) asm volatile("s_mov_b32 m0, %2\n\ts_nop 0\n\tglobal_load_lds_dword %0, %1 sc1" ::"v"(0u), "s"(p.ws + WS_CTL), "i"(M_PF) : "memory");
    };
    const int tg = w >> 1, ng = w & 1;
    f32x4 acc[3][2], asum = {0, 0, 0, 0}, asq = {0, 0, 0, 0}, acs[3];
#pragma unroll
    for (int e = 0; e < 3; ++e) { acc[e][0] = f32x4{0, 0, 0, 0}; acc[e][1] = f32x4{0, 0, 0, 0}; acs[e] = f32x4{0, 0, 0, 0}; }
    const int xb = (32 * tg + 16 * ng + l15) * 128 + ((g ^ (l15 & 7)) << 4);
    int ab[3];
#pragma unroll
    for (int e = 0; e < 3; ++e) ab[e] = WOFF + (8 * g + q4) * 192 + ((((3 * ng + e) * 32) ^ ((g & 1) << 5))) + 8 * p4;
    bf16x8 xf[2][2], af[2][3];
    auto rdp = [&](int st, int buf, int part) {
      ldsp sl = lds + ((st >> 1) % 5) * SLOT; const int ks = st & 1;
      if (part < 2) xf[buf][part] = ld128(sl + (xb ^ (ks << 6) ^ (part * 2048)));
      af[buf][part] = cat(ldtr(sl + ab[part] + (32 * ks) * 192), ldtr(sl + ab[part] + (32 * ks + 4) * 192));
    };
    auto rdf = [&](int st, int buf) { rdp(st, buf, 0); rdp(st, buf, 1); rdp(st, buf, 2); };
    WAITV(4);
    BAR();
    FS(3);
    rdf(0, 0);
    sfor<0, 14>([&](auto ST) {
      constexpr int st = decltype(ST)::value, kc = st >> 1, cb = st & 1;
      if constexpr ((st & 1) && st < 13) {
        if constexpr (kc == 0) WAITV(4); else if constexpr (kc == 1) WAITV(9); else if constexpr (kc <= 3) WAITV(6); else if constexpr (kc == 4) WAITV(4); else WAITV(0);
        BARN();
      }
      if constexpr (st == 2) pt_issue();
      SCHED();
      if constexpr (st < 13) rdp(st + 1, (st + 1) & 1, 0);
      acc[0][0] = MFMA(af[cb][0], xf[cb][0], acc[0][0]); acc[0][1] = MFMA(af[cb][0], xf[cb][1], acc[0][1]); asum = MFMA(onesf, xf[cb][0], asum); acs[0] = MFMA(af[cb][0], onesf, acs[0]);
      ILV(3);
      SCHED();
      if constexpr (st < 2) { issue1(IC(st + 2), IC(0)); issue1(IC(st + 2), IC(1)); }
      else if constexpr (kc + 3 <= 6) issue1(IC((kc + 3) % 5), IC(2 * (st & 1)));
      SCHED();
      if constexpr (st < 13) rdp(st + 1, (st + 1) & 1, 1);
      acc[1][0] = MFMA(af[cb][1], xf[cb][0], acc[1][0]); acc[1][1] = MFMA(af[cb][1], xf[cb][1], acc[1][1]); asq = MFMA(xf[cb][0], xf[cb][0], asq); acs[1] = MFMA(af[cb][1], onesf, acs[1]);
      ILV(3);
      SCHED();
      if constexpr (st < 2) { issue1(IC(st + 2), IC(2)); issue1(IC(st + 2), IC(3)); }
      else if constexpr (kc + 3 <= 6) issue1(IC((kc + 3) % 5), IC(2 * (st & 1) + 1));
      SCHED();
      if constexpr (st < 13) rdp(st + 1, (st + 1) & 1, 2);
      acc[2][0] = MFMA(af[cb][2], xf[cb][0], acc[2][0]); acc[2][1] = MFMA(af[cb][2], xf[cb][1], acc[2][1]); acs[2] = MFMA(af[cb][2], onesf, acs[2]);
      ILV(2);
      SCHED();
    });
    FS(4);
    STAMP(1);
    epoch = (unsigned)rfl((int)*(LAS unsigned*)(lds + M_PF + 252)) + 1u;
    {
      const unsigned ptb = (ptm == 1) ? WT_W1 : WT_W2, ptn = (ptm == 1) ? 14u : 56u;
      ldsp src = lds + M_PT + (4 * g + q4) * 448 + 8 * p4;
#pragma unroll
      for (int i = 0; i < 2; ++i) {
        const int t = (w + 8 * i < 14) ? w + 8 * i : w;
        const bf16x8 f = cat(ldtr(src + t * 32), ldtr(src + 16 * 448 + t * 32));
        st128_sc1(lane16 + ptb + ((unsigned)(14 * ptG + t) * ptn + (unsigned)pts) * 1024u, p.ws + WS_WT, __builtin_bit_cast(u32x4, f));
      }
    }
    {
      const float d01 = (p4 & 1) ? asq[1] : asq[0], d23 = (p4 & 1) ? asq[3] : asq[2], dq = (p4 & 2) ? d23 : d01;
      const float mean = asum[0] * (1.f / 448.f);
      if (g == q4) *(LAS f32x2*)(lds + M_LN + (32 * tg + 16 * ng + l15) * 8) = f32x2{mean, rsqrtf(fmaxf(dq * (1.f / 448.f) - mean * mean, 0.f) + 1e-5f)};
    }
    BAR();
    FS(5);
    float mu[2], rs[2];
#pragma unroll
    for (int t = 0; t < 2; ++t) { const f32x2 st_ = *(LAS f32x2*)(lds + M_LN + (32 * tg + 16 * (t ^ ng) + l15) * 8); mu[t] = st_[0]; rs[t] = st_[1]; }
    const bool pad0 = (h & 1) && g == 0, pad1 = !(h & 1) && g == 3;
    const int rsw = ((l15 >> 1) & 3) << 4, rowb = (32 * tg + l15) * 64;
    auto epi = [&](auto NG) {
      constexpr int ngc = decltype(NG)::value;
      sfor<0, 3>([&](auto E) {
        constexpr int e = decltype(E)::value, nt = 3 * ngc + e, which = nt >> 1, par = nt & 1;
        constexpr int ibase = (which == 0) ? QIMG : (which == 1) ? KIMG + 16384 : VIMG + 16384;
        const int off = (which == 2) ? ((((2 * par + (g >> 1)) << 4) ^ rsw) + (g & 1) * 8) : (((g << 4) ^ rsw) + par * 8);
        const bool pad = par ? pad1 : pad0;
#pragma unroll
        for (int t = 0; t < 2; ++t) {
          const float sc = (which == 0) ? rs[t] * 0.27264657f : rs[t], nm = -mu[t] * sc;
          const f32x2 v0 = lo2(acc[e][t]) * sc + lo2(acs[e]) * nm, v1 = hi2(acc[e][t]) * sc + hi2(acs[e]) * nm;
          u32x2 o; o.x = cvtpk(v0); o.y = cvtpk(v1);
          o.x = pad ? ((which == 2) ? 0x00003f80u : 0u) : o.x; o.y = pad ? 0u : o.y;
          *(LAS u32x2*)(lds + ibase + (t ^ ngc) * 1024 + rowb + off) = o;
        }
      });
    };
    if (ng) epi(IC(1)); else epi(IC(0));
    if (c < 2) {
      const u32x4 z = {0, 0, 0, 0};
#pragma unroll
      for (int i = 0; i < 2; ++i) { *(LAS u32x4*)(lds + KIMG + tid * 16 + i * 8192) = z; *(LAS u32x4*)(lds + VIMG + tid * 16 + i * 8192) = z; }
    }
    BAR();
    FS(6);
    {
      const char* blob = p.ws + WS_KV + (size_t)(c * 16 + h) * 16384;
      const u32x4 v0 = *(LAS u32x4*)(lds + KIMG + 16384 + tid * 16), v1 = *(LAS u32x4*)(lds + VIMG + 16384 + tid * 16);
      st128_sc1(tid * 16, blob, v0); st128_sc1(8192 + tid * 16, blob, v1);
    }
    FS(7);
    WAITV(0);
    BAR();
    if (tid == 0) { st32_sc1(ctl + FLAG_IDX(0, c, h), epoch); st32_sc1(ctl + FLAG_IDX(4, b >> 4, b & 15), epoch); }
    FS(8);
    STAMP(2);
  }
  const int ns = j & 3, col4 = 128 * ns - (((ns >> 1) & ns) << 5), ngr = 4 - (ns >> 1);
  const int pr = w & 3, wh = w >> 2;
  const int prc = (pr < ngr) ? pr : 0;
  const int th5 = j >> 3, cs5 = j & 7, col0 = 64 * cs5 - 16 * ((cs5 >> 2) * (cs5 & 3)), nblk = 4 - (cs5 >> 2);
  const int ng5 = w & 1, kq = w >> 1;
  const char* wt = p.ws + WS_WT;
  bf16x8 wr[32];
  const int T1 = (448 * (j >> 2) + col4) / 16;
  const unsigned w1o = WT_W1 + (unsigned)(T1 + 2 * prc) * 14u * 1024u;
  const unsigned wro = lane16 + w1o;
  constexpr int WI = 32768;
  unsigned wb0, wb1;
  { const int rin = lane >> 4, pos = lane & 15;
    int blk = (pos >> 1) ^ rin; if (blk >= 2 * ngr) blk -= 2; wb0 = (unsigned)(blk * 32 + (pos & 1) * 16);
    blk = (pos >> 1) ^ (4 + rin); if (blk >= 2 * ngr) blk -= 2; wb1 = (unsigned)(blk * 32 + (pos & 1) * 16); }
  const char* wob = p.wo + col4 * 2;
  {
    if (w < 6) {
      const unsigned rb = (unsigned)(((lane >> 4) + 4 * (24 + w)) * 896) + ((w & 1) ? wb1 : wb0);
      sfor<0, 14>([&](auto I) { constexpr int i = decltype(I)::value; dma<i * 6144, i * 24 * 896, false>(rb, wob, (unsigned)(WI + (24 + w) * 1024)); });
    }
    int pos = lane & 15; if (pos >= 4 * ngr) pos = 4 * ngr - 1;
    int xch = (lane & 15) ^ (((lane >> 4) + 4 * w) & 15); if (xch >= 4 * ngr) xch = 0;
    dma<M_XT, 0, false>((unsigned)(((lane >> 4) + 4 * w) * 896 + xch * 16), p.x + (size_t)(128 * c + 32 * (j >> 2)) * 896 + col4 * 2, (unsigned)(w * 1024));
    if (w == 5 && lane < 16) dma<M_B1, 0, false>((unsigned)(pos * 16), p.b1 + (448 * (j >> 2) + col4) * 2, 0u);
    if (w == 4 && lane < 8) dma<M_B2, 0, false>((unsigned)((lane < 2 * nblk ? lane : 0) * 16), p.b2 + col0 * 2, 0u);
  }
  {
    const int h = j;
    FS(9);
    const int sw = (l15 >> 1) & 3;
    if (w == 7 && c >= 1) poll_flags(ctl, (unsigned)(FLAG_IDX(0, (c >= 2 && (lane & 1)) ? c - 2 : c - 1, h) * 4), epoch);
    if (w == 6) {
      const unsigned tfo = (unsigned)(FLAG_IDX(4, 4 * g, l15) * 4);
#pragma unroll 1
      for (unsigned spins = 0; spins < SPIN_CAP; ++spins) {
        unsigned t0, t1, t2, t3;
        asm volatile("global_load_dword %0, %4, %5 sc1\n\tglobal_load_dword %1, %4, %5 offset:2048 sc1\n\tglobal_load_dword %2, %6, %5 sc1\n\tglobal_load_dword %3, %6, %5 offset:2048 sc1\n\ts_waitcnt vmcnt(0)"
                     : "=&v"(t0), "=&v"(t1), "=&v"(t2), "=&v"(t3) : "v"(tfo), "s"(ctl), "v"(tfo + 4096u) : "memory");
        if (__all(t0 == epoch && t1 == epoch && t2 == epoch && t3 == epoch)) break;
        __builtin_amdgcn_s_sleep(2);
      }
      if (b == 0 && lane == 0) st32_sc1(ctl, epoch);
    }
    BAR();
    FS(10);
    {
      const int tl = w >> 2, src = c - 2 + tl, pc = (w & 3) * 2048;
      if (src >= 0) {
        const char* blob = p.ws + WS_KV + (size_t)(src * 16 + h) * 16384 + pc;
        dma2c_sc1<KIMG>(lane16, blob, (unsigned)(tl * 8192 + pc)); dma2c_sc1<VIMG>(lane16, blob + 8192, (unsigned)(tl * 8192 + pc));
      }
    }
    const bf16x8 qf = ld128(lds + QIMG + (16 * w + l15) * 64 + ((g ^ sw) << 4));
    WAITV(0);
    BAR();
    FS(11);
    STAMP(3);
    STAMP(4);
    if (w >= 4) ldfrags<7>(wr, wro, wt);
    f32x4 sc[17];
    const float NEG = -__builtin_inff();
    ldsp kb = lds + KIMG + (16 * w + l15) * 64 + ((g ^ sw) << 4);
    {
      bf16x8 kf[2][6];
#pragma unroll
      for (int u = 0; u < 6; ++u) kf[0][u] = ld128(kb + u * 1024);
#pragma unroll
      for (int bt = 0; bt < 3; ++bt) {
        if (bt < 2) {
#pragma unroll
          for (int u = 0; u < 6; ++u) if (6 * (bt + 1) + u < 17) kf[(bt + 1) & 1][u] = ld128(kb + (6 * (bt + 1) + u) * 1024);
        }
        SCHED();
#pragma unroll
        for (int u = 0; u < 6; ++u) if (6 * bt + u < 17) sc[6 * bt + u] = MFMA(kf[bt & 1][u], qf, (f32x4{0, 0, 0, 0}));
        SCHED();
      }
    }
    FS(12);
    if (w >= 4) ldfrags<7>(wr + 7, wro + 7 * 1024, wt);
    const int vsw = (2 * g + (q4 >> 1)) & 3;
    ldsp vb0 = lds + VIMG + (16 * w + 4 * g + q4) * 64 + (((p4 >> 1) ^ vsw) << 4) + (p4 & 1) * 8;
    ldsp vb1 = lds + VIMG + (16 * w + 4 * g + q4) * 64 + (((2 + (p4 >> 1)) ^ vsw) << 4) + (p4 & 1) * 8;
    bf16x8 vf[2][3][2];
    auto rdv = [&](int bt) {
#pragma unroll
      for (int i = 0; i < 3; ++i) { const int uu = 3 * bt + i, ua = 2 * uu, ub = (uu < 8) ? 2 * uu + 1 : 15;
        vf[bt & 1][i][0] = cat(ldtr(vb0 + ua * 1024), ldtr(vb0 + ub * 1024)); vf[bt & 1][i][1] = cat(ldtr(vb1 + ua * 1024), ldtr(vb1 + ub * 1024)); }
    };
    rdv(0);
    SCHED();
    if (c < 2) {
      const int tmin = (2 - c) * 8;
#pragma unroll
      for (int u = 0; u < 16; ++u) if (w + u < tmin) sc[u] = f32x4{NEG, NEG, NEG, NEG};
    }
#pragma unroll
    for (int r = 0; r < 4; ++r) { sc[0][r] = (4 * g + r <= l15) ? NEG : sc[0][r]; sc[16][r] = (4 * g + r > l15) ? NEG : sc[16][r]; }
    float m = max3(sc[0][0], sc[0][1], sc[0][2]), m2 = max3(sc[0][3], sc[1][0], sc[1][1]);
    m = max3(m, sc[1][2], sc[1][3]);
#pragma unroll
    for (int u = 2; u < 17; ++u) { if (u & 1) m = max3(max3(m, sc[u][0], sc[u][1]), sc[u][2], sc[u][3]); else m2 = max3(max3(m2, sc[u][0], sc[u][1]), sc[u][2], sc[u][3]); }
    m = max3(m, m2, m2);
    m = red_g_max(m);
    FS(13);
    SCHED();
    if (w >= 4) ldfrags<7>(wr + 14, wro + 14 * 1024, wt);
    unsigned pk[17][2];
    const f32x2 mm = {m, m};
    f32x4 oa[2] = {f32x4{0, 0, 0, 0}, f32x4{0, 0, 0, 0}};
#pragma unroll
    for (int bt = 0; bt < 3; ++bt) {
#pragma unroll
      for (int u = 6 * bt; u < 6 * bt + 6 && u < 17; ++u) {
        const f32x2 d0 = lo2(sc[u]) - mm, d1 = hi2(sc[u]) - mm;
        pk[u][0] = cvtpk(__builtin_amdgcn_exp2f(d0[0]), __builtin_amdgcn_exp2f(d0[1]));
        pk[u][1] = cvtpk(__builtin_amdgcn_exp2f(d1[0]), __builtin_amdgcn_exp2f(d1[1]));
      }
      if (bt < 2) rdv(bt + 1);
      if (bt == 0 && w >= 4) ldfrags<7>(wr + 21, wro + 21 * 1024, wt);
      SCHED();
#pragma unroll
      for (int i = 0; i < 3; ++i) {
        const int uu = 3 * bt + i;
        u32x4 pu; pu.x = pk[2 * uu][0]; pu.y = pk[2 * uu][1];
        if (uu < 8) { pu.z = pk[2 * uu + 1][0]; pu.w = pk[2 * uu + 1][1]; } else { pu.z = 0; pu.w = 0; }
        const bf16x8 pf = __builtin_bit_cast(bf16x8, pu);
        oa[0] = MFMA(vf[bt & 1][i][0], pf, oa[0]); oa[1] = MFMA(vf[bt & 1][i][1], pf, oa[1]);
      }
      SCHED();
    }
    FS(15);
    int tz2 = tid; asm volatile("" : "+v"(tz2));
    const int l15b = tz2 & 15, gb = (tz2 & 63) >> 4, swb = (l15b >> 1) & 3;
    const float lsel = (h & 1) ? oa[0][0] : oa[1][0];
    const float l = __uint_as_float((unsigned)__builtin_amdgcn_ds_bpermute((((h & 1) ? 0 : 48) + l15b) * 4, (int)__float_as_uint(lsel)));
    const float inv = __builtin_amdgcn_rcpf(l);
    u32x4 ov;
    ov.x = cvtpk(lo2(oa[0]) * inv); ov.y = cvtpk(hi2(oa[0]) * inv); ov.z = cvtpk(lo2(oa[1]) * inv); ov.w = cvtpk(hi2(oa[1]) * inv);
    { const bool z0 = (h & 1) && gb == 0, z1 = !(h & 1) && gb == 3; ov.x = z0 ? 0u : ov.x; ov.y = z0 ? 0u : ov.y; ov.z = z1 ? 0u : ov.z; ov.w = z1 ? 0u : ov.w; }
    st128_sc1((unsigned)((16 * w + l15b) * 64 + ((gb ^ swb) << 4)), p.ws + WS_O + (size_t)(c * 16 + h) * 8192, ov);
    FS(16);
    BAR();
    {
      const unsigned rb = (unsigned)(((lane >> 4) + 12 * w) * 896), va = rb + ((w & 1) ? wb1 : wb0), vb = rb + ((w & 1) ? wb0 : wb1), lb = (unsigned)(WI + 3 * w * 1024);
      dma<0, 0, false>(va, wob, lb); dma<1024, 4 * 896, false>(vb, wob, lb); dma<2048, 8 * 896, false>(va, wob, lb);
      const int ins = (w < 4) ? 108 + w : 3 * w + 2;
      dma<0, 0, false>((unsigned)(((lane >> 4) + 4 * ins) * 896) + ((ins & 1) ? wb1 : wb0), wob, (unsigned)(WI + ins * 1024));
    }
    WAITV(4);
    BAR();
    if (tid == 0) st32_sc1(ctl + FLAG_IDX(1, c, h), epoch);
    FS(18);
    STAMP(5);
  }
  {
    LANEVARS();
    constexpr int OT = 0;
    const int tq = j >> 2;
    WAITV(0);
    BAR();
    if (w < 4) {
      const int rx = 4 * (g & 1) + q4;
      ldsp ab0 = lds + WI + (4 * g + q4) * 256 + (((2 * prc) ^ rx) << 5) + 8 * p4;
      ldsp ab1 = lds + WI + (4 * g + q4) * 256 + (((2 * prc + 1) ^ rx) << 5) + 8 * p4;
      ldsp ab0h = ab0 + 65536, ab1h = ab1 + 65536;
#pragma unroll
      for (int s = 0; s < 16; ++s) {
        const int rb = (28 * s - 4 * (s & 1)) * 256;
        if (rb + 4096 < 65536) { wr[s] = cat(ldtr(ab0 + rb), ldtr(ab0 + rb + 4096)); wr[16 + s] = cat(ldtr(ab1 + rb), ldtr(ab1 + rb + 4096)); }
        else { wr[s] = cat(ldtr(ab0h + (rb - 65536)), ldtr(ab0h + (rb - 65536 + 4096))); wr[16 + s] = cat(ldtr(ab1h + (rb - 65536)), ldtr(ab1h + (rb - 65536 + 4096))); }
      }
    } else {
      __builtin_amdgcn_s_setprio(3);
      poll_flags(ctl, (unsigned)(FLAG_IDX(1, c, l15) * 4), epoch);
      const char* ob_ = p.ws + WS_O + (size_t)(c * 16 + 4 * (w - 4)) * 8192 + tq * 2048;
      const unsigned lb = (unsigned)(OT + (w - 4) * 8192);
      dma2c_sc1<0>(lane16, ob_, lb); dma2c_sc1<2048>(lane16 + 8192, ob_, lb); dma2c_sc1<4096>(lane16 + 16384, ob_, lb); dma2c_sc1<6144>(lane16 + 24576, ob_, lb);
      __builtin_amdgcn_s_setprio(0);
    }
    FS(19);
    WAITV(0);
    BAR();
    FS(20);
    STAMP(6);
    STAMP(7);
    if (w < 4) {
      f32x4 a0[2], a1[2];
#pragma unroll
      for (int t = 0; t < 2; ++t) { a0[t] = f32x4{0, 0, 0, 0}; a1[t] = f32x4{0, 0, 0, 0}; }
      ldsp ob = lds + OT + l15 * 64 + ((g ^ ((l15 >> 1) & 3)) << 4);
      u32x2 xr[2][2];
#pragma unroll
      for (int t = 0; t < 2; ++t) { ldsp xp = lds + M_XT + (16 * t + l15) * 256 + (g & 1) * 8;
        xr[t][0] = *(LAS u32x2*)(xp + (((4 * prc + (g >> 1)) ^ l15) << 4)); xr[t][1] = *(LAS u32x2*)(xp + (((4 * prc + 2 + (g >> 1)) ^ l15) << 4)); }
      bf16x8 of[4][2];
#pragma unroll
      for (int s = 0; s < 3; ++s) { of[s][0] = ld128(ob + s * 2048); of[s][1] = ld128(ob + s * 2048 + 1024); }
#pragma unroll
      for (int s = 0; s < 16; ++s) {
        if (s + 3 < 16) { of[(s + 3) & 3][0] = ld128(ob + (s + 3) * 2048); of[(s + 3) & 3][1] = ld128(ob + (s + 3) * 2048 + 1024); }
        SCHED();
#pragma unroll
        for (int t = 0; t < 2; ++t) { a0[t] = MFMA(wr[s], of[s & 3][t], a0[t]); a1[t] = MFMA(wr[16 + s], of[s & 3][t], a1[t]); }
        SCHED();
      }
      FS(21);
      const unsigned ho = (unsigned)((128 * c + 32 * tq + l15) * 896 + (col4 / 32 + pr) * 64 + g * 16);
#pragma unroll
      for (int t = 0; t < 2; ++t) {
        u32x4 hv;
        hv.x = cvtpk(lo2(a0[t]) + bf2(xr[t][0].x)); hv.y = cvtpk(hi2(a0[t]) + bf2(xr[t][0].y));
        hv.z = cvtpk(lo2(a1[t]) + bf2(xr[t][1].x)); hv.w = cvtpk(hi2(a1[t]) + bf2(xr[t][1].y));
        if (pr < ngr) st128_sc1(ho + t * 16 * 896, p.ws + WS_H, hv);
        float s1 = 0.f, s2 = 0.f;
#pragma unroll
        for (int i = 0; i < 4; ++i) { s1 = dot2(hv[i], ONES, s1); s2 = dot2(hv[i], hv[i], s2); }
        s1 = red_g_sum(s1); s2 = red_g_sum(s2);
        if (pr >= ngr) { s1 = 0.f; s2 = 0.f; }
        if (g == 0) ((LAS f32x2*)(lds + M_PART))[(16 * t + l15) * 4 + pr] = f32x2{s1, s2};
      }
    } else {
      pin<28>(wr);
#pragma unroll
      for (int i = 0; i < 28; ++i) *(LAS bf16x8*)(lds + 32768 + (w - 4) * 28672 + i * 1024 + lane * 16) = wr[i];
    }
    FS(22);
    BAR();
    if (tid < 32) {
      const f32x4 u0 = *(LAS f32x4*)(lds + M_PART + tid * 32), u1 = *(LAS f32x4*)(lds + M_PART + tid * 32 + 16);
      st64_sc1((unsigned)((128 * c + 32 * tq + tid) * 32 + ns * 8), p.ws + WS_ST,
               u32x2{__float_as_uint((u0[0] + u0[2]) + (u1[0] + u1[2])), __float_as_uint((u0[1] + u0[3]) + (u1[1] + u1[3]))});
    }
    if (w < 4) {
#pragma unroll
      for (int i = 0; i < 28; ++i) wr[i] = ld128(lds + 32768 + w * 28672 + i * 1024 + lane * 16);
    }
    FS(23);
    if (w < 4) WAITV(0);
    BAR();
    if (tid == 0) st32_sc1(ctl + FLAG_IDX(2, c, j), epoch);
    FS(25);
    STAMP(8);
  }
  {
    LANEVARS();
    const int f0_ = 448 * (j >> 2) + col4, th = wh;
    FS(26);
    f32x4 c0 = {0, 0, 0, 0}, c1 = {0, 0, 0, 0};
    pin<28>(wr);
#pragma unroll
    for (int s = 0; s < 14; ++s) { c0 = MFMA(wr[s], onesf, c0); c1 = MFMA(wr[14 + s], onesf, c1); }
    asm volatile("" : "+v"(c0), "+v"(c1));
    SCHED();
    if (w == 7) poll_flags(ctl, (unsigned)(FLAG_IDX(2, c, l15) * 4), epoch);
    FS(29);
    BAR();
    const unsigned vo = (unsigned)((16 * w + (lane >> 3)) * 896) + x8, wl2k = (unsigned)(w * 2048);
    const char* hb = p.ws + WS_H + (size_t)c * 128 * 896;
    auto issue4 = [&](auto KC) { constexpr int kc = decltype(KC)::value; dma<kc * 16384, kc * 128, true>(vo, hb, wl2k); dma<kc * 16384 + 1024, kc * 128 + 8 * 896, true>(vo, hb, wl2k); };
    if (lane < 32) dma<M_TILE, 0, true>(lane16, p.ws + WS_ST + (size_t)(128 * c + 16 * w) * 32, (unsigned)(w * 512));
    issue4(IC(0)); RAWBAR(); issue4(IC(1));
    FS(31);
    f32x4 acc[2][4];
#pragma unroll
    for (int t = 0; t < 4; ++t) { acc[0][t] = f32x4{0, 0, 0, 0}; acc[1][t] = f32x4{0, 0, 0, 0}; }
    const int xb = (64 * th + l15) * 128 + ((g ^ (l15 & 7)) << 4);
    bf16x8 xf[2][4];
    auto rdh = [&](int st, int buf, int half) {
      ldsp r = lds + (st >> 1) * 16384 + (xb ^ ((st & 1) << 6));
      xf[buf][2 * half] = ld128(r + 2 * half * 2048); xf[buf][2 * half + 1] = ld128(r + (2 * half + 1) * 2048);
    };
    auto rdf = [&](int st, int buf) { rdh(st, buf, 0); rdh(st, buf, 1); };
    bf16x8 bf[2][14];
    const unsigned w2o0 = WT_W2 + ((unsigned)(col0 / 16 + 2 * ng5) * 56u + 14u * kq) * 1024u, w2o1 = (2 * ng5 + 1 < nblk) ? w2o0 + 56u * 1024u : w2o0;
    WAITV(2); BAR();
    FS(32);
    STAMP(9);
    STAMP(10);
    rdf(0, 0);
    sfor<0, 14>([&](auto ST) {
      constexpr int st = decltype(ST)::value;
      if constexpr (st == 1) WAITV(2); if constexpr (st == 3) WAITV(4); if constexpr (st == 5) WAITV(6); if constexpr (st == 7) WAITV(8); if constexpr (st == 9) WAITV(10); if constexpr (st == 11) WAITV(12);
      if constexpr ((st & 1) && st < 13) BARN();
      if constexpr (st == 2) {
        if (tid < 128) {
          const f32x4 u0 = *(LAS f32x4*)(lds + M_TILE + tid * 32), u1 = *(LAS f32x4*)(lds + M_TILE + tid * 32 + 16);
          const float mean = ((u0[0] + u0[2]) + (u1[0] + u1[2])) * (1.f / 448.f);
          const float rstd = rsqrtf(fmaxf(((u0[1] + u0[3]) + (u1[1] + u1[3])) * (1.f / 448.f) - mean * mean, 0.f) + 1e-5f);
          *(LAS f32x2*)(lds + M_LN + tid * 8) = f32x2{rstd, -mean * rstd};
        }
      }
      SCHED();
      if constexpr (st < 13) rdh(st + 1, (st + 1) & 1, 0);
#pragma unroll
      for (int t = 0; t < 2; ++t) { acc[0][t] = MFMA(wr[st], xf[st & 1][t], acc[0][t]); acc[1][t] = MFMA(wr[14 + st], xf[st & 1][t], acc[1][t]); }
      ILV(2);
      SCHED();
      if constexpr (st <= 4) issue4(IC(st + 2));
      if constexpr (st >= 5) { constexpr int s = st - 5; ldfrags<1>(&bf[0][s], lane16 + w2o0 + s * 1024, wt); ldfrags<1>(&bf[1][s], lane16 + w2o1 + s * 1024, wt); }
      SCHED();
      if constexpr (st < 13) rdh(st + 1, (st + 1) & 1, 1);
#pragma unroll
      for (int t = 2; t < 4; ++t) { acc[0][t] = MFMA(wr[st], xf[st & 1][t], acc[0][t]); acc[1][t] = MFMA(wr[14 + st], xf[st & 1][t], acc[1][t]); }
      ILV(2);
      SCHED();
    });
    FS(33);
    STAMP(11);
    {
      const u32x2 bb0 = *(LAS u32x2*)(lds + M_B1 + (32 * prc + 4 * g) * 2), bb1 = *(LAS u32x2*)(lds + M_B1 + (32 * prc + 4 * g) * 2 + 32);
      const f32x2 b00 = bf2(bb0.x), b01 = bf2(bb0.y), b10 = bf2(bb1.x), b11 = bf2(bb1.y);
      const unsigned ho = (unsigned)((128 * c + 64 * th + l15) * 3584 + (f0_ / 32 + pr) * 64 + g * 16);
      f32x2 ln[4];
#pragma unroll
      for (int t = 0; t < 4; ++t) ln[t] = *(LAS f32x2*)(lds + M_LN + (64 * th + 16 * t + l15) * 8);
#pragma unroll
      for (int t = 0; t < 4; ++t) {
        const float a = ln[t][0], nm = ln[t][1];
        f32x2 z[4] = {lo2(acc[0][t]) * a + (lo2(c0) * nm + b00), hi2(acc[0][t]) * a + (hi2(c0) * nm + b01), lo2(acc[1][t]) * a + (lo2(c1) * nm + b10), hi2(acc[1][t]) * a + (hi2(c1) * nm + b11)};
        u32x4 hv;
#pragma unroll
        for (int i = 0; i < 4; ++i) {
          f32x2 r = {__builtin_elementwise_maximum(z[i][0], 0.f), __builtin_elementwise_maximum(z[i][1], 0.f)};
          hv[i] = cvtpk(r * r);
        }
        if (pr < ngr) st128_sc1(ho + t * 16 * 3584, p.ws + WS_H1, hv);
      }
    }
    FS(34);
    WAITV(0);
    BAR();
    if (tid == 0) st32_sc1(ctl + FLAG_IDX(3, c, j), epoch);
    FS(38);
    STAMP(12);
    if (w != 7) {
      ldfrags<5>(&bf[0][9], lane16 + w2o0 + 9 * 1024, wt); ldfrags<5>(&bf[1][9], lane16 + w2o1 + 9 * 1024, wt);
    }
    if (w >= 5 && w < 7) {
      const int m0_ = col0 >> 5;
      const char* hr = p.ws + WS_H + (size_t)(128 * c + 64 * th5) * 896;
      sfor<0, 6>([&](auto I) { constexpr int i = decltype(I)::value;
        const unsigned u = (unsigned)((w - 5) * 384 + i * 64 + lane), r0 = (u * 43691u) >> 19, c0_ = u - 12u * r0; unsigned cc0 = m0_ * 4 + c0_; if (cc0 > 55) cc0 = 55;
        dma<M_TILE + i * 1024, 0, true>(r0 * 896 + cc0 * 16, hr, (unsigned)((w - 5) * 6144)); });
    }
    const unsigned vo5 = (unsigned)((32 * ng5 + (lane >> 3)) * 3584 + kq * 896) + x8, wl4k = (unsigned)(w * 4096);
    const char* hb5 = p.ws + WS_H1 + (size_t)(128 * c + 64 * th5) * 3584;
    auto issue5 = [&](auto I) {
      constexpr int i = decltype(I)::value, sa = (i & 3) * 32768;
      sfor<0, 4>([&](auto II) { constexpr int ii = decltype(II)::value; dma<sa + ii * 1024, i * 128 + ii * 8 * 3584, true>(vo5, hb5, wl4k); });
    };
    FS(39);
    if (w == 7) {
      poll_flags(ctl, (unsigned)(FLAG_IDX(3, c, l15) * 4), epoch);
    }
    FS(40);
    BAR();
    FS(41);
    STAMP(13);
    issue5(IC(0));
    if (w == 7) {
      ldfrags<5>(&bf[0][9], lane16 + w2o0 + 9 * 1024, wt); ldfrags<5>(&bf[1][9], lane16 + w2o1 + 9 * 1024, wt);
    }
    FS(42);
    f32x4 ac5[2][4];
#pragma unroll
    for (int t = 0; t < 4; ++t) { ac5[0][t] = f32x4{0, 0, 0, 0}; ac5[1][t] = f32x4{0, 0, 0, 0}; }
    int yb[4];
#pragma unroll
    for (int t = 0; t < 4; ++t) yb[t] = kq * 8192 + (16 * ((t + kq) & 3) + l15) * 128 + ((g ^ (l15 & 7)) << 4);
    bf16x8 yf[2][4];
    auto rdz = [&](int st, int buf, int half) {
      const int sa = ((st >> 1) & 3) * 32768;
      yf[buf][2 * half] = ld128(lds + sa + (yb[2 * half] ^ ((st & 1) << 6))); yf[buf][2 * half + 1] = ld128(lds + sa + (yb[2 * half + 1] ^ ((st & 1) << 6)));
    };
    auto rdy = [&](int st, int buf) { rdz(st, buf, 0); rdz(st, buf, 1); };
    WAITV(0);
    pin<14>(bf[0]); pin<14>(bf[1]);
    BAR();
    FS(43);
    STAMP(14);
    rdy(0, 0);
    sfor<0, 14>([&](auto ST) {
      constexpr int st = decltype(ST)::value;
      if constexpr ((st & 1) && st < 13) {
        if constexpr (st < 11) WAITV(4); else WAITV(0);
        BARN();
      }
      SCHED();
      if constexpr (st < 13) rdz(st + 1, (st + 1) & 1, 0);
#pragma unroll
      for (int t = 0; t < 2; ++t) { ac5[0][t] = MFMA(bf[0][st], yf[st & 1][t], ac5[0][t]); ac5[1][t] = MFMA(bf[1][st], yf[st & 1][t], ac5[1][t]); }
      ILV(2);
      SCHED();
      if constexpr (st == 0) { issue5(IC(1)); issue5(IC(2)); }
      if constexpr ((st & 1) && st <= 7) issue5(IC((st + 5) / 2));
      SCHED();
      if constexpr (st < 13) rdz(st + 1, (st + 1) & 1, 1);
#pragma unroll
      for (int t = 2; t < 4; ++t) { ac5[0][t] = MFMA(bf[0][st], yf[st & 1][t], ac5[0][t]); ac5[1][t] = MFMA(bf[1][st], yf[st & 1][t], ac5[1][t]); }
      ILV(2);
      SCHED();
    });
    FS(44);
    BAR();
#pragma unroll
    for (int t = 1; t < 4; ++t) {
      ldsp d = lds + ((((t + kq) & 3) * 3 + (t - 1)) * 2 + ng5) * 2048 + lane * 16;
      *(LAS f32x4*)d = ac5[0][t]; *(LAS f32x4*)(d + 1024) = ac5[1][t];
    }
    const int tokl = 16 * kq + l15;
    const int t0 = col0 / 16 + 2 * ng5, t1 = (2 * ng5 + 1 < nblk) ? t0 + 1 : t0, m0_ = col0 >> 5;
    const u32x2 hr0 = *(LAS u32x2*)(lds + M_TILE + tokl * 192 + ((t0 >> 1) - m0_) * 64 + g * 16 + (t0 & 1) * 8);
    const u32x2 hr1 = *(LAS u32x2*)(lds + M_TILE + tokl * 192 + ((t1 >> 1) - m0_) * 64 + g * 16 + (t1 & 1) * 8);
    const u32x2 b20 = *(LAS u32x2*)(lds + M_B2 + (32 * ng5 + 4 * g) * 2), b21 = *(LAS u32x2*)(lds + M_B2 + ((2 * ng5 + 1 < nblk) ? (32 * ng5 + 16) : 32 * ng5) * 2 + 8 * g);
    f32x2 r00 = lo2(ac5[0][0]) + (bf2(b20.x) + bf2(hr0.x)), r01 = hi2(ac5[0][0]) + (bf2(b20.y) + bf2(hr0.y));
    f32x2 r10 = lo2(ac5[1][0]) + (bf2(b21.x) + bf2(hr1.x)), r11 = hi2(ac5[1][0]) + (bf2(b21.y) + bf2(hr1.y));
    BAR();
    FS(45);
#pragma unroll
    for (int t = 1; t < 4; ++t) {
      ldsp d = lds + ((kq * 3 + (t - 1)) * 2 + ng5) * 2048 + lane * 16;
      const f32x4 u0 = *(LAS f32x4*)d, u1 = *(LAS f32x4*)(d + 1024);
      r00 += lo2(u0); r01 += hi2(u0); r10 += lo2(u1); r11 += hi2(u1);
    }
    GAS char* op = (GAS char*)p.out + (size_t)(128 * c + 64 * th5 + tokl) * 896 + (col0 + 32 * ng5 + 4 * g) * 2;
    *(GAS u32x2*)op = u32x2{cvtpk(r00), cvtpk(r01)};
    if (2 * ng5 + 1 < nblk) *(GAS u32x2*)(op + 32) = u32x2{cvtpk(r10), cvtpk(r11)};
    STAMP(15);
    FS(63);
  }
}

extern "C" void kernel_launch(void* const* d_in, const int* in_sizes, int n_in, void* d_out, int out_size, void* d_ws, size_t ws_size, hipStream_t stream) {
  Params p;
  p.x = (const char*)d_in[0]; p.wqkv = (const char*)d_in[1]; p.wo = (const char*)d_in[2];
  p.w1 = (const char*)d_in[4]; p.b1 = (const char*)d_in[5]; p.w2 = (const char*)d_in[6]; p.b2 = (const char*)d_in[7];
  p.out = (char*)d_out; p.ws = (char*)d_ws;
  (void)hipFuncSetAttribute((const void*)fused_block, hipFuncAttributeMaxDynamicSharedMemorySize, LDS_BYTES);
  hipLaunchKernelGGL(fused_block, dim3(256), dim3(512), LDS_BYTES, stream, p);
}
```

```cpp
#include <hip/hip_runtime.h>
#include <hip/hip_bf16.h>
#include <stdint.h>
#include <type_traits>

#define LAS __attribute__((address_space(3)))
#define GAS __attribute__((address_space(1)))
typedef short bf16x8 __attribute__((ext_vector_type(8)));
typedef short s16x4 __attribute__((ext_vector_type(4)));
typedef short v4i16_t __attribute__((ext_vector_type(4)));
typedef float f32x4 __attribute__((ext_vector_type(4)));
typedef float f32x2 __attribute__((ext_vector_type(2)));
typedef unsigned u32x4 __attribute__((ext_vector_type(4)));
typedef unsigned u32x2 __attribute__((ext_vector_type(2)));
typedef __bf16 bf16x2_t __attribute__((ext_vector_type(2)));
typedef LAS char* ldsp;

constexpr int SEQ = 2048;
constexpr size_t WS_CTL = 0;
constexpr size_t WS_KV = 262144;
constexpr size_t WS_O = WS_KV + 256 * 16384;
constexpr size_t WS_H = WS_O + 256 * 8192;
constexpr size_t WS_H1 = WS_H + (size_t)SEQ * 896;
constexpr size_t WS_ST = WS_H1 + (size_t)SEQ * 3584;
constexpr size_t WS_WT = WS_ST + (size_t)SEQ * 32;
constexpr unsigned WT_W1 = 0, WT_W2 = WT_W1 + 1568 * 1024, WT_BYTES = WT_W2 + 1568 * 1024;
constexpr size_t WS_DBG = WS_WT + WT_BYTES;
constexpr int LDS_BYTES = 160 * 1024;
constexpr int M_PT = 143360;
constexpr int M_XT = 147456;
constexpr int M_TILE = 131072;
constexpr int MISC = 157696;
constexpr int M_LN = MISC, M_B1 = MISC + 1024, M_B2 = MISC + 1280, M_PF = MISC + 1536, M_PART = MISC + 2048, M_FILL = MISC + 3072;
constexpr unsigned SPIN_CAP = 1u << 16;

#define FLAG_IDX(ph, c, j) (64 + (((ph) * 16 + (c)) * 16 + (j)) * 32)

#define WAITV(n) asm volatile("s_waitcnt vmcnt(" #n ")" ::: "memory")
#define BAR() do { asm volatile("s_waitcnt lgkmcnt(0)" ::: "memory"); __builtin_amdgcn_s_barrier(); asm volatile("" ::: "memory"); } while (0)
#define BARN() do { asm volatile("" ::: "memory"); __builtin_amdgcn_s_barrier(); asm volatile("" ::: "memory"); } while (0)
#define SCHED() __builtin_amdgcn_sched_barrier(0)
#define RAWBAR() __builtin_amdgcn_s_barrier()
#define ILV(n) do { _Pragma("unroll") for (int i_ = 0; i_ < (n); ++i_) { __builtin_amdgcn_sched_group_barrier(0x008, 1, 0); __builtin_amdgcn_sched_group_barrier(0x100, 1, 0); } } while (0)
#define MFMA(a, b, c) __builtin_amdgcn_mfma_f32_16x16x32_bf16((a), (b), (c), 0, 0, 0)
#define MACC(acc, a, b) asm volatile("v_mfma_f32_16x16x32_bf16 %0, %1, %2, %0" : "+v"(acc) : "v"(a), "v"(b))
#define MACCN(acc, a, b) asm volatile("s_nop 1\n\tv_mfma_f32_16x16x32_bf16 %0, %1, %2, %0" : "+v"(acc) : "v"(a), "v"(b))
#define NOPS() asm volatile("s_nop 7\n\ts_nop 3")
#define KEEP(x) asm volatile("" : "+v"(x))
#define IC(x) std::integral_constant<int, (x)>{}

template <int I, int N, class F> __device__ __forceinline__ void sfor(F&& f) { if constexpr (I < N) { f(std::integral_constant<int, I>{}); sfor<I + 1, N>(f); } }

__device__ __forceinline__ unsigned cvtpk(float lo, float hi) { f32x2 v = {lo, hi}; bf16x2_t b = __builtin_convertvector(v, bf16x2_t); return __builtin_bit_cast(unsigned, b); }
__device__ __forceinline__ unsigned cvtpk(f32x2 v) { bf16x2_t b = __builtin_convertvector(v, bf16x2_t); return __builtin_bit_cast(unsigned, b); }
__device__ __forceinline__ float dot2(unsigned a, unsigned b, float c) { return __builtin_amdgcn_fdot2_f32_bf16(__builtin_bit_cast(bf16x2_t, a), __builtin_bit_cast(bf16x2_t, b), c, false); }
__device__ __forceinline__ float bflo(unsigned u) { return __uint_as_float(u << 16); }
__device__ __forceinline__ float bfhi(unsigned u) { return __uint_as_float(u & 0xffff0000u); }
__device__ __forceinline__ f32x2 bf2(unsigned u) { return f32x2{bflo(u), bfhi(u)}; }
__device__ __forceinline__ f32x2 lo2(f32x4 v) { return f32x2{v[0], v[1]}; }
__device__ __forceinline__ f32x2 hi2(f32x4 v) { return f32x2{v[2], v[3]}; }
__device__ __forceinline__ bf16x8 ld128(ldsp p) { return *(const LAS bf16x8*)p; }
__device__ __forceinline__ s16x4 ldtr(ldsp p) { return __builtin_bit_cast(s16x4, __builtin_amdgcn_ds_read_tr16_b64_v4i16((LAS v4i16_t*)p)); }
__device__ __forceinline__ bf16x8 cat(s16x4 lo, s16x4 hi) { return __builtin_shufflevector(lo, hi, 0, 1, 2, 3, 4, 5, 6, 7); }
__device__ __forceinline__ int rfl(int x) { return __builtin_amdgcn_readfirstlane(x); }
template <int LADD, int VADD, bool SC1>
__device__ __forceinline__ void dma(unsigned voff, const void* sbase, unsigned lbase) {
  const unsigned va = voff + (unsigned)VADD;
  if constexpr (SC1) asm volatile("s_add_i32 m0, %2, %3\n\ts_nop 0\n\tglobal_load_lds_dwordx4 %0, %1 sc1" ::"v"(va), "s"(sbase), "s"(lbase), "i"(LADD) : "memory", "scc");
  else asm volatile("s_add_i32 m0, %2, %3\n\ts_nop 0\n\tglobal_load_lds_dwordx4 %0, %1" ::"v"(va), "s"(sbase), "s"(lbase), "i"(LADD) : "memory", "scc");
}
template <int LADD>
__device__ __forceinline__ void dma2c_sc1(unsigned voff, const void* sbase, unsigned lbase) {
  asm volatile("s_add_i32 m0, %2, %3\n\ts_nop 0\n\tglobal_load_lds_dwordx4 %0, %1 sc1\n\tglobal_load_lds_dwordx4 %0, %1 offset:1024 sc1" ::"v"(voff), "s"(sbase), "s"(lbase), "i"(LADD) : "memory", "scc");
}
template <int OFF>
__device__ __forceinline__ void ldfrag1(bf16x8& f, unsigned voff, const void* sbase) {
  asm volatile("global_load_dwordx4 %0, %1, %2 offset:%3 sc1" : "=v"(f) : "v"(voff), "s"(sbase), "i"(OFF) : "memory");
}
__device__ __forceinline__ void pin1(bf16x8& f) { asm volatile("" : "+v"(f)); }
template <int N>
__device__ __forceinline__ void ldfrags(bf16x8* f, unsigned voff, const void* sbase) {
  sfor<0, N>([&](auto I) { constexpr int i = decltype(I)::value; ldfrag1<(i & 3) * 1024>(f[i], voff + (unsigned)((i >> 2) * 4096), sbase); });
}
template <int N>
__device__ __forceinline__ void pin(bf16x8* f) { sfor<0, N>([&](auto I) { pin1(f[decltype(I)::value]); }); }
__device__ __forceinline__ void st128_sc1(unsigned voff, const void* sbase, u32x4 v) {
  asm volatile("global_store_dwordx4 %0, %1, %2 sc1\n\ts_nop 1" ::"v"(voff), "v"(v), "s"(sbase) : "memory");
}
__device__ __forceinline__ void st64_sc1(unsigned voff, const void* sbase, u32x2 v) {
  asm volatile("global_store_dwordx2 %0, %1, %2 sc1\n\ts_nop 1" ::"v"(voff), "v"(v), "s"(sbase) : "memory");
}
__device__ __forceinline__ void st32_sc1(const void* sbase, unsigned v) {
  asm volatile("global_store_dword %0, %1, %2 sc1\n\ts_nop 1" ::"v"(0u), "v"(v), "s"(sbase) : "memory");
}
__device__ __forceinline__ float max3(float a, float b, float c) { return __builtin_elementwise_maximum(__builtin_elementwise_maximum(a, b), c); }
__device__ __forceinline__ float red_g_sum(float v) {
  auto a = __builtin_amdgcn_permlane16_swap(__float_as_uint(v), __float_as_uint(v), false, false);
  v = __uint_as_float(a[0]) + __uint_as_float(a[1]);
  auto b = __builtin_amdgcn_permlane32_swap(__float_as_uint(v), __float_as_uint(v), false, false);
  return __uint_as_float(b[0]) + __uint_as_float(b[1]);
}
__device__ __forceinline__ float red_g_max(float v) {
  auto a = __builtin_amdgcn_permlane16_swap(__float_as_uint(v), __float_as_uint(v), false, false);
  v = __builtin_elementwise_maximum(__uint_as_float(a[0]), __uint_as_float(a[1]));
  auto b = __builtin_amdgcn_permlane32_swap(__float_as_uint(v), __float_as_uint(v), false, false);
  return __builtin_elementwise_maximum(__uint_as_float(b[0]), __uint_as_float(b[1]));
}
__device__ __forceinline__ void poll_flags(const unsigned* base, unsigned voff, unsigned epoch) {
#pragma unroll 1
  for (unsigned spins = 0; spins < SPIN_CAP; ++spins) {
    unsigned v;
    asm volatile("global_load_dword %0, %1, %2 sc1\n\ts_waitcnt vmcnt(0)" : "=&v"(v) : "v"(voff), "s"(base) : "memory");
    if (__all(v == epoch)) break;
    __builtin_amdgcn_s_sleep(1);
  }
}

struct Params {
  const char* x; const char* wqkv; const char* wo; const char* w1; const char* b1; const char* w2; const char* b2;
  char* out; char* ws;
};

#define FS(i) do {} while (0)
#define STAMP(i) do {} while (0)

#define LANEVARS() int tz_ = tid; asm volatile("" : "+v"(tz_)); const int lane = tz_ & 63, l15 = lane & 15, g = lane >> 4, q4 = l15 >> 2, p4 = l15 & 3; \
  const unsigned lane16 = (unsigned)lane * 16u, x8 = (unsigned)(((lane & 7) ^ ((lane >> 3) & 7)) << 4); (void)q4; (void)p4; (void)g; (void)x8; (void)lane16

__global__ void __launch_bounds__(512, 2) fused_block(Params p) {
  extern __shared__ __attribute__((aligned(16))) char smem[];
  ldsp lds = (ldsp)smem;
  const int tid = threadIdx.x, lane = tid & 63, w = rfl(tid >> 6);
  const int l15 = lane & 15, g = lane >> 4, q4 = l15 >> 2, p4 = l15 & 3;
  const int b = blockIdx.x, r_ = b >> 3;
  const int c = 2 * (b & 7) + (r_ >> 4), j = r_ & 15;
  const unsigned* ctl = (const unsigned*)(p.ws + WS_CTL);
  unsigned epoch = 0;
  FS(0);
  STAMP(0);
  const unsigned ONES = 0x3f803f80u;
  const unsigned lane16 = (unsigned)lane * 16u;
  const unsigned x8 = (unsigned)(((lane & 7) ^ ((lane >> 3) & 7)) << 4);

  constexpr int KIMG = 0, VIMG = 24576, QIMG = 49152;
  {
    const int h = j;
    constexpr int SLOT = 28672, WOFF = 16384;
    const unsigned iw = (unsigned)(w >> 2);
    unsigned voff[4];
    {
      const unsigned xo = (unsigned)((32 * w + (lane >> 3)) * 896) + x8;
#pragma unroll
      for (int ii = 0; ii < 4; ++ii) {
        const unsigned u = (unsigned)(192 * (w & 3) + 64 * ii + lane), row = (u * 43691u) >> 19, rem = u - 12u * row;
        const unsigned piece = rem >> 2, c16 = rem & 3, hb = (c16 >> 1) ^ ((row >> 3) & 1);
        const unsigned wo_ = (ii < 3) ? row * 2688u + piece * 896u + hb * 32u + (c16 & 1) * 16u : 0u;
        voff[ii] = iw ? wo_ : xo + ii * 7168u;
      }
    }
    const unsigned long long mW = 0ull - (unsigned long long)iw;
    const unsigned long long bx_ = (unsigned long long)(uintptr_t)p.x + (unsigned)(c * 128 * 896), bw_ = (unsigned long long)(uintptr_t)p.wqkv + (unsigned)(56 * h - 8 * (h & 1));
    const char* gb = (const char*)(uintptr_t)(bx_ + ((bw_ - bx_) & mW));
    const unsigned kstep = 128 + iw * (64 * 2688 - 128), ldst = w * 4096 + iw * (WOFF + (w - 4) * 3072 - w * 4096);
    const unsigned smask = iw - 1u, l3 = iw ? (unsigned)M_FILL : ldst + 3072u;
    auto issue1 = [&](auto SL, auto II) {
      constexpr int sl = decltype(SL)::value, ii = decltype(II)::value;
      if constexpr (ii < 3) dma<sl * SLOT + ii * 1024, 0, false>(voff[ii], gb, ldst);
      else { dma<0, 0, false>(voff[3], gb, l3 + (smask & (unsigned)(sl * SLOT))); gb += kstep; }
    };
    auto issue = [&](auto SL) { issue1(SL, IC(0)); issue1(SL, IC(1)); issue1(SL, IC(2)); issue1(SL, IC(3)); };
    FS(1);
    issue(IC(0)); RAWBAR(); issue(IC(1));
    FS(2);
    const int ptq0 = (b < 224) ? b : 223, ptm = (ptq0 >= 112) ? 2 : 1, ptq = ptq0 - ((ptm == 2) ? 112 : 0);
    const int pts = (ptm == 1) ? ptq >> 3 : ptq >> 1, ptG = (ptm == 1) ? ptq & 7 : ptq & 1;
    auto pt_issue = [&]() {
      const unsigned stride = (ptm == 1) ? 3584u : 896u;
      const char* src = ((ptm == 1) ? p.w1 : p.w2) + (size_t)(32 * pts * stride + ptG * 448);
      const int wq = (w < 7) ? w : 6;
#pragma unroll
      for (int i = 0; i < 2; ++i) { const unsigned u = (unsigned)((2 * wq + i) * 64 + lane), row = (u * 9363u) >> 18, c16 = u - 28u * row;
        dma<M_PT, 0, false>(row * stride + c16 * 16u, src, (unsigned)((2 * wq + i) * 1024)); }
      if (lane == 63) asm volatile("s_mov_b32 m0, %2\n\ts_nop 0\n\tglobal_load_lds_dword %0, %1 sc1" ::"v"(0u), "s"(p.ws + WS_CTL), "i"(M_PF) : "memory");
    };
    const int tg = w >> 1, ng = w & 1;
    f32x4 acc[3][2], asum = {0, 0, 0, 0}, asq = {0, 0, 0, 0}, acs[3];
#pragma unroll
    for (int e = 0; e < 3; ++e) { acc[e][0] = f32x4{0, 0, 0, 0}; acc[e][1] = f32x4{0, 0, 0, 0}; acs[e] = f32x4{0, 0, 0, 0}; }
    const int xb = (32 * tg + 16 * ng + l15) * 128 + ((g ^ (l15 & 7)) << 4);
    int ab[3];
#pragma unroll
    for (int e = 0; e < 3; ++e) ab[e] = WOFF + (8 * g + q4) * 192 + ((((3 * ng + e) * 32) ^ ((g & 1) << 5))) + 8 * p4;
    bf16x8 xf[2][2];
    s16x4 al[2][3], ah[2][3];
    auto rdx = [&](int st, int buf, int t) { xf[buf][t] = ld128(lds + ((st >> 1) % 5) * SLOT + (xb ^ ((st & 1) << 6) ^ (t * 2048))); };
    auto rdl = [&](int st, int buf, int e) { al[buf][e] = ldtr(lds + ((st >> 1) % 5) * SLOT + ab[e] + (32 * (st & 1)) * 192); };
    auto rdh_ = [&](int st, int buf, int e) { ah[buf][e] = ldtr(lds + ((st >> 1) % 5) * SLOT + ab[e] + (32 * (st & 1) + 4) * 192); };
    auto rdf = [&](int st, int buf) { rdx(st, buf, 0); rdx(st, buf, 1);
#pragma unroll
      for (int e = 0; e < 3; ++e) { rdl(st, buf, e); rdh_(st, buf, e); } };
    bf16x8 onesf = __builtin_bit_cast(bf16x8, (u32x4{ONES, ONES, ONES, ONES}));
    KEEP(onesf); KEEP(asum); KEEP(asq);
#pragma unroll
    for (int e = 0; e < 3; ++e) { KEEP(acc[e][0]); KEEP(acc[e][1]); KEEP(acs[e]); }
    WAITV(4);
    BAR();
    FS(3);
    rdf(0, 0);
    sfor<0, 14>([&](auto ST) {
      constexpr int st = decltype(ST)::value, kc = st >> 1, cb = st & 1;
      if constexpr ((st & 1) && st < 13) {
        if constexpr (kc == 0) WAITV(4); else if constexpr (kc == 1) WAITV(9); else if constexpr (kc <= 3) WAITV(6); else if constexpr (kc == 4) WAITV(4); else WAITV(0);
        BARN();
      }
      if constexpr (st == 2) pt_issue();
      constexpr int nb = (st + 1) & 1; constexpr bool more = st < 13;
      const bf16x8 a0 = cat(al[cb][0], ah[cb][0]), a1 = cat(al[cb][1], ah[cb][1]), a2 = cat(al[cb][2], ah[cb][2]);
      SCHED();
      MACC(acc[0][0], a0, xf[cb][0]); if constexpr (more) rdx(st + 1, nb, 0);
      MACC(acc[0][1], a0, xf[cb][1]); if constexpr (more) rdl(st + 1, nb, 0);
      MACC(asum, onesf, xf[cb][0]); if constexpr (more) rdh_(st + 1, nb, 0);
      MACC(acs[0], a0, onesf);
      SCHED();
      if constexpr (st < 2) { issue1(IC(st + 2), IC(0)); issue1(IC(st + 2), IC(1)); }
      else if constexpr (kc + 3 <= 6) issue1(IC((kc + 3) % 5), IC(2 * (st & 1)));
      SCHED();
      MACC(acc[1][0], a1, xf[cb][0]); if constexpr (more) rdx(st + 1, nb, 1);
      MACC(acc[1][1], a1, xf[cb][1]); if constexpr (more) rdl(st + 1, nb, 1);
      MACC(asq, xf[cb][0], xf[cb][0]); if constexpr (more) rdh_(st + 1, nb, 1);
      MACC(acs[1], a1, onesf);
      SCHED();
      if constexpr (st < 2) { issue1(IC(st + 2), IC(2)); issue1(IC(st + 2), IC(3)); }
      else if constexpr (kc + 3 <= 6) issue1(IC((kc + 3) % 5), IC(2 * (st & 1) + 1));
      SCHED();
      MACC(acc[2][0], a2, xf[cb][0]); if constexpr (more) rdl(st + 1, nb, 2);
      MACC(acc[2][1], a2, xf[cb][1]); if constexpr (more) rdh_(st + 1, nb, 2);
      MACC(acs[2], a2, onesf);
      SCHED();
    });
    NOPS(); KEEP(asum); KEEP(asq);
#pragma unroll
    for (int e = 0; e < 3; ++e) { KEEP(acc[e][0]); KEEP(acc[e][1]); KEEP(acs[e]); }
    FS(4);
    STAMP(1);
    epoch = (unsigned)rfl((int)*(LAS unsigned*)(lds + M_PF + 252)) + 1u;
    {
      const unsigned ptb = (ptm == 1) ? WT_W1 : WT_W2, ptn = (ptm == 1) ? 14u : 56u;
      ldsp src = lds + M_PT + (4 * g + q4) * 448 + 8 * p4;
#pragma unroll
      for (int i = 0; i < 2; ++i) {
        const int t = (w + 8 * i < 14) ? w + 8 * i : w;
        const bf16x8 f = cat(ldtr(src + t * 32), ldtr(src + 16 * 448 + t * 32));
        st128_sc1(lane16 + ptb + ((unsigned)(14 * ptG + t) * ptn + (unsigned)pts) * 1024u, p.ws + WS_WT, __builtin_bit_cast(u32x4, f));
      }
    }
    {
      const float d01 = (p4 & 1) ? asq[1] : asq[0], d23 = (p4 & 1) ? asq[3] : asq[2], dq = (p4 & 2) ? d23 : d01;
      float mean = asum[0] * (1.f / 448.f), dq2 = dq;
      KEEP(mean); KEEP(dq2);
      if (g == q4) *(LAS f32x2*)(lds + M_LN + (32 * tg + 16 * ng + l15) * 8) = f32x2{mean, rsqrtf(fmaxf(dq2 * (1.f / 448.f) - mean * mean, 0.f) + 1e-5f)};
    }
    BAR();
    FS(5);
    float mu[2], rs[2];
#pragma unroll
    for (int t = 0; t < 2; ++t) { const f32x2 st_ = *(LAS f32x2*)(lds + M_LN + (32 * tg + 16 * (t ^ ng) + l15) * 8); mu[t] = st_[0]; rs[t] = st_[1]; }
    const bool pad0 = (h & 1) && g == 0, pad1 = !(h & 1) && g == 3;
    const int rsw = ((l15 >> 1) & 3) << 4, rowb = (32 * tg + l15) * 64;
    auto epi = [&](auto NG) {
      constexpr int ngc = decltype(NG)::value;
      sfor<0, 3>([&](auto E) {
        constexpr int e = decltype(E)::value, nt = 3 * ngc + e, which = nt >> 1, par = nt & 1;
        constexpr int ibase = (which == 0) ? QIMG : (which == 1) ? KIMG + 16384 : VIMG + 16384;
        const int off = (which == 2) ? ((((2 * par + (g >> 1)) << 4) ^ rsw) + (g & 1) * 8) : (((g << 4) ^ rsw) + par * 8);
        const bool pad = par ? pad1 : pad0;
#pragma unroll
        for (int t = 0; t < 2; ++t) {
          const float sc = (which == 0) ? rs[t] * 0.27264657f : rs[t], nm = -mu[t] * sc;
          f32x2 sc2 = {sc, sc}, nm2 = {nm, nm};
          KEEP(sc2); KEEP(nm2);
          const f32x2 v0 = lo2(acc[e][t]) * sc2 + lo2(acs[e]) * nm2, v1 = hi2(acc[e][t]) * sc2 + hi2(acs[e]) * nm2;
          u32x2 o; o.x = cvtpk(v0); o.y = cvtpk(v1);
          o.x = pad ? ((which == 2) ? 0x00003f80u : 0u) : o.x; o.y = pad ? 0u : o.y;
          *(LAS u32x2*)(lds + ibase + (t ^ ngc) * 1024 + rowb + off) = o;
        }
      });
    };
    if (ng) epi(IC(1)); else epi(IC(0));
    if (c < 2) {
      const u32x4 z = {0, 0, 0, 0};
#pragma unroll
      for (int i = 0; i < 2; ++i) { *(LAS u32x4*)(lds + KIMG + tid * 16 + i * 8192) = z; *(LAS u32x4*)(lds + VIMG + tid * 16 + i * 8192) = z; }
    }
    BAR();
    FS(6);
    {
      const char* blob = p.ws + WS_KV + (size_t)(c * 16 + h) * 16384;
      const u32x4 v0 = *(LAS u32x4*)(lds + KIMG + 16384 + tid * 16), v1 = *(LAS u32x4*)(lds + VIMG + 16384 + tid * 16);
      st128_sc1(tid * 16, blob, v0); st128_sc1(8192 + tid * 16, blob, v1);
    }
    FS(7);
    WAITV(0);
    BAR();
    if (tid == 0) { st32_sc1(ctl + FLAG_IDX(0, c, h), epoch); st32_sc1(ctl + FLAG_IDX(4, b >> 4, b & 15), epoch); }
    FS(8);
    STAMP(2);
  }
  const int ns = j & 3, col4 = 128 * ns - (((ns >> 1) & ns) << 5), ngr = 4 - (ns >> 1);
  const int pr = w & 3, wh = w >> 2;
  const int prc = (pr < ngr) ? pr : 0;
  const int th5 = j >> 3, cs5 = j & 7, col0 = 64 * cs5 - 16 * ((cs5 >> 2) * (cs5 & 3)), nblk = 4 - (cs5 >> 2);
  const int ng5 = w & 1, kq = w >> 1;
  const char* wt = p.ws + WS_WT;
  bf16x8 wr[32];
  const int T1 = (448 * (j >> 2) + col4) / 16;
  const unsigned w1o = WT_W1 + (unsigned)(T1 + 2 * prc) * 14u * 1024u;
  const unsigned wro = lane16 + w1o;
  constexpr int WI = 32768;
  unsigned wb0, wb1;
  { const int rin = lane >> 4, pos = lane & 15;
    int blk = (pos >> 1) ^ rin; if (blk >= 2 * ngr) blk -= 2; wb0 = (unsigned)(blk * 32 + (pos & 1) * 16);
    blk = (pos >> 1) ^ (4 + rin); if (blk >= 2 * ngr) blk -= 2; wb1 = (unsigned)(blk * 32 + (pos & 1) * 16); }
  const char* wob = p.wo + col4 * 2;
  {
    if (w < 6) {
      const unsigned rb = (unsigned)(((lane >> 4) + 4 * (24 + w)) * 896) + ((w & 1) ? wb1 : wb0);
      sfor<0, 14>([&](auto I) { constexpr int i = decltype(I)::value; dma<i * 6144, i * 24 * 896, false>(rb, wob, (unsigned)(WI + (24 + w) * 1024)); });
    }
    int pos = lane & 15; if (pos >= 4 * ngr) pos = 4 * ngr - 1;
    int xch = (lane & 15) ^ (((lane >> 4) + 4 * w) & 15); if (xch >= 4 * ngr) xch = 0;
    dma<M_XT, 0, false>((unsigned)(((lane >> 4) + 4 * w) * 896 + xch * 16), p.x + (size_t)(128 * c + 32 * (j >> 2)) * 896 + col4 * 2, (unsigned)(w * 1024));
    if (w == 5 && lane < 16) dma<M_B1, 0, false>((unsigned)(pos * 16), p.b1 + (448 * (j >> 2) + col4) * 2, 0u);
    if (w == 4 && lane < 8) dma<M_B2, 0, false>((unsigned)((lane < 2 * nblk ? lane : 0) * 16), p.b2 + col0 * 2, 0u);
  }
  {
    const int h = j;
    FS(9);
    const int sw = (l15 >> 1) & 3;
    if (w == 7 && c >= 1) poll_flags(ctl, (unsigned)(FLAG_IDX(0, (c >= 2 && (lane & 1)) ? c - 2 : c - 1, h) * 4), epoch);
    if (w == 6) {
      const unsigned tfo = (unsigned)(FLAG_IDX(4, 4 * g, l15) * 4);
#pragma unroll 1
      for (unsigned spins = 0; spins < SPIN_CAP; ++spins) {
        unsigned t0, t1, t2, t3;
        asm volatile("global_load_dword %0, %4, %5 sc1\n\tglobal_load_dword %1, %4, %5 offset:2048 sc1\n\tglobal_load_dword %2, %6, %5 sc1\n\tglobal_load_dword %3, %6, %5 offset:2048 sc1\n\ts_waitcnt vmcnt(0)"
                     : "=&v"(t0), "=&v"(t1), "=&v"(t2), "=&v"(t3) : "v"(tfo), "s"(ctl), "v"(tfo + 4096u) : "memory");
        if (__all(t0 == epoch && t1 == epoch && t2 == epoch && t3 == epoch)) break;
        __builtin_amdgcn_s_sleep(2);
      }
      if (b == 0 && lane == 0) st32_sc1(ctl, epoch);
    }
    BAR();
    FS(10);
    {
      const int tl = w >> 2, src = c - 2 + tl, pc = (w & 3) * 2048;
      if (src >= 0) {
        const char* blob = p.ws + WS_KV + (size_t)(src * 16 + h) * 16384 + pc;
        dma2c_sc1<KIMG>(lane16, blob, (unsigned)(tl * 8192 + pc)); dma2c_sc1<VIMG>(lane16, blob + 8192, (unsigned)(tl * 8192 + pc));
      }
    }
    const bf16x8 qf = ld128(lds + QIMG + (16 * w + l15) * 64 + ((g ^ sw) << 4));
    WAITV(0);
    BAR();
    FS(11);
    STAMP(3);
    STAMP(4);
    if (w >= 4) ldfrags<7>(wr, wro, wt);
    f32x4 sc[17];
    const float NEG = -__builtin_inff();
    ldsp kb = lds + KIMG + (16 * w + l15) * 64 + ((g ^ sw) << 4);
    {
      bf16x8 kf[2][6];
#pragma unroll
      for (int u = 0; u < 6; ++u) kf[0][u] = ld128(kb + u * 1024);
#pragma unroll
      for (int bt = 0; bt < 3; ++bt) {
        if (bt < 2) {
#pragma unroll
          for (int u = 0; u < 6; ++u) if (6 * (bt + 1) + u < 17) kf[(bt + 1) & 1][u] = ld128(kb + (6 * (bt + 1) + u) * 1024);
        }
        SCHED();
#pragma unroll
        for (int u = 0; u < 6; ++u) if (6 * bt + u < 17) sc[6 * bt + u] = MFMA(kf[bt & 1][u], qf, (f32x4{0, 0, 0, 0}));
        SCHED();
      }
    }
    FS(12);
    if (w >= 4) ldfrags<7>(wr + 7, wro + 7 * 1024, wt);
    const int vsw = (2 * g + (q4 >> 1)) & 3;
    ldsp vb0 = lds + VIMG + (16 * w + 4 * g + q4) * 64 + (((p4 >> 1) ^ vsw) << 4) + (p4 & 1) * 8;
    ldsp vb1 = lds + VIMG + (16 * w + 4 * g + q4) * 64 + (((2 + (p4 >> 1)) ^ vsw) << 4) + (p4 & 1) * 8;
    bf16x8 vf[2][3][2];
    auto rdv = [&](int bt) {
#pragma unroll
      for (int i = 0; i < 3; ++i) { const int uu = 3 * bt + i, ua = 2 * uu, ub = (uu < 8) ? 2 * uu + 1 : 15;
        vf[bt & 1][i][0] = cat(ldtr(vb0 + ua * 1024), ldtr(vb0 + ub * 1024)); vf[bt & 1][i][1] = cat(ldtr(vb1 + ua * 1024), ldtr(vb1 + ub * 1024)); }
    };
    rdv(0);
    SCHED();
    if (c < 2) {
      const int tmin = (2 - c) * 8;
#pragma unroll
      for (int u = 0; u < 16; ++u) if (w + u < tmin) sc[u] = f32x4{NEG, NEG, NEG, NEG};
    }
#pragma unroll
    for (int r = 0; r < 4; ++r) { sc[0][r] = (4 * g + r <= l15) ? NEG : sc[0][r]; sc[16][r] = (4 * g + r > l15) ? NEG : sc[16][r]; }
    float m = max3(sc[0][0], sc[0][1], sc[0][2]), m2 = max3(sc[0][3], sc[1][0], sc[1][1]);
    m = max3(m, sc[1][2], sc[1][3]);
#pragma unroll
    for (int u = 2; u < 17; ++u) { if (u & 1) m = max3(max3(m, sc[u][0], sc[u][1]), sc[u][2], sc[u][3]); else m2 = max3(max3(m2, sc[u][0], sc[u][1]), sc[u][2], sc[u][3]); }
    m = max3(m, m2, m2);
    m = red_g_max(m);
    FS(13);
    SCHED();
    if (w >= 4) ldfrags<7>(wr + 14, wro + 14 * 1024, wt);
    unsigned pk[17][2];
    f32x2 mm = {-m, -m};
    KEEP(mm);
    f32x4 oa[2] = {f32x4{0, 0, 0, 0}, f32x4{0, 0, 0, 0}};
    KEEP(oa[0]); KEEP(oa[1]);
#pragma unroll
    for (int bt = 0; bt < 3; ++bt) {
#pragma unroll
      for (int u = 6 * bt; u < 6 * bt + 6 && u < 17; ++u) {
        const f32x2 d0 = lo2(sc[u]) + mm, d1 = hi2(sc[u]) + mm;
        pk[u][0] = cvtpk(__builtin_amdgcn_exp2f(d0[0]), __builtin_amdgcn_exp2f(d0[1]));
        pk[u][1] = cvtpk(__builtin_amdgcn_exp2f(d1[0]), __builtin_amdgcn_exp2f(d1[1]));
      }
      if (bt < 2) rdv(bt + 1);
      if (bt == 0 && w >= 4) ldfrags<7>(wr + 21, wro + 21 * 1024, wt);
      SCHED();
#pragma unroll
      for (int i = 0; i < 3; ++i) {
        const int uu = 3 * bt + i;
        u32x4 pu; pu.x = pk[2 * uu][0]; pu.y = pk[2 * uu][1];
        if (uu < 8) { pu.z = pk[2 * uu + 1][0]; pu.w = pk[2 * uu + 1][1]; } else { pu.z = 0; pu.w = 0; }
        const bf16x8 pf = __builtin_bit_cast(bf16x8, pu);
        MACCN(oa[0], vf[bt & 1][i][0], pf); MACC(oa[1], vf[bt & 1][i][1], pf);
      }
      SCHED();
    }
    NOPS(); KEEP(oa[0]); KEEP(oa[1]);
    FS(15);
    int tz2 = tid; asm volatile("" : "+v"(tz2));
    const int l15b = tz2 & 15, gb = (tz2 & 63) >> 4, swb = (l15b >> 1) & 3;
    const float lsel = (h & 1) ? oa[0][0] : oa[1][0];
    const float l = __uint_as_float((unsigned)__builtin_amdgcn_ds_bpermute((((h & 1) ? 0 : 48) + l15b) * 4, (int)__float_as_uint(lsel)));
    const float inv1 = __builtin_amdgcn_rcpf(l);
    f32x2 inv = {inv1, inv1};
    KEEP(inv);
    u32x4 ov;
    ov.x = cvtpk(lo2(oa[0]) * inv); ov.y = cvtpk(hi2(oa[0]) * inv); ov.z = cvtpk(lo2(oa[1]) * inv); ov.w = cvtpk(hi2(oa[1]) * inv);
    { const bool z0 = (h & 1) && gb == 0, z1 = !(h & 1) && gb == 3; ov.x = z0 ? 0u : ov.x; ov.y = z0 ? 0u : ov.y; ov.z = z1 ? 0u : ov.z; ov.w = z1 ? 0u : ov.w; }
    st128_sc1((unsigned)((16 * w + l15b) * 64 + ((gb ^ swb) << 4)), p.ws + WS_O + (size_t)(c * 16 + h) * 8192, ov);
    FS(16);
    BAR();
    {
      const unsigned rb = (unsigned)(((lane >> 4) + 12 * w) * 896), va = rb + ((w & 1) ? wb1 : wb0), vb = rb + ((w & 1) ? wb0 : wb1), lb = (unsigned)(WI + 3 * w * 1024);
      dma<0, 0, false>(va, wob, lb); dma<1024, 4 * 896, false>(vb, wob, lb); dma<2048, 8 * 896, false>(va, wob, lb);
      const int ins = (w < 4) ? 108 + w : 3 * w + 2;
      dma<0, 0, false>((unsigned)(((lane >> 4) + 4 * ins) * 896) + ((ins & 1) ? wb1 : wb0), wob, (unsigned)(WI + ins * 1024));
    }
    WAITV(4);
    BAR();
    if (tid == 0) st32_sc1(ctl + FLAG_IDX(1, c, h), epoch);
    FS(18);
    STAMP(5);
  }
  {
    LANEVARS();
    constexpr int OT = 0;
    const int tq = j >> 2;
    WAITV(0);
    BAR();
    if (w < 4) {
      const int rx = 4 * (g & 1) + q4;
      ldsp ab0 = lds + WI + (4 * g + q4) * 256 + (((2 * prc) ^ rx) << 5) + 8 * p4;
      ldsp ab1 = lds + WI + (4 * g + q4) * 256 + (((2 * prc + 1) ^ rx) << 5) + 8 * p4;
      ldsp ab0h = ab0 + 65536, ab1h = ab1 + 65536;
#pragma unroll
      for (int s = 0; s < 16; ++s) {
        const int rb = (28 * s - 4 * (s & 1)) * 256;
        if (rb + 4096 < 65536) { wr[s] = cat(ldtr(ab0 + rb), ldtr(ab0 + rb + 4096)); wr[16 + s] = cat(ldtr(ab1 + rb), ldtr(ab1 + rb + 4096)); }
        else { wr[s] = cat(ldtr(ab0h + (rb - 65536)), ldtr(ab0h + (rb - 65536 + 4096))); wr[16 + s] = cat(ldtr(ab1h + (rb - 65536)), ldtr(ab1h + (rb - 65536 + 4096))); }
      }
    } else {
      __builtin_amdgcn_s_setprio(3);
      poll_flags(ctl, (unsigned)(FLAG_IDX(1, c, l15) * 4), epoch);
      const char* ob_ = p.ws + WS_O + (size_t)(c * 16 + 4 * (w - 4)) * 8192 + tq * 2048;
      const unsigned lb = (unsigned)(OT + (w - 4) * 8192);
      dma2c_sc1<0>(lane16, ob_, lb); dma2c_sc1<2048>(lane16 + 8192, ob_, lb); dma2c_sc1<4096>(lane16 + 16384, ob_, lb); dma2c_sc1<6144>(lane16 + 24576, ob_, lb);
      __builtin_amdgcn_s_setprio(0);
    }
    FS(19);
    WAITV(0);
    BAR();
    FS(20);
    STAMP(6);
    STAMP(7);
    if (w < 4) {
      f32x4 a0[2], a1[2];
#pragma unroll
      for (int t = 0; t < 2; ++t) { a0[t] = f32x4{0, 0, 0, 0}; a1[t] = f32x4{0, 0, 0, 0}; }
      ldsp ob = lds + OT + l15 * 64 + ((g ^ ((l15 >> 1) & 3)) << 4);
      u32x2 xr[2][2];
#pragma unroll
      for (int t = 0; t < 2; ++t) { ldsp xp = lds + M_XT + (16 * t + l15) * 256 + (g & 1) * 8;
        xr[t][0] = *(LAS u32x2*)(xp + (((4 * prc + (g >> 1)) ^ l15) << 4)); xr[t][1] = *(LAS u32x2*)(xp + (((4 * prc + 2 + (g >> 1)) ^ l15) << 4)); }
#pragma unroll
      for (int t = 0; t < 2; ++t) { KEEP(a0[t]); KEEP(a1[t]); }
      bf16x8 of[4][2];
#pragma unroll
      for (int s = 0; s < 3; ++s) { of[s][0] = ld128(ob + s * 2048); of[s][1] = ld128(ob + s * 2048 + 1024); }
#pragma unroll
      for (int s = 0; s < 16; ++s) {
        if (s + 3 < 16) { of[(s + 3) & 3][0] = ld128(ob + (s + 3) * 2048); of[(s + 3) & 3][1] = ld128(ob + (s + 3) * 2048 + 1024); }
        SCHED();
#pragma unroll
        for (int t = 0; t < 2; ++t) { MACC(a0[t], wr[s], of[s & 3][t]); MACC(a1[t], wr[16 + s], of[s & 3][t]); }
        SCHED();
      }
      NOPS();
#pragma unroll
      for (int t = 0; t < 2; ++t) { KEEP(a0[t]); KEEP(a1[t]); }
      FS(21);
      const unsigned ho = (unsigned)((128 * c + 32 * tq + l15) * 896 + (col4 / 32 + pr) * 64 + g * 16);
#pragma unroll
      for (int t = 0; t < 2; ++t) {
        u32x4 hv;
        hv.x = cvtpk(lo2(a0[t]) + bf2(xr[t][0].x)); hv.y = cvtpk(hi2(a0[t]) + bf2(xr[t][0].y));
        hv.z = cvtpk(lo2(a1[t]) + bf2(xr[t][1].x)); hv.w = cvtpk(hi2(a1[t]) + bf2(xr[t][1].y));
        if (pr < ngr) st128_sc1(ho + t * 16 * 896, p.ws + WS_H, hv);
        float s1 = 0.f, s2 = 0.f;
#pragma unroll
        for (int i = 0; i < 4; ++i) { s1 = dot2(hv[i], ONES, s1); s2 = dot2(hv[i], hv[i], s2); }
        s1 = red_g_sum(s1); s2 = red_g_sum(s2);
        if (pr >= ngr) { s1 = 0.f; s2 = 0.f; }
        if (g == 0) ((LAS f32x2*)(lds + M_PART))[(16 * t + l15) * 4 + pr] = f32x2{s1, s2};
      }
    } else {
      pin<28>(wr);
#pragma unroll
      for (int i = 0; i < 28; ++i) *(LAS bf16x8*)(lds + 32768 + (w - 4) * 28672 + i * 1024 + lane * 16) = wr[i];
    }
    FS(22);
    BAR();
    if (tid < 32) {
      const f32x4 u0 = *(LAS f32x4*)(lds + M_PART + tid * 32), u1 = *(LAS f32x4*)(lds + M_PART + tid * 32 + 16);
      st64_sc1((unsigned)((128 * c + 32 * tq + tid) * 32 + ns * 8), p.ws + WS_ST,
               u32x2{__float_as_uint((u0[0] + u0[2]) + (u1[0] + u1[2])), __float_as_uint((u0[1] + u0[3]) + (u1[1] + u1[3]))});
    }
    if (w < 4) {
#pragma unroll
      for (int i = 0; i < 28; ++i) wr[i] = ld128(lds + 32768 + w * 28672 + i * 1024 + lane * 16);
    }
    FS(23);
    if (w < 4) WAITV(0);
    BAR();
    if (tid == 0) st32_sc1(ctl + FLAG_IDX(2, c, j), epoch);
    FS(25);
    STAMP(8);
  }
  {
    LANEVARS();
    const int f0_ = 448 * (j >> 2) + col4, th = wh;
    FS(26);
    f32x4 c0 = {0, 0, 0, 0}, c1 = {0, 0, 0, 0};
    pin<28>(wr);
    bf16x8 onesf = __builtin_bit_cast(bf16x8, (u32x4{ONES, ONES, ONES, ONES}));
    KEEP(onesf); KEEP(c0); KEEP(c1);
    asm volatile("s_nop 1");
#pragma unroll
    for (int s = 0; s < 14; ++s) { MACC(c0, wr[s], onesf); MACC(c1, wr[14 + s], onesf); }
    NOPS(); KEEP(c0); KEEP(c1);
    SCHED();
    if (w == 7) poll_flags(ctl, (unsigned)(FLAG_IDX(2, c, l15) * 4), epoch);
    FS(29);
    BAR();
    const unsigned vo = (unsigned)((16 * w + (lane >> 3)) * 896) + x8, wl2k = (unsigned)(w * 2048);
    const char* hb = p.ws + WS_H + (size_t)c * 128 * 896;
    auto issue4 = [&](auto KC) { constexpr int kc = decltype(KC)::value; dma<kc * 16384, kc * 128, true>(vo, hb, wl2k); dma<kc * 16384 + 1024, kc * 128 + 8 * 896, true>(vo, hb, wl2k); };
    if (lane < 32) dma<M_TILE, 0, true>(lane16, p.ws + WS_ST + (size_t)(128 * c + 16 * w) * 32, (unsigned)(w * 512));
    issue4(IC(0)); RAWBAR(); issue4(IC(1));
    FS(31);
    f32x4 acc[2][4];
#pragma unroll
    for (int t = 0; t < 4; ++t) { acc[0][t] = f32x4{0, 0, 0, 0}; acc[1][t] = f32x4{0, 0, 0, 0}; }
    const int xb = (64 * th + l15) * 128 + ((g ^ (l15 & 7)) << 4);
    bf16x8 xf[2][4];
    auto rd1 = [&](int st, int buf, int t) { xf[buf][t] = ld128(lds + (st >> 1) * 16384 + (xb ^ ((st & 1) << 6)) + t * 2048); };
    auto rdf = [&](int st, int buf) { rd1(st, buf, 0); rd1(st, buf, 1); rd1(st, buf, 2); rd1(st, buf, 3); };
#pragma unroll
    for (int t = 0; t < 4; ++t) { KEEP(acc[0][t]); KEEP(acc[1][t]); }
    bf16x8 bf[2][14];
    const unsigned w2o0 = WT_W2 + ((unsigned)(col0 / 16 + 2 * ng5) * 56u + 14u * kq) * 1024u, w2o1 = (2 * ng5 + 1 < nblk) ? w2o0 + 56u * 1024u : w2o0;
    WAITV(2); BAR();
    FS(32);
    STAMP(9);
    STAMP(10);
    rdf(0, 0);
    sfor<0, 14>([&](auto ST) {
      constexpr int st = decltype(ST)::value;
      if constexpr (st == 1) WAITV(2); if constexpr (st == 3) WAITV(4); if constexpr (st == 5) WAITV(6); if constexpr (st == 7) WAITV(8); if constexpr (st == 9) WAITV(10); if constexpr (st == 11) WAITV(12);
      if constexpr ((st & 1) && st < 13) BARN();
      if constexpr (st == 2) {
        if (tid < 128) {
          const f32x4 u0 = *(LAS f32x4*)(lds + M_TILE + tid * 32), u1 = *(LAS f32x4*)(lds + M_TILE + tid * 32 + 16);
          float mean = ((u0[0] + u0[2]) + (u1[0] + u1[2])) * (1.f / 448.f), sq = (u0[1] + u0[3]) + (u1[1] + u1[3]);
          KEEP(mean); KEEP(sq);
          const float rstd = rsqrtf(fmaxf(sq * (1.f / 448.f) - mean * mean, 0.f) + 1e-5f);
          *(LAS f32x2*)(lds + M_LN + tid * 8) = f32x2{rstd, -mean * rstd};
        }
      }
      SCHED();
#pragma unroll
      for (int t = 0; t < 2; ++t) { MACC(acc[0][t], wr[st], xf[st & 1][t]); if constexpr (st < 13) rd1(st + 1, (st + 1) & 1, t); MACC(acc[1][t], wr[14 + st], xf[st & 1][t]); }
      SCHED();
      if constexpr (st <= 4) issue4(IC(st + 2));
      if constexpr (st >= 5) { constexpr int s = st - 5; ldfrags<1>(&bf[0][s], lane16 + w2o0 + s * 1024, wt); ldfrags<1>(&bf[1][s], lane16 + w2o1 + s * 1024, wt); }
      SCHED();
#pragma unroll
      for (int t = 2; t < 4; ++t) { MACC(acc[0][t], wr[st], xf[st & 1][t]); if constexpr (st < 13) rd1(st + 1, (st + 1) & 1, t); MACC(acc[1][t], wr[14 + st], xf[st & 1][t]); }
      SCHED();
    });
    NOPS();
#pragma unroll
    for (int t = 0; t < 4; ++t) { KEEP(acc[0][t]); KEEP(acc[1][t]); }
    FS(33);
    STAMP(11);
    {
      const u32x2 bb0 = *(LAS u32x2*)(lds + M_B1 + (32 * prc + 4 * g) * 2), bb1 = *(LAS u32x2*)(lds + M_B1 + (32 * prc + 4 * g) * 2 + 32);
      const f32x2 b00 = bf2(bb0.x), b01 = bf2(bb0.y), b10 = bf2(bb1.x), b11 = bf2(bb1.y);
      const unsigned ho = (unsigned)((128 * c + 64 * th + l15) * 3584 + (f0_ / 32 + pr) * 64 + g * 16);
      f32x2 ln[4];
#pragma unroll
      for (int t = 0; t < 4; ++t) ln[t] = *(LAS f32x2*)(lds + M_LN + (64 * th + 16 * t + l15) * 8);
#pragma unroll
      for (int t = 0; t < 4; ++t) {
        const float a = ln[t][0], nm = ln[t][1];
        f32x2 aa = {a, a}, nn = {nm, nm};
        KEEP(aa); KEEP(nn);
        f32x2 z[4] = {lo2(acc[0][t]) * aa + (lo2(c0) * nn + b00), hi2(acc[0][t]) * aa + (hi2(c0) * nn + b01), lo2(acc[1][t]) * aa + (lo2(c1) * nn + b10), hi2(acc[1][t]) * aa + (hi2(c1) * nn + b11)};
        u32x4 hv;
#pragma unroll
        for (int i = 0; i < 4; ++i) {
          f32x2 r = {__builtin_elementwise_maximum(z[i][0], 0.f), __builtin_elementwise_maximum(z[i][1], 0.f)};
          hv[i] = cvtpk(r * r);
        }
        if (pr < ngr) st128_sc1(ho + t * 16 * 3584, p.ws + WS_H1, hv);
      }
    }
    FS(34);
    WAITV(0);
    BAR();
    if (tid == 0) st32_sc1(ctl + FLAG_IDX(3, c, j), epoch);
    FS(38);
    STAMP(12);
    if (w != 7) {
      ldfrags<5>(&bf[0][9], lane16 + w2o0 + 9 * 1024, wt); ldfrags<5>(&bf[1][9], lane16 + w2o1 + 9 * 1024, wt);
    }
    if (w >= 5 && w < 7) {
      const int m0_ = col0 >> 5;
      const char* hr = p.ws + WS_H + (size_t)(128 * c + 64 * th5) * 896;
      sfor<0, 6>([&](auto I) { constexpr int i = decltype(I)::value;
        const unsigned u = (unsigned)((w - 5) * 384 + i * 64 + lane), r0 = (u * 43691u) >> 19, c0_ = u - 12u * r0; unsigned cc0 = m0_ * 4 + c0_; if (cc0 > 55) cc0 = 55;
        dma<M_TILE + i * 1024, 0, true>(r0 * 896 + cc0 * 16, hr, (unsigned)((w - 5) * 6144)); });
    }
    const unsigned vo5 = (unsigned)((32 * ng5 + (lane >> 3)) * 3584 + kq * 896) + x8, wl4k = (unsigned)(w * 4096);
    const char* hb5 = p.ws + WS_H1 + (size_t)(128 * c + 64 * th5) * 3584;
    auto issue5 = [&](auto I) {
      constexpr int i = decltype(I)::value, sa = (i & 3) * 32768;
      sfor<0, 4>([&](auto II) { constexpr int ii = decltype(II)::value; dma<sa + ii * 1024, i * 128 + ii * 8 * 3584, true>(vo5, hb5, wl4k); });
    };
    FS(39);
    if (w == 7) {
      poll_flags(ctl, (unsigned)(FLAG_IDX(3, c, l15) * 4), epoch);
    }
    FS(40);
    BAR();
    FS(41);
    STAMP(13);
    issue5(IC(0));
    if (w == 7) {
      ldfrags<5>(&bf[0][9], lane16 + w2o0 + 9 * 1024, wt); ldfrags<5>(&bf[1][9], lane16 + w2o1 + 9 * 1024, wt);
    }
    FS(42);
    f32x4 ac5[2][4];
#pragma unroll
    for (int t = 0; t < 4; ++t) { ac5[0][t] = f32x4{0, 0, 0, 0}; ac5[1][t] = f32x4{0, 0, 0, 0}; }
    int yb[4];
#pragma unroll
    for (int t = 0; t < 4; ++t) yb[t] = kq * 8192 + (16 * ((t + kq) & 3) + l15) * 128 + ((g ^ (l15 & 7)) << 4);
    bf16x8 yf[2][4];
    auto rz1 = [&](int st, int buf, int t) { yf[buf][t] = ld128(lds + ((st >> 1) & 3) * 32768 + (yb[t] ^ ((st & 1) << 6))); };
    auto rdy = [&](int st, int buf) { rz1(st, buf, 0); rz1(st, buf, 1); rz1(st, buf, 2); rz1(st, buf, 3); };
#pragma unroll
    for (int t = 0; t < 4; ++t) { KEEP(ac5[0][t]); KEEP(ac5[1][t]); }
    WAITV(0);
    pin<14>(bf[0]); pin<14>(bf[1]);
    BAR();
    FS(43);
    STAMP(14);
    rdy(0, 0);
    sfor<0, 14>([&](auto ST) {
      constexpr int st = decltype(ST)::value;
      if constexpr ((st & 1) && st < 13) {
        if constexpr (st < 11) WAITV(4); else WAITV(0);
        BARN();
      }
      SCHED();
#pragma unroll
      for (int t = 0; t < 2; ++t) { MACC(ac5[0][t], bf[0][st], yf[st & 1][t]); if constexpr (st < 13) rz1(st + 1, (st + 1) & 1, t); MACC(ac5[1][t], bf[1][st], yf[st & 1][t]); }
      SCHED();
      if constexpr (st == 0) { issue5(IC(1)); issue5(IC(2)); }
      if constexpr ((st & 1) && st <= 7) issue5(IC((st + 5) / 2));
      SCHED();
#pragma unroll
      for (int t = 2; t < 4; ++t) { MACC(ac5[0][t], bf[0][st], yf[st & 1][t]); if constexpr (st < 13) rz1(st + 1, (st + 1) & 1, t); MACC(ac5[1][t], bf[1][st], yf[st & 1][t]); }
      SCHED();
    });
    NOPS();
#pragma unroll
    for (int t = 0; t < 4; ++t) { KEEP(ac5[0][t]); KEEP(ac5[1][t]); }
    FS(44);
    BAR();
#pragma unroll
    for (int t = 1; t < 4; ++t) {
      ldsp d = lds + ((((t + kq) & 3) * 3 + (t - 1)) * 2 + ng5) * 2048 + lane * 16;
      *(LAS f32x4*)d = ac5[0][t]; *(LAS f32x4*)(d + 1024) = ac5[1][t];
    }
    const int tokl = 16 * kq + l15;
    const int t0 = col0 / 16 + 2 * ng5, t1 = (2 * ng5 + 1 < nblk) ? t0 + 1 : t0, m0_ = col0 >> 5;
    const u32x2 hr0 = *(LAS u32x2*)(lds + M_TILE + tokl * 192 + ((t0 >> 1) - m0_) * 64 + g * 16 + (t0 & 1) * 8);
    const u32x2 hr1 = *(LAS u32x2*)(lds + M_TILE + tokl * 192 + ((t1 >> 1) - m0_) * 64 + g * 16 + (t1 & 1) * 8);
    const u32x2 b20 = *(LAS u32x2*)(lds + M_B2 + (32 * ng5 + 4 * g) * 2), b21 = *(LAS u32x2*)(lds + M_B2 + ((2 * ng5 + 1 < nblk) ? (32 * ng5 + 16) : 32 * ng5) * 2 + 8 * g);
    f32x2 r00 = lo2(ac5[0][0]) + (bf2(b20.x) + bf2(hr0.x)), r01 = hi2(ac5[0][0]) + (bf2(b20.y) + bf2(hr0.y));
    f32x2 r10 = lo2(ac5[1][0]) + (bf2(b21.x) + bf2(hr1.x)), r11 = hi2(ac5[1][0]) + (bf2(b21.y) + bf2(hr1.y));
    BAR();
    FS(45);
#pragma unroll
    for (int t = 1; t < 4; ++t) {
      ldsp d = lds + ((kq * 3 + (t - 1)) * 2 + ng5) * 2048 + lane * 16;
      const f32x4 u0 = *(LAS f32x4*)d, u1 = *(LAS f32x4*)(d + 1024);
      r00 += lo2(u0); r01 += hi2(u0); r10 += lo2(u1); r11 += hi2(u1);
    }
    GAS char* op = (GAS char*)p.out + (size_t)(128 * c + 64 * th5 + tokl) * 896 + (col0 + 32 * ng5 + 4 * g) * 2;
    *(GAS u32x2*)op = u32x2{cvtpk(r00), cvtpk(r01)};
    if (2 * ng5 + 1 < nblk) *(GAS u32x2*)(op + 32) = u32x2{cvtpk(r10), cvtpk(r11)};
    STAMP(15);
    FS(63);
  }
}

extern "C" void kernel_launch(void* const* d_in, const int* in_sizes, int n_in, void* d_out, int out_size, void* d_ws, size_t ws_size, hipStream_t stream) {
  Params p;
  p.x = (const char*)d_in[0]; p.wqkv = (const char*)d_in[1]; p.wo = (const char*)d_in[2];
  p.w1 = (const char*)d_in[4]; p.b1 = (const char*)d_in[5]; p.w2 = (const char*)d_in[6]; p.b2 = (const char*)d_in[7];
  p.out = (char*)d_out; p.ws = (char*)d_ws;
  (void)hipFuncSetAttribute((const void*)fused_block, hipFuncAttributeMaxDynamicSharedMemorySize, LDS_BYTES);
  hipLaunchKernelGGL(fused_block, dim3(256), dim3(512), LDS_BYTES, stream, p);
}
```

```cpp
#define MAPA
#include <hip/hip_runtime.h>
#include <hip/hip_bf16.h>
#include <stdint.h>
#include <type_traits>

#define LAS __attribute__((address_space(3)))
#define GAS __attribute__((address_space(1)))
typedef short bf16x8 __attribute__((ext_vector_type(8)));
typedef short s16x4 __attribute__((ext_vector_type(4)));
typedef short v4i16_t __attribute__((ext_vector_type(4)));
typedef float f32x4 __attribute__((ext_vector_type(4)));
typedef float f32x2 __attribute__((ext_vector_type(2)));
typedef unsigned u32x4 __attribute__((ext_vector_type(4)));
typedef unsigned u32x2 __attribute__((ext_vector_type(2)));
typedef __bf16 bf16x2_t __attribute__((ext_vector_type(2)));
typedef LAS char* ldsp;

constexpr int SEQ = 2048;
constexpr size_t WS_CTL = 0;
constexpr size_t WS_KV = 262144;
constexpr size_t WS_O = WS_KV + 256 * 16384;
constexpr size_t WS_H = WS_O + 256 * 8192;
constexpr size_t WS_H1 = WS_H + (size_t)SEQ * 896;
constexpr size_t WS_ST = WS_H1 + (size_t)SEQ * 3584;
constexpr size_t WS_WT = WS_ST + (size_t)SEQ * 32;
constexpr unsigned WT_W1 = 0, WT_W2 = WT_W1 + 1568 * 1024, WT_BYTES = WT_W2 + 1568 * 1024;
constexpr size_t WS_DBG = WS_WT + WT_BYTES;
constexpr int LDS_BYTES = 160 * 1024;
constexpr int M_PT = 143360;
constexpr int M_XT = 147456;
constexpr int M_TILE = 131072;
constexpr int MISC = 157696;
constexpr int M_LN = MISC, M_B1 = MISC + 1024, M_B2 = MISC + 1280, M_PF = MISC + 1536, M_PART = MISC + 2048, M_FILL = MISC + 3072;
constexpr unsigned SPIN_CAP = 1u << 16;

#define FLAG_IDX(ph, c, j) (64 + (((ph) * 16 + (c)) * 16 + (j)) * 32)

#define WAITV(n) asm volatile("s_waitcnt vmcnt(" #n ")" ::: "memory")
#define BAR() do { asm volatile("s_waitcnt lgkmcnt(0)" ::: "memory"); __builtin_amdgcn_s_barrier(); asm volatile("" ::: "memory"); } while (0)
#define BARN() do { asm volatile("" ::: "memory"); __builtin_amdgcn_s_barrier(); asm volatile("" ::: "memory"); } while (0)
#define SCHED() __builtin_amdgcn_sched_barrier(0)
#define RAWBAR() __builtin_amdgcn_s_barrier()
#define ILV(n) do { _Pragma("unroll") for (int i_ = 0; i_ < (n); ++i_) { __builtin_amdgcn_sched_group_barrier(0x008, 1, 0); __builtin_amdgcn_sched_group_barrier(0x100, 1, 0); } } while (0)
#define MFMA(a, b, c) __builtin_amdgcn_mfma_f32_16x16x32_bf16((a), (b), (c), 0, 0, 0)
#define MACC(acc, a, b) asm volatile("v_mfma_f32_16x16x32_bf16 %0, %1, %2, %0" : "+v"(acc) : "v"(a), "v"(b))
#define MACCN(acc, a, b) asm volatile("s_nop 1\n\tv_mfma_f32_16x16x32_bf16 %0, %1, %2, %0" : "+v"(acc) : "v"(a), "v"(b))
#define NOPS() asm volatile("s_nop 7\n\ts_nop 3")
#define KEEP(x) asm volatile("" : "+v"(x))
#define IC(x) std::integral_constant<int, (x)>{}

template <int I, int N, class F> __device__ __forceinline__ void sfor(F&& f) { if constexpr (I < N) { f(std::integral_constant<int, I>{}); sfor<I + 1, N>(f); } }

__device__ __forceinline__ unsigned cvtpk(float lo, float hi) { f32x2 v = {lo, hi}; bf16x2_t b = __builtin_convertvector(v, bf16x2_t); return __builtin_bit_cast(unsigned, b); }
__device__ __forceinline__ unsigned cvtpk(f32x2 v) { bf16x2_t b = __builtin_convertvector(v, bf16x2_t); return __builtin_bit_cast(unsigned, b); }
__device__ __forceinline__ float dot2(unsigned a, unsigned b, float c) { return __builtin_amdgcn_fdot2_f32_bf16(__builtin_bit_cast(bf16x2_t, a), __builtin_bit_cast(bf16x2_t, b), c, false); }
__device__ __forceinline__ float bflo(unsigned u) { return __uint_as_float(u << 16); }
__device__ __forceinline__ float bfhi(unsigned u) { return __uint_as_float(u & 0xffff0000u); }
__device__ __forceinline__ f32x2 bf2(unsigned u) { return f32x2{bflo(u), bfhi(u)}; }
__device__ __forceinline__ f32x2 lo2(f32x4 v) { return f32x2{v[0], v[1]}; }
__device__ __forceinline__ f32x2 hi2(f32x4 v) { return f32x2{v[2], v[3]}; }
__device__ __forceinline__ bf16x8 ld128(ldsp p) { return *(const LAS bf16x8*)p; }
__device__ __forceinline__ s16x4 ldtr(ldsp p) { return __builtin_bit_cast(s16x4, __builtin_amdgcn_ds_read_tr16_b64_v4i16((LAS v4i16_t*)p)); }
__device__ __forceinline__ bf16x8 cat(s16x4 lo, s16x4 hi) { return __builtin_shufflevector(lo, hi, 0, 1, 2, 3, 4, 5, 6, 7); }
__device__ __forceinline__ int rfl(int x) { return __builtin_amdgcn_readfirstlane(x); }
template <int LADD, int VADD, bool SC1>
__device__ __forceinline__ void dma(unsigned voff, const void* sbase, unsigned lbase) {
  const unsigned va = voff + (unsigned)VADD;
  if constexpr (SC1) asm volatile("s_add_i32 m0, %2, %3\n\ts_nop 0\n\tglobal_load_lds_dwordx4 %0, %1 sc1" ::"v"(va), "s"(sbase), "s"(lbase), "i"(LADD) : "memory", "scc");
  else asm volatile("s_add_i32 m0, %2, %3\n\ts_nop 0\n\tglobal_load_lds_dwordx4 %0, %1" ::"v"(va), "s"(sbase), "s"(lbase), "i"(LADD) : "memory", "scc");
}
template <int LADD>
__device__ __forceinline__ void dma2c_sc1(unsigned voff, const void* sbase, unsigned lbase) {
  asm volatile("s_add_i32 m0, %2, %3\n\ts_nop 0\n\tglobal_load_lds_dwordx4 %0, %1 sc1\n\tglobal_load_lds_dwordx4 %0, %1 offset:1024 sc1" ::"v"(voff), "s"(sbase), "s"(lbase), "i"(LADD) : "memory", "scc");
}
template <int OFF>
__device__ __forceinline__ void ldfrag1(bf16x8& f, unsigned voff, const void* sbase) {
  asm volatile("global_load_dwordx4 %0, %1, %2 offset:%3 sc1" : "=v"(f) : "v"(voff), "s"(sbase), "i"(OFF) : "memory");
}
__device__ __forceinline__ void pin1(bf16x8& f) { asm volatile("" : "+v"(f)); }
template <int N>
__device__ __forceinline__ void ldfrags(bf16x8* f, unsigned voff, const void* sbase) {
  sfor<0, N>([&](auto I) { constexpr int i = decltype(I)::value; ldfrag1<(i & 3) * 1024>(f[i], voff + (unsigned)((i >> 2) * 4096), sbase); });
}
template <int N>
__device__ __forceinline__ void pin(bf16x8* f) { sfor<0, N>([&](auto I) { pin1(f[decltype(I)::value]); }); }
__device__ __forceinline__ void st128_sc1(unsigned voff, const void* sbase, u32x4 v) {
  asm volatile("global_store_dwordx4 %0, %1, %2 sc1\n\ts_nop 1" ::"v"(voff), "v"(v), "s"(sbase) : "memory");
}
__device__ __forceinline__ void st64_sc1(unsigned voff, const void* sbase, u32x2 v) {
  asm volatile("global_store_dwordx2 %0, %1, %2 sc1\n\ts_nop 1" ::"v"(voff), "v"(v), "s"(sbase) : "memory");
}
__device__ __forceinline__ void st32_sc1(const void* sbase, unsigned v) {
  asm volatile("global_store_dword %0, %1, %2 sc1\n\ts_nop 1" ::"v"(0u), "v"(v), "s"(sbase) : "memory");
}
__device__ __forceinline__ float max3(float a, float b, float c) { return __builtin_elementwise_maximum(__builtin_elementwise_maximum(a, b), c); }
__device__ __forceinline__ float red_g_sum(float v) {
  auto a = __builtin_amdgcn_permlane16_swap(__float_as_uint(v), __float_as_uint(v), false, false);
  v = __uint_as_float(a[0]) + __uint_as_float(a[1]);
  auto b = __builtin_amdgcn_permlane32_swap(__float_as_uint(v), __float_as_uint(v), false, false);
  return __uint_as_float(b[0]) + __uint_as_float(b[1]);
}
__device__ __forceinline__ float red_g_max(float v) {
  auto a = __builtin_amdgcn_permlane16_swap(__float_as_uint(v), __float_as_uint(v), false, false);
  v = __builtin_elementwise_maximum(__uint_as_float(a[0]), __uint_as_float(a[1]));
  auto b = __builtin_amdgcn_permlane32_swap(__float_as_uint(v), __float_as_uint(v), false, false);
  return __builtin_elementwise_maximum(__uint_as_float(b[0]), __uint_as_float(b[1]));
}
__device__ __forceinline__ void poll_flags(const unsigned* base, unsigned voff, unsigned epoch) {
#pragma unroll 1
  for (unsigned spins = 0; spins < SPIN_CAP; ++spins) {
    unsigned v;
    asm volatile("global_load_dword %0, %1, %2 sc1\n\ts_waitcnt vmcnt(0)" : "=&v"(v) : "v"(voff), "s"(base) : "memory");
    if (__all(v == epoch)) break;
    __builtin_amdgcn_s_sleep(1);
  }
}

struct Params {
  const char* x; const char* wqkv; const char* wo; const char* w1; const char* b1; const char* w2; const char* b2;
  char* out; char* ws;
};

#define FS(i) do {} while (0)
#define STAMP(i) do {} while (0)

#define LANEVARS() int tz_ = tid; asm volatile("" : "+v"(tz_)); const int lane = tz_ & 63, l15 = lane & 15, g = lane >> 4, q4 = l15 >> 2, p4 = l15 & 3; \
  const unsigned lane16 = (unsigned)lane * 16u, x8 = (unsigned)(((lane & 7) ^ ((lane >> 3) & 7)) << 4); (void)q4; (void)p4; (void)g; (void)x8; (void)lane16

__global__ void __launch_bounds__(512, 2) fused_block(Params p) {
  extern __shared__ __attribute__((aligned(16))) char smem[];
  ldsp lds = (ldsp)smem;
  const int tid = threadIdx.x, lane = tid & 63, w = rfl(tid >> 6);
  const int l15 = lane & 15, g = lane >> 4, q4 = l15 >> 2, p4 = l15 & 3;
  const int b = blockIdx.x, r_ = b >> 3;
  const int c = 2 * (b & 7) + (r_ >> 4), j = r_ & 15;
  const int ca = 4 * ((b & 7) >> 1) + (r_ >> 3), ha = 8 * (b & 1) + (r_ & 7);
  const unsigned* ctl = (const unsigned*)(p.ws + WS_CTL);
  unsigned epoch = 0;
  FS(0);
  STAMP(0);
  const unsigned ONES = 0x3f803f80u;
  const unsigned lane16 = (unsigned)lane * 16u;
  const unsigned x8 = (unsigned)(((lane & 7) ^ ((lane >> 3) & 7)) << 4);

  constexpr int KIMG = 0, VIMG = 24576, QIMG = 49152;
  {
    const int h = ha;
    constexpr int SLOT = 28672, WOFF = 16384;
    const unsigned iw = (unsigned)(w >> 2);
    unsigned voff[4];
    {
      const unsigned xo = (unsigned)((32 * w + (lane >> 3)) * 896) + x8;
#pragma unroll
      for (int ii = 0; ii < 4; ++ii) {
        const unsigned u = (unsigned)(192 * (w & 3) + 64 * ii + lane), row = (u * 43691u) >> 19, rem = u - 12u * row;
        const unsigned piece = rem >> 2, c16 = rem & 3, hb = (c16 >> 1) ^ ((row >> 3) & 1);
        const unsigned wo_ = (ii < 3) ? row * 2688u + piece * 896u + hb * 32u + (c16 & 1) * 16u : 0u;
        voff[ii] = iw ? wo_ : xo + ii * 7168u;
      }
    }
    const unsigned long long mW = 0ull - (unsigned long long)iw;
    const unsigned long long bx_ = (unsigned long long)(uintptr_t)p.x + (unsigned)(ca * 128 * 896), bw_ = (unsigned long long)(uintptr_t)p.wqkv + (unsigned)(56 * h - 8 * (h & 1));
    const char* gb = (const char*)(uintptr_t)(bx_ + ((bw_ - bx_) & mW));
    const unsigned kstep = 128 + iw * (64 * 2688 - 128), ldst = w * 4096 + iw * (WOFF + (w - 4) * 3072 - w * 4096);
    const unsigned smask = iw - 1u, l3 = iw ? (unsigned)M_FILL : ldst + 3072u;
    auto issue1 = [&](auto SL, auto II) {
      constexpr int sl = decltype(SL)::value, ii = decltype(II)::value;
      if constexpr (ii < 3) dma<sl * SLOT + ii * 1024, 0, false>(voff[ii], gb, ldst);
      else { dma<0, 0, false>(voff[3], gb, l3 + (smask & (unsigned)(sl * SLOT))); gb += kstep; }
    };
    auto issue = [&](auto SL) { issue1(SL, IC(0)); issue1(SL, IC(1)); issue1(SL, IC(2)); issue1(SL, IC(3)); };
    FS(1);
    issue(IC(0)); RAWBAR(); issue(IC(1));
    FS(2);
    const int ptq0 = (b < 224) ? b : 223, ptm = (ptq0 >= 112) ? 2 : 1, ptq = ptq0 - ((ptm == 2) ? 112 : 0);
    const int pts = (ptm == 1) ? ptq >> 3 : ptq >> 1, ptG = (ptm == 1) ? ptq & 7 : ptq & 1;
    auto pt_issue = [&]() {
      const unsigned stride = (ptm == 1) ? 3584u : 896u;
      const char* src = ((ptm == 1) ? p.w1 : p.w2) + (size_t)(32 * pts * stride + ptG * 448);
      const int wq = (w < 7) ? w : 6;
#pragma unroll
      for (int i = 0; i < 2; ++i) { const unsigned u = (unsigned)((2 * wq + i) * 64 + lane), row = (u * 9363u) >> 18, c16 = u - 28u * row;
        dma<M_PT, 0, false>(row * stride + c16 * 16u, src, (unsigned)((2 * wq + i) * 1024)); }
      if (lane == 63) asm volatile("s_mov_b32 m0, %2\n\ts_nop 0\n\tglobal_load_lds_dword %0, %1 sc1" ::"v"(0u), "s"(p.ws + WS_CTL), "i"(M_PF) : "memory");
    };
    const int tg = w >> 1, ng = w & 1;
    f32x4 acc[3][2], asum = {0, 0, 0, 0}, asq = {0, 0, 0, 0}, acs[3];
#pragma unroll
    for (int e = 0; e < 3; ++e) { acc[e][0] = f32x4{0, 0, 0, 0}; acc[e][1] = f32x4{0, 0, 0, 0}; acs[e] = f32x4{0, 0, 0, 0}; }
    const int xb = (32 * tg + 16 * ng + l15) * 128 + ((g ^ (l15 & 7)) << 4);
    int ab[3];
#pragma unroll
    for (int e = 0; e < 3; ++e) ab[e] = WOFF + (8 * g + q4) * 192 + ((((3 * ng + e) * 32) ^ ((g & 1) << 5))) + 8 * p4;
    bf16x8 xf[2][2];
    s16x4 al[2][3], ah[2][3];
    auto rdx = [&](int st, int buf, int t) { xf[buf][t] = ld128(lds + ((st >> 1) % 5) * SLOT + (xb ^ ((st & 1) << 6) ^ (t * 2048))); };
    auto rdl = [&](int st, int buf, int e) { al[buf][e] = ldtr(lds + ((st >> 1) % 5) * SLOT + ab[e] + (32 * (st & 1)) * 192); };
    auto rdh_ = [&](int st, int buf, int e) { ah[buf][e] = ldtr(lds + ((st >> 1) % 5) * SLOT + ab[e] + (32 * (st & 1) + 4) * 192); };
    auto rdf = [&](int st, int buf) { rdx(st, buf, 0); rdx(st, buf, 1);
#pragma unroll
      for (int e = 0; e < 3; ++e) { rdl(st, buf, e); rdh_(st, buf, e); } };
    bf16x8 onesf = __builtin_bit_cast(bf16x8, (u32x4{ONES, ONES, ONES, ONES}));
    KEEP(onesf); KEEP(asum); KEEP(asq);
#pragma unroll
    for (int e = 0; e < 3; ++e) { KEEP(acc[e][0]); KEEP(acc[e][1]); KEEP(acs[e]); }
    WAITV(4);
    BAR();
    FS(3);
    rdf(0, 0);
    sfor<0, 14>([&](auto ST) {
      constexpr int st = decltype(ST)::value, kc = st >> 1, cb = st & 1;
      if constexpr ((st & 1) && st < 13) {
        if constexpr (kc == 0) WAITV(4); else if constexpr (kc == 1) WAITV(9); else if constexpr (kc <= 3) WAITV(6); else if constexpr (kc == 4) WAITV(4); else WAITV(0);
        BARN();
      }
      if constexpr (st == 2) pt_issue();
      constexpr int nb = (st + 1) & 1; constexpr bool more = st < 13;
      const bf16x8 a0 = cat(al[cb][0], ah[cb][0]), a1 = cat(al[cb][1], ah[cb][1]), a2 = cat(al[cb][2], ah[cb][2]);
      SCHED();
      MACC(acc[0][0], a0, xf[cb][0]); if constexpr (more) rdx(st + 1, nb, 0);
      MACC(acc[0][1], a0, xf[cb][1]); if constexpr (more) rdl(st + 1, nb, 0);
      MACC(asum, onesf, xf[cb][0]); if constexpr (more) rdh_(st + 1, nb, 0);
      MACC(acs[0], a0, onesf);
      SCHED();
      if constexpr (st < 2) { issue1(IC(st + 2), IC(0)); issue1(IC(st + 2), IC(1)); }
      else if constexpr (kc + 3 <= 6) issue1(IC((kc + 3) % 5), IC(2 * (st & 1)));
      SCHED();
      MACC(acc[1][0], a1, xf[cb][0]); if constexpr (more) rdx(st + 1, nb, 1);
      MACC(acc[1][1], a1, xf[cb][1]); if constexpr (more) rdl(st + 1, nb, 1);
      MACC(asq, xf[cb][0], xf[cb][0]); if constexpr (more) rdh_(st + 1, nb, 1);
      MACC(acs[1], a1, onesf);
      SCHED();
      if constexpr (st < 2) { issue1(IC(st + 2), IC(2)); issue1(IC(st + 2), IC(3)); }
      else if constexpr (kc + 3 <= 6) issue1(IC((kc + 3) % 5), IC(2 * (st & 1) + 1));
      SCHED();
      MACC(acc[2][0], a2, xf[cb][0]); if constexpr (more) rdl(st + 1, nb, 2);
      MACC(acc[2][1], a2, xf[cb][1]); if constexpr (more) rdh_(st + 1, nb, 2);
      MACC(acs[2], a2, onesf);
      SCHED();
    });
    NOPS(); KEEP(asum); KEEP(asq);
#pragma unroll
    for (int e = 0; e < 3; ++e) { KEEP(acc[e][0]); KEEP(acc[e][1]); KEEP(acs[e]); }
    FS(4);
    STAMP(1);
    epoch = (unsigned)rfl((int)*(LAS unsigned*)(lds + M_PF + 252)) + 1u;
    {
      const unsigned ptb = (ptm == 1) ? WT_W1 : WT_W2, ptn = (ptm == 1) ? 14u : 56u;
      ldsp src = lds + M_PT + (4 * g + q4) * 448 + 8 * p4;
#pragma unroll
      for (int i = 0; i < 2; ++i) {
        const int t = (w + 8 * i < 14) ? w + 8 * i : w;
        const bf16x8 f = cat(ldtr(src + t * 32), ldtr(src + 16 * 448 + t * 32));
        st128_sc1(lane16 + ptb + ((unsigned)(14 * ptG + t) * ptn + (unsigned)pts) * 1024u, p.ws + WS_WT, __builtin_bit_cast(u32x4, f));
      }
    }
    {
      const float d01 = (p4 & 1) ? asq[1] : asq[0], d23 = (p4 & 1) ? asq[3] : asq[2], dq = (p4 & 2) ? d23 : d01;
      float mean = asum[0] * (1.f / 448.f), dq2 = dq;
      KEEP(mean); KEEP(dq2);
      if (g == q4) *(LAS f32x2*)(lds + M_LN + (32 * tg + 16 * ng + l15) * 8) = f32x2{mean, rsqrtf(fmaxf(dq2 * (1.f / 448.f) - mean * mean, 0.f) + 1e-5f)};
    }
    BAR();
    FS(5);
    float mu[2], rs[2];
#pragma unroll
    for (int t = 0; t < 2; ++t) { const f32x2 st_ = *(LAS f32x2*)(lds + M_LN + (32 * tg + 16 * (t ^ ng) + l15) * 8); mu[t] = st_[0]; rs[t] = st_[1]; }
    const bool pad0 = (h & 1) && g == 0, pad1 = !(h & 1) && g == 3;
    const int rsw = ((l15 >> 1) & 3) << 4, rowb = (32 * tg + l15) * 64;
    auto epi = [&](auto NG) {
      constexpr int ngc = decltype(NG)::value;
      sfor<0, 3>([&](auto E) {
        constexpr int e = decltype(E)::value, nt = 3 * ngc + e, which = nt >> 1, par = nt & 1;
        constexpr int ibase = (which == 0) ? QIMG : (which == 1) ? KIMG + 16384 : VIMG + 16384;
        const int off = (which == 2) ? ((((2 * par + (g >> 1)) << 4) ^ rsw) + (g & 1) * 8) : (((g << 4) ^ rsw) + par * 8);
        const bool pad = par ? pad1 : pad0;
#pragma unroll
        for (int t = 0; t < 2; ++t) {
          const float sc = (which == 0) ? rs[t] * 0.27264657f : rs[t], nm = -mu[t] * sc;
          f32x2 sc2 = {sc, sc}, nm2 = {nm, nm};
          KEEP(sc2); KEEP(nm2);
          const f32x2 v0 = lo2(acc[e][t]) * sc2 + lo2(acs[e]) * nm2, v1 = hi2(acc[e][t]) * sc2 + hi2(acs[e]) * nm2;
          u32x2 o; o.x = cvtpk(v0); o.y = cvtpk(v1);
          o.x = pad ? ((which == 2) ? 0x00003f80u : 0u) : o.x; o.y = pad ? 0u : o.y;
          *(LAS u32x2*)(lds + ibase + (t ^ ngc) * 1024 + rowb + off) = o;
        }
      });
    };
    if (ng) epi(IC(1)); else epi(IC(0));
    if (ca < 2) {
      const u32x4 z = {0, 0, 0, 0};
#pragma unroll
      for (int i = 0; i < 2; ++i) { *(LAS u32x4*)(lds + KIMG + tid * 16 + i * 8192) = z; *(LAS u32x4*)(lds + VIMG + tid * 16 + i * 8192) = z; }
    }
    BAR();
    FS(6);
    {
      const char* blob = p.ws + WS_KV + (size_t)(ca * 16 + h) * 16384;
      const u32x4 v0 = *(LAS u32x4*)(lds + KIMG + 16384 + tid * 16), v1 = *(LAS u32x4*)(lds + VIMG + 16384 + tid * 16);
      st128_sc1(tid * 16, blob, v0); st128_sc1(8192 + tid * 16, blob, v1);
    }
    FS(7);
    WAITV(0);
    BAR();
    if (tid == 0) { st32_sc1(ctl + FLAG_IDX(0, ca, h), epoch); st32_sc1(ctl + FLAG_IDX(4, b >> 4, b & 15), epoch); }
    FS(8);
    STAMP(2);
  }
  const int ns = j & 3, col4 = 128 * ns - (((ns >> 1) & ns) << 5), ngr = 4 - (ns >> 1);
  const int pr = w & 3, wh = w >> 2;
  const int prc = (pr < ngr) ? pr : 0;
  const int th5 = j >> 3, cs5 = j & 7, col0 = 64 * cs5 - 16 * ((cs5 >> 2) * (cs5 & 3)), nblk = 4 - (cs5 >> 2);
  const int ng5 = w & 1, kq = w >> 1;
  const char* wt = p.ws + WS_WT;
  bf16x8 wr[32];
  const int T1 = (448 * (j >> 2) + col4) / 16;
  const unsigned w1o = WT_W1 + (unsigned)(T1 + 2 * prc) * 14u * 1024u;
  const unsigned wro = lane16 + w1o;
  constexpr int WI = 32768;
  unsigned wb0, wb1;
  { const int rin = lane >> 4, pos = lane & 15;
    int blk = (pos >> 1) ^ rin; if (blk >= 2 * ngr) blk -= 2; wb0 = (unsigned)(blk * 32 + (pos & 1) * 16);
    blk = (pos >> 1) ^ (4 + rin); if (blk >= 2 * ngr) blk -= 2; wb1 = (unsigned)(blk * 32 + (pos & 1) * 16); }
  const char* wob = p.wo + col4 * 2;
  {
    if (w < 6) {
      const unsigned rb = (unsigned)(((lane >> 4) + 4 * (24 + w)) * 896) + ((w & 1) ? wb1 : wb0);
      sfor<0, 14>([&](auto I) { constexpr int i = decltype(I)::value; dma<i * 6144, i * 24 * 896, false>(rb, wob, (unsigned)(WI + (24 + w) * 1024)); });
    }
    int pos = lane & 15; if (pos >= 4 * ngr) pos = 4 * ngr - 1;
    int xch = (lane & 15) ^ (((lane >> 4) + 4 * w) & 15); if (xch >= 4 * ngr) xch = 0;
    dma<M_XT, 0, false>((unsigned)(((lane >> 4) + 4 * w) * 896 + xch * 16), p.x + (size_t)(128 * c + 32 * (j >> 2)) * 896 + col4 * 2, (unsigned)(w * 1024));
    if (w == 5 && lane < 16) dma<M_B1, 0, false>((unsigned)(pos * 16), p.b1 + (448 * (j >> 2) + col4) * 2, 0u);
    if (w == 4 && lane < 8) dma<M_B2, 0, false>((unsigned)((lane < 2 * nblk ? lane : 0) * 16), p.b2 + col0 * 2, 0u);
  }
  {
    const int h = ha;
    FS(9);
    const int sw = (l15 >> 1) & 3;
    if (w == 7 && ca >= 1) poll_flags(ctl, (unsigned)(FLAG_IDX(0, (ca >= 2 && (lane & 1)) ? ca - 2 : ca - 1, h) * 4), epoch);
    if (w == 6) {
      const unsigned tfo = (unsigned)(FLAG_IDX(4, 4 * g, l15) * 4);
#pragma unroll 1
      for (unsigned spins = 0; spins < SPIN_CAP; ++spins) {
        unsigned t0, t1, t2, t3;
        asm volatile("global_load_dword %0, %4, %5 sc1\n\tglobal_load_dword %1, %4, %5 offset:2048 sc1\n\tglobal_load_dword %2, %6, %5 sc1\n\tglobal_load_dword %3, %6, %5 offset:2048 sc1\n\ts_waitcnt vmcnt(0)"
                     : "=&v"(t0), "=&v"(t1), "=&v"(t2), "=&v"(t3) : "v"(tfo), "s"(ctl), "v"(tfo + 4096u) : "memory");
        if (__all(t0 == epoch && t1 == epoch && t2 == epoch && t3 == epoch)) break;
        __builtin_amdgcn_s_sleep(2);
      }
      if (b == 0 && lane == 0) st32_sc1(ctl, epoch);
    }
    BAR();
    FS(10);
    {
      const int tl = w >> 2, src = ca - 2 + tl, pc = (w & 3) * 2048;
      if (src >= 0) {
        const char* blob = p.ws + WS_KV + (size_t)(src * 16 + h) * 16384 + pc;
        dma2c_sc1<KIMG>(lane16, blob, (unsigned)(tl * 8192 + pc)); dma2c_sc1<VIMG>(lane16, blob + 8192, (unsigned)(tl * 8192 + pc));
      }
    }
    const bf16x8 qf = ld128(lds + QIMG + (16 * w + l15) * 64 + ((g ^ sw) << 4));
    WAITV(0);
    BAR();
    FS(11);
    STAMP(3);
    STAMP(4);
    if (w >= 4) ldfrags<7>(wr, wro, wt);
    f32x4 sc[17];
    const float NEG = -__builtin_inff();
    ldsp kb = lds + KIMG + (16 * w + l15) * 64 + ((g ^ sw) << 4);
    {
      bf16x8 kf[2][6];
#pragma unroll
      for (int u = 0; u < 6; ++u) kf[0][u] = ld128(kb + u * 1024);
#pragma unroll
      for (int bt = 0; bt < 3; ++bt) {
        if (bt < 2) {
#pragma unroll
          for (int u = 0; u < 6; ++u) if (6 * (bt + 1) + u < 17) kf[(bt + 1) & 1][u] = ld128(kb + (6 * (bt + 1) + u) * 1024);
        }
        SCHED();
#pragma unroll
        for (int u = 0; u < 6; ++u) if (6 * bt + u < 17) sc[6 * bt + u] = MFMA(kf[bt & 1][u], qf, (f32x4{0, 0, 0, 0}));
        SCHED();
      }
    }
    FS(12);
    if (w >= 4) ldfrags<7>(wr + 7, wro + 7 * 1024, wt);
    const int vsw = (2 * g + (q4 >> 1)) & 3;
    ldsp vb0 = lds + VIMG + (16 * w + 4 * g + q4) * 64 + (((p4 >> 1) ^ vsw) << 4) + (p4 & 1) * 8;
    ldsp vb1 = lds + VIMG + (16 * w + 4 * g + q4) * 64 + (((2 + (p4 >> 1)) ^ vsw) << 4) + (p4 & 1) * 8;
    bf16x8 vf[2][3][2];
    auto rdv = [&](int bt) {
#pragma unroll
      for (int i = 0; i < 3; ++i) { const int uu = 3 * bt + i, ua = 2 * uu, ub = (uu < 8) ? 2 * uu + 1 : 15;
        vf[bt & 1][i][0] = cat(ldtr(vb0 + ua * 1024), ldtr(vb0 + ub * 1024)); vf[bt & 1][i][1] = cat(ldtr(vb1 + ua * 1024), ldtr(vb1 + ub * 1024)); }
    };
    rdv(0);
    SCHED();
    if (ca < 2) {
      const int tmin = (2 - ca) * 8;
#pragma unroll
      for (int u = 0; u < 16; ++u) if (w + u < tmin) sc[u] = f32x4{NEG, NEG, NEG, NEG};
    }
#pragma unroll
    for (int r = 0; r < 4; ++r) { sc[0][r] = (4 * g + r <= l15) ? NEG : sc[0][r]; sc[16][r] = (4 * g + r > l15) ? NEG : sc[16][r]; }
    float m = max3(sc[0][0], sc[0][1], sc[0][2]), m2 = max3(sc[0][3], sc[1][0], sc[1][1]);
    m = max3(m, sc[1][2], sc[1][3]);
#pragma unroll
    for (int u = 2; u < 17; ++u) { if (u & 1) m = max3(max3(m, sc[u][0], sc[u][1]), sc[u][2], sc[u][3]); else m2 = max3(max3(m2, sc[u][0], sc[u][1]), sc[u][2], sc[u][3]); }
    m = max3(m, m2, m2);
    m = red_g_max(m);
    FS(13);
    SCHED();
    if (w >= 4) ldfrags<7>(wr + 14, wro + 14 * 1024, wt);
    unsigned pk[17][2];
    f32x2 mm = {-m, -m};
    KEEP(mm);
    f32x4 oa[2] = {f32x4{0, 0, 0, 0}, f32x4{0, 0, 0, 0}};
    KEEP(oa[0]); KEEP(oa[1]);
#pragma unroll
    for (int bt = 0; bt < 3; ++bt) {
#pragma unroll
      for (int u = 6 * bt; u < 6 * bt + 6 && u < 17; ++u) {
        const f32x2 d0 = lo2(sc[u]) + mm, d1 = hi2(sc[u]) + mm;
        pk[u][0] = cvtpk(__builtin_amdgcn_exp2f(d0[0]), __builtin_amdgcn_exp2f(d0[1]));
        pk[u][1] = cvtpk(__builtin_amdgcn_exp2f(d1[0]), __builtin_amdgcn_exp2f(d1[1]));
      }
      if (bt < 2) rdv(bt + 1);
      if (bt == 0 && w >= 4) ldfrags<7>(wr + 21, wro + 21 * 1024, wt);
      SCHED();
#pragma unroll
      for (int i = 0; i < 3; ++i) {
        const int uu = 3 * bt + i;
        u32x4 pu; pu.x = pk[2 * uu][0]; pu.y = pk[2 * uu][1];
        if (uu < 8) { pu.z = pk[2 * uu + 1][0]; pu.w = pk[2 * uu + 1][1]; } else { pu.z = 0; pu.w = 0; }
        const bf16x8 pf = __builtin_bit_cast(bf16x8, pu);
        MACCN(oa[0], vf[bt & 1][i][0], pf); MACC(oa[1], vf[bt & 1][i][1], pf);
      }
      SCHED();
    }
    NOPS(); KEEP(oa[0]); KEEP(oa[1]);
    FS(15);
    int tz2 = tid; asm volatile("" : "+v"(tz2));
    const int l15b = tz2 & 15, gb = (tz2 & 63) >> 4, swb = (l15b >> 1) & 3;
    const float lsel = (h & 1) ? oa[0][0] : oa[1][0];
    const float l = __uint_as_float((unsigned)__builtin_amdgcn_ds_bpermute((((h & 1) ? 0 : 48) + l15b) * 4, (int)__float_as_uint(lsel)));
    const float inv1 = __builtin_amdgcn_rcpf(l);
    f32x2 inv = {inv1, inv1};
    KEEP(inv);
    u32x4 ov;
    ov.x = cvtpk(lo2(oa[0]) * inv); ov.y = cvtpk(hi2(oa[0]) * inv); ov.z = cvtpk(lo2(oa[1]) * inv); ov.w = cvtpk(hi2(oa[1]) * inv);
    { const bool z0 = (h & 1) && gb == 0, z1 = !(h & 1) && gb == 3; ov.x = z0 ? 0u : ov.x; ov.y = z0 ? 0u : ov.y; ov.z = z1 ? 0u : ov.z; ov.w = z1 ? 0u : ov.w; }
    st128_sc1((unsigned)((16 * w + l15b) * 64 + ((gb ^ swb) << 4)), p.ws + WS_O + (size_t)(ca * 16 + h) * 8192, ov);
    FS(16);
    BAR();
    {
      const unsigned rb = (unsigned)(((lane >> 4) + 12 * w) * 896), va = rb + ((w & 1) ? wb1 : wb0), vb = rb + ((w & 1) ? wb0 : wb1), lb = (unsigned)(WI + 3 * w * 1024);
      dma<0, 0, false>(va, wob, lb); dma<1024, 4 * 896, false>(vb, wob, lb); dma<2048, 8 * 896, false>(va, wob, lb);
      const int ins = (w < 4) ? 108 + w : 3 * w + 2;
      dma<0, 0, false>((unsigned)(((lane >> 4) + 4 * ins) * 896) + ((ins & 1) ? wb1 : wb0), wob, (unsigned)(WI + ins * 1024));
    }
    WAITV(4);
    BAR();
    if (tid == 0) st32_sc1(ctl + FLAG_IDX(1, ca, h), epoch);
    FS(18);
    STAMP(5);
  }
  {
    LANEVARS();
    constexpr int OT = 0;
    const int tq = j >> 2;
    WAITV(0);
    BAR();
    if (w < 4) {
      const int rx = 4 * (g & 1) + q4;
      ldsp ab0 = lds + WI + (4 * g + q4) * 256 + (((2 * prc) ^ rx) << 5) + 8 * p4;
      ldsp ab1 = lds + WI + (4 * g + q4) * 256 + (((2 * prc + 1) ^ rx) << 5) + 8 * p4;
      ldsp ab0h = ab0 + 65536, ab1h = ab1 + 65536;
#pragma unroll
      for (int s = 0; s < 16; ++s) {
        const int rb = (28 * s - 4 * (s & 1)) * 256;
        if (rb + 4096 < 65536) { wr[s] = cat(ldtr(ab0 + rb), ldtr(ab0 + rb + 4096)); wr[16 + s] = cat(ldtr(ab1 + rb), ldtr(ab1 + rb + 4096)); }
        else { wr[s] = cat(ldtr(ab0h + (rb - 65536)), ldtr(ab0h + (rb - 65536 + 4096))); wr[16 + s] = cat(ldtr(ab1h + (rb - 65536)), ldtr(ab1h + (rb - 65536 + 4096))); }
      }
    } else {
      __builtin_amdgcn_s_setprio(3);
      poll_flags(ctl, (unsigned)(FLAG_IDX(1, c, l15) * 4), epoch);
      const char* ob_ = p.ws + WS_O + (size_t)(c * 16 + 4 * (w - 4)) * 8192 + tq * 2048;
      const unsigned lb = (unsigned)(OT + (w - 4) * 8192);
      dma2c_sc1<0>(lane16, ob_, lb); dma2c_sc1<2048>(lane16 + 8192, ob_, lb); dma2c_sc1<4096>(lane16 + 16384, ob_, lb); dma2c_sc1<6144>(lane16 + 24576, ob_, lb);
      __builtin_amdgcn_s_setprio(0);
    }
    FS(19);
    WAITV(0);
    BAR();
    FS(20);
    STAMP(6);
    STAMP(7);
    if (w < 4) {
      f32x4 a0[2], a1[2];
#pragma unroll
      for (int t = 0; t < 2; ++t) { a0[t] = f32x4{0, 0, 0, 0}; a1[t] = f32x4{0, 0, 0, 0}; }
      ldsp ob = lds + OT + l15 * 64 + ((g ^ ((l15 >> 1) & 3)) << 4);
      u32x2 xr[2][2];
#pragma unroll
      for (int t = 0; t < 2; ++t) { ldsp xp = lds + M_XT + (16 * t + l15) * 256 + (g & 1) * 8;
        xr[t][0] = *(LAS u32x2*)(xp + (((4 * prc + (g >> 1)) ^ l15) << 4)); xr[t][1] = *(LAS u32x2*)(xp + (((4 * prc + 2 + (g >> 1)) ^ l15) << 4)); }
#pragma unroll
      for (int t = 0; t < 2; ++t) { KEEP(a0[t]); KEEP(a1[t]); }
      bf16x8 of[4][2];
#pragma unroll
      for (int s = 0; s < 3; ++s) { of[s][0] = ld128(ob + s * 2048); of[s][1] = ld128(ob + s * 2048 + 1024); }
#pragma unroll
      for (int s = 0; s < 16; ++s) {
        if (s + 3 < 16) { of[(s + 3) & 3][0] = ld128(ob + (s + 3) * 2048); of[(s + 3) & 3][1] = ld128(ob + (s + 3) * 2048 + 1024); }
        SCHED();
#pragma unroll
        for (int t = 0; t < 2; ++t) { MACC(a0[t], wr[s], of[s & 3][t]); MACC(a1[t], wr[16 + s], of[s & 3][t]); }
        SCHED();
      }
      NOPS();
#pragma unroll
      for (int t = 0; t < 2; ++t) { KEEP(a0[t]); KEEP(a1[t]); }
      FS(21);
      const unsigned ho = (unsigned)((128 * c + 32 * tq + l15) * 896 + (col4 / 32 + pr) * 64 + g * 16);
#pragma unroll
      for (int t = 0; t < 2; ++t) {
        u32x4 hv;
        hv.x = cvtpk(lo2(a0[t]) + bf2(xr[t][0].x)); hv.y = cvtpk(hi2(a0[t]) + bf2(xr[t][0].y));
        hv.z = cvtpk(lo2(a1[t]) + bf2(xr[t][1].x)); hv.w = cvtpk(hi2(a1[t]) + bf2(xr[t][1].y));
        if (pr < ngr) st128_sc1(ho + t * 16 * 896, p.ws + WS_H, hv);
        float s1 = 0.f, s2 = 0.f;
#pragma unroll
        for (int i = 0; i < 4; ++i) { s1 = dot2(hv[i], ONES, s1); s2 = dot2(hv[i], hv[i], s2); }
        s1 = red_g_sum(s1); s2 = red_g_sum(s2);
        if (pr >= ngr) { s1 = 0.f; s2 = 0.f; }
        if (g == 0) ((LAS f32x2*)(lds + M_PART))[(16 * t + l15) * 4 + pr] = f32x2{s1, s2};
      }
    } else {
      pin<28>(wr);
#pragma unroll
      for (int i = 0; i < 28; ++i) *(LAS bf16x8*)(lds + 32768 + (w - 4) * 28672 + i * 1024 + lane * 16) = wr[i];
    }
    FS(22);
    BAR();
    if (tid < 32) {
      const f32x4 u0 = *(LAS f32x4*)(lds + M_PART + tid * 32), u1 = *(LAS f32x4*)(lds + M_PART + tid * 32 + 16);
      st64_sc1((unsigned)((128 * c + 32 * tq + tid) * 32 + ns * 8), p.ws + WS_ST,
               u32x2{__float_as_uint((u0[0] + u0[2]) + (u1[0] + u1[2])), __float_as_uint((u0[1] + u0[3]) + (u1[1] + u1[3]))});
    }
    if (w < 4) {
#pragma unroll
      for (int i = 0; i < 28; ++i) wr[i] = ld128(lds + 32768 + w * 28672 + i * 1024 + lane * 16);
    }
    FS(23);
    if (w < 4) WAITV(0);
    BAR();
    if (tid == 0) st32_sc1(ctl + FLAG_IDX(2, c, j), epoch);
    FS(25);
    STAMP(8);
  }
  {
    LANEVARS();
    const int f0_ = 448 * (j >> 2) + col4, th = wh;
    FS(26);
    f32x4 c0 = {0, 0, 0, 0}, c1 = {0, 0, 0, 0};
    pin<28>(wr);
    bf16x8 onesf = __builtin_bit_cast(bf16x8, (u32x4{ONES, ONES, ONES, ONES}));
    KEEP(onesf); KEEP(c0); KEEP(c1);
    asm volatile("s_nop 1");
#pragma unroll
    for (int s = 0; s < 14; ++s) { MACC(c0, wr[s], onesf); MACC(c1, wr[14 + s], onesf); }
    NOPS(); KEEP(c0); KEEP(c1);
    SCHED();
    if (w == 7) poll_flags(ctl, (unsigned)(FLAG_IDX(2, c, l15) * 4), epoch);
    FS(29);
    BAR();
    const unsigned vo = (unsigned)((16 * w + (lane >> 3)) * 896) + x8, wl2k = (unsigned)(w * 2048);
    const char* hb = p.ws + WS_H + (size_t)c * 128 * 896;
    auto issue4 = [&](auto KC) { constexpr int kc = decltype(KC)::value; dma<kc * 16384, kc * 128, true>(vo, hb, wl2k); dma<kc * 16384 + 1024, kc * 128 + 8 * 896, true>(vo, hb, wl2k); };
    if (lane < 32) dma<M_TILE, 0, true>(lane16, p.ws + WS_ST + (size_t)(128 * c + 16 * w) * 32, (unsigned)(w * 512));
    issue4(IC(0)); RAWBAR(); issue4(IC(1));
    FS(31);
    f32x4 acc[2][4];
#pragma unroll
    for (int t = 0; t < 4; ++t) { acc[0][t] = f32x4{0, 0, 0, 0}; acc[1][t] = f32x4{0, 0, 0, 0}; }
    const int xb = (64 * th + l15) * 128 + ((g ^ (l15 & 7)) << 4);
    bf16x8 xf[2][4];
    auto rd1 = [&](int st, int buf, int t) { xf[buf][t] = ld128(lds + (st >> 1) * 16384 + (xb ^ ((st & 1) << 6)) + t * 2048); };
    auto rdf = [&](int st, int buf) { rd1(st, buf, 0); rd1(st, buf, 1); rd1(st, buf, 2); rd1(st, buf, 3); };
#pragma unroll
    for (int t = 0; t < 4; ++t) { KEEP(acc[0][t]); KEEP(acc[1][t]); }
    bf16x8 bf[2][14];
    const unsigned w2o0 = WT_W2 + ((unsigned)(col0 / 16 + 2 * ng5) * 56u + 14u * kq) * 1024u, w2o1 = (2 * ng5 + 1 < nblk) ? w2o0 + 56u * 1024u : w2o0;
    WAITV(2); BAR();
    FS(32);
    STAMP(9);
    STAMP(10);
    rdf(0, 0);
    sfor<0, 14>([&](auto ST) {
      constexpr int st = decltype(ST)::value;
      if constexpr (st == 1) WAITV(2); if constexpr (st == 3) WAITV(4); if constexpr (st == 5) WAITV(6); if constexpr (st == 7) WAITV(8); if constexpr (st == 9) WAITV(10); if constexpr (st == 11) WAITV(12);
      if constexpr ((st & 1) && st < 13) BARN();
      if constexpr (st == 2) {
        if (tid < 128) {
          const f32x4 u0 = *(LAS f32x4*)(lds + M_TILE + tid * 32), u1 = *(LAS f32x4*)(lds + M_TILE + tid * 32 + 16);
          float mean = ((u0[0] + u0[2]) + (u1[0] + u1[2])) * (1.f / 448.f), sq = (u0[1] + u0[3]) + (u1[1] + u1[3]);
          KEEP(mean); KEEP(sq);
          const float rstd = rsqrtf(fmaxf(sq * (1.f / 448.f) - mean * mean, 0.f) + 1e-5f);
          *(LAS f32x2*)(lds + M_LN + tid * 8) = f32x2{rstd, -mean * rstd};
        }
      }
      SCHED();
#pragma unroll
      for (int t = 0; t < 2; ++t) { MACC(acc[0][t], wr[st], xf[st & 1][t]); if constexpr (st < 13) rd1(st + 1, (st + 1) & 1, t); MACC(acc[1][t], wr[14 + st], xf[st & 1][t]); }
      SCHED();
      if constexpr (st <= 4) issue4(IC(st + 2));
      if constexpr (st >= 5) { constexpr int s = st - 5; ldfrags<1>(&bf[0][s], lane16 + w2o0 + s * 1024, wt); ldfrags<1>(&bf[1][s], lane16 + w2o1 + s * 1024, wt); }
      SCHED();
#pragma unroll
      for (int t = 2; t < 4; ++t) { MACC(acc[0][t], wr[st], xf[st & 1][t]); if constexpr (st < 13) rd1(st + 1, (st + 1) & 1, t); MACC(acc[1][t], wr[14 + st], xf[st & 1][t]); }
      SCHED();
    });
    NOPS();
#pragma unroll
    for (int t = 0; t < 4; ++t) { KEEP(acc[0][t]); KEEP(acc[1][t]); }
    FS(33);
    STAMP(11);
    {
      const u32x2 bb0 = *(LAS u32x2*)(lds + M_B1 + (32 * prc + 4 * g) * 2), bb1 = *(LAS u32x2*)(lds + M_B1 + (32 * prc + 4 * g) * 2 + 32);
      const f32x2 b00 = bf2(bb0.x), b01 = bf2(bb0.y), b10 = bf2(bb1.x), b11 = bf2(bb1.y);
      const unsigned ho = (unsigned)((128 * c + 64 * th + l15) * 3584 + (f0_ / 32 + pr) * 64 + g * 16);
      f32x2 ln[4];
#pragma unroll
      for (int t = 0; t < 4; ++t) ln[t] = *(LAS f32x2*)(lds + M_LN + (64 * th + 16 * t + l15) * 8);
#pragma unroll
      for (int t = 0; t < 4; ++t) {
        const float a = ln[t][0], nm = ln[t][1];
        f32x2 aa = {a, a}, nn = {nm, nm};
        KEEP(aa); KEEP(nn);
        f32x2 z[4] = {lo2(acc[0][t]) * aa + (lo2(c0) * nn + b00), hi2(acc[0][t]) * aa + (hi2(c0) * nn + b01), lo2(acc[1][t]) * aa + (lo2(c1) * nn + b10), hi2(acc[1][t]) * aa + (hi2(c1) * nn + b11)};
        u32x4 hv;
#pragma unroll
        for (int i = 0; i < 4; ++i) {
          f32x2 r = {__builtin_elementwise_maximum(z[i][0], 0.f), __builtin_elementwise_maximum(z[i][1], 0.f)};
          hv[i] = cvtpk(r * r);
        }
        if (pr < ngr) st128_sc1(ho + t * 16 * 3584, p.ws + WS_H1, hv);
      }
    }
    FS(34);
    WAITV(0);
    BAR();
    if (tid == 0) st32_sc1(ctl + FLAG_IDX(3, c, j), epoch);
    FS(38);
    STAMP(12);
    if (w != 7) {
      ldfrags<5>(&bf[0][9], lane16 + w2o0 + 9 * 1024, wt); ldfrags<5>(&bf[1][9], lane16 + w2o1 + 9 * 1024, wt);
    }
    if (w >= 5 && w < 7) {
      const int m0_ = col0 >> 5;
      const char* hr = p.ws + WS_H + (size_t)(128 * c + 64 * th5) * 896;
      sfor<0, 6>([&](auto I) { constexpr int i = decltype(I)::value;
        const unsigned u = (unsigned)((w - 5) * 384 + i * 64 + lane), r0 = (u * 43691u) >> 19, c0_ = u - 12u * r0; unsigned cc0 = m0_ * 4 + c0_; if (cc0 > 55) cc0 = 55;
        dma<M_TILE + i * 1024, 0, true>(r0 * 896 + cc0 * 16, hr, (unsigned)((w - 5) * 6144)); });
    }
    const unsigned vo5 = (unsigned)((32 * ng5 + (lane >> 3)) * 3584 + kq * 896) + x8, wl4k = (unsigned)(w * 4096);
    const char* hb5 = p.ws + WS_H1 + (size_t)(128 * c + 64 * th5) * 3584;
    auto issue5 = [&](auto I) {
      constexpr int i = decltype(I)::value, sa = (i & 3) * 32768;
      sfor<0, 4>([&](auto II) { constexpr int ii = decltype(II)::value; dma<sa + ii * 1024, i * 128 + ii * 8 * 3584, true>(vo5, hb5, wl4k); });
    };
    FS(39);
    if (w == 7) {
      poll_flags(ctl, (unsigned)(FLAG_IDX(3, c, l15) * 4), epoch);
    }
    FS(40);
    BAR();
    FS(41);
    STAMP(13);
    issue5(IC(0));
    if (w == 7) {
      ldfrags<5>(&bf[0][9], lane16 + w2o0 + 9 * 1024, wt); ldfrags<5>(&bf[1][9], lane16 + w2o1 + 9 * 1024, wt);
    }
    FS(42);
    f32x4 ac5[2][4];
#pragma unroll
    for (int t = 0; t < 4; ++t) { ac5[0][t] = f32x4{0, 0, 0, 0}; ac5[1][t] = f32x4{0, 0, 0, 0}; }
    int yb[4];
#pragma unroll
    for (int t = 0; t < 4; ++t) yb[t] = kq * 8192 + (16 * ((t + kq) & 3) + l15) * 128 + ((g ^ (l15 & 7)) << 4);
    bf16x8 yf[2][4];
    auto rz1 = [&](int st, int buf, int t) { yf[buf][t] = ld128(lds + ((st >> 1) & 3) * 32768 + (yb[t] ^ ((st & 1) << 6))); };
    auto rdy = [&](int st, int buf) { rz1(st, buf, 0); rz1(st, buf, 1); rz1(st, buf, 2); rz1(st, buf, 3); };
#pragma unroll
    for (int t = 0; t < 4; ++t) { KEEP(ac5[0][t]); KEEP(ac5[1][t]); }
    WAITV(0);
    pin<14>(bf[0]); pin<14>(bf[1]);
    BAR();
    FS(43);
    STAMP(14);
    rdy(0, 0);
    sfor<0, 14>([&](auto ST) {
      constexpr int st = decltype(ST)::value;
      if constexpr ((st & 1) && st < 13) {
        if constexpr (st < 11) WAITV(4); else WAITV(0);
        BARN();
      }
      SCHED();
#pragma unroll
      for (int t = 0; t < 2; ++t) { MACC(ac5[0][t], bf[0][st], yf[st & 1][t]); if constexpr (st < 13) rz1(st + 1, (st + 1) & 1, t); MACC(ac5[1][t], bf[1][st], yf[st & 1][t]); }
      SCHED();
      if constexpr (st == 0) { issue5(IC(1)); issue5(IC(2)); }
      if constexpr ((st & 1) && st <= 7) issue5(IC((st + 5) / 2));
      SCHED();
#pragma unroll
      for (int t = 2; t < 4; ++t) { MACC(ac5[0][t], bf[0][st], yf[st & 1][t]); if constexpr (st < 13) rz1(st + 1, (st + 1) & 1, t); MACC(ac5[1][t], bf[1][st], yf[st & 1][t]); }
      SCHED();
    });
    NOPS();
#pragma unroll
    for (int t = 0; t < 4; ++t) { KEEP(ac5[0][t]); KEEP(ac5[1][t]); }
    FS(44);
    BAR();
#pragma unroll
    for (int t = 1; t < 4; ++t) {
      ldsp d = lds + ((((t + kq) & 3) * 3 + (t - 1)) * 2 + ng5) * 2048 + lane * 16;
      *(LAS f32x4*)d = ac5[0][t]; *(LAS f32x4*)(d + 1024) = ac5[1][t];
    }
    const int tokl = 16 * kq + l15;
    const int t0 = col0 / 16 + 2 * ng5, t1 = (2 * ng5 + 1 < nblk) ? t0 + 1 : t0, m0_ = col0 >> 5;
    const u32x2 hr0 = *(LAS u32x2*)(lds + M_TILE + tokl * 192 + ((t0 >> 1) - m0_) * 64 + g * 16 + (t0 & 1) * 8);
    const u32x2 hr1 = *(LAS u32x2*)(lds + M_TILE + tokl * 192 + ((t1 >> 1) - m0_) * 64 + g * 16 + (t1 & 1) * 8);
    const u32x2 b20 = *(LAS u32x2*)(lds + M_B2 + (32 * ng5 + 4 * g) * 2), b21 = *(LAS u32x2*)(lds + M_B2 + ((2 * ng5 + 1 < nblk) ? (32 * ng5 + 16) : 32 * ng5) * 2 + 8 * g);
    f32x2 r00 = lo2(ac5[0][0]) + (bf2(b20.x) + bf2(hr0.x)), r01 = hi2(ac5[0][0]) + (bf2(b20.y) + bf2(hr0.y));
    f32x2 r10 = lo2(ac5[1][0]) + (bf2(b21.x) + bf2(hr1.x)), r11 = hi2(ac5[1][0]) + (bf2(b21.y) + bf2(hr1.y));
    BAR();
    FS(45);
#pragma unroll
    for (int t = 1; t < 4; ++t) {
      ldsp d = lds + ((kq * 3 + (t - 1)) * 2 + ng5) * 2048 + lane * 16;
      const f32x4 u0 = *(LAS f32x4*)d, u1 = *(LAS f32x4*)(d + 1024);
      r00 += lo2(u0); r01 += hi2(u0); r10 += lo2(u1); r11 += hi2(u1);
    }
    GAS char* op = (GAS char*)p.out + (size_t)(128 * c + 64 * th5 + tokl) * 896 + (col0 + 32 * ng5 + 4 * g) * 2;
    *(GAS u32x2*)op = u32x2{cvtpk(r00), cvtpk(r01)};
    if (2 * ng5 + 1 < nblk) *(GAS u32x2*)(op + 32) = u32x2{cvtpk(r10), cvtpk(r11)};
    STAMP(15);
    FS(63);
  }
}

extern "C" void kernel_launch(void* const* d_in, const int* in_sizes, int n_in, void* d_out, int out_size, void* d_ws, size_t ws_size, hipStream_t stream) {
  Params p;
  p.x = (const char*)d_in[0]; p.wqkv = (const char*)d_in[1]; p.wo = (const char*)d_in[2];
  p.w1 = (const char*)d_in[4]; p.b1 = (const char*)d_in[5]; p.w2 = (const char*)d_in[6]; p.b2 = (const char*)d_in[7];
  p.out = (char*)d_out; p.ws = (char*)d_ws;
  (void)hipFuncSetAttribute((const void*)fused_block, hipFuncAttributeMaxDynamicSharedMemorySize, LDS_BYTES);
  hipLaunchKernelGGL(fused_block, dim3(256), dim3(512), LDS_BYTES, stream, p);
}
```

```cpp
#define MAPA
#define E1_CODE
#include <hip/hip_runtime.h>
#include <hip/hip_bf16.h>
#include <stdint.h>
#include <type_traits>

#define LAS __attribute__((address_space(3)))
#define GAS __attribute__((address_space(1)))
typedef short bf16x8 __attribute__((ext_vector_type(8)));
typedef short s16x4 __attribute__((ext_vector_type(4)));
typedef short v4i16_t __attribute__((ext_vector_type(4)));
typedef float f32x4 __attribute__((ext_vector_type(4)));
typedef float f32x2 __attribute__((ext_vector_type(2)));
typedef unsigned u32x4 __attribute__((ext_vector_type(4)));
typedef unsigned u32x2 __attribute__((ext_vector_type(2)));
typedef __bf16 bf16x2_t __attribute__((ext_vector_type(2)));
typedef LAS char* ldsp;

constexpr int SEQ = 2048;
constexpr size_t WS_CTL = 0;
constexpr size_t WS_KV = 262144;
constexpr size_t WS_O = WS_KV + 256 * 16384;
constexpr size_t WS_H = WS_O + 256 * 8192;
constexpr size_t WS_H1 = WS_H + (size_t)SEQ * 896;
constexpr size_t WS_ST = WS_H1 + (size_t)SEQ * 3584;
constexpr size_t WS_WT = WS_ST + (size_t)SEQ * 32;
constexpr unsigned WT_W1 = 0, WT_W2 = WT_W1 + 1568 * 1024, WT_BYTES = WT_W2 + 1568 * 1024;
constexpr size_t WS_DBG = WS_WT + WT_BYTES;
constexpr int LDS_BYTES = 160 * 1024;
constexpr int M_PT = 143360;
constexpr int M_XT = 147456;
constexpr int M_TILE = 131072;
constexpr int MISC = 157696;
constexpr int M_LN = MISC, M_B1 = MISC + 1024, M_B2 = MISC + 1280, M_PF = MISC + 1536, M_PART = MISC + 2048, M_FILL = MISC + 3072;
constexpr unsigned SPIN_CAP = 1u << 16;

#define FLAG_IDX(ph, c, j) (64 + (((ph) * 16 + (c)) * 16 + (j)) * 32)

#define WAITV(n) asm volatile("s_waitcnt vmcnt(" #n ")" ::: "memory")
#define BAR() do { asm volatile("s_waitcnt lgkmcnt(0)" ::: "memory"); __builtin_amdgcn_s_barrier(); asm volatile("" ::: "memory"); } while (0)
#define BARN() do { asm volatile("" ::: "memory"); __builtin_amdgcn_s_barrier(); asm volatile("" ::: "memory"); } while (0)
#define SCHED() __builtin_amdgcn_sched_barrier(0)
#define RAWBAR() __builtin_amdgcn_s_barrier()
#define ILV(n) do { _Pragma("unroll") for (int i_ = 0; i_ < (n); ++i_) { __builtin_amdgcn_sched_group_barrier(0x008, 1, 0); __builtin_amdgcn_sched_group_barrier(0x100, 1, 0); } } while (0)
#define MFMA(a, b, c) __builtin_amdgcn_mfma_f32_16x16x32_bf16((a), (b), (c), 0, 0, 0)
#define MACC(acc, a, b) asm volatile("v_mfma_f32_16x16x32_bf16 %0, %1, %2, %0" : "+v"(acc) : "v"(a), "v"(b))
#define MACCN(acc, a, b) asm volatile("s_nop 1\n\tv_mfma_f32_16x16x32_bf16 %0, %1, %2, %0" : "+v"(acc) : "v"(a), "v"(b))
#define NOPS() asm volatile("s_nop 7\n\ts_nop 3")
#define KEEP(x) asm volatile("" : "+v"(x))
#define IC(x) std::integral_constant<int, (x)>{}

template <int I, int N, class F> __device__ __forceinline__ void sfor(F&& f) { if constexpr (I < N) { f(std::integral_constant<int, I>{}); sfor<I + 1, N>(f); } }

__device__ __forceinline__ unsigned cvtpk(float lo, float hi) { f32x2 v = {lo, hi}; bf16x2_t b = __builtin_convertvector(v, bf16x2_t); return __builtin_bit_cast(unsigned, b); }
__device__ __forceinline__ unsigned cvtpk(f32x2 v) { bf16x2_t b = __builtin_convertvector(v, bf16x2_t); return __builtin_bit_cast(unsigned, b); }
__device__ __forceinline__ float dot2(unsigned a, unsigned b, float c) { return __builtin_amdgcn_fdot2_f32_bf16(__builtin_bit_cast(bf16x2_t, a), __builtin_bit_cast(bf16x2_t, b), c, false); }
__device__ __forceinline__ float bflo(unsigned u) { return __uint_as_float(u << 16); }
__device__ __forceinline__ float bfhi(unsigned u) { return __uint_as_float(u & 0xffff0000u); }
__device__ __forceinline__ f32x2 bf2(unsigned u) { return f32x2{bflo(u), bfhi(u)}; }
__device__ __forceinline__ f32x2 lo2(f32x4 v) { return f32x2{v[0], v[1]}; }
__device__ __forceinline__ f32x2 hi2(f32x4 v) { return f32x2{v[2], v[3]}; }
__device__ __forceinline__ bf16x8 ld128(ldsp p) { return *(const LAS bf16x8*)p; }
__device__ __forceinline__ s16x4 ldtr(ldsp p) { return __builtin_bit_cast(s16x4, __builtin_amdgcn_ds_read_tr16_b64_v4i16((LAS v4i16_t*)p)); }
__device__ __forceinline__ bf16x8 cat(s16x4 lo, s16x4 hi) { return __builtin_shufflevector(lo, hi, 0, 1, 2, 3, 4, 5, 6, 7); }
__device__ __forceinline__ int rfl(int x) { return __builtin_amdgcn_readfirstlane(x); }
template <int LADD, int VADD, bool SC1>
__device__ __forceinline__ void dma(unsigned voff, const void* sbase, unsigned lbase) {
  const unsigned va = voff + (unsigned)VADD;
  if constexpr (SC1) asm volatile("s_add_i32 m0, %2, %3\n\ts_nop 0\n\tglobal_load_lds_dwordx4 %0, %1 sc1" ::"v"(va), "s"(sbase), "s"(lbase), "i"(LADD) : "memory", "scc");
  else asm volatile("s_add_i32 m0, %2, %3\n\ts_nop 0\n\tglobal_load_lds_dwordx4 %0, %1" ::"v"(va), "s"(sbase), "s"(lbase), "i"(LADD) : "memory", "scc");
}
template <int LADD>
__device__ __forceinline__ void dma2c_sc1(unsigned voff, const void* sbase, unsigned lbase) {
  asm volatile("s_add_i32 m0, %2, %3\n\ts_nop 0\n\tglobal_load_lds_dwordx4 %0, %1 sc1\n\tglobal_load_lds_dwordx4 %0, %1 offset:1024 sc1" ::"v"(voff), "s"(sbase), "s"(lbase), "i"(LADD) : "memory", "scc");
}
template <int OFF>
__device__ __forceinline__ void ldfrag1(bf16x8& f, unsigned voff, const void* sbase) {
  asm volatile("global_load_dwordx4 %0, %1, %2 offset:%3 sc1" : "=v"(f) : "v"(voff), "s"(sbase), "i"(OFF) : "memory");
}
__device__ __forceinline__ void pin1(bf16x8& f) { asm volatile("" : "+v"(f)); }
template <int N>
__device__ __forceinline__ void ldfrags(bf16x8* f, unsigned voff, const void* sbase) {
  sfor<0, N>([&](auto I) { constexpr int i = decltype(I)::value; ldfrag1<(i & 3) * 1024>(f[i], voff + (unsigned)((i >> 2) * 4096), sbase); });
}
template <int N>
__device__ __forceinline__ void pin(bf16x8* f) { sfor<0, N>([&](auto I) { pin1(f[decltype(I)::value]); }); }
__device__ __forceinline__ void st128_sc1(unsigned voff, const void* sbase, u32x4 v) {
  asm volatile("global_store_dwordx4 %0, %1, %2 sc1\n\ts_nop 1" ::"v"(voff), "v"(v), "s"(sbase) : "memory");
}
__device__ __forceinline__ void st64_sc1(unsigned voff, const void* sbase, u32x2 v) {
  asm volatile("global_store_dwordx2 %0, %1, %2 sc1\n\ts_nop 1" ::"v"(voff), "v"(v), "s"(sbase) : "memory");
}
__device__ __forceinline__ void st32_sc1(const void* sbase, unsigned v) {
  asm volatile("global_store_dword %0, %1, %2 sc1\n\ts_nop 1" ::"v"(0u), "v"(v), "s"(sbase) : "memory");
}
__device__ __forceinline__ float max3(float a, float b, float c) { return __builtin_elementwise_maximum(__builtin_elementwise_maximum(a, b), c); }
__device__ __forceinline__ float red_g_sum(float v) {
  auto a = __builtin_amdgcn_permlane16_swap(__float_as_uint(v), __float_as_uint(v), false, false);
  v = __uint_as_float(a[0]) + __uint_as_float(a[1]);
  auto b = __builtin_amdgcn_permlane32_swap(__float_as_uint(v), __float_as_uint(v), false, false);
  return __uint_as_float(b[0]) + __uint_as_float(b[1]);
}
__device__ __forceinline__ float red_g_max(float v) {
  auto a = __builtin_amdgcn_permlane16_swap(__float_as_uint(v), __float_as_uint(v), false, false);
  v = __builtin_elementwise_maximum(__uint_as_float(a[0]), __uint_as_float(a[1]));
  auto b = __builtin_amdgcn_permlane32_swap(__float_as_uint(v), __float_as_uint(v), false, false);
  return __builtin_elementwise_maximum(__uint_as_float(b[0]), __uint_as_float(b[1]));
}
__device__ __forceinline__ void poll_flags(const unsigned* base, unsigned voff, unsigned epoch) {
#pragma unroll 1
  for (unsigned spins = 0; spins < SPIN_CAP; ++spins) {
    unsigned v;
    asm volatile("global_load_dword %0, %1, %2 sc1\n\ts_waitcnt vmcnt(0)" : "=&v"(v) : "v"(voff), "s"(base) : "memory");
    if (__all(v == epoch)) break;
    __builtin_amdgcn_s_sleep(1);
  }
}

struct Params {
  const char* x; const char* wqkv; const char* wo; const char* w1; const char* b1; const char* w2; const char* b2;
  char* out; char* ws;
};

#define FS(i) do {} while (0)
#define STAMP(i) do {} while (0)

#define LANEVARS() int tz_ = tid; asm volatile("" : "+v"(tz_)); const int lane = tz_ & 63, l15 = lane & 15, g = lane >> 4, q4 = l15 >> 2, p4 = l15 & 3; \
  const unsigned lane16 = (unsigned)lane * 16u, x8 = (unsigned)(((lane & 7) ^ ((lane >> 3) & 7)) << 4); (void)q4; (void)p4; (void)g; (void)x8; (void)lane16

__global__ void __launch_bounds__(512, 2) fused_block(Params p) {
  extern __shared__ __attribute__((aligned(16))) char smem[];
  ldsp lds = (ldsp)smem;
  const int tid = threadIdx.x, lane = tid & 63, w = rfl(tid >> 6);
  const int l15 = lane & 15, g = lane >> 4, q4 = l15 >> 2, p4 = l15 & 3;
  const int b = blockIdx.x, r_ = b >> 3;
  const int c = 2 * (b & 7) + (r_ >> 4), j = r_ & 15;
  const int ca = 4 * ((b & 7) >> 1) + (r_ >> 3), ha = 8 * (b & 1) + (r_ & 7);
  const unsigned* ctl = (const unsigned*)(p.ws + WS_CTL);
  unsigned epoch = 0;
  FS(0);
  STAMP(0);
  const unsigned ONES = 0x3f803f80u;
  const unsigned lane16 = (unsigned)lane * 16u;
  const unsigned x8 = (unsigned)(((lane & 7) ^ ((lane >> 3) & 7)) << 4);

  constexpr int KIMG = 0, VIMG = 24576, QIMG = 49152;
  {
    const int h = ha;
    constexpr int SLOT = 28672, WOFF = 16384;
    const unsigned iw = (unsigned)(w >> 2);
    unsigned voff[4];
    {
      const unsigned xo = (unsigned)((32 * w + (lane >> 3)) * 896) + x8;
#pragma unroll
      for (int ii = 0; ii < 4; ++ii) {
        const unsigned u = (unsigned)(192 * (w & 3) + 64 * ii + lane), row = (u * 43691u) >> 19, rem = u - 12u * row;
        const unsigned piece = rem >> 2, c16 = rem & 3, hb = (c16 >> 1) ^ ((row >> 3) & 1);
        const unsigned wo_ = (ii < 3) ? row * 2688u + piece * 896u + hb * 32u + (c16 & 1) * 16u : 0u;
        voff[ii] = iw ? wo_ : xo + ii * 7168u;
      }
    }
    const unsigned long long mW = 0ull - (unsigned long long)iw;
    const unsigned long long bx_ = (unsigned long long)(uintptr_t)p.x + (unsigned)(ca * 128 * 896), bw_ = (unsigned long long)(uintptr_t)p.wqkv + (unsigned)(56 * h - 8 * (h & 1));
    const char* gb = (const char*)(uintptr_t)(bx_ + ((bw_ - bx_) & mW));
    const unsigned kstep = 128 + iw * (64 * 2688 - 128), ldst = w * 4096 + iw * (WOFF + (w - 4) * 3072 - w * 4096);
    const unsigned smask = iw - 1u, l3 = iw ? (unsigned)M_FILL : ldst + 3072u;
    auto issue1 = [&](auto SL, auto II) {
      constexpr int sl = decltype(SL)::value, ii = decltype(II)::value;
      if constexpr (ii < 3) dma<sl * SLOT + ii * 1024, 0, false>(voff[ii], gb, ldst);
      else { dma<0, 0, false>(voff[3], gb, l3 + (smask & (unsigned)(sl * SLOT))); gb += kstep; }
    };
    auto issue = [&](auto SL) { issue1(SL, IC(0)); issue1(SL, IC(1)); issue1(SL, IC(2)); issue1(SL, IC(3)); };
    FS(1);
    issue(IC(0));
    RAWBAR(); issue(IC(1));
    {
      unsigned long long pc; asm volatile("s_getpc_b64 %0" : "=s"(pc));
      const unsigned long long a = (pc & ~127ull) + (unsigned long long)((r_ * 8 + w) * 128);
      if (lane == 0) asm volatile("s_mov_b32 m0, %1\n\ts_nop 0\n\tglobal_load_lds_dword %0, off" ::"v"(a), "i"(M_FILL) : "memory");
    }

    FS(2);
    const int ptq0 = (b < 224) ? b : 223, ptm = (ptq0 >= 112) ? 2 : 1, ptq = ptq0 - ((ptm == 2) ? 112 : 0);
    const int pts = (ptm == 1) ? ptq >> 3 : ptq >> 1, ptG = (ptm == 1) ? ptq & 7 : ptq & 1;
    auto pt_issue = [&]() {
      const unsigned stride = (ptm == 1) ? 3584u : 896u;
      const char* src = ((ptm == 1) ? p.w1 : p.w2) + (size_t)(32 * pts * stride + ptG * 448);
      const int wq = (w < 7) ? w : 6;
#pragma unroll
      for (int i = 0; i < 2; ++i) { const unsigned u = (unsigned)((2 * wq + i) * 64 + lane), row = (u * 9363u) >> 18, c16 = u - 28u * row;
        dma<M_PT, 0, false>(row * stride + c16 * 16u, src, (unsigned)((2 * wq + i) * 1024)); }
      if (lane == 63) asm volatile("s_mov_b32 m0, %2\n\ts_nop 0\n\tglobal_load_lds_dword %0, %1 sc1" ::"v"(0u), "s"(p.ws + WS_CTL), "i"(M_PF) : "memory");
    };
    const int tg = w >> 1, ng = w & 1;
    f32x4 acc[3][2], asum = {0, 0, 0, 0}, asq = {0, 0, 0, 0}, acs[3];
#pragma unroll
    for (int e = 0; e < 3; ++e) { acc[e][0] = f32x4{0, 0, 0, 0}; acc[e][1] = f32x4{0, 0, 0, 0}; acs[e] = f32x4{0, 0, 0, 0}; }
    const int xb = (32 * tg + 16 * ng + l15) * 128 + ((g ^ (l15 & 7)) << 4);
    int ab[3];
#pragma unroll
    for (int e = 0; e < 3; ++e) ab[e] = WOFF + (8 * g + q4) * 192 + ((((3 * ng + e) * 32) ^ ((g & 1) << 5))) + 8 * p4;
    bf16x8 xf[2][2];
    s16x4 al[2][3], ah[2][3];
    auto rdx = [&](int st, int buf, int t) { xf[buf][t] = ld128(lds + ((st >> 1) % 5) * SLOT + (xb ^ ((st & 1) << 6) ^ (t * 2048))); };
    auto rdl = [&](int st, int buf, int e) { al[buf][e] = ldtr(lds + ((st >> 1) % 5) * SLOT + ab[e] + (32 * (st & 1)) * 192); };
    auto rdh_ = [&](int st, int buf, int e) { ah[buf][e] = ldtr(lds + ((st >> 1) % 5) * SLOT + ab[e] + (32 * (st & 1) + 4) * 192); };
    auto rdf = [&](int st, int buf) { rdx(st, buf, 0); rdx(st, buf, 1);
#pragma unroll
      for (int e = 0; e < 3; ++e) { rdl(st, buf, e); rdh_(st, buf, e); } };
    bf16x8 onesf = __builtin_bit_cast(bf16x8, (u32x4{ONES, ONES, ONES, ONES}));
    KEEP(onesf); KEEP(asum); KEEP(asq);
#pragma unroll
    for (int e = 0; e < 3; ++e) { KEEP(acc[e][0]); KEEP(acc[e][1]); KEEP(acs[e]); }
    WAITV(5);
    BAR();
    FS(3);
    rdf(0, 0);
    sfor<0, 14>([&](auto ST) {
      constexpr int st = decltype(ST)::value, kc = st >> 1, cb = st & 1;
      if constexpr ((st & 1) && st < 13) {
        if constexpr (kc == 0) WAITV(5);
        if constexpr (kc == 0) {} else if constexpr (kc == 1) WAITV(9); else if constexpr (kc <= 3) WAITV(6); else if constexpr (kc == 4) WAITV(4); else WAITV(0);
        BARN();
      }
      if constexpr (st == 2) pt_issue();
      constexpr int nb = (st + 1) & 1; constexpr bool more = st < 13;
      const bf16x8 a0 = cat(al[cb][0], ah[cb][0]), a1 = cat(al[cb][1], ah[cb][1]), a2 = cat(al[cb][2], ah[cb][2]);
      SCHED();
      MACC(acc[0][0], a0, xf[cb][0]); if constexpr (more) rdx(st + 1, nb, 0);
      MACC(acc[0][1], a0, xf[cb][1]); if constexpr (more) rdl(st + 1, nb, 0);
      MACC(asum, onesf, xf[cb][0]); if constexpr (more) rdh_(st + 1, nb, 0);
      MACC(acs[0], a0, onesf);
      SCHED();
      if constexpr (st < 2) { issue1(IC(st + 2), IC(0)); issue1(IC(st + 2), IC(1)); }
      else if constexpr (kc + 3 <= 6) issue1(IC((kc + 3) % 5), IC(2 * (st & 1)));
      SCHED();
      MACC(acc[1][0], a1, xf[cb][0]); if constexpr (more) rdx(st + 1, nb, 1);
      MACC(acc[1][1], a1, xf[cb][1]); if constexpr (more) rdl(st + 1, nb, 1);
      MACC(asq, xf[cb][0], xf[cb][0]); if constexpr (more) rdh_(st + 1, nb, 1);
      MACC(acs[1], a1, onesf);
      SCHED();
      if constexpr (st < 2) { issue1(IC(st + 2), IC(2)); issue1(IC(st + 2), IC(3)); }
      else if constexpr (kc + 3 <= 6) issue1(IC((kc + 3) % 5), IC(2 * (st & 1) + 1));
      SCHED();
      MACC(acc[2][0], a2, xf[cb][0]); if constexpr (more) rdl(st + 1, nb, 2);
      MACC(acc[2][1], a2, xf[cb][1]); if constexpr (more) rdh_(st + 1, nb, 2);
      MACC(acs[2], a2, onesf);
      SCHED();
    });
    NOPS(); KEEP(asum); KEEP(asq);
#pragma unroll
    for (int e = 0; e < 3; ++e) { KEEP(acc[e][0]); KEEP(acc[e][1]); KEEP(acs[e]); }
    FS(4);
    STAMP(1);
    epoch = (unsigned)rfl((int)*(LAS unsigned*)(lds + M_PF + 252)) + 1u;
    {
      const unsigned ptb = (ptm == 1) ? WT_W1 : WT_W2, ptn = (ptm == 1) ? 14u : 56u;
      ldsp src = lds + M_PT + (4 * g + q4) * 448 + 8 * p4;
#pragma unroll
      for (int i = 0; i < 2; ++i) {
        const int t = (w + 8 * i < 14) ? w + 8 * i : w;
        const bf16x8 f = cat(ldtr(src + t * 32), ldtr(src + 16 * 448 + t * 32));
        st128_sc1(lane16 + ptb + ((unsigned)(14 * ptG + t) * ptn + (unsigned)pts) * 1024u, p.ws + WS_WT, __builtin_bit_cast(u32x4, f));
      }
    }
    {
      const float d01 = (p4 & 1) ? asq[1] : asq[0], d23 = (p4 & 1) ? asq[3] : asq[2], dq = (p4 & 2) ? d23 : d01;
      float mean = asum[0] * (1.f / 448.f), dq2 = dq;
      KEEP(mean); KEEP(dq2);
      if (g == q4) *(LAS f32x2*)(lds + M_LN + (32 * tg + 16 * ng + l15) * 8) = f32x2{mean, rsqrtf(fmaxf(dq2 * (1.f / 448.f) - mean * mean, 0.f) + 1e-5f)};
    }
    BAR();
    FS(5);
    float mu[2], rs[2];
#pragma unroll
    for (int t = 0; t < 2; ++t) { const f32x2 st_ = *(LAS f32x2*)(lds + M_LN + (32 * tg + 16 * (t ^ ng) + l15) * 8); mu[t] = st_[0]; rs[t] = st_[1]; }
    const bool pad0 = (h & 1) && g == 0, pad1 = !(h & 1) && g == 3;
    const int rsw = ((l15 >> 1) & 3) << 4, rowb = (32 * tg + l15) * 64;
    auto epi = [&](auto NG) {
      constexpr int ngc = decltype(NG)::value;
      sfor<0, 3>([&](auto E) {
        constexpr int e = decltype(E)::value, nt = 3 * ngc + e, which = nt >> 1, par = nt & 1;
        constexpr int ibase = (which == 0) ? QIMG : (which == 1) ? KIMG + 16384 : VIMG + 16384;
        const int off = (which == 2) ? ((((2 * par + (g >> 1)) << 4) ^ rsw) + (g & 1) * 8) : (((g << 4) ^ rsw) + par * 8);
        const bool pad = par ? pad1 : pad0;
#pragma unroll
        for (int t = 0; t < 2; ++t) {
          const float sc = (which == 0) ? rs[t] * 0.27264657f : rs[t], nm = -mu[t] * sc;
          f32x2 sc2 = {sc, sc}, nm2 = {nm, nm};
          KEEP(sc2); KEEP(nm2);
          const f32x2 v0 = lo2(acc[e][t]) * sc2 + lo2(acs[e]) * nm2, v1 = hi2(acc[e][t]) * sc2 + hi2(acs[e]) * nm2;
          u32x2 o; o.x = cvtpk(v0); o.y = cvtpk(v1);
          o.x = pad ? ((which == 2) ? 0x00003f80u : 0u) : o.x; o.y = pad ? 0u : o.y;
          *(LAS u32x2*)(lds + ibase + (t ^ ngc) * 1024 + rowb + off) = o;
        }
      });
    };
    if (ng) epi(IC(1)); else epi(IC(0));
    if (ca < 2) {
      const u32x4 z = {0, 0, 0, 0};
#pragma unroll
      for (int i = 0; i < 2; ++i) { *(LAS u32x4*)(lds + KIMG + tid * 16 + i * 8192) = z; *(LAS u32x4*)(lds + VIMG + tid * 16 + i * 8192) = z; }
    }
    BAR();
    FS(6);
    {
      const char* blob = p.ws + WS_KV + (size_t)(ca * 16 + h) * 16384;
      const u32x4 v0 = *(LAS u32x4*)(lds + KIMG + 16384 + tid * 16), v1 = *(LAS u32x4*)(lds + VIMG + 16384 + tid * 16);
      st128_sc1(tid * 16, blob, v0); st128_sc1(8192 + tid * 16, blob, v1);
    }
    FS(7);
    WAITV(0);
    BAR();
    if (tid == 0) { st32_sc1(ctl + FLAG_IDX(0, ca, h), epoch); st32_sc1(ctl + FLAG_IDX(4, b >> 4, b & 15), epoch); }
    FS(8);
    STAMP(2);
  }
  const int ns = j & 3, col4 = 128 * ns - (((ns >> 1) & ns) << 5), ngr = 4 - (ns >> 1);
  const int pr = w & 3, wh = w >> 2;
  const int prc = (pr < ngr) ? pr : 0;
  const int th5 = j >> 3, cs5 = j & 7, col0 = 64 * cs5 - 16 * ((cs5 >> 2) * (cs5 & 3)), nblk = 4 - (cs5 >> 2);
  const int ng5 = w & 1, kq = w >> 1;
  const char* wt = p.ws + WS_WT;
  bf16x8 wr[32];
  const int T1 = (448 * (j >> 2) + col4) / 16;
  const unsigned w1o = WT_W1 + (unsigned)(T1 + 2 * prc) * 14u * 1024u;
  const unsigned wro = lane16 + w1o;
  constexpr int WI = 32768;
  unsigned wb0, wb1;
  { const int rin = lane >> 4, pos = lane & 15;
    int blk = (pos >> 1) ^ rin; if (blk >= 2 * ngr) blk -= 2; wb0 = (unsigned)(blk * 32 + (pos & 1) * 16);
    blk = (pos >> 1) ^ (4 + rin); if (blk >= 2 * ngr) blk -= 2; wb1 = (unsigned)(blk * 32 + (pos & 1) * 16); }
  const char* wob = p.wo + col4 * 2;
  {
    if (w < 6) {
      const unsigned rb = (unsigned)(((lane >> 4) + 4 * (24 + w)) * 896) + ((w & 1) ? wb1 : wb0);
      sfor<0, 14>([&](auto I) { constexpr int i = decltype(I)::value; dma<i * 6144, i * 24 * 896, false>(rb, wob, (unsigned)(WI + (24 + w) * 1024)); });
    }
    int pos = lane & 15; if (pos >= 4 * ngr) pos = 4 * ngr - 1;
    int xch = (lane & 15) ^ (((lane >> 4) + 4 * w) & 15); if (xch >= 4 * ngr) xch = 0;
    dma<M_XT, 0, false>((unsigned)(((lane >> 4) + 4 * w) * 896 + xch * 16), p.x + (size_t)(128 * c + 32 * (j >> 2)) * 896 + col4 * 2, (unsigned)(w * 1024));
    if (w == 5 && lane < 16) dma<M_B1, 0, false>((unsigned)(pos * 16), p.b1 + (448 * (j >> 2) + col4) * 2, 0u);
    if (w == 4 && lane < 8) dma<M_B2, 0, false>((unsigned)((lane < 2 * nblk ? lane : 0) * 16), p.b2 + col0 * 2, 0u);
  }
  {
    const int h = ha;
    FS(9);
    const int sw = (l15 >> 1) & 3;
    if (w == 7 && ca >= 1) poll_flags(ctl, (unsigned)(FLAG_IDX(0, (ca >= 2 && (lane & 1)) ? ca - 2 : ca - 1, h) * 4), epoch);
    if (w == 6) {
      const unsigned tfo = (unsigned)(FLAG_IDX(4, 4 * g, l15) * 4);
#pragma unroll 1
      for (unsigned spins = 0; spins < SPIN_CAP; ++spins) {
        unsigned t0, t1, t2, t3;
        asm volatile("global_load_dword %0, %4, %5 sc1\n\tglobal_load_dword %1, %4, %5 offset:2048 sc1\n\tglobal_load_dword %2, %6, %5 sc1\n\tglobal_load_dword %3, %6, %5 offset:2048 sc1\n\ts_waitcnt vmcnt(0)"
                     : "=&v"(t0), "=&v"(t1), "=&v"(t2), "=&v"(t3) : "v"(tfo), "s"(ctl), "v"(tfo + 4096u) : "memory");
        if (__all(t0 == epoch && t1 == epoch && t2 == epoch && t3 == epoch)) break;
        __builtin_amdgcn_s_sleep(2);
      }
      if (b == 0 && lane == 0) st32_sc1(ctl, epoch);
    }
    BAR();
    FS(10);
    {
      const int tl = w >> 2, src = ca - 2 + tl, pc = (w & 3) * 2048;
      if (src >= 0) {
        const char* blob = p.ws + WS_KV + (size_t)(src * 16 + h) * 16384 + pc;
        dma2c_sc1<KIMG>(lane16, blob, (unsigned)(tl * 8192 + pc)); dma2c_sc1<VIMG>(lane16, blob + 8192, (unsigned)(tl * 8192 + pc));
      }
    }
    const bf16x8 qf = ld128(lds + QIMG + (16 * w + l15) * 64 + ((g ^ sw) << 4));
    WAITV(0);
    BAR();
    FS(11);
    STAMP(3);
    STAMP(4);
    if (w >= 4) ldfrags<7>(wr, wro, wt);
    f32x4 sc[17];
    const float NEG = -__builtin_inff();
    ldsp kb = lds + KIMG + (16 * w + l15) * 64 + ((g ^ sw) << 4);
    {
      bf16x8 kf[2][6];
#pragma unroll
      for (int u = 0; u < 6; ++u) kf[0][u] = ld128(kb + u * 1024);
#pragma unroll
      for (int bt = 0; bt < 3; ++bt) {
        if (bt < 2) {
#pragma unroll
          for (int u = 0; u < 6; ++u) if (6 * (bt + 1) + u < 17) kf[(bt + 1) & 1][u] = ld128(kb + (6 * (bt + 1) + u) * 1024);
        }
        SCHED();
#pragma unroll
        for (int u = 0; u < 6; ++u) if (6 * bt + u < 17) sc[6 * bt + u] = MFMA(kf[bt & 1][u], qf, (f32x4{0, 0, 0, 0}));
        SCHED();
      }
    }
    FS(12);
    if (w >= 4) ldfrags<7>(wr + 7, wro + 7 * 1024, wt);
    const int vsw = (2 * g + (q4 >> 1)) & 3;
    ldsp vb0 = lds + VIMG + (16 * w + 4 * g + q4) * 64 + (((p4 >> 1) ^ vsw) << 4) + (p4 & 1) * 8;
    ldsp vb1 = lds + VIMG + (16 * w + 4 * g + q4) * 64 + (((2 + (p4 >> 1)) ^ vsw) << 4) + (p4 & 1) * 8;
    bf16x8 vf[2][3][2];
    auto rdv = [&](int bt) {
#pragma unroll
      for (int i = 0; i < 3; ++i) { const int uu = 3 * bt + i, ua = 2 * uu, ub = (uu < 8) ? 2 * uu + 1 : 15;
        vf[bt & 1][i][0] = cat(ldtr(vb0 + ua * 1024), ldtr(vb0 + ub * 1024)); vf[bt & 1][i][1] = cat(ldtr(vb1 + ua * 1024), ldtr(vb1 + ub * 1024)); }
    };
    rdv(0);
    SCHED();
    if (ca < 2) {
      const int tmin = (2 - ca) * 8;
#pragma unroll
      for (int u = 0; u < 16; ++u) if (w + u < tmin) sc[u] = f32x4{NEG, NEG, NEG, NEG};
    }
#pragma unroll
    for (int r = 0; r < 4; ++r) { sc[0][r] = (4 * g + r <= l15) ? NEG : sc[0][r]; sc[16][r] = (4 * g + r > l15) ? NEG : sc[16][r]; }
    float m = max3(sc[0][0], sc[0][1], sc[0][2]), m2 = max3(sc[0][3], sc[1][0], sc[1][1]);
    m = max3(m, sc[1][2], sc[1][3]);
#pragma unroll
    for (int u = 2; u < 17; ++u) { if (u & 1) m = max3(max3(m, sc[u][0], sc[u][1]), sc[u][2], sc[u][3]); else m2 = max3(max3(m2, sc[u][0], sc[u][1]), sc[u][2], sc[u][3]); }
    m = max3(m, m2, m2);
    m = red_g_max(m);
    FS(13);
    SCHED();
    if (w >= 4) ldfrags<7>(wr + 14, wro + 14 * 1024, wt);
    unsigned pk[17][2];
    f32x2 mm = {-m, -m};
    KEEP(mm);
    f32x4 oa[2] = {f32x4{0, 0, 0, 0}, f32x4{0, 0, 0, 0}};
    KEEP(oa[0]); KEEP(oa[1]);
#pragma unroll
    for (int bt = 0; bt < 3; ++bt) {
#pragma unroll
      for (int u = 6 * bt; u < 6 * bt + 6 && u < 17; ++u) {
        const f32x2 d0 = lo2(sc[u]) + mm, d1 = hi2(sc[u]) + mm;
        pk[u][0] = cvtpk(__builtin_amdgcn_exp2f(d0[0]), __builtin_amdgcn_exp2f(d0[1]));
        pk[u][1] = cvtpk(__builtin_amdgcn_exp2f(d1[0]), __builtin_amdgcn_exp2f(d1[1]));
      }
      if (bt < 2) rdv(bt + 1);
      if (bt == 0 && w >= 4) ldfrags<7>(wr + 21, wro + 21 * 1024, wt);
      SCHED();
#pragma unroll
      for (int i = 0; i < 3; ++i) {
        const int uu = 3 * bt + i;
        u32x4 pu; pu.x = pk[2 * uu][0]; pu.y = pk[2 * uu][1];
        if (uu < 8) { pu.z = pk[2 * uu + 1][0]; pu.w = pk[2 * uu + 1][1]; } else { pu.z = 0; pu.w = 0; }
        const bf16x8 pf = __builtin_bit_cast(bf16x8, pu);
        MACCN(oa[0], vf[bt & 1][i][0], pf); MACC(oa[1], vf[bt & 1][i][1], pf);
      }
      SCHED();
    }
    NOPS(); KEEP(oa[0]); KEEP(oa[1]);
    FS(15);
    int tz2 = tid; asm volatile("" : "+v"(tz2));
    const int l15b = tz2 & 15, gb = (tz2 & 63) >> 4, swb = (l15b >> 1) & 3;
    const float lsel = (h & 1) ? oa[0][0] : oa[1][0];
    const float l = __uint_as_float((unsigned)__builtin_amdgcn_ds_bpermute((((h & 1) ? 0 : 48) + l15b) * 4, (int)__float_as_uint(lsel)));
    const float inv1 = __builtin_amdgcn_rcpf(l);
    f32x2 inv = {inv1, inv1};
    KEEP(inv);
    u32x4 ov;
    ov.x = cvtpk(lo2(oa[0]) * inv); ov.y = cvtpk(hi2(oa[0]) * inv); ov.z = cvtpk(lo2(oa[1]) * inv); ov.w = cvtpk(hi2(oa[1]) * inv);
    { const bool z0 = (h & 1) && gb == 0, z1 = !(h & 1) && gb == 3; ov.x = z0 ? 0u : ov.x; ov.y = z0 ? 0u : ov.y; ov.z = z1 ? 0u : ov.z; ov.w = z1 ? 0u : ov.w; }
    st128_sc1((unsigned)((16 * w + l15b) * 64 + ((gb ^ swb) << 4)), p.ws + WS_O + (size_t)(ca * 16 + h) * 8192, ov);
    FS(16);
    BAR();
    {
      const unsigned rb = (unsigned)(((lane >> 4) + 12 * w) * 896), va = rb + ((w & 1) ? wb1 : wb0), vb = rb + ((w & 1) ? wb0 : wb1), lb = (unsigned)(WI + 3 * w * 1024);
      dma<0, 0, false>(va, wob, lb); dma<1024, 4 * 896, false>(vb, wob, lb); dma<2048, 8 * 896, false>(va, wob, lb);
      const int ins = (w < 4) ? 108 + w : 3 * w + 2;
      dma<0, 0, false>((unsigned)(((lane >> 4) + 4 * ins) * 896) + ((ins & 1) ? wb1 : wb0), wob, (unsigned)(WI + ins * 1024));
    }
    WAITV(4);
    BAR();
    if (tid == 0) st32_sc1(ctl + FLAG_IDX(1, ca, h), epoch);
    FS(18);
    STAMP(5);
  }
  {
    LANEVARS();
    constexpr int OT = 0;
    const int tq = j >> 2;
    WAITV(0);
    BAR();
    if (w < 4) {
      const int rx = 4 * (g & 1) + q4;
      ldsp ab0 = lds + WI + (4 * g + q4) * 256 + (((2 * prc) ^ rx) << 5) + 8 * p4;
      ldsp ab1 = lds + WI + (4 * g + q4) * 256 + (((2 * prc + 1) ^ rx) << 5) + 8 * p4;
      ldsp ab0h = ab0 + 65536, ab1h = ab1 + 65536;
#pragma unroll
      for (int s = 0; s < 16; ++s) {
        const int rb = (28 * s - 4 * (s & 1)) * 256;
        if (rb + 4096 < 65536) { wr[s] = cat(ldtr(ab0 + rb), ldtr(ab0 + rb + 4096)); wr[16 + s] = cat(ldtr(ab1 + rb), ldtr(ab1 + rb + 4096)); }
        else { wr[s] = cat(ldtr(ab0h + (rb - 65536)), ldtr(ab0h + (rb - 65536 + 4096))); wr[16 + s] = cat(ldtr(ab1h + (rb - 65536)), ldtr(ab1h + (rb - 65536 + 4096))); }
      }
    } else {
      __builtin_amdgcn_s_setprio(3);
      poll_flags(ctl, (unsigned)(FLAG_IDX(1, c, l15) * 4), epoch);
      const char* ob_ = p.ws + WS_O + (size_t)(c * 16 + 4 * (w - 4)) * 8192 + tq * 2048;
      const unsigned lb = (unsigned)(OT + (w - 4) * 8192);
      dma2c_sc1<0>(lane16, ob_, lb); dma2c_sc1<2048>(lane16 + 8192, ob_, lb); dma2c_sc1<4096>(lane16 + 16384, ob_, lb); dma2c_sc1<6144>(lane16 + 24576, ob_, lb);
      __builtin_amdgcn_s_setprio(0);
    }
    FS(19);
    WAITV(0);
    BAR();
    FS(20);
    STAMP(6);
    STAMP(7);
    if (w < 4) {
      f32x4 a0[2], a1[2];
#pragma unroll
      for (int t = 0; t < 2; ++t) { a0[t] = f32x4{0, 0, 0, 0}; a1[t] = f32x4{0, 0, 0, 0}; }
      ldsp ob = lds + OT + l15 * 64 + ((g ^ ((l15 >> 1) & 3)) << 4);
      u32x2 xr[2][2];
#pragma unroll
      for (int t = 0; t < 2; ++t) { ldsp xp = lds + M_XT + (16 * t + l15) * 256 + (g & 1) * 8;
        xr[t][0] = *(LAS u32x2*)(xp + (((4 * prc + (g >> 1)) ^ l15) << 4)); xr[t][1] = *(LAS u32x2*)(xp + (((4 * prc + 2 + (g >> 1)) ^ l15) << 4)); }
#pragma unroll
      for (int t = 0; t < 2; ++t) { KEEP(a0[t]); KEEP(a1[t]); }
      bf16x8 of[4][2];
#pragma unroll
      for (int s = 0; s < 3; ++s) { of[s][0] = ld128(ob + s * 2048); of[s][1] = ld128(ob + s * 2048 + 1024); }
#pragma unroll
      for (int s = 0; s < 16; ++s) {
        if (s + 3 < 16) { of[(s + 3) & 3][0] = ld128(ob + (s + 3) * 2048); of[(s + 3) & 3][1] = ld128(ob + (s + 3) * 2048 + 1024); }
        SCHED();
#pragma unroll
        for (int t = 0; t < 2; ++t) { MACC(a0[t], wr[s], of[s & 3][t]); MACC(a1[t], wr[16 + s], of[s & 3][t]); }
        SCHED();
      }
      NOPS();
#pragma unroll
      for (int t = 0; t < 2; ++t) { KEEP(a0[t]); KEEP(a1[t]); }
      FS(21);
      const unsigned ho = (unsigned)((128 * c + 32 * tq + l15) * 896 + (col4 / 32 + pr) * 64 + g * 16);
#pragma unroll
      for (int t = 0; t < 2; ++t) {
        u32x4 hv;
        hv.x = cvtpk(lo2(a0[t]) + bf2(xr[t][0].x)); hv.y = cvtpk(hi2(a0[t]) + bf2(xr[t][0].y));
        hv.z = cvtpk(lo2(a1[t]) + bf2(xr[t][1].x)); hv.w = cvtpk(hi2(a1[t]) + bf2(xr[t][1].y));
        if (pr < ngr) st128_sc1(ho + t * 16 * 896, p.ws + WS_H, hv);
        float s1 = 0.f, s2 = 0.f;
#pragma unroll
        for (int i = 0; i < 4; ++i) { s1 = dot2(hv[i], ONES, s1); s2 = dot2(hv[i], hv[i], s2); }
        s1 = red_g_sum(s1); s2 = red_g_sum(s2);
        if (pr >= ngr) { s1 = 0.f; s2 = 0.f; }
        if (g == 0) ((LAS f32x2*)(lds + M_PART))[(16 * t + l15) * 4 + pr] = f32x2{s1, s2};
      }
    } else {
      pin<28>(wr);
#pragma unroll
      for (int i = 0; i < 28; ++i) *(LAS bf16x8*)(lds + 32768 + (w - 4) * 28672 + i * 1024 + lane * 16) = wr[i];
    }
    FS(22);
    BAR();
    if (tid < 32) {
      const f32x4 u0 = *(LAS f32x4*)(lds + M_PART + tid * 32), u1 = *(LAS f32x4*)(lds + M_PART + tid * 32 + 16);
      st64_sc1((unsigned)((128 * c + 32 * tq + tid) * 32 + ns * 8), p.ws + WS_ST,
               u32x2{__float_as_uint((u0[0] + u0[2]) + (u1[0] + u1[2])), __float_as_uint((u0[1] + u0[3]) + (u1[1] + u1[3]))});
    }
    if (w < 4) {
#pragma unroll
      for (int i = 0; i < 28; ++i) wr[i] = ld128(lds + 32768 + w * 28672 + i * 1024 + lane * 16);
    }
    FS(23);
    if (w < 4) WAITV(0);
    BAR();
    if (tid == 0) st32_sc1(ctl + FLAG_IDX(2, c, j), epoch);
    FS(25);
    STAMP(8);
  }
  {
    LANEVARS();
    const int f0_ = 448 * (j >> 2) + col4, th = wh;
    FS(26);
    f32x4 c0 = {0, 0, 0, 0}, c1 = {0, 0, 0, 0};
    pin<28>(wr);
    bf16x8 onesf = __builtin_bit_cast(bf16x8, (u32x4{ONES, ONES, ONES, ONES}));
    KEEP(onesf); KEEP(c0); KEEP(c1);
    asm volatile("s_nop 1");
#pragma unroll
    for (int s = 0; s < 14; ++s) { MACC(c0, wr[s], onesf); MACC(c1, wr[14 + s], onesf); }
    NOPS(); KEEP(c0); KEEP(c1);
    SCHED();
    if (w == 7) poll_flags(ctl, (unsigned)(FLAG_IDX(2, c, l15) * 4), epoch);
    FS(29);
    BAR();
    const unsigned vo = (unsigned)((16 * w + (lane >> 3)) * 896) + x8, wl2k = (unsigned)(w * 2048);
    const char* hb = p.ws + WS_H + (size_t)c * 128 * 896;
    auto issue4 = [&](auto KC) { constexpr int kc = decltype(KC)::value; dma<kc * 16384, kc * 128, true>(vo, hb, wl2k); dma<kc * 16384 + 1024, kc * 128 + 8 * 896, true>(vo, hb, wl2k); };
    if (lane < 32) dma<M_TILE, 0, true>(lane16, p.ws + WS_ST + (size_t)(128 * c + 16 * w) * 32, (unsigned)(w * 512));
    issue4(IC(0)); RAWBAR(); issue4(IC(1));
    FS(31);
    f32x4 acc[2][4];
#pragma unroll
    for (int t = 0; t < 4; ++t) { acc[0][t] = f32x4{0, 0, 0, 0}; acc[1][t] = f32x4{0, 0, 0, 0}; }
    const int xb = (64 * th + l15) * 128 + ((g ^ (l15 & 7)) << 4);
    bf16x8 xf[2][4];
    auto rd1 = [&](int st, int buf, int t) { xf[buf][t] = ld128(lds + (st >> 1) * 16384 + (xb ^ ((st & 1) << 6)) + t * 2048); };
    auto rdf = [&](int st, int buf) { rd1(st, buf, 0); rd1(st, buf, 1); rd1(st, buf, 2); rd1(st, buf, 3); };
#pragma unroll
    for (int t = 0; t < 4; ++t) { KEEP(acc[0][t]); KEEP(acc[1][t]); }
    bf16x8 bf[2][14];
    const unsigned w2o0 = WT_W2 + ((unsigned)(col0 / 16 + 2 * ng5) * 56u + 14u * kq) * 1024u, w2o1 = (2 * ng5 + 1 < nblk) ? w2o0 + 56u * 1024u : w2o0;
    WAITV(2); BAR();
    FS(32);
    STAMP(9);
    STAMP(10);
    rdf(0, 0);
    sfor<0, 14>([&](auto ST) {
      constexpr int st = decltype(ST)::value;
      if constexpr (st == 1) WAITV(2); if constexpr (st == 3) WAITV(4); if constexpr (st == 5) WAITV(6); if constexpr (st == 7) WAITV(8); if constexpr (st == 9) WAITV(10); if constexpr (st == 11) WAITV(12);
      if constexpr ((st & 1) && st < 13) BARN();
      if constexpr (st == 2) {
        if (tid < 128) {
          const f32x4 u0 = *(LAS f32x4*)(lds + M_TILE + tid * 32), u1 = *(LAS f32x4*)(lds + M_TILE + tid * 32 + 16);
          float mean = ((u0[0] + u0[2]) + (u1[0] + u1[2])) * (1.f / 448.f), sq = (u0[1] + u0[3]) + (u1[1] + u1[3]);
          KEEP(mean); KEEP(sq);
          const float rstd = rsqrtf(fmaxf(sq * (1.f / 448.f) - mean * mean, 0.f) + 1e-5f);
          *(LAS f32x2*)(lds + M_LN + tid * 8) = f32x2{rstd, -mean * rstd};
        }
      }
      SCHED();
#pragma unroll
      for (int t = 0; t < 2; ++t) { MACC(acc[0][t], wr[st], xf[st & 1][t]); if constexpr (st < 13) rd1(st + 1, (st + 1) & 1, t); MACC(acc[1][t], wr[14 + st], xf[st & 1][t]); }
      SCHED();
      if constexpr (st <= 4) issue4(IC(st + 2));
      if constexpr (st >= 5) { constexpr int s = st - 5; ldfrags<1>(&bf[0][s], lane16 + w2o0 + s * 1024, wt); ldfrags<1>(&bf[1][s], lane16 + w2o1 + s * 1024, wt); }
      SCHED();
#pragma unroll
      for (int t = 2; t < 4; ++t) { MACC(acc[0][t], wr[st], xf[st & 1][t]); if constexpr (st < 13) rd1(st + 1, (st + 1) & 1, t); MACC(acc[1][t], wr[14 + st], xf[st & 1][t]); }
      SCHED();
    });
    NOPS();
#pragma unroll
    for (int t = 0; t < 4; ++t) { KEEP(acc[0][t]); KEEP(acc[1][t]); }
    FS(33);
    STAMP(11);
    {
      const u32x2 bb0 = *(LAS u32x2*)(lds + M_B1 + (32 * prc + 4 * g) * 2), bb1 = *(LAS u32x2*)(lds + M_B1 + (32 * prc + 4 * g) * 2 + 32);
      const f32x2 b00 = bf2(bb0.x), b01 = bf2(bb0.y), b10 = bf2(bb1.x), b11 = bf2(bb1.y);
      const unsigned ho = (unsigned)((128 * c + 64 * th + l15) * 3584 + (f0_ / 32 + pr) * 64 + g * 16);
      f32x2 ln[4];
#pragma unroll
      for (int t = 0; t < 4; ++t) ln[t] = *(LAS f32x2*)(lds + M_LN + (64 * th + 16 * t + l15) * 8);
#pragma unroll
      for (int t = 0; t < 4; ++t) {
        const float a = ln[t][0], nm = ln[t][1];
        f32x2 aa = {a, a}, nn = {nm, nm};
        KEEP(aa); KEEP(nn);
        f32x2 z[4] = {lo2(acc[0][t]) * aa + (lo2(c0) * nn + b00), hi2(acc[0][t]) * aa + (hi2(c0) * nn + b01), lo2(acc[1][t]) * aa + (lo2(c1) * nn + b10), hi2(acc[1][t]) * aa + (hi2(c1) * nn + b11)};
        u32x4 hv;
#pragma unroll
        for (int i = 0; i < 4; ++i) {
          f32x2 r = {__builtin_elementwise_maximum(z[i][0], 0.f), __builtin_elementwise_maximum(z[i][1], 0.f)};
          hv[i] = cvtpk(r * r);
        }
        if (pr < ngr) st128_sc1(ho + t * 16 * 3584, p.ws + WS_H1, hv);
      }
    }
    FS(34);
    WAITV(0);
    BAR();
    if (tid == 0) st32_sc1(ctl + FLAG_IDX(3, c, j), epoch);
    FS(38);
    STAMP(12);
    if (w != 7) {
      ldfrags<5>(&bf[0][9], lane16 + w2o0 + 9 * 1024, wt); ldfrags<5>(&bf[1][9], lane16 + w2o1 + 9 * 1024, wt);
    }
    if (w >= 5 && w < 7) {
      const int m0_ = col0 >> 5;
      const char* hr = p.ws + WS_H + (size_t)(128 * c + 64 * th5) * 896;
      sfor<0, 6>([&](auto I) { constexpr int i = decltype(I)::value;
        const unsigned u = (unsigned)((w - 5) * 384 + i * 64 + lane), r0 = (u * 43691u) >> 19, c0_ = u - 12u * r0; unsigned cc0 = m0_ * 4 + c0_; if (cc0 > 55) cc0 = 55;
        dma<M_TILE + i * 1024, 0, true>(r0 * 896 + cc0 * 16, hr, (unsigned)((w - 5) * 6144)); });
    }
    const unsigned vo5 = (unsigned)((32 * ng5 + (lane >> 3)) * 3584 + kq * 896) + x8, wl4k = (unsigned)(w * 4096);
    const char* hb5 = p.ws + WS_H1 + (size_t)(128 * c + 64 * th5) * 3584;
    auto issue5 = [&](auto I) {
      constexpr int i = decltype(I)::value, sa = (i & 3) * 32768;
      sfor<0, 4>([&](auto II) { constexpr int ii = decltype(II)::value; dma<sa + ii * 1024, i * 128 + ii * 8 * 3584, true>(vo5, hb5, wl4k); });
    };
    FS(39);
    if (w == 7) {
      poll_flags(ctl, (unsigned)(FLAG_IDX(3, c, l15) * 4), epoch);
    }
    FS(40);
    BAR();
    FS(41);
    STAMP(13);
    issue5(IC(0));
    if (w == 7) {
      ldfrags<5>(&bf[0][9], lane16 + w2o0 + 9 * 1024, wt); ldfrags<5>(&bf[1][9], lane16 + w2o1 + 9 * 1024, wt);
    }
    FS(42);
    f32x4 ac5[2][4];
#pragma unroll
    for (int t = 0; t < 4; ++t) { ac5[0][t] = f32x4{0, 0, 0, 0}; ac5[1][t] = f32x4{0, 0, 0, 0}; }
    int yb[4];
#pragma unroll
    for (int t = 0; t < 4; ++t) yb[t] = kq * 8192 + (16 * ((t + kq) & 3) + l15) * 128 + ((g ^ (l15 & 7)) << 4);
    bf16x8 yf[2][4];
    auto rz1 = [&](int st, int buf, int t) { yf[buf][t] = ld128(lds + ((st >> 1) & 3) * 32768 + (yb[t] ^ ((st & 1) << 6))); };
    auto rdy = [&](int st, int buf) { rz1(st, buf, 0); rz1(st, buf, 1); rz1(st, buf, 2); rz1(st, buf, 3); };
#pragma unroll
    for (int t = 0; t < 4; ++t) { KEEP(ac5[0][t]); KEEP(ac5[1][t]); }
    WAITV(0);
    pin<14>(bf[0]); pin<14>(bf[1]);
    BAR();
    FS(43);
    STAMP(14);
    rdy(0, 0);
    sfor<0, 14>([&](auto ST) {
      constexpr int st = decltype(ST)::value;
      if constexpr ((st & 1) && st < 13) {
        if constexpr (st < 11) WAITV(4); else WAITV(0);
        BARN();
      }
      SCHED();
#pragma unroll
      for (int t = 0; t < 2; ++t) { MACC(ac5[0][t], bf[0][st], yf[st & 1][t]); if constexpr (st < 13) rz1(st + 1, (st + 1) & 1, t); MACC(ac5[1][t], bf[1][st], yf[st & 1][t]); }
      SCHED();
      if constexpr (st == 0) { issue5(IC(1)); issue5(IC(2)); }
      if constexpr ((st & 1) && st <= 7) issue5(IC((st + 5) / 2));
      SCHED();
#pragma unroll
      for (int t = 2; t < 4; ++t) { MACC(ac5[0][t], bf[0][st], yf[st & 1][t]); if constexpr (st < 13) rz1(st + 1, (st + 1) & 1, t); MACC(ac5[1][t], bf[1][st], yf[st & 1][t]); }
      SCHED();
    });
    NOPS();
#pragma unroll
    for (int t = 0; t < 4; ++t) { KEEP(ac5[0][t]); KEEP(ac5[1][t]); }
    FS(44);
    BAR();
#pragma unroll
    for (int t = 1; t < 4; ++t) {
      ldsp d = lds + ((((t + kq) & 3) * 3 + (t - 1)) * 2 + ng5) * 2048 + lane * 16;
      *(LAS f32x4*)d = ac5[0][t]; *(LAS f32x4*)(d + 1024) = ac5[1][t];
    }
    const int tokl = 16 * kq + l15;
    const int t0 = col0 / 16 + 2 * ng5, t1 = (2 * ng5 + 1 < nblk) ? t0 + 1 : t0, m0_ = col0 >> 5;
    const u32x2 hr0 = *(LAS u32x2*)(lds + M_TILE + tokl * 192 + ((t0 >> 1) - m0_) * 64 + g * 16 + (t0 & 1) * 8);
    const u32x2 hr1 = *(LAS u32x2*)(lds + M_TILE + tokl * 192 + ((t1 >> 1) - m0_) * 64 + g * 16 + (t1 & 1) * 8);
    const u32x2 b20 = *(LAS u32x2*)(lds + M_B2 + (32 * ng5 + 4 * g) * 2), b21 = *(LAS u32x2*)(lds + M_B2 + ((2 * ng5 + 1 < nblk) ? (32 * ng5 + 16) : 32 * ng5) * 2 + 8 * g);
    f32x2 r00 = lo2(ac5[0][0]) + (bf2(b20.x) + bf2(hr0.x)), r01 = hi2(ac5[0][0]) + (bf2(b20.y) + bf2(hr0.y));
    f32x2 r10 = lo2(ac5[1][0]) + (bf2(b21.x) + bf2(hr1.x)), r11 = hi2(ac5[1][0]) + (bf2(b21.y) + bf2(hr1.y));
    BAR();
    FS(45);
#pragma unroll
    for (int t = 1; t < 4; ++t) {
      ldsp d = lds + ((kq * 3 + (t - 1)) * 2 + ng5) * 2048 + lane * 16;
      const f32x4 u0 = *(LAS f32x4*)d, u1 = *(LAS f32x4*)(d + 1024);
      r00 += lo2(u0); r01 += hi2(u0); r10 += lo2(u1); r11 += hi2(u1);
    }
    GAS char* op = (GAS char*)p.out + (size_t)(128 * c + 64 * th5 + tokl) * 896 + (col0 + 32 * ng5 + 4 * g) * 2;
    *(GAS u32x2*)op = u32x2{cvtpk(r00), cvtpk(r01)};
    if (2 * ng5 + 1 < nblk) *(GAS u32x2*)(op + 32) = u32x2{cvtpk(r10), cvtpk(r11)};
    STAMP(15);
    FS(63);
  }
}

extern "C" void kernel_launch(void* const* d_in, const int* in_sizes, int n_in, void* d_out, int out_size, void* d_ws, size_t ws_size, hipStream_t stream) {
  Params p;
  p.x = (const char*)d_in[0]; p.wqkv = (const char*)d_in[1]; p.wo = (const char*)d_in[2];
  p.w1 = (const char*)d_in[4]; p.b1 = (const char*)d_in[5]; p.w2 = (const char*)d_in[6]; p.b2 = (const char*)d_in[7];
  p.out = (char*)d_out; p.ws = (char*)d_ws;
  (void)hipFuncSetAttribute((const void*)fused_block, hipFuncAttributeMaxDynamicSharedMemorySize, LDS_BYTES);
  hipLaunchKernelGGL(fused_block, dim3(256), dim3(512), LDS_BYTES, stream, p);
}
```

```cpp
#define NP 3
#define MAPA
#define E1_CODE
#include <hip/hip_runtime.h>
#include <hip/hip_bf16.h>
#include <stdint.h>
#include <type_traits>

#define LAS __attribute__((address_space(3)))
#define GAS __attribute__((address_space(1)))
typedef short bf16x8 __attribute__((ext_vector_type(8)));
typedef short s16x4 __attribute__((ext_vector_type(4)));
typedef short v4i16_t __attribute__((ext_vector_type(4)));
typedef float f32x4 __attribute__((ext_vector_type(4)));
typedef float f32x2 __attribute__((ext_vector_type(2)));
typedef unsigned u32x4 __attribute__((ext_vector_type(4)));
typedef unsigned u32x2 __attribute__((ext_vector_type(2)));
typedef __bf16 bf16x2_t __attribute__((ext_vector_type(2)));
typedef LAS char* ldsp;

constexpr int SEQ = 2048;
constexpr size_t WS_CTL = 0;
constexpr size_t WS_KV = 262144;
constexpr size_t WS_O = WS_KV + 256 * 16384;
constexpr size_t WS_H = WS_O + 256 * 8192;
constexpr size_t WS_H1 = WS_H + (size_t)SEQ * 896;
constexpr size_t WS_ST = WS_H1 + (size_t)SEQ * 3584;
constexpr size_t WS_WT = WS_ST + (size_t)SEQ * 32;
constexpr unsigned WT_W1 = 0, WT_W2 = WT_W1 + 1568 * 1024, WT_BYTES = WT_W2 + 1568 * 1024;
constexpr size_t WS_DBG = WS_WT + WT_BYTES;
constexpr int LDS_BYTES = 160 * 1024;
constexpr int M_PT = 143360;
constexpr int M_XT = 147456;
constexpr int M_TILE = 131072;
constexpr int MISC = 157696;
constexpr int M_LN = MISC, M_B1 = MISC + 1024, M_B2 = MISC + 1280, M_PF = MISC + 1536, M_PART = MISC + 2048, M_FILL = MISC + 3072;
constexpr unsigned SPIN_CAP = 1u << 16;

#define FLAG_IDX(ph, c, j) (64 + (((ph) * 16 + (c)) * 16 + (j)) * 32)

#define WAITV(n) asm volatile("s_waitcnt vmcnt(" #n ")" ::: "memory")
template <int N> __device__ __forceinline__ void waitv() { asm volatile("s_waitcnt vmcnt(%0)" ::"i"(N) : "memory"); }
#define BAR() do { asm volatile("s_waitcnt lgkmcnt(0)" ::: "memory"); __builtin_amdgcn_s_barrier(); asm volatile("" ::: "memory"); } while (0)
#define BARN() do { asm volatile("" ::: "memory"); __builtin_amdgcn_s_barrier(); asm volatile("" ::: "memory"); } while (0)
#define SCHED() __builtin_amdgcn_sched_barrier(0)
#define RAWBAR() __builtin_amdgcn_s_barrier()
#define ILV(n) do { _Pragma("unroll") for (int i_ = 0; i_ < (n); ++i_) { __builtin_amdgcn_sched_group_barrier(0x008, 1, 0); __builtin_amdgcn_sched_group_barrier(0x100, 1, 0); } } while (0)
#define MFMA(a, b, c) __builtin_amdgcn_mfma_f32_16x16x32_bf16((a), (b), (c), 0, 0, 0)
#define MACC(acc, a, b) asm volatile("v_mfma_f32_16x16x32_bf16 %0, %1, %2, %0" : "+v"(acc) : "v"(a), "v"(b))
#define MACCN(acc, a, b) asm volatile("s_nop 1\n\tv_mfma_f32_16x16x32_bf16 %0, %1, %2, %0" : "+v"(acc) : "v"(a), "v"(b))
#define NOPS() asm volatile("s_nop 7\n\ts_nop 3")
#define KEEP(x) asm volatile("" : "+v"(x))
#define IC(x) std::integral_constant<int, (x)>{}

template <int I, int N, class F> __device__ __forceinline__ void sfor(F&& f) { if constexpr (I < N) { f(std::integral_constant<int, I>{}); sfor<I + 1, N>(f); } }

__device__ __forceinline__ unsigned cvtpk(float lo, float hi) { f32x2 v = {lo, hi}; bf16x2_t b = __builtin_convertvector(v, bf16x2_t); return __builtin_bit_cast(unsigned, b); }
__device__ __forceinline__ unsigned cvtpk(f32x2 v) { bf16x2_t b = __builtin_convertvector(v, bf16x2_t); return __builtin_bit_cast(unsigned, b); }
__device__ __forceinline__ float dot2(unsigned a, unsigned b, float c) { return __builtin_amdgcn_fdot2_f32_bf16(__builtin_bit_cast(bf16x2_t, a), __builtin_bit_cast(bf16x2_t, b), c, false); }
__device__ __forceinline__ float bflo(unsigned u) { return __uint_as_float(u << 16); }
__device__ __forceinline__ float bfhi(unsigned u) { return __uint_as_float(u & 0xffff0000u); }
__device__ __forceinline__ f32x2 bf2(unsigned u) { return f32x2{bflo(u), bfhi(u)}; }
__device__ __forceinline__ f32x2 lo2(f32x4 v) { return f32x2{v[0], v[1]}; }
__device__ __forceinline__ f32x2 hi2(f32x4 v) { return f32x2{v[2], v[3]}; }
__device__ __forceinline__ bf16x8 ld128(ldsp p) { return *(const LAS bf16x8*)p; }
__device__ __forceinline__ s16x4 ldtr(ldsp p) { return __builtin_bit_cast(s16x4, __builtin_amdgcn_ds_read_tr16_b64_v4i16((LAS v4i16_t*)p)); }
__device__ __forceinline__ bf16x8 cat(s16x4 lo, s16x4 hi) { return __builtin_shufflevector(lo, hi, 0, 1, 2, 3, 4, 5, 6, 7); }
__device__ __forceinline__ int rfl(int x) { return __builtin_amdgcn_readfirstlane(x); }
template <int LADD, int VADD, bool SC1>
__device__ __forceinline__ void dma(unsigned voff, const void* sbase, unsigned lbase) {
  const unsigned va = voff + (unsigned)VADD;
  if constexpr (SC1) asm volatile("s_add_i32 m0, %2, %3\n\ts_nop 0\n\tglobal_load_lds_dwordx4 %0, %1 sc1" ::"v"(va), "s"(sbase), "s"(lbase), "i"(LADD) : "memory", "scc");
  else asm volatile("s_add_i32 m0, %2, %3\n\ts_nop 0\n\tglobal_load_lds_dwordx4 %0, %1" ::"v"(va), "s"(sbase), "s"(lbase), "i"(LADD) : "memory", "scc");
}
template <int LADD>
__device__ __forceinline__ void dma2c_sc1(unsigned voff, const void* sbase, unsigned lbase) {
  asm volatile("s_add_i32 m0, %2, %3\n\ts_nop 0\n\tglobal_load_lds_dwordx4 %0, %1 sc1\n\tglobal_load_lds_dwordx4 %0, %1 offset:1024 sc1" ::"v"(voff), "s"(sbase), "s"(lbase), "i"(LADD) : "memory", "scc");
}
template <int OFF>
__device__ __forceinline__ void ldfrag1(bf16x8& f, unsigned voff, const void* sbase) {
  asm volatile("global_load_dwordx4 %0, %1, %2 offset:%3 sc1" : "=v"(f) : "v"(voff), "s"(sbase), "i"(OFF) : "memory");
}
__device__ __forceinline__ void pin1(bf16x8& f) { asm volatile("" : "+v"(f)); }
template <int N>
__device__ __forceinline__ void ldfrags(bf16x8* f, unsigned voff, const void* sbase) {
  sfor<0, N>([&](auto I) { constexpr int i = decltype(I)::value; ldfrag1<(i & 3) * 1024>(f[i], voff + (unsigned)((i >> 2) * 4096), sbase); });
}
template <int N>
__device__ __forceinline__ void pin(bf16x8* f) { sfor<0, N>([&](auto I) { pin1(f[decltype(I)::value]); }); }
__device__ __forceinline__ void st128_sc1(unsigned voff, const void* sbase, u32x4 v) {
  asm volatile("global_store_dwordx4 %0, %1, %2 sc1\n\ts_nop 1" ::"v"(voff), "v"(v), "s"(sbase) : "memory");
}
__device__ __forceinline__ void st64_sc1(unsigned voff, const void* sbase, u32x2 v) {
  asm volatile("global_store_dwordx2 %0, %1, %2 sc1\n\ts_nop 1" ::"v"(voff), "v"(v), "s"(sbase) : "memory");
}
__device__ __forceinline__ void st32_sc1(const void* sbase, unsigned v) {
  asm volatile("global_store_dword %0, %1, %2 sc1\n\ts_nop 1" ::"v"(0u), "v"(v), "s"(sbase) : "memory");
}
__device__ __forceinline__ float max3(float a, float b, float c) { return __builtin_elementwise_maximum(__builtin_elementwise_maximum(a, b), c); }
__device__ __forceinline__ float red_g_sum(float v) {
  auto a = __builtin_amdgcn_permlane16_swap(__float_as_uint(v), __float_as_uint(v), false, false);
  v = __uint_as_float(a[0]) + __uint_as_float(a[1]);
  auto b = __builtin_amdgcn_permlane32_swap(__float_as_uint(v), __float_as_uint(v), false, false);
  return __uint_as_float(b[0]) + __uint_as_float(b[1]);
}
__device__ __forceinline__ float red_g_max(float v) {
  auto a = __builtin_amdgcn_permlane16_swap(__float_as_uint(v), __float_as_uint(v), false, false);
  v = __builtin_elementwise_maximum(__uint_as_float(a[0]), __uint_as_float(a[1]));
  auto b = __builtin_amdgcn_permlane32_swap(__float_as_uint(v), __float_as_uint(v), false, false);
  return __builtin_elementwise_maximum(__uint_as_float(b[0]), __uint_as_float(b[1]));
}
__device__ __forceinline__ void poll_flags(const unsigned* base, unsigned voff, unsigned epoch) {
#pragma unroll 1
  for (unsigned spins = 0; spins < SPIN_CAP; ++spins) {
    unsigned v;
    asm volatile("global_load_dword %0, %1, %2 sc1\n\ts_waitcnt vmcnt(0)" : "=&v"(v) : "v"(voff), "s"(base) : "memory");
    if (__all(v == epoch)) break;
    __builtin_amdgcn_s_sleep(1);
  }
}

struct Params {
  const char* x; const char* wqkv; const char* wo; const char* w1; const char* b1; const char* w2; const char* b2;
  char* out; char* ws;
};

#define FS(i) do {} while (0)
#define STAMP(i) do {} while (0)

#define LANEVARS() int tz_ = tid; asm volatile("" : "+v"(tz_)); const int lane = tz_ & 63, l15 = lane & 15, g = lane >> 4, q4 = l15 >> 2, p4 = l15 & 3; \
  const unsigned lane16 = (unsigned)lane * 16u, x8 = (unsigned)(((lane & 7) ^ ((lane >> 3) & 7)) << 4); (void)q4; (void)p4; (void)g; (void)x8; (void)lane16

__global__ void __launch_bounds__(512, 2) fused_block(Params p) {
  extern __shared__ __attribute__((aligned(16))) char smem[];
  ldsp lds = (ldsp)smem;
  const int tid = threadIdx.x, lane = tid & 63, w = rfl(tid >> 6);
  const int l15 = lane & 15, g = lane >> 4, q4 = l15 >> 2, p4 = l15 & 3;
  const int b = blockIdx.x, r_ = b >> 3;
  const int c = 2 * (b & 7) + (r_ >> 4), j = r_ & 15;
  const int ca = 4 * ((b & 7) >> 1) + (r_ >> 3), ha = 8 * (b & 1) + (r_ & 7);
  const unsigned* ctl = (const unsigned*)(p.ws + WS_CTL);
  unsigned epoch = 0;
  FS(0);
  STAMP(0);
  const unsigned ONES = 0x3f803f80u;
  const unsigned lane16 = (unsigned)lane * 16u;
  const unsigned x8 = (unsigned)(((lane & 7) ^ ((lane >> 3) & 7)) << 4);

  constexpr int KIMG = 0, VIMG = 24576, QIMG = 49152;
  {
    const int h = ha;
    constexpr int SLOT = 28672, WOFF = 16384;
    const unsigned iw = (unsigned)(w >> 2);
    unsigned voff[4];
    {
      const unsigned xo = (unsigned)((32 * w + (lane >> 3)) * 896) + x8;
#pragma unroll
      for (int ii = 0; ii < 4; ++ii) {
        const unsigned u = (unsigned)(192 * (w & 3) + 64 * ii + lane), row = (u * 43691u) >> 19, rem = u - 12u * row;
        const unsigned piece = rem >> 2, c16 = rem & 3, hb = (c16 >> 1) ^ ((row >> 3) & 1);
        const unsigned wo_ = (ii < 3) ? row * 2688u + piece * 896u + hb * 32u + (c16 & 1) * 16u : 0u;
        voff[ii] = iw ? wo_ : xo + ii * 7168u;
      }
    }
    const unsigned long long mW = 0ull - (unsigned long long)iw;
    const unsigned long long bx_ = (unsigned long long)(uintptr_t)p.x + (unsigned)(ca * 128 * 896), bw_ = (unsigned long long)(uintptr_t)p.wqkv + (unsigned)(56 * h - 8 * (h & 1));
    const char* gb = (const char*)(uintptr_t)(bx_ + ((bw_ - bx_) & mW));
    const unsigned kstep = 128 + iw * (64 * 2688 - 128), ldst = w * 4096 + iw * (WOFF + (w - 4) * 3072 - w * 4096);
    const unsigned smask = iw - 1u, l3 = iw ? (unsigned)M_FILL : ldst + 3072u;
    auto issue1 = [&](auto SL, auto II) {
      constexpr int sl = decltype(SL)::value, ii = decltype(II)::value;
      if constexpr (ii < 3) dma<sl * SLOT + ii * 1024, 0, false>(voff[ii], gb, ldst);
      else { dma<0, 0, false>(voff[3], gb, l3 + (smask & (unsigned)(sl * SLOT))); gb += kstep; }
    };
    auto issue = [&](auto SL) { issue1(SL, IC(0)); issue1(SL, IC(1)); issue1(SL, IC(2)); issue1(SL, IC(3)); };
    FS(1);
    sfor<0, NP>([&](auto S_) { issue(S_); if constexpr (decltype(S_)::value + 1 < NP) RAWBAR(); });
    {
      unsigned long long pc; asm volatile("s_getpc_b64 %0" : "=s"(pc));
      const unsigned long long a = (pc & ~127ull) + (unsigned long long)((r_ * 8 + w) * 128);
      if (lane == 0) asm volatile("s_mov_b32 m0, %1\n\ts_nop 0\n\tglobal_load_lds_dword %0, off" ::"v"(a), "i"(M_FILL) : "memory");
    }
    FS(2);
    const int ptq0 = (b < 224) ? b : 223, ptm = (ptq0 >= 112) ? 2 : 1, ptq = ptq0 - ((ptm == 2) ? 112 : 0);
    const int pts = (ptm == 1) ? ptq >> 3 : ptq >> 1, ptG = (ptm == 1) ? ptq & 7 : ptq & 1;
    auto pt_issue = [&]() {
      const unsigned stride = (ptm == 1) ? 3584u : 896u;
      const char* src = ((ptm == 1) ? p.w1 : p.w2) + (size_t)(32 * pts * stride + ptG * 448);
      const int wq = (w < 7) ? w : 6;
#pragma unroll
      for (int i = 0; i < 2; ++i) { const unsigned u = (unsigned)((2 * wq + i) * 64 + lane), row = (u * 9363u) >> 18, c16 = u - 28u * row;
        dma<M_PT, 0, false>(row * stride + c16 * 16u, src, (unsigned)((2 * wq + i) * 1024)); }
      if (lane == 63) asm volatile("s_mov_b32 m0, %2\n\ts_nop 0\n\tglobal_load_lds_dword %0, %1 sc1" ::"v"(0u), "s"(p.ws + WS_CTL), "i"(M_PF) : "memory");
    };
    pt_issue();
    const int tg = w >> 1, ng = w & 1;
    f32x4 acc[3][2], asum = {0, 0, 0, 0}, asq = {0, 0, 0, 0}, acs[3];
#pragma unroll
    for (int e = 0; e < 3; ++e) { acc[e][0] = f32x4{0, 0, 0, 0}; acc[e][1] = f32x4{0, 0, 0, 0}; acs[e] = f32x4{0, 0, 0, 0}; }
    const int xb = (32 * tg + 16 * ng + l15) * 128 + ((g ^ (l15 & 7)) << 4);
    int ab[3];
#pragma unroll
    for (int e = 0; e < 3; ++e) ab[e] = WOFF + (8 * g + q4) * 192 + ((((3 * ng + e) * 32) ^ ((g & 1) << 5))) + 8 * p4;
    bf16x8 xf[2][2];
    s16x4 al[2][3], ah[2][3];
    auto rdx = [&](int st, int buf, int t) { xf[buf][t] = ld128(lds + ((st >> 1) % 5) * SLOT + (xb ^ ((st & 1) << 6) ^ (t * 2048))); };
    auto rdl = [&](int st, int buf, int e) { al[buf][e] = ldtr(lds + ((st >> 1) % 5) * SLOT + ab[e] + (32 * (st & 1)) * 192); };
    auto rdh_ = [&](int st, int buf, int e) { ah[buf][e] = ldtr(lds + ((st >> 1) % 5) * SLOT + ab[e] + (32 * (st & 1) + 4) * 192); };
    auto rdf = [&](int st, int buf) { rdx(st, buf, 0); rdx(st, buf, 1);
#pragma unroll
      for (int e = 0; e < 3; ++e) { rdl(st, buf, e); rdh_(st, buf, e); } };
    bf16x8 onesf = __builtin_bit_cast(bf16x8, (u32x4{ONES, ONES, ONES, ONES}));
    KEEP(onesf); KEEP(asum); KEEP(asq);
#pragma unroll
    for (int e = 0; e < 3; ++e) { KEEP(acc[e][0]); KEEP(acc[e][1]); KEEP(acs[e]); }
    constexpr int ISS[14] = {NP <= 3 ? 3 : NP == 4 ? 4 : -1, NP <= 3 ? 4 : -1, -1, 5, -1, 6, -1, -1, -1, -1, -1, -1, -1, -1};
    constexpr int WT[7] = {NP == 3 ? 12 : NP == 4 ? 16 : 20, NP == 3 ? 12 : 16, 12, NP == 3 ? 8 : 12, NP == 5 ? 12 : 8, 4, 0};
    waitv<WT[0]>();
    BAR();
    FS(3);
    rdf(0, 0);
    sfor<0, 14>([&](auto ST) {
      constexpr int st = decltype(ST)::value, kc = st >> 1, cb = st & 1;
      if constexpr ((st & 1) && st < 13) {
        waitv<WT[kc + 1]>();
        BARN();
      }
      constexpr int nb = (st + 1) & 1; constexpr bool more = st < 13;
      const bf16x8 a0 = cat(al[cb][0], ah[cb][0]), a1 = cat(al[cb][1], ah[cb][1]), a2 = cat(al[cb][2], ah[cb][2]);
      SCHED();
      MACC(acc[0][0], a0, xf[cb][0]); if constexpr (more) rdx(st + 1, nb, 0);
      MACC(acc[0][1], a0, xf[cb][1]); if constexpr (more) rdl(st + 1, nb, 0);
      MACC(asum, onesf, xf[cb][0]); if constexpr (more) rdh_(st + 1, nb, 0);
      MACC(acs[0], a0, onesf);
      SCHED();
      if constexpr (ISS[st] >= 0) { issue1(IC(ISS[st] % 5), IC(0)); issue1(IC(ISS[st] % 5), IC(1)); }
      SCHED();
      MACC(acc[1][0], a1, xf[cb][0]); if constexpr (more) rdx(st + 1, nb, 1);
      MACC(acc[1][1], a1, xf[cb][1]); if constexpr (more) rdl(st + 1, nb, 1);
      MACC(asq, xf[cb][0], xf[cb][0]); if constexpr (more) rdh_(st + 1, nb, 1);
      MACC(acs[1], a1, onesf);
      SCHED();
      if constexpr (ISS[st] >= 0) { issue1(IC(ISS[st] % 5), IC(2)); issue1(IC(ISS[st] % 5), IC(3)); }
      SCHED();
      MACC(acc[2][0], a2, xf[cb][0]); if constexpr (more) rdl(st + 1, nb, 2);
      MACC(acc[2][1], a2, xf[cb][1]); if constexpr (more) rdh_(st + 1, nb, 2);
      MACC(acs[2], a2, onesf);
      SCHED();
    });
    NOPS(); KEEP(asum); KEEP(asq);
#pragma unroll
    for (int e = 0; e < 3; ++e) { KEEP(acc[e][0]); KEEP(acc[e][1]); KEEP(acs[e]); }
    FS(4);
    STAMP(1);
    epoch = (unsigned)rfl((int)*(LAS unsigned*)(lds + M_PF + 252)) + 1u;
    {
      const unsigned ptb = (ptm == 1) ? WT_W1 : WT_W2, ptn = (ptm == 1) ? 14u : 56u;
      ldsp src = lds + M_PT + (4 * g + q4) * 448 + 8 * p4;
#pragma unroll
      for (int i = 0; i < 2; ++i) {
        const int t = (w + 8 * i < 14) ? w + 8 * i : w;
        const bf16x8 f = cat(ldtr(src + t * 32), ldtr(src + 16 * 448 + t * 32));
        st128_sc1(lane16 + ptb + ((unsigned)(14 * ptG + t) * ptn + (unsigned)pts) * 1024u, p.ws + WS_WT, __builtin_bit_cast(u32x4, f));
      }
    }
    {
      const float d01 = (p4 & 1) ? asq[1] : asq[0], d23 = (p4 & 1) ? asq[3] : asq[2], dq = (p4 & 2) ? d23 : d01;
      float mean = asum[0] * (1.f / 448.f), dq2 = dq;
      KEEP(mean); KEEP(dq2);
      if (g == q4) *(LAS f32x2*)(lds + M_LN + (32 * tg + 16 * ng + l15) * 8) = f32x2{mean, rsqrtf(fmaxf(dq2 * (1.f / 448.f) - mean * mean, 0.f) + 1e-5f)};
    }
    BAR();
    FS(5);
    float mu[2], rs[2];
#pragma unroll
    for (int t = 0; t < 2; ++t) { const f32x2 st_ = *(LAS f32x2*)(lds + M_LN + (32 * tg + 16 * (t ^ ng) + l15) * 8); mu[t] = st_[0]; rs[t] = st_[1]; }
    const bool pad0 = (h & 1) && g == 0, pad1 = !(h & 1) && g == 3;
    const int rsw = ((l15 >> 1) & 3) << 4, rowb = (32 * tg + l15) * 64;
    auto epi = [&](auto NG) {
      constexpr int ngc = decltype(NG)::value;
      sfor<0, 3>([&](auto E) {
        constexpr int e = decltype(E)::value, nt = 3 * ngc + e, which = nt >> 1, par = nt & 1;
        constexpr int ibase = (which == 0) ? QIMG : (which == 1) ? KIMG + 16384 : VIMG + 16384;
        const int off = (which == 2) ? ((((2 * par + (g >> 1)) << 4) ^ rsw) + (g & 1) * 8) : (((g << 4) ^ rsw) + par * 8);
        const bool pad = par ? pad1 : pad0;
#pragma unroll
        for (int t = 0; t < 2; ++t) {
          const float sc = (which == 0) ? rs[t] * 0.27264657f : rs[t], nm = -mu[t] * sc;
          f32x2 sc2 = {sc, sc}, nm2 = {nm, nm};
          KEEP(sc2); KEEP(nm2);
          const f32x2 v0 = lo2(acc[e][t]) * sc2 + lo2(acs[e]) * nm2, v1 = hi2(acc[e][t]) * sc2 + hi2(acs[e]) * nm2;
          u32x2 o; o.x = cvtpk(v0); o.y = cvtpk(v1);
          o.x = pad ? ((which == 2) ? 0x00003f80u : 0u) : o.x; o.y = pad ? 0u : o.y;
          *(LAS u32x2*)(lds + ibase + (t ^ ngc) * 1024 + rowb + off) = o;
        }
      });
    };
    if (ng) epi(IC(1)); else epi(IC(0));
    if (ca < 2) {
      const u32x4 z = {0, 0, 0, 0};
#pragma unroll
      for (int i = 0; i < 2; ++i) { *(LAS u32x4*)(lds + KIMG + tid * 16 + i * 8192) = z; *(LAS u32x4*)(lds + VIMG + tid * 16 + i * 8192) = z; }
    }
    BAR();
    FS(6);
    {
      const char* blob = p.ws + WS_KV + (size_t)(ca * 16 + h) * 16384;
      const u32x4 v0 = *(LAS u32x4*)(lds + KIMG + 16384 + tid * 16), v1 = *(LAS u32x4*)(lds + VIMG + 16384 + tid * 16);
      st128_sc1(tid * 16, blob, v0); st128_sc1(8192 + tid * 16, blob, v1);
    }
    FS(7);
    WAITV(0);
    BAR();
    if (tid == 0) { st32_sc1(ctl + FLAG_IDX(0, ca, h), epoch); st32_sc1(ctl + FLAG_IDX(4, b >> 4, b & 15), epoch); }
    FS(8);
    STAMP(2);
  }
  const int ns = j & 3, col4 = 128 * ns - (((ns >> 1) & ns) << 5), ngr = 4 - (ns >> 1);
  const int pr = w & 3, wh = w >> 2;
  const int prc = (pr < ngr) ? pr : 0;
  const int th5 = j >> 3, cs5 = j & 7, col0 = 64 * cs5 - 16 * ((cs5 >> 2) * (cs5 & 3)), nblk = 4 - (cs5 >> 2);
  const int ng5 = w & 1, kq = w >> 1;
  const char* wt = p.ws + WS_WT;
  bf16x8 wr[32];
  const int T1 = (448 * (j >> 2) + col4) / 16;
  const unsigned w1o = WT_W1 + (unsigned)(T1 + 2 * prc) * 14u * 1024u;
  const unsigned wro = lane16 + w1o;
  constexpr int WI = 32768;
  unsigned wb0, wb1;
  { const int rin = lane >> 4, pos = lane & 15;
    int blk = (pos >> 1) ^ rin; if (blk >= 2 * ngr) blk -= 2; wb0 = (unsigned)(blk * 32 + (pos & 1) * 16);
    blk = (pos >> 1) ^ (4 + rin); if (blk >= 2 * ngr) blk -= 2; wb1 = (unsigned)(blk * 32 + (pos & 1) * 16); }
  const char* wob = p.wo + col4 * 2;
  {
    if (w < 6) {
      const unsigned rb = (unsigned)(((lane >> 4) + 4 * (24 + w)) * 896) + ((w & 1) ? wb1 : wb0);
      sfor<0, 14>([&](auto I) { constexpr int i = decltype(I)::value; dma<i * 6144, i * 24 * 896, false>(rb, wob, (unsigned)(WI + (24 + w) * 1024)); });
    }
    int pos = lane & 15; if (pos >= 4 * ngr) pos = 4 * ngr - 1;
    int xch = (lane & 15) ^ (((lane >> 4) + 4 * w) & 15); if (xch >= 4 * ngr) xch = 0;
    dma<M_XT, 0, false>((unsigned)(((lane >> 4) + 4 * w) * 896 + xch * 16), p.x + (size_t)(128 * c + 32 * (j >> 2)) * 896 + col4 * 2, (unsigned)(w * 1024));
    if (w == 5 && lane < 16) dma<M_B1, 0, false>((unsigned)(pos * 16), p.b1 + (448 * (j >> 2) + col4) * 2, 0u);
    if (w == 4 && lane < 8) dma<M_B2, 0, false>((unsigned)((lane < 2 * nblk ? lane : 0) * 16), p.b2 + col0 * 2, 0u);
  }
  {
    const int h = ha;
    FS(9);
    const int sw = (l15 >> 1) & 3;
    if (w == 7 && ca >= 1) poll_flags(ctl, (unsigned)(FLAG_IDX(0, (ca >= 2 && (lane & 1)) ? ca - 2 : ca - 1, h) * 4), epoch);
    if (w == 6) {
      const unsigned tfo = (unsigned)(FLAG_IDX(4, 4 * g, l15) * 4);
#pragma unroll 1
      for (unsigned spins = 0; spins < SPIN_CAP; ++spins) {
        unsigned t0, t1, t2, t3;
        asm volatile("global_load_dword %0, %4, %5 sc1\n\tglobal_load_dword %1, %4, %5 offset:2048 sc1\n\tglobal_load_dword %2, %6, %5 sc1\n\tglobal_load_dword %3, %6, %5 offset:2048 sc1\n\ts_waitcnt vmcnt(0)"
                     : "=&v"(t0), "=&v"(t1), "=&v"(t2), "=&v"(t3) : "v"(tfo), "s"(ctl), "v"(tfo + 4096u) : "memory");
        if (__all(t0 == epoch && t1 == epoch && t2 == epoch && t3 == epoch)) break;
        __builtin_amdgcn_s_sleep(2);
      }
      if (b == 0 && lane == 0) st32_sc1(ctl, epoch);
    }
    BAR();
    FS(10);
    {
      const int tl = w >> 2, src = ca - 2 + tl, pc = (w & 3) * 2048;
      if (src >= 0) {
        const char* blob = p.ws + WS_KV + (size_t)(src * 16 + h) * 16384 + pc;
        dma2c_sc1<KIMG>(lane16, blob, (unsigned)(tl * 8192 + pc)); dma2c_sc1<VIMG>(lane16, blob + 8192, (unsigned)(tl * 8192 + pc));
      }
    }
    const bf16x8 qf = ld128(lds + QIMG + (16 * w + l15) * 64 + ((g ^ sw) << 4));
    WAITV(0);
    BAR();
    FS(11);
    STAMP(3);
    STAMP(4);
    if (w >= 4) ldfrags<7>(wr, wro, wt);
    f32x4 sc[17];
    const float NEG = -__builtin_inff();
    ldsp kb = lds + KIMG + (16 * w + l15) * 64 + ((g ^ sw) << 4);
    {
      bf16x8 kf[2][6];
#pragma unroll
      for (int u = 0; u < 6; ++u) kf[0][u] = ld128(kb + u * 1024);
#pragma unroll
      for (int bt = 0; bt < 3; ++bt) {
        if (bt < 2) {
#pragma unroll
          for (int u = 0; u < 6; ++u) if (6 * (bt + 1) + u < 17) kf[(bt + 1) & 1][u] = ld128(kb + (6 * (bt + 1) + u) * 1024);
        }
        SCHED();
#pragma unroll
        for (int u = 0; u < 6; ++u) if (6 * bt + u < 17) sc[6 * bt + u] = MFMA(kf[bt & 1][u], qf, (f32x4{0, 0, 0, 0}));
        SCHED();
      }
    }
    FS(12);
    if (w >= 4) ldfrags<7>(wr + 7, wro + 7 * 1024, wt);
    const int vsw = (2 * g + (q4 >> 1)) & 3;
    ldsp vb0 = lds + VIMG + (16 * w + 4 * g + q4) * 64 + (((p4 >> 1) ^ vsw) << 4) + (p4 & 1) * 8;
    ldsp vb1 = lds + VIMG + (16 * w + 4 * g + q4) * 64 + (((2 + (p4 >> 1)) ^ vsw) << 4) + (p4 & 1) * 8;
    bf16x8 vf[2][3][2];
    auto rdv = [&](int bt) {
#pragma unroll
      for (int i = 0; i < 3; ++i) { const int uu = 3 * bt + i, ua = 2 * uu, ub = (uu < 8) ? 2 * uu + 1 : 15;
        vf[bt & 1][i][0] = cat(ldtr(vb0 + ua * 1024), ldtr(vb0 + ub * 1024)); vf[bt & 1][i][1] = cat(ldtr(vb1 + ua * 1024), ldtr(vb1 + ub * 1024)); }
    };
    rdv(0);
    SCHED();
    if (ca < 2) {
      const int tmin = (2 - ca) * 8;
#pragma unroll
      for (int u = 0; u < 16; ++u) if (w + u < tmin) sc[u] = f32x4{NEG, NEG, NEG, NEG};
    }
#pragma unroll
    for (int r = 0; r < 4; ++r) { sc[0][r] = (4 * g + r <= l15) ? NEG : sc[0][r]; sc[16][r] = (4 * g + r > l15) ? NEG : sc[16][r]; }
    float m = max3(sc[0][0], sc[0][1], sc[0][2]), m2 = max3(sc[0][3], sc[1][0], sc[1][1]);
    m = max3(m, sc[1][2], sc[1][3]);
#pragma unroll
    for (int u = 2; u < 17; ++u) { if (u & 1) m = max3(max3(m, sc[u][0], sc[u][1]), sc[u][2], sc[u][3]); else m2 = max3(max3(m2, sc[u][0], sc[u][1]), sc[u][2], sc[u][3]); }
    m = max3(m, m2, m2);
    m = red_g_max(m);
    FS(13);
    SCHED();
    if (w >= 4) ldfrags<7>(wr + 14, wro + 14 * 1024, wt);
    unsigned pk[17][2];
    f32x2 mm = {-m, -m};
    KEEP(mm);
    f32x4 oa[2] = {f32x4{0, 0, 0, 0}, f32x4{0, 0, 0, 0}};
    KEEP(oa[0]); KEEP(oa[1]);
#pragma unroll
    for (int bt = 0; bt < 3; ++bt) {
#pragma unroll
      for (int u = 6 * bt; u < 6 * bt + 6 && u < 17; ++u) {
        const f32x2 d0 = lo2(sc[u]) + mm, d1 = hi2(sc[u]) + mm;
        pk[u][0] = cvtpk(__builtin_amdgcn_exp2f(d0[0]), __builtin_amdgcn_exp2f(d0[1]));
        pk[u][1] = cvtpk(__builtin_amdgcn_exp2f(d1[0]), __builtin_amdgcn_exp2f(d1[1]));
      }
      if (bt < 2) rdv(bt + 1);
      if (bt == 0 && w >= 4) ldfrags<7>(wr + 21, wro + 21 * 1024, wt);
      SCHED();
#pragma unroll
      for (int i = 0; i < 3; ++i) {
        const int uu = 3 * bt + i;
        u32x4 pu; pu.x = pk[2 * uu][0]; pu.y = pk[2 * uu][1];
        if (uu < 8) { pu.z = pk[2 * uu + 1][0]; pu.w = pk[2 * uu + 1][1]; } else { pu.z = 0; pu.w = 0; }
        const bf16x8 pf = __builtin_bit_cast(bf16x8, pu);
        MACCN(oa[0], vf[bt & 1][i][0], pf); MACC(oa[1], vf[bt & 1][i][1], pf);
      }
      SCHED();
    }
    NOPS(); KEEP(oa[0]); KEEP(oa[1]);
    FS(15);
    int tz2 = tid; asm volatile("" : "+v"(tz2));
    const int l15b = tz2 & 15, gb = (tz2 & 63) >> 4, swb = (l15b >> 1) & 3;
    const float lsel = (h & 1) ? oa[0][0] : oa[1][0];
    const float l = __uint_as_float((unsigned)__builtin_amdgcn_ds_bpermute((((h & 1) ? 0 : 48) + l15b) * 4, (int)__float_as_uint(lsel)));
    const float inv1 = __builtin_amdgcn_rcpf(l);
    f32x2 inv = {inv1, inv1};
    KEEP(inv);
    u32x4 ov;
    ov.x = cvtpk(lo2(oa[0]) * inv); ov.y = cvtpk(hi2(oa[0]) * inv); ov.z = cvtpk(lo2(oa[1]) * inv); ov.w = cvtpk(hi2(oa[1]) * inv);
    { const bool z0 = (h & 1) && gb == 0, z1 = !(h & 1) && gb == 3; ov.x = z0 ? 0u : ov.x; ov.y = z0 ? 0u : ov.y; ov.z = z1 ? 0u : ov.z; ov.w = z1 ? 0u : ov.w; }
    st128_sc1((unsigned)((16 * w + l15b) * 64 + ((gb ^ swb) << 4)), p.ws + WS_O + (size_t)(ca * 16 + h) * 8192, ov);
    FS(16);
    BAR();
    {
      const unsigned rb = (unsigned)(((lane >> 4) + 12 * w) * 896), va = rb + ((w & 1) ? wb1 : wb0), vb = rb + ((w & 1) ? wb0 : wb1), lb = (unsigned)(WI + 3 * w * 1024);
      dma<0, 0, false>(va, wob, lb); dma<1024, 4 * 896, false>(vb, wob, lb); dma<2048, 8 * 896, false>(va, wob, lb);
      const int ins = (w < 4) ? 108 + w : 3 * w + 2;
      dma<0, 0, false>((unsigned)(((lane >> 4) + 4 * ins) * 896) + ((ins & 1) ? wb1 : wb0), wob, (unsigned)(WI + ins * 1024));
    }
    WAITV(4);
    BAR();
    if (tid == 0) st32_sc1(ctl + FLAG_IDX(1, ca, h), epoch);
    FS(18);
    STAMP(5);
  }
  {
    LANEVARS();
    constexpr int OT = 0;
    const int tq = j >> 2;
    WAITV(0);
    BAR();
    if (w < 4) {
      const int rx = 4 * (g & 1) + q4;
      ldsp ab0 = lds + WI + (4 * g + q4) * 256 + (((2 * prc) ^ rx) << 5) + 8 * p4;
      ldsp ab1 = lds + WI + (4 * g + q4) * 256 + (((2 * prc + 1) ^ rx) << 5) + 8 * p4;
      ldsp ab0h = ab0 + 65536, ab1h = ab1 + 65536;
#pragma unroll
      for (int s = 0; s < 16; ++s) {
        const int rb = (28 * s - 4 * (s & 1)) * 256;
        if (rb + 4096 < 65536) { wr[s] = cat(ldtr(ab0 + rb), ldtr(ab0 + rb + 4096)); wr[16 + s] = cat(ldtr(ab1 + rb), ldtr(ab1 + rb + 4096)); }
        else { wr[s] = cat(ldtr(ab0h + (rb - 65536)), ldtr(ab0h + (rb - 65536 + 4096))); wr[16 + s] = cat(ldtr(ab1h + (rb - 65536)), ldtr(ab1h + (rb - 65536 + 4096))); }
      }
    } else {
      __builtin_amdgcn_s_setprio(3);
      poll_flags(ctl, (unsigned)(FLAG_IDX(1, c, l15) * 4), epoch);
      const char* ob_ = p.ws + WS_O + (size_t)(c * 16 + 4 * (w - 4)) * 8192 + tq * 2048;
      const unsigned lb = (unsigned)(OT + (w - 4) * 8192);
      dma2c_sc1<0>(lane16, ob_, lb); dma2c_sc1<2048>(lane16 + 8192, ob_, lb); dma2c_sc1<4096>(lane16 + 16384, ob_, lb); dma2c_sc1<6144>(lane16 + 24576, ob_, lb);
      __builtin_amdgcn_s_setprio(0);
    }
    FS(19);
    WAITV(0);
    BAR();
    FS(20);
    STAMP(6);
    STAMP(7);
    if (w < 4) {
      f32x4 a0[2], a1[2];
#pragma unroll
      for (int t = 0; t < 2; ++t) { a0[t] = f32x4{0, 0, 0, 0}; a1[t] = f32x4{0, 0, 0, 0}; }
      ldsp ob = lds + OT + l15 * 64 + ((g ^ ((l15 >> 1) & 3)) << 4);
      u32x2 xr[2][2];
#pragma unroll
      for (int t = 0; t < 2; ++t) { ldsp xp = lds + M_XT + (16 * t + l15) * 256 + (g & 1) * 8;
        xr[t][0] = *(LAS u32x2*)(xp + (((4 * prc + (g >> 1)) ^ l15) << 4)); xr[t][1] = *(LAS u32x2*)(xp + (((4 * prc + 2 + (g >> 1)) ^ l15) << 4)); }
#pragma unroll
      for (int t = 0; t < 2; ++t) { KEEP(a0[t]); KEEP(a1[t]); }
      bf16x8 of[4][2];
#pragma unroll
      for (int s = 0; s < 3; ++s) { of[s][0] = ld128(ob + s * 2048); of[s][1] = ld128(ob + s * 2048 + 1024); }
#pragma unroll
      for (int s = 0; s < 16; ++s) {
        if (s + 3 < 16) { of[(s + 3) & 3][0] = ld128(ob + (s + 3) * 2048); of[(s + 3) & 3][1] = ld128(ob + (s + 3) * 2048 + 1024); }
        SCHED();
#pragma unroll
        for (int t = 0; t < 2; ++t) { MACC(a0[t], wr[s], of[s & 3][t]); MACC(a1[t], wr[16 + s], of[s & 3][t]); }
        SCHED();
      }
      NOPS();
#pragma unroll
      for (int t = 0; t < 2; ++t) { KEEP(a0[t]); KEEP(a1[t]); }
      FS(21);
      const unsigned ho = (unsigned)((128 * c + 32 * tq + l15) * 896 + (col4 / 32 + pr) * 64 + g * 16);
#pragma unroll
      for (int t = 0; t < 2; ++t) {
        u32x4 hv;
        hv.x = cvtpk(lo2(a0[t]) + bf2(xr[t][0].x)); hv.y = cvtpk(hi2(a0[t]) + bf2(xr[t][0].y));
        hv.z = cvtpk(lo2(a1[t]) + bf2(xr[t][1].x)); hv.w = cvtpk(hi2(a1[t]) + bf2(xr[t][1].y));
        if (pr < ngr) st128_sc1(ho + t * 16 * 896, p.ws + WS_H, hv);
        float s1 = 0.f, s2 = 0.f;
#pragma unroll
        for (int i = 0; i < 4; ++i) { s1 = dot2(hv[i], ONES, s1); s2 = dot2(hv[i], hv[i], s2); }
        s1 = red_g_sum(s1); s2 = red_g_sum(s2);
        if (pr >= ngr) { s1 = 0.f; s2 = 0.f; }
        if (g == 0) ((LAS f32x2*)(lds + M_PART))[(16 * t + l15) * 4 + pr] = f32x2{s1, s2};
      }
    } else {
      pin<28>(wr);
#pragma unroll
      for (int i = 0; i < 28; ++i) *(LAS bf16x8*)(lds + 32768 + (w - 4) * 28672 + i * 1024 + lane * 16) = wr[i];
    }
    FS(22);
    BAR();
    if (tid < 32) {
      const f32x4 u0 = *(LAS f32x4*)(lds + M_PART + tid * 32), u1 = *(LAS f32x4*)(lds + M_PART + tid * 32 + 16);
      st64_sc1((unsigned)((128 * c + 32 * tq + tid) * 32 + ns * 8), p.ws + WS_ST,
               u32x2{__float_as_uint((u0[0] + u0[2]) + (u1[0] + u1[2])), __float_as_uint((u0[1] + u0[3]) + (u1[1] + u1[3]))});
    }
    if (w < 4) {
#pragma unroll
      for (int i = 0; i < 28; ++i) wr[i] = ld128(lds + 32768 + w * 28672 + i * 1024 + lane * 16);
    }
    FS(23);
    if (w < 4) WAITV(0);
    BAR();
    if (tid == 0) st32_sc1(ctl + FLAG_IDX(2, c, j), epoch);
    FS(25);
    STAMP(8);
  }
  {
    LANEVARS();
    const int f0_ = 448 * (j >> 2) + col4, th = wh;
    FS(26);
    f32x4 c0 = {0, 0, 0, 0}, c1 = {0, 0, 0, 0};
    pin<28>(wr);
    bf16x8 onesf = __builtin_bit_cast(bf16x8, (u32x4{ONES, ONES, ONES, ONES}));
    KEEP(onesf); KEEP(c0); KEEP(c1);
    asm volatile("s_nop 1");
#pragma unroll
    for (int s = 0; s < 14; ++s) { MACC(c0, wr[s], onesf); MACC(c1, wr[14 + s], onesf); }
    NOPS(); KEEP(c0); KEEP(c1);
    SCHED();
    if (w == 7) poll_flags(ctl, (unsigned)(FLAG_IDX(2, c, l15) * 4), epoch);
    FS(29);
    BAR();
    const unsigned vo = (unsigned)((16 * w + (lane >> 3)) * 896) + x8, wl2k = (unsigned)(w * 2048);
    const char* hb = p.ws + WS_H + (size_t)c * 128 * 896;
    auto issue4 = [&](auto KC) { constexpr int kc = decltype(KC)::value; dma<kc * 16384, kc * 128, true>(vo, hb, wl2k); dma<kc * 16384 + 1024, kc * 128 + 8 * 896, true>(vo, hb, wl2k); };
    if (lane < 32) dma<M_TILE, 0, true>(lane16, p.ws + WS_ST + (size_t)(128 * c + 16 * w) * 32, (unsigned)(w * 512));
    issue4(IC(0)); RAWBAR(); issue4(IC(1));
    FS(31);
    f32x4 acc[2][4];
#pragma unroll
    for (int t = 0; t < 4; ++t) { acc[0][t] = f32x4{0, 0, 0, 0}; acc[1][t] = f32x4{0, 0, 0, 0}; }
    const int xb = (64 * th + l15) * 128 + ((g ^ (l15 & 7)) << 4);
    bf16x8 xf[2][4];
    auto rd1 = [&](int st, int buf, int t) { xf[buf][t] = ld128(lds + (st >> 1) * 16384 + (xb ^ ((st & 1) << 6)) + t * 2048); };
    auto rdf = [&](int st, int buf) { rd1(st, buf, 0); rd1(st, buf, 1); rd1(st, buf, 2); rd1(st, buf, 3); };
#pragma unroll
    for (int t = 0; t < 4; ++t) { KEEP(acc[0][t]); KEEP(acc[1][t]); }
    bf16x8 bf[2][14];
    const unsigned w2o0 = WT_W2 + ((unsigned)(col0 / 16 + 2 * ng5) * 56u + 14u * kq) * 1024u, w2o1 = (2 * ng5 + 1 < nblk) ? w2o0 + 56u * 1024u : w2o0;
    WAITV(2); BAR();
    FS(32);
    STAMP(9);
    STAMP(10);
    rdf(0, 0);
    sfor<0, 14>([&](auto ST) {
      constexpr int st = decltype(ST)::value;
      if constexpr (st == 1) WAITV(2); if constexpr (st == 3) WAITV(4); if constexpr (st == 5) WAITV(6); if constexpr (st == 7) WAITV(8); if constexpr (st == 9) WAITV(10); if constexpr (st == 11) WAITV(12);
      if constexpr ((st & 1) && st < 13) BARN();
      if constexpr (st == 2) {
        if (tid < 128) {
          const f32x4 u0 = *(LAS f32x4*)(lds + M_TILE + tid * 32), u1 = *(LAS f32x4*)(lds + M_TILE + tid * 32 + 16);
          float mean = ((u0[0] + u0[2]) + (u1[0] + u1[2])) * (1.f / 448.f), sq = (u0[1] + u0[3]) + (u1[1] + u1[3]);
          KEEP(mean); KEEP(sq);
          const float rstd = rsqrtf(fmaxf(sq * (1.f / 448.f) - mean * mean, 0.f) + 1e-5f);
          *(LAS f32x2*)(lds + M_LN + tid * 8) = f32x2{rstd, -mean * rstd};
        }
      }
      SCHED();
#pragma unroll
      for (int t = 0; t < 2; ++t) { MACC(acc[0][t], wr[st], xf[st & 1][t]); if constexpr (st < 13) rd1(st + 1, (st + 1) & 1, t); MACC(acc[1][t], wr[14 + st], xf[st & 1][t]); }
      SCHED();
      if constexpr (st <= 4) issue4(IC(st + 2));
      if constexpr (st >= 5) { constexpr int s = st - 5; ldfrags<1>(&bf[0][s], lane16 + w2o0 + s * 1024, wt); ldfrags<1>(&bf[1][s], lane16 + w2o1 + s * 1024, wt); }
      SCHED();
#pragma unroll
      for (int t = 2; t < 4; ++t) { MACC(acc[0][t], wr[st], xf[st & 1][t]); if constexpr (st < 13) rd1(st + 1, (st + 1) & 1, t); MACC(acc[1][t], wr[14 + st], xf[st & 1][t]); }
      SCHED();
    });
    NOPS();
#pragma unroll
    for (int t = 0; t < 4; ++t) { KEEP(acc[0][t]); KEEP(acc[1][t]); }
    FS(33);
    STAMP(11);
    {
      const u32x2 bb0 = *(LAS u32x2*)(lds + M_B1 + (32 * prc + 4 * g) * 2), bb1 = *(LAS u32x2*)(lds + M_B1 + (32 * prc + 4 * g) * 2 + 32);
      const f32x2 b00 = bf2(bb0.x), b01 = bf2(bb0.y), b10 = bf2(bb1.x), b11 = bf2(bb1.y);
      const unsigned ho = (unsigned)((128 * c + 64 * th + l15) * 3584 + (f0_ / 32 + pr) * 64 + g * 16);
      f32x2 ln[4];
#pragma unroll
      for (int t = 0; t < 4; ++t) ln[t] = *(LAS f32x2*)(lds + M_LN + (64 * th + 16 * t + l15) * 8);
#pragma unroll
      for (int t = 0; t < 4; ++t) {
        const float a = ln[t][0], nm = ln[t][1];
        f32x2 aa = {a, a}, nn = {nm, nm};
        KEEP(aa); KEEP(nn);
        f32x2 z[4] = {lo2(acc[0][t]) * aa + (lo2(c0) * nn + b00), hi2(acc[0][t]) * aa + (hi2(c0) * nn + b01), lo2(acc[1][t]) * aa + (lo2(c1) * nn + b10), hi2(acc[1][t]) * aa + (hi2(c1) * nn + b11)};
        u32x4 hv;
#pragma unroll
        for (int i = 0; i < 4; ++i) {
          f32x2 r = {__builtin_elementwise_maximum(z[i][0], 0.f), __builtin_elementwise_maximum(z[i][1], 0.f)};
          hv[i] = cvtpk(r * r);
        }
        if (pr < ngr) st128_sc1(ho + t * 16 * 3584, p.ws + WS_H1, hv);
      }
    }
    FS(34);
    WAITV(0);
    BAR();
    if (tid == 0) st32_sc1(ctl + FLAG_IDX(3, c, j), epoch);
    FS(38);
    STAMP(12);
    if (w != 7) {
      ldfrags<5>(&bf[0][9], lane16 + w2o0 + 9 * 1024, wt); ldfrags<5>(&bf[1][9], lane16 + w2o1 + 9 * 1024, wt);
    }
    if (w >= 5 && w < 7) {
      const int m0_ = col0 >> 5;
      const char* hr = p.ws + WS_H + (size_t)(128 * c + 64 * th5) * 896;
      sfor<0, 6>([&](auto I) { constexpr int i = decltype(I)::value;
        const unsigned u = (unsigned)((w - 5) * 384 + i * 64 + lane), r0 = (u * 43691u) >> 19, c0_ = u - 12u * r0; unsigned cc0 = m0_ * 4 + c0_; if (cc0 > 55) cc0 = 55;
        dma<M_TILE + i * 1024, 0, true>(r0 * 896 + cc0 * 16, hr, (unsigned)((w - 5) * 6144)); });
    }
    const unsigned vo5 = (unsigned)((32 * ng5 + (lane >> 3)) * 3584 + kq * 896) + x8, wl4k = (unsigned)(w * 4096);
    const char* hb5 = p.ws + WS_H1 + (size_t)(128 * c + 64 * th5) * 3584;
    auto issue5 = [&](auto I) {
      constexpr int i = decltype(I)::value, sa = (i & 3) * 32768;
      sfor<0, 4>([&](auto II) { constexpr int ii = decltype(II)::value; dma<sa + ii * 1024, i * 128 + ii * 8 * 3584, true>(vo5, hb5, wl4k); });
    };
    FS(39);
    if (w == 7) {
      poll_flags(ctl, (unsigned)(FLAG_IDX(3, c, l15) * 4), epoch);
    }
    FS(40);
    BAR();
    FS(41);
    STAMP(13);
    issue5(IC(0));
    if (w == 7) {
      ldfrags<5>(&bf[0][9], lane16 + w2o0 + 9 * 1024, wt); ldfrags<5>(&bf[1][9], lane16 + w2o1 + 9 * 1024, wt);
    }
    FS(42);
    f32x4 ac5[2][4];
#pragma unroll
    for (int t = 0; t < 4; ++t) { ac5[0][t] = f32x4{0, 0, 0, 0}; ac5[1][t] = f32x4{0, 0, 0, 0}; }
    int yb[4];
#pragma unroll
    for (int t = 0; t < 4; ++t) yb[t] = kq * 8192 + (16 * ((t + kq) & 3) + l15) * 128 + ((g ^ (l15 & 7)) << 4);
    bf16x8 yf[2][4];
    auto rz1 = [&](int st, int buf, int t) { yf[buf][t] = ld128(lds + ((st >> 1) & 3) * 32768 + (yb[t] ^ ((st & 1) << 6))); };
    auto rdy = [&](int st, int buf) { rz1(st, buf, 0); rz1(st, buf, 1); rz1(st, buf, 2); rz1(st, buf, 3); };
#pragma unroll
    for (int t = 0; t < 4; ++t) { KEEP(ac5[0][t]); KEEP(ac5[1][t]); }
    WAITV(0);
    pin<14>(bf[0]); pin<14>(bf[1]);
    BAR();
    FS(43);
    STAMP(14);
    rdy(0, 0);
    sfor<0, 14>([&](auto ST) {
      constexpr int st = decltype(ST)::value;
      if constexpr ((st & 1) && st < 13) {
        if constexpr (st < 11) WAITV(4); else WAITV(0);
        BARN();
      }
      SCHED();
#pragma unroll
      for (int t = 0; t < 2; ++t) { MACC(ac5[0][t], bf[0][st], yf[st & 1][t]); if constexpr (st < 13) rz1(st + 1, (st + 1) & 1, t); MACC(ac5[1][t], bf[1][st], yf[st & 1][t]); }
      SCHED();
      if constexpr (st == 0) { issue5(IC(1)); issue5(IC(2)); }
      if constexpr ((st & 1) && st <= 7) issue5(IC((st + 5) / 2));
      SCHED();
#pragma unroll
      for (int t = 2; t < 4; ++t) { MACC(ac5[0][t], bf[0][st], yf[st & 1][t]); if constexpr (st < 13) rz1(st + 1, (st + 1) & 1, t); MACC(ac5[1][t], bf[1][st], yf[st & 1][t]); }
      SCHED();
    });
    NOPS();
#pragma unroll
    for (int t = 0; t < 4; ++t) { KEEP(ac5[0][t]); KEEP(ac5[1][t]); }
    FS(44);
    BAR();
#pragma unroll
    for (int t = 1; t < 4; ++t) {
      ldsp d = lds + ((((t + kq) & 3) * 3 + (t - 1)) * 2 + ng5) * 2048 + lane * 16;
      *(LAS f32x4*)d = ac5[0][t]; *(LAS f32x4*)(d + 1024) = ac5[1][t];
    }
    const int tokl = 16 * kq + l15;
    const int t0 = col0 / 16 + 2 * ng5, t1 = (2 * ng5 + 1 < nblk) ? t0 + 1 : t0, m0_ = col0 >> 5;
    const u32x2 hr0 = *(LAS u32x2*)(lds + M_TILE + tokl * 192 + ((t0 >> 1) - m0_) * 64 + g * 16 + (t0 & 1) * 8);
    const u32x2 hr1 = *(LAS u32x2*)(lds + M_TILE + tokl * 192 + ((t1 >> 1) - m0_) * 64 + g * 16 + (t1 & 1) * 8);
    const u32x2 b20 = *(LAS u32x2*)(lds + M_B2 + (32 * ng5 + 4 * g) * 2), b21 = *(LAS u32x2*)(lds + M_B2 + ((2 * ng5 + 1 < nblk) ? (32 * ng5 + 16) : 32 * ng5) * 2 + 8 * g);
    f32x2 r00 = lo2(ac5[0][0]) + (bf2(b20.x) + bf2(hr0.x)), r01 = hi2(ac5[0][0]) + (bf2(b20.y) + bf2(hr0.y));
    f32x2 r10 = lo2(ac5[1][0]) + (bf2(b21.x) + bf2(hr1.x)), r11 = hi2(ac5[1][0]) + (bf2(b21.y) + bf2(hr1.y));
    BAR();
    FS(45);
#pragma unroll
    for (int t = 1; t < 4; ++t) {
      ldsp d = lds + ((kq * 3 + (t - 1)) * 2 + ng5) * 2048 + lane * 16;
      const f32x4 u0 = *(LAS f32x4*)d, u1 = *(LAS f32x4*)(d + 1024);
      r00 += lo2(u0); r01 += hi2(u0); r10 += lo2(u1); r11 += hi2(u1);
    }
    GAS char* op = (GAS char*)p.out + (size_t)(128 * c + 64 * th5 + tokl) * 896 + (col0 + 32 * ng5 + 4 * g) * 2;
    *(GAS u32x2*)op = u32x2{cvtpk(r00), cvtpk(r01)};
    if (2 * ng5 + 1 < nblk) *(GAS u32x2*)(op + 32) = u32x2{cvtpk(r10), cvtpk(r11)};
    STAMP(15);
    FS(63);
  }
}

extern "C" void kernel_launch(void* const* d_in, const int* in_sizes, int n_in, void* d_out, int out_size, void* d_ws, size_t ws_size, hipStream_t stream) {
  Params p;
  p.x = (const char*)d_in[0]; p.wqkv = (const char*)d_in[1]; p.wo = (const char*)d_in[2];
  p.w1 = (const char*)d_in[4]; p.b1 = (const char*)d_in[5]; p.w2 = (const char*)d_in[6]; p.b2 = (const char*)d_in[7];
  p.out = (char*)d_out; p.ws = (char*)d_ws;
  (void)hipFuncSetAttribute((const void*)fused_block, hipFuncAttributeMaxDynamicSharedMemorySize, LDS_BYTES);
  hipLaunchKernelGGL(fused_block, dim3(256), dim3(512), LDS_BYTES, stream, p);
}
```

```cpp
#define MAPA
#define E1_CODE
#include <hip/hip_runtime.h>
#include <hip/hip_bf16.h>
#include <stdint.h>
#include <type_traits>

#define LAS __attribute__((address_space(3)))
#define GAS __attribute__((address_space(1)))
typedef short bf16x8 __attribute__((ext_vector_type(8)));
typedef short s16x4 __attribute__((ext_vector_type(4)));
typedef short v4i16_t __attribute__((ext_vector_type(4)));
typedef float f32x4 __attribute__((ext_vector_type(4)));
typedef float f32x2 __attribute__((ext_vector_type(2)));
typedef unsigned u32x4 __attribute__((ext_vector_type(4)));
typedef unsigned u32x2 __attribute__((ext_vector_type(2)));
typedef __bf16 bf16x2_t __attribute__((ext_vector_type(2)));
typedef LAS char* ldsp;

constexpr int SEQ = 2048;
constexpr size_t WS_CTL = 0;
constexpr size_t WS_KV = 262144;
constexpr size_t WS_O = WS_KV + 256 * 16384;
constexpr size_t WS_H = WS_O + 256 * 8192;
constexpr size_t WS_H1 = WS_H + (size_t)SEQ * 896;
constexpr size_t WS_ST = WS_H1 + (size_t)SEQ * 3584;
constexpr size_t WS_WT = WS_ST + (size_t)SEQ * 32;
constexpr unsigned WT_W1 = 0, WT_W2 = WT_W1 + 1568 * 1024, WT_BYTES = WT_W2 + 1568 * 1024;
constexpr size_t WS_DBG = WS_WT + WT_BYTES;
constexpr int LDS_BYTES = 160 * 1024;
constexpr int M_PT = 143360;
constexpr int M_XT = 147456;
constexpr int M_TILE = 131072;
constexpr int MISC = 157696;
constexpr int M_LN = MISC, M_B1 = MISC + 1024, M_B2 = MISC + 1280, M_PF = MISC + 1536, M_PART = MISC + 2048, M_FILL = MISC + 3072;
constexpr unsigned SPIN_CAP = 1u << 16;

#define FLAG_IDX(ph, c, j) (64 + (((ph) * 16 + (c)) * 16 + (j)) * 32)

#define WAITV(n) asm volatile("s_waitcnt vmcnt(" #n ")" ::: "memory")
#define BAR() do { asm volatile("s_waitcnt lgkmcnt(0)" ::: "memory"); __builtin_amdgcn_s_barrier(); asm volatile("" ::: "memory"); } while (0)
#define BARN() do { asm volatile("" ::: "memory"); __builtin_amdgcn_s_barrier(); asm volatile("" ::: "memory"); } while (0)
#define SCHED() __builtin_amdgcn_sched_barrier(0)
#define RAWBAR() __builtin_amdgcn_s_barrier()
#define ILV(n) do { _Pragma("unroll") for (int i_ = 0; i_ < (n); ++i_) { __builtin_amdgcn_sched_group_barrier(0x008, 1, 0); __builtin_amdgcn_sched_group_barrier(0x100, 1, 0); } } while (0)
#define MFMA(a, b, c) __builtin_amdgcn_mfma_f32_16x16x32_bf16((a), (b), (c), 0, 0, 0)
#define MACC(acc, a, b) asm volatile("v_mfma_f32_16x16x32_bf16 %0, %1, %2, %0" : "+v"(acc) : "v"(a), "v"(b))
#define MACCN(acc, a, b) asm volatile("s_nop 1\n\tv_mfma_f32_16x16x32_bf16 %0, %1, %2, %0" : "+v"(acc) : "v"(a), "v"(b))
#define NOPS() asm volatile("s_nop 7\n\ts_nop 3")
#define KEEP(x) asm volatile("" : "+v"(x))
#define IC(x) std::integral_constant<int, (x)>{}

template <int I, int N, class F> __device__ __forceinline__ void sfor(F&& f) { if constexpr (I < N) { f(std::integral_constant<int, I>{}); sfor<I + 1, N>(f); } }

__device__ __forceinline__ unsigned cvtpk(float lo, float hi) { f32x2 v = {lo, hi}; bf16x2_t b = __builtin_convertvector(v, bf16x2_t); return __builtin_bit_cast(unsigned, b); }
__device__ __forceinline__ unsigned cvtpk(f32x2 v) { bf16x2_t b = __builtin_convertvector(v, bf16x2_t); return __builtin_bit_cast(unsigned, b); }
__device__ __forceinline__ float dot2(unsigned a, unsigned b, float c) { return __builtin_amdgcn_fdot2_f32_bf16(__builtin_bit_cast(bf16x2_t, a), __builtin_bit_cast(bf16x2_t, b), c, false); }
__device__ __forceinline__ float bflo(unsigned u) { return __uint_as_float(u << 16); }
__device__ __forceinline__ float bfhi(unsigned u) { return __uint_as_float(u & 0xffff0000u); }
__device__ __forceinline__ f32x2 bf2(unsigned u) { return f32x2{bflo(u), bfhi(u)}; }
__device__ __forceinline__ f32x2 lo2(f32x4 v) { return f32x2{v[0], v[1]}; }
__device__ __forceinline__ f32x2 hi2(f32x4 v) { return f32x2{v[2], v[3]}; }
__device__ __forceinline__ bf16x8 ld128(ldsp p) { return *(const LAS bf16x8*)p; }
__device__ __forceinline__ s16x4 ldtr(ldsp p) { return __builtin_bit_cast(s16x4, __builtin_amdgcn_ds_read_tr16_b64_v4i16((LAS v4i16_t*)p)); }
__device__ __forceinline__ bf16x8 cat(s16x4 lo, s16x4 hi) { return __builtin_shufflevector(lo, hi, 0, 1, 2, 3, 4, 5, 6, 7); }
__device__ __forceinline__ int rfl(int x) { return __builtin_amdgcn_readfirstlane(x); }
template <int LADD, int VADD, bool SC1>
__device__ __forceinline__ void dma(unsigned voff, const void* sbase, unsigned lbase) {
  const unsigned va = voff + (unsigned)VADD;
  if constexpr (SC1) asm volatile("s_add_i32 m0, %2, %3\n\ts_nop 0\n\tglobal_load_lds_dwordx4 %0, %1 sc1" ::"v"(va), "s"(sbase), "s"(lbase), "i"(LADD) : "memory", "scc");
  else asm volatile("s_add_i32 m0, %2, %3\n\ts_nop 0\n\tglobal_load_lds_dwordx4 %0, %1" ::"v"(va), "s"(sbase), "s"(lbase), "i"(LADD) : "memory", "scc");
}
template <int LADD>
__device__ __forceinline__ void dma2c_sc1(unsigned voff, const void* sbase, unsigned lbase) {
  asm volatile("s_add_i32 m0, %2, %3\n\ts_nop 0\n\tglobal_load_lds_dwordx4 %0, %1 sc1\n\tglobal_load_lds_dwordx4 %0, %1 offset:1024 sc1" ::"v"(voff), "s"(sbase), "s"(lbase), "i"(LADD) : "memory", "scc");
}
template <int OFF>
__device__ __forceinline__ void ldfrag1(bf16x8& f, unsigned voff, const void* sbase) {
  asm volatile("global_load_dwordx4 %0, %1, %2 offset:%3 sc1" : "=v"(f) : "v"(voff), "s"(sbase), "i"(OFF) : "memory");
}
__device__ __forceinline__ void pin1(bf16x8& f) { asm volatile("" : "+v"(f)); }
template <int N>
__device__ __forceinline__ void ldfrags(bf16x8* f, unsigned voff, const void* sbase) {
  sfor<0, N>([&](auto I) { constexpr int i = decltype(I)::value; ldfrag1<(i & 3) * 1024>(f[i], voff + (unsigned)((i >> 2) * 4096), sbase); });
}
template <int N>
__device__ __forceinline__ void pin(bf16x8* f) { sfor<0, N>([&](auto I) { pin1(f[decltype(I)::value]); }); }
__device__ __forceinline__ void st128_sc1(unsigned voff, const void* sbase, u32x4 v) {
  asm volatile("global_store_dwordx4 %0, %1, %2 sc1\n\ts_nop 1" ::"v"(voff), "v"(v), "s"(sbase) : "memory");
}
__device__ __forceinline__ void st64_sc1(unsigned voff, const void* sbase, u32x2 v) {
  asm volatile("global_store_dwordx2 %0, %1, %2 sc1\n\ts_nop 1" ::"v"(voff), "v"(v), "s"(sbase) : "memory");
}
__device__ __forceinline__ void st32_sc1(const void* sbase, unsigned v) {
  asm volatile("global_store_dword %0, %1, %2 sc1\n\ts_nop 1" ::"v"(0u), "v"(v), "s"(sbase) : "memory");
}
__device__ __forceinline__ float max3(float a, float b, float c) { return __builtin_elementwise_maximum(__builtin_elementwise_maximum(a, b), c); }
__device__ __forceinline__ float red_g_sum(float v) {
  auto a = __builtin_amdgcn_permlane16_swap(__float_as_uint(v), __float_as_uint(v), false, false);
  v = __uint_as_float(a[0]) + __uint_as_float(a[1]);
  auto b = __builtin_amdgcn_permlane32_swap(__float_as_uint(v), __float_as_uint(v), false, false);
  return __uint_as_float(b[0]) + __uint_as_float(b[1]);
}
__device__ __forceinline__ float red_g_max(float v) {
  auto a = __builtin_amdgcn_permlane16_swap(__float_as_uint(v), __float_as_uint(v), false, false);
  v = __builtin_elementwise_maximum(__uint_as_float(a[0]), __uint_as_float(a[1]));
  auto b = __builtin_amdgcn_permlane32_swap(__float_as_uint(v), __float_as_uint(v), false, false);
  return __builtin_elementwise_maximum(__uint_as_float(b[0]), __uint_as_float(b[1]));
}
__device__ __forceinline__ void poll_flags(const unsigned* base, unsigned voff, unsigned epoch) {
#pragma unroll 1
  for (unsigned spins = 0; spins < SPIN_CAP; ++spins) {
    unsigned v;
    asm volatile("global_load_dword %0, %1, %2 sc1\n\ts_waitcnt vmcnt(0)" : "=&v"(v) : "v"(voff), "s"(base) : "memory");
    if (__all(v == epoch)) break;
    __builtin_amdgcn_s_sleep(1);
  }
}

struct Params {
  const char* x; const char* wqkv; const char* wo; const char* w1; const char* b1; const char* w2; const char* b2;
  char* out; char* ws;
};

#define FS(i) do {} while (0)
#define STAMP(i) do {} while (0)

#define LANEVARS() int tz_ = tid; asm volatile("" : "+v"(tz_)); const int lane = tz_ & 63, l15 = lane & 15, g = lane >> 4, q4 = l15 >> 2, p4 = l15 & 3; \
  const unsigned lane16 = (unsigned)lane * 16u, x8 = (unsigned)(((lane & 7) ^ ((lane >> 3) & 7)) << 4); (void)q4; (void)p4; (void)g; (void)x8; (void)lane16

__global__ void __launch_bounds__(512, 2) fused_block(Params p) {
  extern __shared__ __attribute__((aligned(16))) char smem[];
  ldsp lds = (ldsp)smem;
  const int tid = threadIdx.x, lane = tid & 63, w = rfl(tid >> 6);
  const int l15 = lane & 15, g = lane >> 4, q4 = l15 >> 2, p4 = l15 & 3;
  const int b = blockIdx.x, r_ = b >> 3;
  const int c = 2 * (b & 7) + (r_ >> 4), j = r_ & 15;
  const int ca = 4 * ((b & 7) >> 1) + (r_ >> 3), ha = 8 * (b & 1) + (r_ & 7);
  const unsigned* ctl = (const unsigned*)(p.ws + WS_CTL);
  unsigned epoch = 0;
  FS(0);
  STAMP(0);
  const unsigned ONES = 0x3f803f80u;
  const unsigned lane16 = (unsigned)lane * 16u;
  const unsigned x8 = (unsigned)(((lane & 7) ^ ((lane >> 3) & 7)) << 4);

  constexpr int KIMG = 0, VIMG = 24576, QIMG = 49152;
  {
    const int h = ha;
    constexpr int SLOT = 28672, WOFF = 16384;
    const unsigned iw = (unsigned)(w >> 2);
    unsigned voff[4];
    {
      const unsigned xo = (unsigned)((32 * w + (lane >> 3)) * 896) + x8;
#pragma unroll
      for (int ii = 0; ii < 4; ++ii) {
        const unsigned u = (unsigned)(192 * (w & 3) + 64 * ii + lane), row = (u * 43691u) >> 19, rem = u - 12u * row;
        const unsigned piece = rem >> 2, c16 = rem & 3, hb = (c16 >> 1) ^ ((row >> 3) & 1);
        const unsigned wo_ = (ii < 3) ? row * 2688u + piece * 896u + hb * 32u + (c16 & 1) * 16u : 0u;
        voff[ii] = iw ? wo_ : xo + ii * 7168u;
      }
    }
    const unsigned long long mW = 0ull - (unsigned long long)iw;
    const unsigned long long bx_ = (unsigned long long)(uintptr_t)p.x + (unsigned)(ca * 128 * 896), bw_ = (unsigned long long)(uintptr_t)p.wqkv + (unsigned)(56 * h - 8 * (h & 1));
    const char* gb = (const char*)(uintptr_t)(bx_ + ((bw_ - bx_) & mW));
    const unsigned kstep = 128 + iw * (64 * 2688 - 128), ldst = w * 4096 + iw * (WOFF + (w - 4) * 3072 - w * 4096);
    const unsigned smask = iw - 1u, l3 = iw ? (unsigned)M_FILL : ldst + 3072u;
    auto issue1 = [&](auto SL, auto II) {
      constexpr int sl = decltype(SL)::value, ii = decltype(II)::value;
      if constexpr (ii < 3) dma<sl * SLOT + ii * 1024, 0, false>(voff[ii], gb, ldst);
      else { dma<0, 0, false>(voff[3], gb, l3 + (smask & (unsigned)(sl * SLOT))); gb += kstep; }
    };
    auto issue = [&](auto SL) { issue1(SL, IC(0)); issue1(SL, IC(1)); issue1(SL, IC(2)); issue1(SL, IC(3)); };
    FS(1);
    issue(IC(0));
    RAWBAR(); issue(IC(1));
    {
      unsigned long long pc; asm volatile("s_getpc_b64 %0" : "=s"(pc));
      const unsigned long long a = (pc & ~127ull) + (unsigned long long)((r_ * 8 + w) * 128);
      if (lane == 0) asm volatile("s_mov_b32 m0, %1\n\ts_nop 0\n\tglobal_load_lds_dword %0, off" ::"v"(a), "i"(M_FILL) : "memory");
    }

    FS(2);
    const int ptq0 = (b < 224) ? b : 223, ptm = (ptq0 >= 112) ? 2 : 1, ptq = ptq0 - ((ptm == 2) ? 112 : 0);
    const int pts = (ptm == 1) ? ptq >> 3 : ptq >> 1, ptG = (ptm == 1) ? ptq & 7 : ptq & 1;
    auto pt_issue = [&]() {
      const unsigned stride = (ptm == 1) ? 3584u : 896u;
      const char* src = ((ptm == 1) ? p.w1 : p.w2) + (size_t)(32 * pts * stride + ptG * 448);
      const int wq = (w < 7) ? w : 6;
#pragma unroll
      for (int i = 0; i < 2; ++i) { const unsigned u = (unsigned)((2 * wq + i) * 64 + lane), row = (u * 9363u) >> 18, c16 = u - 28u * row;
        dma<M_PT, 0, false>(row * stride + c16 * 16u, src, (unsigned)((2 * wq + i) * 1024)); }
      if (lane == 63) asm volatile("s_mov_b32 m0, %2\n\ts_nop 0\n\tglobal_load_lds_dword %0, %1 sc1" ::"v"(0u), "s"(p.ws + WS_CTL), "i"(M_PF) : "memory");
    };
    const int tg = w >> 1, ng = w & 1;
    f32x4 acc[3][2], asum = {0, 0, 0, 0}, asq = {0, 0, 0, 0}, acs[3];
#pragma unroll
    for (int e = 0; e < 3; ++e) { acc[e][0] = f32x4{0, 0, 0, 0}; acc[e][1] = f32x4{0, 0, 0, 0}; acs[e] = f32x4{0, 0, 0, 0}; }
    const int xb = (32 * tg + 16 * ng + l15) * 128 + ((g ^ (l15 & 7)) << 4);
    int ab[3];
#pragma unroll
    for (int e = 0; e < 3; ++e) ab[e] = WOFF + (8 * g + q4) * 192 + ((((3 * ng + e) * 32) ^ ((g & 1) << 5))) + 8 * p4;
    bf16x8 xf[2][2];
    s16x4 al[2][3], ah[2][3];
    auto rdx = [&](int st, int buf, int t) { xf[buf][t] = ld128(lds + ((st >> 1) % 5) * SLOT + (xb ^ ((st & 1) << 6) ^ (t * 2048))); };
    auto rdl = [&](int st, int buf, int e) { al[buf][e] = ldtr(lds + ((st >> 1) % 5) * SLOT + ab[e] + (32 * (st & 1)) * 192); };
    auto rdh_ = [&](int st, int buf, int e) { ah[buf][e] = ldtr(lds + ((st >> 1) % 5) * SLOT + ab[e] + (32 * (st & 1) + 4) * 192); };
    auto rdf = [&](int st, int buf) { rdx(st, buf, 0); rdx(st, buf, 1);
#pragma unroll
      for (int e = 0; e < 3; ++e) { rdl(st, buf, e); rdh_(st, buf, e); } };
    bf16x8 onesf = __builtin_bit_cast(bf16x8, (u32x4{ONES, ONES, ONES, ONES}));
    KEEP(onesf); KEEP(asum); KEEP(asq);
#pragma unroll
    for (int e = 0; e < 3; ++e) { KEEP(acc[e][0]); KEEP(acc[e][1]); KEEP(acs[e]); }
    WAITV(5);
    BAR();
    FS(3);
    rdf(0, 0);
    sfor<0, 14>([&](auto ST) {
      constexpr int st = decltype(ST)::value, kc = st >> 1, cb = st & 1;
      if constexpr ((st & 1) && st < 13) {
        if constexpr (kc == 0) WAITV(5);
        if constexpr (kc == 0) {} else if constexpr (kc == 1) WAITV(9); else if constexpr (kc <= 3) WAITV(6); else if constexpr (kc == 4) WAITV(4); else WAITV(0);
        BARN();
      }
      if constexpr (st == 2) pt_issue();
      constexpr int nb = (st + 1) & 1; constexpr bool more = st < 13;
      const bf16x8 a0 = cat(al[cb][0], ah[cb][0]), a1 = cat(al[cb][1], ah[cb][1]), a2 = cat(al[cb][2], ah[cb][2]);
      SCHED();
      MACC(acc[0][0], a0, xf[cb][0]); if constexpr (more) rdx(st + 1, nb, 0);
      MACC(acc[0][1], a0, xf[cb][1]); if constexpr (more) rdl(st + 1, nb, 0);
      MACC(asum, onesf, xf[cb][0]); if constexpr (more) rdh_(st + 1, nb, 0);
      MACC(acs[0], a0, onesf);
      SCHED();
      if constexpr (st < 2) { issue1(IC(st + 2), IC(0)); issue1(IC(st + 2), IC(1)); }
      else if constexpr (kc + 3 <= 6) issue1(IC((kc + 3) % 5), IC(2 * (st & 1)));
      SCHED();
      MACC(acc[1][0], a1, xf[cb][0]); if constexpr (more) rdx(st + 1, nb, 1);
      MACC(acc[1][1], a1, xf[cb][1]); if constexpr (more) rdl(st + 1, nb, 1);
      MACC(asq, xf[cb][0], xf[cb][0]); if constexpr (more) rdh_(st + 1, nb, 1);
      MACC(acs[1], a1, onesf);
      SCHED();
      if constexpr (st < 2) { issue1(IC(st + 2), IC(2)); issue1(IC(st + 2), IC(3)); }
      else if constexpr (kc + 3 <= 6) issue1(IC((kc + 3) % 5), IC(2 * (st & 1) + 1));
      SCHED();
      MACC(acc[2][0], a2, xf[cb][0]); if constexpr (more) rdl(st + 1, nb, 2);
      MACC(acc[2][1], a2, xf[cb][1]); if constexpr (more) rdh_(st + 1, nb, 2);
      MACC(acs[2], a2, onesf);
      SCHED();
    });
    NOPS(); KEEP(asum); KEEP(asq);
#pragma unroll
    for (int e = 0; e < 3; ++e) { KEEP(acc[e][0]); KEEP(acc[e][1]); KEEP(acs[e]); }
    FS(4);
    STAMP(1);
    epoch = (unsigned)rfl((int)*(LAS unsigned*)(lds + M_PF + 252)) + 1u;
    {
      const unsigned ptb = (ptm == 1) ? WT_W1 : WT_W2, ptn = (ptm == 1) ? 14u : 56u;
      ldsp src = lds + M_PT + (4 * g + q4) * 448 + 8 * p4;
#pragma unroll
      for (int i = 0; i < 2; ++i) {
        const int t = (w + 8 * i < 14) ? w + 8 * i : w;
        const bf16x8 f = cat(ldtr(src + t * 32), ldtr(src + 16 * 448 + t * 32));
        st128_sc1(lane16 + ptb + ((unsigned)(14 * ptG + t) * ptn + (unsigned)pts) * 1024u, p.ws + WS_WT, __builtin_bit_cast(u32x4, f));
      }
    }
    {
      const float d01 = (p4 & 1) ? asq[1] : asq[0], d23 = (p4 & 1) ? asq[3] : asq[2], dq = (p4 & 2) ? d23 : d01;
      float mean = asum[0] * (1.f / 448.f), dq2 = dq;
      KEEP(mean); KEEP(dq2);
      if (g == q4) *(LAS f32x2*)(lds + M_LN + (32 * tg + 16 * ng + l15) * 8) = f32x2{mean, rsqrtf(fmaxf(dq2 * (1.f / 448.f) - mean * mean, 0.f) + 1e-5f)};
    }
    BAR();
    FS(5);
    float mu[2], rs[2];
#pragma unroll
    for (int t = 0; t < 2; ++t) { const f32x2 st_ = *(LAS f32x2*)(lds + M_LN + (32 * tg + 16 * (t ^ ng) + l15) * 8); mu[t] = st_[0]; rs[t] = st_[1]; }
    const bool pad0 = (h & 1) && g == 0, pad1 = !(h & 1) && g == 3;
    const int rsw = ((l15 >> 1) & 3) << 4, rowb = (32 * tg + l15) * 64;
    auto epi = [&](auto NG) {
      constexpr int ngc = decltype(NG)::value;
      sfor<0, 3>([&](auto E) {
        constexpr int e = decltype(E)::value, nt = 3 * ngc + e, which = nt >> 1, par = nt & 1;
        constexpr int ibase = (which == 0) ? QIMG : (which == 1) ? KIMG + 16384 : VIMG + 16384;
        const int off = (which == 2) ? ((((2 * par + (g >> 1)) << 4) ^ rsw) + (g & 1) * 8) : (((g << 4) ^ rsw) + par * 8);
        const bool pad = par ? pad1 : pad0;
#pragma unroll
        for (int t = 0; t < 2; ++t) {
          const float sc = (which == 0) ? rs[t] * 0.27264657f : rs[t], nm = -mu[t] * sc;
          f32x2 sc2 = {sc, sc}, nm2 = {nm, nm};
          KEEP(sc2); KEEP(nm2);
          const f32x2 v0 = lo2(acc[e][t]) * sc2 + lo2(acs[e]) * nm2, v1 = hi2(acc[e][t]) * sc2 + hi2(acs[e]) * nm2;
          u32x2 o; o.x = cvtpk(v0); o.y = cvtpk(v1);
          o.x = pad ? ((which == 2) ? 0x00003f80u : 0u) : o.x; o.y = pad ? 0u : o.y;
          *(LAS u32x2*)(lds + ibase + (t ^ ngc) * 1024 + rowb + off) = o;
        }
      });
    };
    if (ng) epi(IC(1)); else epi(IC(0));
    if (ca < 2) {
      const u32x4 z = {0, 0, 0, 0};
#pragma unroll
      for (int i = 0; i < 2; ++i) { *(LAS u32x4*)(lds + KIMG + tid * 16 + i * 8192) = z; *(LAS u32x4*)(lds + VIMG + tid * 16 + i * 8192) = z; }
    }
    BAR();
    FS(6);
    {
      const char* blob = p.ws + WS_KV + (size_t)(ca * 16 + h) * 16384;
      const u32x4 v0 = *(LAS u32x4*)(lds + KIMG + 16384 + tid * 16), v1 = *(LAS u32x4*)(lds + VIMG + 16384 + tid * 16);
      st128_sc1(tid * 16, blob, v0); st128_sc1(8192 + tid * 16, blob, v1);
    }
    FS(7);
    WAITV(0);
    BAR();
    if (tid == 0) { st32_sc1(ctl + FLAG_IDX(0, ca, h), epoch); st32_sc1(ctl + FLAG_IDX(4, b >> 4, b & 15), epoch); }
    FS(8);
    STAMP(2);
  }
  const int ns = j & 3, col4 = 128 * ns - (((ns >> 1) & ns) << 5), ngr = 4 - (ns >> 1);
  const int pr = w & 3, wh = w >> 2;
  const int prc = (pr < ngr) ? pr : 0;
  const int th5 = j >> 3, cs5 = j & 7, col0 = 64 * cs5 - 16 * ((cs5 >> 2) * (cs5 & 3)), nblk = 4 - (cs5 >> 2);
  const int ng5 = w & 1, kq = w >> 1;
  const char* wt = p.ws + WS_WT;
  bf16x8 wr[32];
  const int T1 = (448 * (j >> 2) + col4) / 16;
  const unsigned w1o = WT_W1 + (unsigned)(T1 + 2 * prc) * 14u * 1024u;
  const unsigned wro = lane16 + w1o;
  constexpr int WI = 32768;
  unsigned wb0, wb1;
  { const int rin = lane >> 4, pos = lane & 15;
    int blk = (pos >> 1) ^ rin; if (blk >= 2 * ngr) blk -= 2; wb0 = (unsigned)(blk * 32 + (pos & 1) * 16);
    blk = (pos >> 1) ^ (4 + rin); if (blk >= 2 * ngr) blk -= 2; wb1 = (unsigned)(blk * 32 + (pos & 1) * 16); }
  const char* wob = p.wo + col4 * 2;
  {
    if (w < 6) {
      const unsigned rb = (unsigned)(((lane >> 4) + 4 * (24 + w)) * 896) + ((w & 1) ? wb1 : wb0);
      sfor<0, 14>([&](auto I) { constexpr int i = decltype(I)::value; dma<i * 6144, i * 24 * 896, false>(rb, wob, (unsigned)(WI + (24 + w) * 1024)); });
    }
    int pos = lane & 15; if (pos >= 4 * ngr) pos = 4 * ngr - 1;
    int xch = (lane & 15) ^ (((lane >> 4) + 4 * w) & 15); if (xch >= 4 * ngr) xch = 0;
    dma<M_XT, 0, false>((unsigned)(((lane >> 4) + 4 * w) * 896 + xch * 16), p.x + (size_t)(128 * c + 32 * (j >> 2)) * 896 + col4 * 2, (unsigned)(w * 1024));
    if (w == 5 && lane < 16) dma<M_B1, 0, false>((unsigned)(pos * 16), p.b1 + (448 * (j >> 2) + col4) * 2, 0u);
    if (w == 4 && lane < 8) dma<M_B2, 0, false>((unsigned)((lane < 2 * nblk ? lane : 0) * 16), p.b2 + col0 * 2, 0u);
  }
  {
    const int h = ha;
    FS(9);
    const int sw = (l15 >> 1) & 3;
    if (w == 7 && ca >= 1) poll_flags(ctl, (unsigned)(FLAG_IDX(0, (ca >= 2 && (lane & 1)) ? ca - 2 : ca - 1, h) * 4), epoch);
    if (w == 6) {
      const unsigned tfo = (unsigned)(FLAG_IDX(4, 4 * g, l15) * 4);
#pragma unroll 1
      for (unsigned spins = 0; spins < SPIN_CAP; ++spins) {
        unsigned t0, t1, t2, t3;
        asm volatile("global_load_dword %0, %4, %5 sc1\n\tglobal_load_dword %1, %4, %5 offset:2048 sc1\n\tglobal_load_dword %2, %6, %5 sc1\n\tglobal_load_dword %3, %6, %5 offset:2048 sc1\n\ts_waitcnt vmcnt(0)"
                     : "=&v"(t0), "=&v"(t1), "=&v"(t2), "=&v"(t3) : "v"(tfo), "s"(ctl), "v"(tfo + 4096u) : "memory");
        if (__all(t0 == epoch && t1 == epoch && t2 == epoch && t3 == epoch)) break;
        __builtin_amdgcn_s_sleep(2);
      }
      if (b == 0 && lane == 0) st32_sc1(ctl, epoch);
    }
    BAR();
    FS(10);
    {
      const int tl = w >> 2, src = ca - 2 + tl, pc = (w & 3) * 2048;
      if (src >= 0) {
        const char* blob = p.ws + WS_KV + (size_t)(src * 16 + h) * 16384 + pc;
        dma2c_sc1<KIMG>(lane16, blob, (unsigned)(tl * 8192 + pc)); dma2c_sc1<VIMG>(lane16, blob + 8192, (unsigned)(tl * 8192 + pc));
      }
    }
    const bf16x8 qf = ld128(lds + QIMG + (16 * w + l15) * 64 + ((g ^ sw) << 4));
    WAITV(0);
    BAR();
    FS(11);
    STAMP(3);
    STAMP(4);
    if (w >= 4) ldfrags<7>(wr, wro, wt);
    f32x4 sc[17];
    const float NEG = -__builtin_inff();
    ldsp kb = lds + KIMG + (16 * w + l15) * 64 + ((g ^ sw) << 4);
    {
      bf16x8 kf[2][6];
#pragma unroll
      for (int u = 0; u < 6; ++u) kf[0][u] = ld128(kb + u * 1024);
#pragma unroll
      for (int bt = 0; bt < 3; ++bt) {
        if (bt < 2) {
#pragma unroll
          for (int u = 0; u < 6; ++u) if (6 * (bt + 1) + u < 17) kf[(bt + 1) & 1][u] = ld128(kb + (6 * (bt + 1) + u) * 1024);
        }
        SCHED();
#pragma unroll
        for (int u = 0; u < 6; ++u) if (6 * bt + u < 17) sc[6 * bt + u] = MFMA(kf[bt & 1][u], qf, (f32x4{0, 0, 0, 0}));
        SCHED();
      }
    }
    FS(12);
    if (w >= 4) ldfrags<7>(wr + 7, wro + 7 * 1024, wt);
    const int vsw = (2 * g + (q4 >> 1)) & 3;
    ldsp vb0 = lds + VIMG + (16 * w + 4 * g + q4) * 64 + (((p4 >> 1) ^ vsw) << 4) + (p4 & 1) * 8;
    ldsp vb1 = lds + VIMG + (16 * w + 4 * g + q4) * 64 + (((2 + (p4 >> 1)) ^ vsw) << 4) + (p4 & 1) * 8;
    bf16x8 vf[2][3][2];
    auto rdv = [&](int bt) {
#pragma unroll
      for (int i = 0; i < 3; ++i) { const int uu = 3 * bt + i, ua = 2 * uu, ub = (uu < 8) ? 2 * uu + 1 : 15;
        vf[bt & 1][i][0] = cat(ldtr(vb0 + ua * 1024), ldtr(vb0 + ub * 1024)); vf[bt & 1][i][1] = cat(ldtr(vb1 + ua * 1024), ldtr(vb1 + ub * 1024)); }
    };
    rdv(0);
    SCHED();
    if (ca < 2) {
      const int tmin = (2 - ca) * 8;
#pragma unroll
      for (int u = 0; u < 16; ++u) if (w + u < tmin) sc[u] = f32x4{NEG, NEG, NEG, NEG};
    }
#pragma unroll
    for (int r = 0; r < 4; ++r) { sc[0][r] = (4 * g + r <= l15) ? NEG : sc[0][r]; sc[16][r] = (4 * g + r > l15) ? NEG : sc[16][r]; }
    float m = max3(sc[0][0], sc[0][1], sc[0][2]), m2 = max3(sc[0][3], sc[1][0], sc[1][1]);
    m = max3(m, sc[1][2], sc[1][3]);
#pragma unroll
    for (int u = 2; u < 17; ++u) { if (u & 1) m = max3(max3(m, sc[u][0], sc[u][1]), sc[u][2], sc[u][3]); else m2 = max3(max3(m2, sc[u][0], sc[u][1]), sc[u][2], sc[u][3]); }
    m = max3(m, m2, m2);
    m = red_g_max(m);
    FS(13);
    SCHED();
    if (w >= 4) ldfrags<7>(wr + 14, wro + 14 * 1024, wt);
    unsigned pk[17][2];
    f32x2 mm = {-m, -m};
    KEEP(mm);
    f32x4 oa[2] = {f32x4{0, 0, 0, 0}, f32x4{0, 0, 0, 0}};
    KEEP(oa[0]); KEEP(oa[1]);
#pragma unroll
    for (int bt = 0; bt < 3; ++bt) {
#pragma unroll
      for (int u = 6 * bt; u < 6 * bt + 6 && u < 17; ++u) {
        const f32x2 d0 = lo2(sc[u]) + mm, d1 = hi2(sc[u]) + mm;
        pk[u][0] = cvtpk(__builtin_amdgcn_exp2f(d0[0]), __builtin_amdgcn_exp2f(d0[1]));
        pk[u][1] = cvtpk(__builtin_amdgcn_exp2f(d1[0]), __builtin_amdgcn_exp2f(d1[1]));
      }
      if (bt < 2) rdv(bt + 1);
      if (bt == 0 && w >= 4) ldfrags<7>(wr + 21, wro + 21 * 1024, wt);
      SCHED();
#pragma unroll
      for (int i = 0; i < 3; ++i) {
        const int uu = 3 * bt + i;
        u32x4 pu; pu.x = pk[2 * uu][0]; pu.y = pk[2 * uu][1];
        if (uu < 8) { pu.z = pk[2 * uu + 1][0]; pu.w = pk[2 * uu + 1][1]; } else { pu.z = 0; pu.w = 0; }
        const bf16x8 pf = __builtin_bit_cast(bf16x8, pu);
        MACCN(oa[0], vf[bt & 1][i][0], pf); MACC(oa[1], vf[bt & 1][i][1], pf);
      }
      SCHED();
    }
    NOPS(); KEEP(oa[0]); KEEP(oa[1]);
    FS(15);
    int tz2 = tid; asm volatile("" : "+v"(tz2));
    const int l15b = tz2 & 15, gb = (tz2 & 63) >> 4, swb = (l15b >> 1) & 3;
    const float lsel = (h & 1) ? oa[0][0] : oa[1][0];
    const float l = __uint_as_float((unsigned)__builtin_amdgcn_ds_bpermute((((h & 1) ? 0 : 48) + l15b) * 4, (int)__float_as_uint(lsel)));
    const float inv1 = __builtin_amdgcn_rcpf(l);
    f32x2 inv = {inv1, inv1};
    KEEP(inv);
    u32x4 ov;
    ov.x = cvtpk(lo2(oa[0]) * inv); ov.y = cvtpk(hi2(oa[0]) * inv); ov.z = cvtpk(lo2(oa[1]) * inv); ov.w = cvtpk(hi2(oa[1]) * inv);
    { const bool z0 = (h & 1) && gb == 0, z1 = !(h & 1) && gb == 3; ov.x = z0 ? 0u : ov.x; ov.y = z0 ? 0u : ov.y; ov.z = z1 ? 0u : ov.z; ov.w = z1 ? 0u : ov.w; }
    st128_sc1((unsigned)((16 * w + l15b) * 64 + ((gb ^ swb) << 4)), p.ws + WS_O + (size_t)(ca * 16 + h) * 8192, ov);
    FS(16);
    BAR();
    {
      const unsigned rb = (unsigned)(((lane >> 4) + 12 * w) * 896), va = rb + ((w & 1) ? wb1 : wb0), vb = rb + ((w & 1) ? wb0 : wb1), lb = (unsigned)(WI + 3 * w * 1024);
      dma<0, 0, false>(va, wob, lb); dma<1024, 4 * 896, false>(vb, wob, lb); dma<2048, 8 * 896, false>(va, wob, lb);
      const int ins = (w < 4) ? 108 + w : 3 * w + 2;
      dma<0, 0, false>((unsigned)(((lane >> 4) + 4 * ins) * 896) + ((ins & 1) ? wb1 : wb0), wob, (unsigned)(WI + ins * 1024));
    }
    WAITV(4);
    BAR();
    if (tid == 0) st32_sc1(ctl + FLAG_IDX(1, ca, h), epoch);
    FS(18);
    STAMP(5);
  }
  {
    LANEVARS();
    constexpr int OT = 0;
    const int tq = j >> 2;
    WAITV(0);
    BAR();
    if (w < 4) {
      const int rx = 4 * (g & 1) + q4;
      ldsp ab0 = lds + WI + (4 * g + q4) * 256 + (((2 * prc) ^ rx) << 5) + 8 * p4;
      ldsp ab1 = lds + WI + (4 * g + q4) * 256 + (((2 * prc + 1) ^ rx) << 5) + 8 * p4;
      ldsp ab0h = ab0 + 65536, ab1h = ab1 + 65536;
#pragma unroll
      for (int s = 0; s < 16; ++s) {
        const int rb = (28 * s - 4 * (s & 1)) * 256;
        if (rb + 4096 < 65536) { wr[s] = cat(ldtr(ab0 + rb), ldtr(ab0 + rb + 4096)); wr[16 + s] = cat(ldtr(ab1 + rb), ldtr(ab1 + rb + 4096)); }
        else { wr[s] = cat(ldtr(ab0h + (rb - 65536)), ldtr(ab0h + (rb - 65536 + 4096))); wr[16 + s] = cat(ldtr(ab1h + (rb - 65536)), ldtr(ab1h + (rb - 65536 + 4096))); }
      }
    } else {
      __builtin_amdgcn_s_setprio(3);
      poll_flags(ctl, (unsigned)(FLAG_IDX(1, c, l15) * 4), epoch);
      const char* ob_ = p.ws + WS_O + (size_t)(c * 16 + 4 * (w - 4)) * 8192 + tq * 2048;
      const unsigned lb = (unsigned)(OT + (w - 4) * 8192);
      dma2c_sc1<0>(lane16, ob_, lb); dma2c_sc1<2048>(lane16 + 8192, ob_, lb); dma2c_sc1<4096>(lane16 + 16384, ob_, lb); dma2c_sc1<6144>(lane16 + 24576, ob_, lb);
      __builtin_amdgcn_s_setprio(0);
    }
    FS(19);
    WAITV(0);
    BAR();
    FS(20);
    STAMP(6);
    STAMP(7);
    if (w < 4) {
      f32x4 a0[2], a1[2];
#pragma unroll
      for (int t = 0; t < 2; ++t) { a0[t] = f32x4{0, 0, 0, 0}; a1[t] = f32x4{0, 0, 0, 0}; }
      ldsp ob = lds + OT + l15 * 64 + ((g ^ ((l15 >> 1) & 3)) << 4);
      u32x2 xr[2][2];
#pragma unroll
      for (int t = 0; t < 2; ++t) { ldsp xp = lds + M_XT + (16 * t + l15) * 256 + (g & 1) * 8;
        xr[t][0] = *(LAS u32x2*)(xp + (((4 * prc + (g >> 1)) ^ l15) << 4)); xr[t][1] = *(LAS u32x2*)(xp + (((4 * prc + 2 + (g >> 1)) ^ l15) << 4)); }
#pragma unroll
      for (int t = 0; t < 2; ++t) { KEEP(a0[t]); KEEP(a1[t]); }
      bf16x8 of[4][2];
#pragma unroll
      for (int s = 0; s < 3; ++s) { of[s][0] = ld128(ob + s * 2048); of[s][1] = ld128(ob + s * 2048 + 1024); }
#pragma unroll
      for (int s = 0; s < 16; ++s) {
        if (s + 3 < 16) { of[(s + 3) & 3][0] = ld128(ob + (s + 3) * 2048); of[(s + 3) & 3][1] = ld128(ob + (s + 3) * 2048 + 1024); }
        SCHED();
#pragma unroll
        for (int t = 0; t < 2; ++t) { MACC(a0[t], wr[s], of[s & 3][t]); MACC(a1[t], wr[16 + s], of[s & 3][t]); }
        SCHED();
      }
      NOPS();
#pragma unroll
      for (int t = 0; t < 2; ++t) { KEEP(a0[t]); KEEP(a1[t]); }
      FS(21);
      const unsigned ho = (unsigned)((128 * c + 32 * tq + l15) * 896 + (col4 / 32 + pr) * 64 + g * 16);
#pragma unroll
      for (int t = 0; t < 2; ++t) {
        u32x4 hv;
        hv.x = cvtpk(lo2(a0[t]) + bf2(xr[t][0].x)); hv.y = cvtpk(hi2(a0[t]) + bf2(xr[t][0].y));
        hv.z = cvtpk(lo2(a1[t]) + bf2(xr[t][1].x)); hv.w = cvtpk(hi2(a1[t]) + bf2(xr[t][1].y));
        if (pr < ngr) st128_sc1(ho + t * 16 * 896, p.ws + WS_H, hv);
        float s1 = 0.f, s2 = 0.f;
#pragma unroll
        for (int i = 0; i < 4; ++i) { s1 = dot2(hv[i], ONES, s1); s2 = dot2(hv[i], hv[i], s2); }
        s1 = red_g_sum(s1); s2 = red_g_sum(s2);
        if (pr >= ngr) { s1 = 0.f; s2 = 0.f; }
        if (g == 0) ((LAS f32x2*)(lds + M_PART))[(16 * t + l15) * 4 + pr] = f32x2{s1, s2};
      }
    } else {
      pin<28>(wr);
#pragma unroll
      for (int i = 0; i < 28; ++i) *(LAS bf16x8*)(lds + 32768 + (w - 4) * 28672 + i * 1024 + lane * 16) = wr[i];
    }
    FS(22);
    BAR();
    if (tid < 32) {
      const f32x4 u0 = *(LAS f32x4*)(lds + M_PART + tid * 32), u1 = *(LAS f32x4*)(lds + M_PART + tid * 32 + 16);
      st64_sc1((unsigned)((128 * c + 32 * tq + tid) * 32 + ns * 8), p.ws + WS_ST,
               u32x2{__float_as_uint((u0[0] + u0[2]) + (u1[0] + u1[2])), __float_as_uint((u0[1] + u0[3]) + (u1[1] + u1[3]))});
    }
    if (w < 4) {
#pragma unroll
      for (int i = 0; i < 28; ++i) wr[i] = ld128(lds + 32768 + w * 28672 + i * 1024 + lane * 16);
    }
    FS(23);
    if (w < 4) WAITV(0);
    BAR();
    if (tid == 0) st32_sc1(ctl + FLAG_IDX(2, c, j), epoch);
    FS(25);
    STAMP(8);
  }
  {
    LANEVARS();
    const int f0_ = 448 * (j >> 2) + col4, th = wh;
    FS(26);
    f32x4 c0 = {0, 0, 0, 0}, c1 = {0, 0, 0, 0};
    pin<28>(wr);
    bf16x8 onesf = __builtin_bit_cast(bf16x8, (u32x4{ONES, ONES, ONES, ONES}));
    KEEP(onesf); KEEP(c0); KEEP(c1);
    asm volatile("s_nop 1");
#pragma unroll
    for (int s = 0; s < 14; ++s) { MACC(c0, wr[s], onesf); MACC(c1, wr[14 + s], onesf); }
    NOPS(); KEEP(c0); KEEP(c1);
    SCHED();
    if (w == 7) poll_flags(ctl, (unsigned)(FLAG_IDX(2, c, l15) * 4), epoch);
    FS(29);
    BAR();
    const unsigned vo = (unsigned)((16 * w + (lane >> 3)) * 896) + x8, wl2k = (unsigned)(w * 2048);
    const char* hb = p.ws + WS_H + (size_t)c * 128 * 896;
    auto issue4 = [&](auto KC) { constexpr int kc = decltype(KC)::value; dma<kc * 16384, kc * 128, true>(vo, hb, wl2k); dma<kc * 16384 + 1024, kc * 128 + 8 * 896, true>(vo, hb, wl2k); };
    if (lane < 32) dma<M_TILE, 0, true>(lane16, p.ws + WS_ST + (size_t)(128 * c + 16 * w) * 32, (unsigned)(w * 512));
    issue4(IC(0)); RAWBAR(); issue4(IC(1));
    FS(31);
    f32x4 acc[2][4];
#pragma unroll
    for (int t = 0; t < 4; ++t) { acc[0][t] = f32x4{0, 0, 0, 0}; acc[1][t] = f32x4{0, 0, 0, 0}; }
    const int xb = (64 * th + l15) * 128 + ((g ^ (l15 & 7)) << 4);
    bf16x8 xf[2][4];
    auto rd1 = [&](int st, int buf, int t) { xf[buf][t] = ld128(lds + (st >> 1) * 16384 + (xb ^ ((st & 1) << 6)) + t * 2048); };
    auto rdf = [&](int st, int buf) { rd1(st, buf, 0); rd1(st, buf, 1); rd1(st, buf, 2); rd1(st, buf, 3); };
#pragma unroll
    for (int t = 0; t < 4; ++t) { KEEP(acc[0][t]); KEEP(acc[1][t]); }
    bf16x8 bf[2][14];
    const unsigned w2o0 = WT_W2 + ((unsigned)(col0 / 16 + 2 * ng5) * 56u + 14u * kq) * 1024u, w2o1 = (2 * ng5 + 1 < nblk) ? w2o0 + 56u * 1024u : w2o0;
    WAITV(2); BAR();
    FS(32);
    STAMP(9);
    STAMP(10);
    rdf(0, 0);
    sfor<0, 14>([&](auto ST) {
      constexpr int st = decltype(ST)::value;
      if constexpr (st == 1) WAITV(2); if constexpr (st == 3) WAITV(4); if constexpr (st == 5) WAITV(6); if constexpr (st == 7) WAITV(8); if constexpr (st == 9) WAITV(10); if constexpr (st == 11) WAITV(12);
      if constexpr ((st & 1) && st < 13) BARN();
      if constexpr (st == 2) {
        if (tid < 128) {
          const f32x4 u0 = *(LAS f32x4*)(lds + M_TILE + tid * 32), u1 = *(LAS f32x4*)(lds + M_TILE + tid * 32 + 16);
          float mean = ((u0[0] + u0[2]) + (u1[0] + u1[2])) * (1.f / 448.f), sq = (u0[1] + u0[3]) + (u1[1] + u1[3]);
          KEEP(mean); KEEP(sq);
          const float rstd = rsqrtf(fmaxf(sq * (1.f / 448.f) - mean * mean, 0.f) + 1e-5f);
          *(LAS f32x2*)(lds + M_LN + tid * 8) = f32x2{rstd, -mean * rstd};
        }
      }
      SCHED();
#pragma unroll
      for (int t = 0; t < 2; ++t) { MACC(acc[0][t], wr[st], xf[st & 1][t]); if constexpr (st < 13) rd1(st + 1, (st + 1) & 1, t); MACC(acc[1][t], wr[14 + st], xf[st & 1][t]); }
      SCHED();
      if constexpr (st <= 4) issue4(IC(st + 2));
      if constexpr (st >= 5) { constexpr int s = st - 5; ldfrags<1>(&bf[0][s], lane16 + w2o0 + s * 1024, wt); ldfrags<1>(&bf[1][s], lane16 + w2o1 + s * 1024, wt); }
      SCHED();
#pragma unroll
      for (int t = 2; t < 4; ++t) { MACC(acc[0][t], wr[st], xf[st & 1][t]); if constexpr (st < 13) rd1(st + 1, (st + 1) & 1, t); MACC(acc[1][t], wr[14 + st], xf[st & 1][t]); }
      SCHED();
    });
    NOPS();
#pragma unroll
    for (int t = 0; t < 4; ++t) { KEEP(acc[0][t]); KEEP(acc[1][t]); }
    FS(33);
    STAMP(11);
    {
      const u32x2 bb0 = *(LAS u32x2*)(lds + M_B1 + (32 * prc + 4 * g) * 2), bb1 = *(LAS u32x2*)(lds + M_B1 + (32 * prc + 4 * g) * 2 + 32);
      const f32x2 b00 = bf2(bb0.x), b01 = bf2(bb0.y), b10 = bf2(bb1.x), b11 = bf2(bb1.y);
      const unsigned ho = (unsigned)((128 * c + 64 * th + l15) * 3584 + (f0_ / 32 + pr) * 64 + g * 16);
      f32x2 ln[4];
#pragma unroll
      for (int t = 0; t < 4; ++t) ln[t] = *(LAS f32x2*)(lds + M_LN + (64 * th + 16 * t + l15) * 8);
#pragma unroll
      for (int t = 0; t < 4; ++t) {
        const float a = ln[t][0], nm = ln[t][1];
        f32x2 aa = {a, a}, nn = {nm, nm};
        KEEP(aa); KEEP(nn);
        f32x2 z[4] = {lo2(acc[0][t]) * aa + (lo2(c0) * nn + b00), hi2(acc[0][t]) * aa + (hi2(c0) * nn + b01), lo2(acc[1][t]) * aa + (lo2(c1) * nn + b10), hi2(acc[1][t]) * aa + (hi2(c1) * nn + b11)};
        u32x4 hv;
#pragma unroll
        for (int i = 0; i < 4; ++i) {
          f32x2 r = {__builtin_elementwise_maximum(z[i][0], 0.f), __builtin_elementwise_maximum(z[i][1], 0.f)};
          hv[i] = cvtpk(r * r);
        }
        if (pr < ngr) st128_sc1(ho + t * 16 * 3584, p.ws + WS_H1, hv);
      }
    }
    FS(34);
    WAITV(0);
    BAR();
    if (tid == 0) st32_sc1(ctl + FLAG_IDX(3, c, j), epoch);
    FS(38);
    STAMP(12);
    if (w != 7) {
      ldfrags<5>(&bf[0][9], lane16 + w2o0 + 9 * 1024, wt); ldfrags<5>(&bf[1][9], lane16 + w2o1 + 9 * 1024, wt);
    }
    if (w >= 5 && w < 7) {
      const int m0_ = col0 >> 5;
      const char* hr = p.ws + WS_H + (size_t)(128 * c + 64 * th5) * 896;
      sfor<0, 6>([&](auto I) { constexpr int i = decltype(I)::value;
        const unsigned u = (unsigned)((w - 5) * 384 + i * 64 + lane), r0 = (u * 43691u) >> 19, c0_ = u - 12u * r0; unsigned cc0 = m0_ * 4 + c0_; if (cc0 > 55) cc0 = 55;
        dma<M_TILE + i * 1024, 0, true>(r0 * 896 + cc0 * 16, hr, (unsigned)((w - 5) * 6144)); });
    }
    const unsigned vo5 = (unsigned)((32 * ng5 + (lane >> 3)) * 3584 + kq * 896) + x8, wl4k = (unsigned)(w * 4096);
    const char* hb5 = p.ws + WS_H1 + (size_t)(128 * c + 64 * th5) * 3584;
    auto issue5 = [&](auto I) {
      constexpr int i = decltype(I)::value, sa = (i & 3) * 32768;
      sfor<0, 4>([&](auto II) { constexpr int ii = decltype(II)::value; dma<sa + ii * 1024, i * 128 + ii * 8 * 3584, true>(vo5, hb5, wl4k); });
    };
    FS(39);
    if (w == 7) {
      poll_flags(ctl, (unsigned)(FLAG_IDX(3, c, l15) * 4), epoch);
    }
    FS(40);
    BAR();
    FS(41);
    STAMP(13);
    issue5(IC(0));
    if (w == 7) {
      ldfrags<5>(&bf[0][9], lane16 + w2o0 + 9 * 1024, wt); ldfrags<5>(&bf[1][9], lane16 + w2o1 + 9 * 1024, wt);
    }
    FS(42);
    f32x4 ac5[2][4];
#pragma unroll
    for (int t = 0; t < 4; ++t) { ac5[0][t] = f32x4{0, 0, 0, 0}; ac5[1][t] = f32x4{0, 0, 0, 0}; }
    int yb[4];
#pragma unroll
    for (int t = 0; t < 4; ++t) yb[t] = kq * 8192 + (16 * ((t + kq) & 3) + l15) * 128 + ((g ^ (l15 & 7)) << 4);
    bf16x8 yf[2][4];
    auto rz1 = [&](int st, int buf, int t) { yf[buf][t] = ld128(lds + ((st >> 1) & 3) * 32768 + (yb[t] ^ ((st & 1) << 6))); };
    auto rdy = [&](int st, int buf) { rz1(st, buf, 0); rz1(st, buf, 1); rz1(st, buf, 2); rz1(st, buf, 3); };
#pragma unroll
    for (int t = 0; t < 4; ++t) { KEEP(ac5[0][t]); KEEP(ac5[1][t]); }
    WAITV(0);
    pin<14>(bf[0]); pin<14>(bf[1]);
    BAR();
    FS(43);
    STAMP(14);
    rdy(0, 0);
    sfor<0, 14>([&](auto ST) {
      constexpr int st = decltype(ST)::value;
      if constexpr ((st & 1) && st < 13) {
        if constexpr (st < 11) WAITV(4); else WAITV(0);
        BARN();
      }
      SCHED();
#pragma unroll
      for (int t = 0; t < 2; ++t) { MACC(ac5[0][t], bf[0][st], yf[st & 1][t]); if constexpr (st < 13) rz1(st + 1, (st + 1) & 1, t); MACC(ac5[1][t], bf[1][st], yf[st & 1][t]); }
      SCHED();
      if constexpr (st == 0) { issue5(IC(1)); issue5(IC(2)); }
      if constexpr ((st & 1) && st <= 7) issue5(IC((st + 5) / 2));
      SCHED();
#pragma unroll
      for (int t = 2; t < 4; ++t) { MACC(ac5[0][t], bf[0][st], yf[st & 1][t]); if constexpr (st < 13) rz1(st + 1, (st + 1) & 1, t); MACC(ac5[1][t], bf[1][st], yf[st & 1][t]); }
      SCHED();
    });
    NOPS();
#pragma unroll
    for (int t = 0; t < 4; ++t) { KEEP(ac5[0][t]); KEEP(ac5[1][t]); }
    FS(44);
    BAR();
#pragma unroll
    for (int t = 1; t < 4; ++t) {
      ldsp d = lds + ((((t + kq) & 3) * 3 + (t - 1)) * 2 + ng5) * 2048 + lane * 16;
      *(LAS f32x4*)d = ac5[0][t]; *(LAS f32x4*)(d + 1024) = ac5[1][t];
    }
    const int tokl = 16 * kq + l15;
    const int t0 = col0 / 16 + 2 * ng5, t1 = (2 * ng5 + 1 < nblk) ? t0 + 1 : t0, m0_ = col0 >> 5;
    const u32x2 hr0 = *(LAS u32x2*)(lds + M_TILE + tokl * 192 + ((t0 >> 1) - m0_) * 64 + g * 16 + (t0 & 1) * 8);
    const u32x2 hr1 = *(LAS u32x2*)(lds + M_TILE + tokl * 192 + ((t1 >> 1) - m0_) * 64 + g * 16 + (t1 & 1) * 8);
    const u32x2 b20 = *(LAS u32x2*)(lds + M_B2 + (32 * ng5 + 4 * g) * 2), b21 = *(LAS u32x2*)(lds + M_B2 + ((2 * ng5 + 1 < nblk) ? (32 * ng5 + 16) : 32 * ng5) * 2 + 8 * g);
    f32x2 r00 = lo2(ac5[0][0]) + (bf2(b20.x) + bf2(hr0.x)), r01 = hi2(ac5[0][0]) + (bf2(b20.y) + bf2(hr0.y));
    f32x2 r10 = lo2(ac5[1][0]) + (bf2(b21.x) + bf2(hr1.x)), r11 = hi2(ac5[1][0]) + (bf2(b21.y) + bf2(hr1.y));
    BAR();
    FS(45);
#pragma unroll
    for (int t = 1; t < 4; ++t) {
      ldsp d = lds + ((kq * 3 + (t - 1)) * 2 + ng5) * 2048 + lane * 16;
      const f32x4 u0 = *(LAS f32x4*)d, u1 = *(LAS f32x4*)(d + 1024);
      r00 += lo2(u0); r01 += hi2(u0); r10 += lo2(u1); r11 += hi2(u1);
    }
    GAS char* op = (GAS char*)p.out + (size_t)(128 * c + 64 * th5 + tokl) * 896 + (col0 + 32 * ng5 + 4 * g) * 2;
    *(GAS u32x2*)op = u32x2{cvtpk(r00), cvtpk(r01)};
    if (2 * ng5 + 1 < nblk) *(GAS u32x2*)(op + 32) = u32x2{cvtpk(r10), cvtpk(r11)};
    STAMP(15);
    FS(63);
  }
}

extern "C" void kernel_launch(void* const* d_in, const int* in_sizes, int n_in, void* d_out, int out_size, void* d_ws, size_t ws_size, hipStream_t stream) {
  Params p;
  p.x = (const char*)d_in[0]; p.wqkv = (const char*)d_in[1]; p.wo = (const char*)d_in[2];
  p.w1 = (const char*)d_in[4]; p.b1 = (const char*)d_in[5]; p.w2 = (const char*)d_in[6]; p.b2 = (const char*)d_in[7];
  p.out = (char*)d_out; p.ws = (char*)d_ws + (((ws_size / 2)) & ~(size_t)0x1fffff);
  (void)hipFuncSetAttribute((const void*)fused_block, hipFuncAttributeMaxDynamicSharedMemorySize, LDS_BYTES);
  hipLaunchKernelGGL(fused_block, dim3(256), dim3(512), LDS_BYTES, stream, p);
}
```

```cpp
#define MAPA
#define E1_CODE
#include <hip/hip_runtime.h>
#include <hip/hip_bf16.h>
#include <stdint.h>
#include <type_traits>

#define LAS __attribute__((address_space(3)))
#define GAS __attribute__((address_space(1)))
typedef short bf16x8 __attribute__((ext_vector_type(8)));
typedef short s16x4 __attribute__((ext_vector_type(4)));
typedef short v4i16_t __attribute__((ext_vector_type(4)));
typedef float f32x4 __attribute__((ext_vector_type(4)));
typedef float f32x2 __attribute__((ext_vector_type(2)));
typedef unsigned u32x4 __attribute__((ext_vector_type(4)));
typedef unsigned u32x2 __attribute__((ext_vector_type(2)));
typedef __bf16 bf16x2_t __attribute__((ext_vector_type(2)));
typedef LAS char* ldsp;

constexpr int SEQ = 2048;
constexpr size_t WS_CTL = 0;
constexpr size_t WS_KV = 262144;
constexpr size_t WS_O = WS_KV + 256 * 16384;
constexpr size_t WS_H = WS_O + 256 * 8192;
constexpr size_t WS_H1 = WS_H + (size_t)SEQ * 896;
constexpr size_t WS_ST = WS_H1 + (size_t)SEQ * 3584;
constexpr size_t WS_WT = WS_ST + (size_t)SEQ * 32;
constexpr unsigned WT_W1 = 0, WT_W2 = WT_W1 + 1568 * 1024, WT_BYTES = WT_W2 + 1568 * 1024;
constexpr size_t WS_DBG = WS_WT + WT_BYTES;
constexpr int LDS_BYTES = 160 * 1024;
constexpr int M_PT = 143360;
constexpr int M_XT = 147456;
constexpr int M_TILE = 131072;
constexpr int MISC = 157696;
constexpr int M_LN = MISC, M_B1 = MISC + 1024, M_B2 = MISC + 1280, M_PF = MISC + 1536, M_PART = MISC + 2048, M_FILL = MISC + 3072;
constexpr unsigned SPIN_CAP = 1u << 16;
constexpr unsigned CODE_LINES = 208;

#define FLAG_IDX(ph, c, j) (64 + (((ph) * 16 + (c)) * 16 + (j)) * 32)

#define WAITV(n) asm volatile("s_waitcnt vmcnt(" #n ")" ::: "memory")
#define BAR() do { asm volatile("s_waitcnt lgkmcnt(0)" ::: "memory"); __builtin_amdgcn_s_barrier(); asm volatile("" ::: "memory"); } while (0)
#define BARN() do { asm volatile("" ::: "memory"); __builtin_amdgcn_s_barrier(); asm volatile("" ::: "memory"); } while (0)
#define SCHED() __builtin_amdgcn_sched_barrier(0)
#define RAWBAR() __builtin_amdgcn_s_barrier()
#define ILV(n) do { _Pragma("unroll") for (int i_ = 0; i_ < (n); ++i_) { __builtin_amdgcn_sched_group_barrier(0x008, 1, 0); __builtin_amdgcn_sched_group_barrier(0x100, 1, 0); } } while (0)
#define MFMA(a, b, c) __builtin_amdgcn_mfma_f32_16x16x32_bf16((a), (b), (c), 0, 0, 0)
#define MACC(acc, a, b) asm volatile("v_mfma_f32_16x16x32_bf16 %0, %1, %2, %0" : "+v"(acc) : "v"(a), "v"(b))
#define MACCN(acc, a, b) asm volatile("s_nop 1\n\tv_mfma_f32_16x16x32_bf16 %0, %1, %2, %0" : "+v"(acc) : "v"(a), "v"(b))
#define NOPS() asm volatile("s_nop 7\n\ts_nop 3")
#define KEEP(x) asm volatile("" : "+v"(x))
#define IC(x) std::integral_constant<int, (x)>{}

template <int I, int N, class F> __device__ __forceinline__ void sfor(F&& f) { if constexpr (I < N) { f(std::integral_constant<int, I>{}); sfor<I + 1, N>(f); } }

__device__ __forceinline__ unsigned cvtpk(float lo, float hi) { f32x2 v = {lo, hi}; bf16x2_t b = __builtin_convertvector(v, bf16x2_t); return __builtin_bit_cast(unsigned, b); }
__device__ __forceinline__ unsigned cvtpk(f32x2 v) { bf16x2_t b = __builtin_convertvector(v, bf16x2_t); return __builtin_bit_cast(unsigned, b); }
__device__ __forceinline__ float dot2(unsigned a, unsigned b, float c) { return __builtin_amdgcn_fdot2_f32_bf16(__builtin_bit_cast(bf16x2_t, a), __builtin_bit_cast(bf16x2_t, b), c, false); }
__device__ __forceinline__ float bflo(unsigned u) { return __uint_as_float(u << 16); }
__device__ __forceinline__ float bfhi(unsigned u) { return __uint_as_float(u & 0xffff0000u); }
__device__ __forceinline__ f32x2 bf2(unsigned u) { return f32x2{bflo(u), bfhi(u)}; }
__device__ __forceinline__ f32x2 lo2(f32x4 v) { return f32x2{v[0], v[1]}; }
__device__ __forceinline__ f32x2 hi2(f32x4 v) { return f32x2{v[2], v[3]}; }
__device__ __forceinline__ bf16x8 ld128(ldsp p) { return *(const LAS bf16x8*)p; }
__device__ __forceinline__ s16x4 ldtr(ldsp p) { return __builtin_bit_cast(s16x4, __builtin_amdgcn_ds_read_tr16_b64_v4i16((LAS v4i16_t*)p)); }
__device__ __forceinline__ bf16x8 cat(s16x4 lo, s16x4 hi) { return __builtin_shufflevector(lo, hi, 0, 1, 2, 3, 4, 5, 6, 7); }
__device__ __forceinline__ int rfl(int x) { return __builtin_amdgcn_readfirstlane(x); }
template <int LADD, int VADD, bool SC1>
__device__ __forceinline__ void dma(unsigned voff, const void* sbase, unsigned lbase) {
  const unsigned va = voff + (unsigned)VADD;
  if constexpr (SC1) asm volatile("s_add_i32 m0, %2, %3\n\ts_nop 0\n\tglobal_load_lds_dwordx4 %0, %1 sc1" ::"v"(va), "s"(sbase), "s"(lbase), "i"(LADD) : "memory", "scc");
  else asm volatile("s_add_i32 m0, %2, %3\n\ts_nop 0\n\tglobal_load_lds_dwordx4 %0, %1" ::"v"(va), "s"(sbase), "s"(lbase), "i"(LADD) : "memory", "scc");
}
template <int LADD>
__device__ __forceinline__ void dma2c_sc1(unsigned voff, const void* sbase, unsigned lbase) {
  asm volatile("s_add_i32 m0, %2, %3\n\ts_nop 0\n\tglobal_load_lds_dwordx4 %0, %1 sc1\n\tglobal_load_lds_dwordx4 %0, %1 offset:1024 sc1" ::"v"(voff), "s"(sbase), "s"(lbase), "i"(LADD) : "memory", "scc");
}
template <int OFF>
__device__ __forceinline__ void ldfrag1(bf16x8& f, unsigned voff, const void* sbase) {
  asm volatile("global_load_dwordx4 %0, %1, %2 offset:%3 sc1" : "=v"(f) : "v"(voff), "s"(sbase), "i"(OFF) : "memory");
}
__device__ __forceinline__ void pin1(bf16x8& f) { asm volatile("" : "+v"(f)); }
template <int N>
__device__ __forceinline__ void ldfrags(bf16x8* f, unsigned voff, const void* sbase) {
  sfor<0, N>([&](auto I) { constexpr int i = decltype(I)::value; ldfrag1<(i & 3) * 1024>(f[i], voff + (unsigned)((i >> 2) * 4096), sbase); });
}
template <int N>
__device__ __forceinline__ void pin(bf16x8* f) { sfor<0, N>([&](auto I) { pin1(f[decltype(I)::value]); }); }
__device__ __forceinline__ void st128_sc1(unsigned voff, const void* sbase, u32x4 v) {
  asm volatile("global_store_dwordx4 %0, %1, %2 sc1\n\ts_nop 1" ::"v"(voff), "v"(v), "s"(sbase) : "memory");
}
__device__ __forceinline__ void st64_sc1(unsigned voff, const void* sbase, u32x2 v) {
  asm volatile("global_store_dwordx2 %0, %1, %2 sc1\n\ts_nop 1" ::"v"(voff), "v"(v), "s"(sbase) : "memory");
}
__device__ __forceinline__ void st32_sc1(const void* sbase, unsigned v) {
  asm volatile("global_store_dword %0, %1, %2 sc1\n\ts_nop 1" ::"v"(0u), "v"(v), "s"(sbase) : "memory");
}
__device__ __forceinline__ float max3(float a, float b, float c) { return __builtin_elementwise_maximum(__builtin_elementwise_maximum(a, b), c); }
__device__ __forceinline__ float red_g_sum(float v) {
  auto a = __builtin_amdgcn_permlane16_swap(__float_as_uint(v), __float_as_uint(v), false, false);
  v = __uint_as_float(a[0]) + __uint_as_float(a[1]);
  auto b = __builtin_amdgcn_permlane32_swap(__float_as_uint(v), __float_as_uint(v), false, false);
  return __uint_as_float(b[0]) + __uint_as_float(b[1]);
}
__device__ __forceinline__ float red_g_max(float v) {
  auto a = __builtin_amdgcn_permlane16_swap(__float_as_uint(v), __float_as_uint(v), false, false);
  v = __builtin_elementwise_maximum(__uint_as_float(a[0]), __uint_as_float(a[1]));
  auto b = __builtin_amdgcn_permlane32_swap(__float_as_uint(v), __float_as_uint(v), false, false);
  return __builtin_elementwise_maximum(__uint_as_float(b[0]), __uint_as_float(b[1]));
}
__device__ __forceinline__ void poll_flags(const unsigned* base, unsigned voff, unsigned epoch) {
#pragma unroll 1
  for (unsigned spins = 0; spins < SPIN_CAP; ++spins) {
    unsigned v;
    asm volatile("global_load_dword %0, %1, %2 sc1\n\ts_waitcnt vmcnt(0)" : "=&v"(v) : "v"(voff), "s"(base) : "memory");
    if (__all(v == epoch)) break;
    __builtin_amdgcn_s_sleep(1);
  }
}

struct Params {
  const char* x; const char* wqkv; const char* wo; const char* w1; const char* b1; const char* w2; const char* b2;
  char* out; char* ws;
};

#define FS(i) do {} while (0)
#define STAMP(i) do {} while (0)

#define LANEVARS() int tz_ = tid; asm volatile("" : "+v"(tz_)); const int lane = tz_ & 63, l15 = lane & 15, g = lane >> 4, q4 = l15 >> 2, p4 = l15 & 3; \
  const unsigned lane16 = (unsigned)lane * 16u, x8 = (unsigned)(((lane & 7) ^ ((lane >> 3) & 7)) << 4); (void)q4; (void)p4; (void)g; (void)x8; (void)lane16

__global__ void __launch_bounds__(512, 2) fused_block(Params p) {
  extern __shared__ __attribute__((aligned(16))) char smem[];
  ldsp lds = (ldsp)smem;
  unsigned long long pc0; asm volatile("s_getpc_b64 %0" : "=s"(pc0));
  const int tid = threadIdx.x, lane = tid & 63, w = rfl(tid >> 6);
  const int l15 = lane & 15, g = lane >> 4, q4 = l15 >> 2, p4 = l15 & 3;
  const int b = blockIdx.x, r_ = b >> 3;
  const int c = 2 * (b & 7) + (r_ >> 4), j = r_ & 15;
  const int ca = 4 * ((b & 7) >> 1) + (r_ >> 3), ha = 8 * (b & 1) + (r_ & 7);
  const unsigned* ctl = (const unsigned*)(p.ws + WS_CTL);
  unsigned epoch = 0;
  FS(0);
  STAMP(0);
  const unsigned ONES = 0x3f803f80u;
  const unsigned lane16 = (unsigned)lane * 16u;
  const unsigned x8 = (unsigned)(((lane & 7) ^ ((lane >> 3) & 7)) << 4);

  constexpr int KIMG = 0, VIMG = 24576, QIMG = 49152;
  {
    const int h = ha;
    constexpr int SLOT = 28672, WOFF = 16384;
    const unsigned iw = (unsigned)(w >> 2);
    unsigned voff[4];
    {
      const unsigned xo = (unsigned)((32 * w + (lane >> 3)) * 896) + x8;
#pragma unroll
      for (int ii = 0; ii < 4; ++ii) {
        const unsigned u = (unsigned)(192 * (w & 3) + 64 * ii + lane), row = (u * 43691u) >> 19, rem = u - 12u * row;
        const unsigned piece = rem >> 2, c16 = rem & 3, hb = (c16 >> 1) ^ ((row >> 3) & 1);
        const unsigned wo_ = (ii < 3) ? row * 2688u + piece * 896u + hb * 32u + (c16 & 1) * 16u : 0u;
        voff[ii] = iw ? wo_ : xo + ii * 7168u;
      }
    }
    const unsigned long long mW = 0ull - (unsigned long long)iw;
    const unsigned long long bx_ = (unsigned long long)(uintptr_t)p.x + (unsigned)(ca * 128 * 896), bw_ = (unsigned long long)(uintptr_t)p.wqkv + (unsigned)(56 * h - 8 * (h & 1));
    const char* gb = (const char*)(uintptr_t)(bx_ + ((bw_ - bx_) & mW));
    const unsigned kstep = 128 + iw * (64 * 2688 - 128), ldst = w * 4096 + iw * (WOFF + (w - 4) * 3072 - w * 4096);
    const unsigned smask = iw - 1u, l3 = iw ? (unsigned)M_FILL : ldst + 3072u;
    auto issue1 = [&](auto SL, auto II) {
      constexpr int sl = decltype(SL)::value, ii = decltype(II)::value;
      if constexpr (ii < 3) dma<sl * SLOT + ii * 1024, 0, false>(voff[ii], gb, ldst);
      else { dma<0, 0, false>(voff[3], gb, l3 + (smask & (unsigned)(sl * SLOT))); gb += kstep; }
    };
    auto issue = [&](auto SL) { issue1(SL, IC(0)); issue1(SL, IC(1)); issue1(SL, IC(2)); issue1(SL, IC(3)); };
    FS(1);
    issue(IC(0));
    RAWBAR(); issue(IC(1));
    {
      unsigned ln = (unsigned)(r_ * 8 + w); if (ln >= CODE_LINES) ln = 0;
      const unsigned long long a = (pc0 & ~255ull) + (unsigned long long)(ln * 128u);
      if (lane == 0) asm volatile("s_mov_b32 m0, %1\n\ts_nop 0\n\tglobal_load_lds_dword %0, off" ::"v"(a), "i"(M_FILL) : "memory");
    }

    FS(2);
    const int ptq0 = (b < 224) ? b : 223, ptm = (ptq0 >= 112) ? 2 : 1, ptq = ptq0 - ((ptm == 2) ? 112 : 0);
    const int pts = (ptm == 1) ? ptq >> 3 : ptq >> 1, ptG = (ptm == 1) ? ptq & 7 : ptq & 1;
    auto pt_issue = [&]() {
      const unsigned stride = (ptm == 1) ? 3584u : 896u;
      const char* src = ((ptm == 1) ? p.w1 : p.w2) + (size_t)(32 * pts * stride + ptG * 448);
      const int wq = (w < 7) ? w : 6;
#pragma unroll
      for (int i = 0; i < 2; ++i) { const unsigned u = (unsigned)((2 * wq + i) * 64 + lane), row = (u * 9363u) >> 18, c16 = u - 28u * row;
        dma<M_PT, 0, false>(row * stride + c16 * 16u, src, (unsigned)((2 * wq + i) * 1024)); }
      if (lane == 63) asm volatile("s_mov_b32 m0, %2\n\ts_nop 0\n\tglobal_load_lds_dword %0, %1 sc1" ::"v"(0u), "s"(p.ws + WS_CTL), "i"(M_PF) : "memory");
    };
    const int tg = w >> 1, ng = w & 1;
    f32x4 acc[3][2], asum = {0, 0, 0, 0}, asq = {0, 0, 0, 0}, acs[3];
#pragma unroll
    for (int e = 0; e < 3; ++e) { acc[e][0] = f32x4{0, 0, 0, 0}; acc[e][1] = f32x4{0, 0, 0, 0}; acs[e] = f32x4{0, 0, 0, 0}; }
    const int xb = (32 * tg + 16 * ng + l15) * 128 + ((g ^ (l15 & 7)) << 4);
    int ab[3];
#pragma unroll
    for (int e = 0; e < 3; ++e) ab[e] = WOFF + (8 * g + q4) * 192 + ((((3 * ng + e) * 32) ^ ((g & 1) << 5))) + 8 * p4;
    bf16x8 xf[2][2];
    s16x4 al[2][3], ah[2][3];
    auto rdx = [&](int st, int buf, int t) { xf[buf][t] = ld128(lds + ((st >> 1) % 5) * SLOT + (xb ^ ((st & 1) << 6) ^ (t * 2048))); };
    auto rdl = [&](int st, int buf, int e) { al[buf][e] = ldtr(lds + ((st >> 1) % 5) * SLOT + ab[e] + (32 * (st & 1)) * 192); };
    auto rdh_ = [&](int st, int buf, int e) { ah[buf][e] = ldtr(lds + ((st >> 1) % 5) * SLOT + ab[e] + (32 * (st & 1) + 4) * 192); };
    auto rdf = [&](int st, int buf) { rdx(st, buf, 0); rdx(st, buf, 1);
#pragma unroll
      for (int e = 0; e < 3; ++e) { rdl(st, buf, e); rdh_(st, buf, e); } };
    bf16x8 onesf = __builtin_bit_cast(bf16x8, (u32x4{ONES, ONES, ONES, ONES}));
    KEEP(onesf); KEEP(asum); KEEP(asq);
#pragma unroll
    for (int e = 0; e < 3; ++e) { KEEP(acc[e][0]); KEEP(acc[e][1]); KEEP(acs[e]); }
    WAITV(5);
    BAR();
    FS(3);
    rdf(0, 0);
    sfor<0, 14>([&](auto ST) {
      constexpr int st = decltype(ST)::value, kc = st >> 1, cb = st & 1;
      if constexpr ((st & 1) && st < 13) {
        if constexpr (kc == 0) WAITV(5);
        if constexpr (kc == 0) {} else if constexpr (kc == 1) WAITV(9); else if constexpr (kc <= 3) WAITV(6); else if constexpr (kc == 4) WAITV(4); else WAITV(0);
        BARN();
      }
      if constexpr (st == 2) pt_issue();
      constexpr int nb = (st + 1) & 1; constexpr bool more = st < 13;
      const bf16x8 a0 = cat(al[cb][0], ah[cb][0]), a1 = cat(al[cb][1], ah[cb][1]), a2 = cat(al[cb][2], ah[cb][2]);
      SCHED();
      MACC(acc[0][0], a0, xf[cb][0]); if constexpr (more) rdx(st + 1, nb, 0);
      MACC(acc[0][1], a0, xf[cb][1]); if constexpr (more) rdl(st + 1, nb, 0);
      MACC(asum, onesf, xf[cb][0]); if constexpr (more) rdh_(st + 1, nb, 0);
      MACC(acs[0], a0, onesf);
      SCHED();
      if constexpr (st < 2) { issue1(IC(st + 2), IC(0)); issue1(IC(st + 2), IC(1)); }
      else if constexpr (kc + 3 <= 6) issue1(IC((kc + 3) % 5), IC(2 * (st & 1)));
      SCHED();
      MACC(acc[1][0], a1, xf[cb][0]); if constexpr (more) rdx(st + 1, nb, 1);
      MACC(acc[1][1], a1, xf[cb][1]); if constexpr (more) rdl(st + 1, nb, 1);
      MACC(asq, xf[cb][0], xf[cb][0]); if constexpr (more) rdh_(st + 1, nb, 1);
      MACC(acs[1], a1, onesf);
      SCHED();
      if constexpr (st < 2) { issue1(IC(st + 2), IC(2)); issue1(IC(st + 2), IC(3)); }
      else if constexpr (kc + 3 <= 6) issue1(IC((kc + 3) % 5), IC(2 * (st & 1) + 1));
      SCHED();
      MACC(acc[2][0], a2, xf[cb][0]); if constexpr (more) rdl(st + 1, nb, 2);
      MACC(acc[2][1], a2, xf[cb][1]); if constexpr (more) rdh_(st + 1, nb, 2);
      MACC(acs[2], a2, onesf);
      SCHED();
    });
    NOPS(); KEEP(asum); KEEP(asq);
#pragma unroll
    for (int e = 0; e < 3; ++e) { KEEP(acc[e][0]); KEEP(acc[e][1]); KEEP(acs[e]); }
    FS(4);
    STAMP(1);
    epoch = (unsigned)rfl((int)*(LAS unsigned*)(lds + M_PF + 252)) + 1u;
    {
      const unsigned ptb = (ptm == 1) ? WT_W1 : WT_W2, ptn = (ptm == 1) ? 14u : 56u;
      ldsp src = lds + M_PT + (4 * g + q4) * 448 + 8 * p4;
#pragma unroll
      for (int i = 0; i < 2; ++i) {
        const int t = (w + 8 * i < 14) ? w + 8 * i : w;
        const bf16x8 f = cat(ldtr(src + t * 32), ldtr(src + 16 * 448 + t * 32));
        st128_sc1(lane16 + ptb + ((unsigned)(14 * ptG + t) * ptn + (unsigned)pts) * 1024u, p.ws + WS_WT, __builtin_bit_cast(u32x4, f));
      }
    }
    {
      const float d01 = (p4 & 1) ? asq[1] : asq[0], d23 = (p4 & 1) ? asq[3] : asq[2], dq = (p4 & 2) ? d23 : d01;
      float mean = asum[0] * (1.f / 448.f), dq2 = dq;
      KEEP(mean); KEEP(dq2);
      if (g == q4) *(LAS f32x2*)(lds + M_LN + (32 * tg + 16 * ng + l15) * 8) = f32x2{mean, rsqrtf(fmaxf(dq2 * (1.f / 448.f) - mean * mean, 0.f) + 1e-5f)};
    }
    BAR();
    FS(5);
    float mu[2], rs[2];
#pragma unroll
    for (int t = 0; t < 2; ++t) { const f32x2 st_ = *(LAS f32x2*)(lds + M_LN + (32 * tg + 16 * (t ^ ng) + l15) * 8); mu[t] = st_[0]; rs[t] = st_[1]; }
    const bool pad0 = (h & 1) && g == 0, pad1 = !(h & 1) && g == 3;
    const int rsw = ((l15 >> 1) & 3) << 4, rowb = (32 * tg + l15) * 64;
    auto epi = [&](auto NG) {
      constexpr int ngc = decltype(NG)::value;
      sfor<0, 3>([&](auto E) {
        constexpr int e = decltype(E)::value, nt = 3 * ngc + e, which = nt >> 1, par = nt & 1;
        constexpr int ibase = (which == 0) ? QIMG : (which == 1) ? KIMG + 16384 : VIMG + 16384;
        const int off = (which == 2) ? ((((2 * par + (g >> 1)) << 4) ^ rsw) + (g & 1) * 8) : (((g << 4) ^ rsw) + par * 8);
        const bool pad = par ? pad1 : pad0;
#pragma unroll
        for (int t = 0; t < 2; ++t) {
          const float sc = (which == 0) ? rs[t] * 0.27264657f : rs[t], nm = -mu[t] * sc;
          f32x2 sc2 = {sc, sc}, nm2 = {nm, nm};
          KEEP(sc2); KEEP(nm2);
          const f32x2 v0 = lo2(acc[e][t]) * sc2 + lo2(acs[e]) * nm2, v1 = hi2(acc[e][t]) * sc2 + hi2(acs[e]) * nm2;
          u32x2 o; o.x = cvtpk(v0); o.y = cvtpk(v1);
          o.x = pad ? ((which == 2) ? 0x00003f80u : 0u) : o.x; o.y = pad ? 0u : o.y;
          *(LAS u32x2*)(lds + ibase + (t ^ ngc) * 1024 + rowb + off) = o;
        }
      });
    };
    if (ng) epi(IC(1)); else epi(IC(0));
    if (ca < 2) {
      const u32x4 z = {0, 0, 0, 0};
#pragma unroll
      for (int i = 0; i < 2; ++i) { *(LAS u32x4*)(lds + KIMG + tid * 16 + i * 8192) = z; *(LAS u32x4*)(lds + VIMG + tid * 16 + i * 8192) = z; }
    }
    BAR();
    FS(6);
    {
      const char* blob = p.ws + WS_KV + (size_t)(ca * 16 + h) * 16384;
      const u32x4 v0 = *(LAS u32x4*)(lds + KIMG + 16384 + tid * 16), v1 = *(LAS u32x4*)(lds + VIMG + 16384 + tid * 16);
      st128_sc1(tid * 16, blob, v0); st128_sc1(8192 + tid * 16, blob, v1);
    }
    FS(7);
    WAITV(0);
    BAR();
    if (tid == 0) { st32_sc1(ctl + FLAG_IDX(0, ca, h), epoch); st32_sc1(ctl + FLAG_IDX(4, b >> 4, b & 15), epoch); }
    FS(8);
    STAMP(2);
  }
  const int ns = j & 3, col4 = 128 * ns - (((ns >> 1) & ns) << 5), ngr = 4 - (ns >> 1);
  const int pr = w & 3, wh = w >> 2;
  const int prc = (pr < ngr) ? pr : 0;
  const int th5 = j >> 3, cs5 = j & 7, col0 = 64 * cs5 - 16 * ((cs5 >> 2) * (cs5 & 3)), nblk = 4 - (cs5 >> 2);
  const int ng5 = w & 1, kq = w >> 1;
  const char* wt = p.ws + WS_WT;
  bf16x8 wr[32];
  const int T1 = (448 * (j >> 2) + col4) / 16;
  const unsigned w1o = WT_W1 + (unsigned)(T1 + 2 * prc) * 14u * 1024u;
  const unsigned wro = lane16 + w1o;
  constexpr int WI = 32768;
  unsigned wb0, wb1;
  { const int rin = lane >> 4, pos = lane & 15;
    int blk = (pos >> 1) ^ rin; if (blk >= 2 * ngr) blk -= 2; wb0 = (unsigned)(blk * 32 + (pos & 1) * 16);
    blk = (pos >> 1) ^ (4 + rin); if (blk >= 2 * ngr) blk -= 2; wb1 = (unsigned)(blk * 32 + (pos & 1) * 16); }
  const char* wob = p.wo + col4 * 2;
  {
    if (w < 6) {
      const unsigned rb = (unsigned)(((lane >> 4) + 4 * (24 + w)) * 896) + ((w & 1) ? wb1 : wb0);
      sfor<0, 14>([&](auto I) { constexpr int i = decltype(I)::value; dma<i * 6144, i * 24 * 896, false>(rb, wob, (unsigned)(WI + (24 + w) * 1024)); });
    }
    int pos = lane & 15; if (pos >= 4 * ngr) pos = 4 * ngr - 1;
    int xch = (lane & 15) ^ (((lane >> 4) + 4 * w) & 15); if (xch >= 4 * ngr) xch = 0;
    dma<M_XT, 0, false>((unsigned)(((lane >> 4) + 4 * w) * 896 + xch * 16), p.x + (size_t)(128 * c + 32 * (j >> 2)) * 896 + col4 * 2, (unsigned)(w * 1024));
    if (w == 5 && lane < 16) dma<M_B1, 0, false>((unsigned)(pos * 16), p.b1 + (448 * (j >> 2) + col4) * 2, 0u);
    if (w == 4 && lane < 8) dma<M_B2, 0, false>((unsigned)((lane < 2 * nblk ? lane : 0) * 16), p.b2 + col0 * 2, 0u);
  }
  {
    const int h = ha;
    FS(9);
    const int sw = (l15 >> 1) & 3;
    if (w == 7 && ca >= 1) poll_flags(ctl, (unsigned)(FLAG_IDX(0, (ca >= 2 && (lane & 1)) ? ca - 2 : ca - 1, h) * 4), epoch);
    if (w == 6) {
      const unsigned tfo = (unsigned)(FLAG_IDX(4, 4 * g, l15) * 4);
#pragma unroll 1
      for (unsigned spins = 0; spins < SPIN_CAP; ++spins) {
        unsigned t0, t1, t2, t3;
        asm volatile("global_load_dword %0, %4, %5 sc1\n\tglobal_load_dword %1, %4, %5 offset:2048 sc1\n\tglobal_load_dword %2, %6, %5 sc1\n\tglobal_load_dword %3, %6, %5 offset:2048 sc1\n\ts_waitcnt vmcnt(0)"
                     : "=&v"(t0), "=&v"(t1), "=&v"(t2), "=&v"(t3) : "v"(tfo), "s"(ctl), "v"(tfo + 4096u) : "memory");
        if (__all(t0 == epoch && t1 == epoch && t2 == epoch && t3 == epoch)) break;
        __builtin_amdgcn_s_sleep(2);
      }
      if (b == 0 && lane == 0) st32_sc1(ctl, epoch);
    }
    BAR();
    FS(10);
    {
      const int tl = w >> 2, src = ca - 2 + tl, pc = (w & 3) * 2048;
      if (src >= 0) {
        const char* blob = p.ws + WS_KV + (size_t)(src * 16 + h) * 16384 + pc;
        dma2c_sc1<KIMG>(lane16, blob, (unsigned)(tl * 8192 + pc)); dma2c_sc1<VIMG>(lane16, blob + 8192, (unsigned)(tl * 8192 + pc));
      }
    }
    const bf16x8 qf = ld128(lds + QIMG + (16 * w + l15) * 64 + ((g ^ sw) << 4));
    WAITV(0);
    BAR();
    FS(11);
    STAMP(3);
    STAMP(4);
    if (w >= 4) ldfrags<7>(wr, wro, wt);
    f32x4 sc[17];
    const float NEG = -__builtin_inff();
    ldsp kb = lds + KIMG + (16 * w + l15) * 64 + ((g ^ sw) << 4);
    {
      bf16x8 kf[2][6];
#pragma unroll
      for (int u = 0; u < 6; ++u) kf[0][u] = ld128(kb + u * 1024);
#pragma unroll
      for (int bt = 0; bt < 3; ++bt) {
        if (bt < 2) {
#pragma unroll
          for (int u = 0; u < 6; ++u) if (6 * (bt + 1) + u < 17) kf[(bt + 1) & 1][u] = ld128(kb + (6 * (bt + 1) + u) * 1024);
        }
        SCHED();
#pragma unroll
        for (int u = 0; u < 6; ++u) if (6 * bt + u < 17) sc[6 * bt + u] = MFMA(kf[bt & 1][u], qf, (f32x4{0, 0, 0, 0}));
        SCHED();
      }
    }
    FS(12);
    if (w >= 4) ldfrags<7>(wr + 7, wro + 7 * 1024, wt);
    const int vsw = (2 * g + (q4 >> 1)) & 3;
    ldsp vb0 = lds + VIMG + (16 * w + 4 * g + q4) * 64 + (((p4 >> 1) ^ vsw) << 4) + (p4 & 1) * 8;
    ldsp vb1 = lds + VIMG + (16 * w + 4 * g + q4) * 64 + (((2 + (p4 >> 1)) ^ vsw) << 4) + (p4 & 1) * 8;
    bf16x8 vf[2][3][2];
    auto rdv = [&](int bt) {
#pragma unroll
      for (int i = 0; i < 3; ++i) { const int uu = 3 * bt + i, ua = 2 * uu, ub = (uu < 8) ? 2 * uu + 1 : 15;
        vf[bt & 1][i][0] = cat(ldtr(vb0 + ua * 1024), ldtr(vb0 + ub * 1024)); vf[bt & 1][i][1] = cat(ldtr(vb1 + ua * 1024), ldtr(vb1 + ub * 1024)); }
    };
    rdv(0);
    SCHED();
    if (ca < 2) {
      const int tmin = (2 - ca) * 8;
#pragma unroll
      for (int u = 0; u < 16; ++u) if (w + u < tmin) sc[u] = f32x4{NEG, NEG, NEG, NEG};
    }
#pragma unroll
    for (int r = 0; r < 4; ++r) { sc[0][r] = (4 * g + r <= l15) ? NEG : sc[0][r]; sc[16][r] = (4 * g + r > l15) ? NEG : sc[16][r]; }
    float m = max3(sc[0][0], sc[0][1], sc[0][2]), m2 = max3(sc[0][3], sc[1][0], sc[1][1]);
    m = max3(m, sc[1][2], sc[1][3]);
#pragma unroll
    for (int u = 2; u < 17; ++u) { if (u & 1) m = max3(max3(m, sc[u][0], sc[u][1]), sc[u][2], sc[u][3]); else m2 = max3(max3(m2, sc[u][0], sc[u][1]), sc[u][2], sc[u][3]); }
    m = max3(m, m2, m2);
    m = red_g_max(m);
    FS(13);
    SCHED();
    if (w >= 4) ldfrags<7>(wr + 14, wro + 14 * 1024, wt);
    unsigned pk[17][2];
    f32x2 mm = {-m, -m};
    KEEP(mm);
    f32x4 oa[2] = {f32x4{0, 0, 0, 0}, f32x4{0, 0, 0, 0}};
    KEEP(oa[0]); KEEP(oa[1]);
#pragma unroll
    for (int bt = 0; bt < 3; ++bt) {
#pragma unroll
      for (int u = 6 * bt; u < 6 * bt + 6 && u < 17; ++u) {
        const f32x2 d0 = lo2(sc[u]) + mm, d1 = hi2(sc[u]) + mm;
        pk[u][0] = cvtpk(__builtin_amdgcn_exp2f(d0[0]), __builtin_amdgcn_exp2f(d0[1]));
        pk[u][1] = cvtpk(__builtin_amdgcn_exp2f(d1[0]), __builtin_amdgcn_exp2f(d1[1]));
      }
      if (bt < 2) rdv(bt + 1);
      if (bt == 0 && w >= 4) ldfrags<7>(wr + 21, wro + 21 * 1024, wt);
      SCHED();
#pragma unroll
      for (int i = 0; i < 3; ++i) {
        const int uu = 3 * bt + i;
        u32x4 pu; pu.x = pk[2 * uu][0]; pu.y = pk[2 * uu][1];
        if (uu < 8) { pu.z = pk[2 * uu + 1][0]; pu.w = pk[2 * uu + 1][1]; } else { pu.z = 0; pu.w = 0; }
        const bf16x8 pf = __builtin_bit_cast(bf16x8, pu);
        MACCN(oa[0], vf[bt & 1][i][0], pf); MACC(oa[1], vf[bt & 1][i][1], pf);
      }
      SCHED();
    }
    NOPS(); KEEP(oa[0]); KEEP(oa[1]);
    FS(15);
    int tz2 = tid; asm volatile("" : "+v"(tz2));
    const int l15b = tz2 & 15, gb = (tz2 & 63) >> 4, swb = (l15b >> 1) & 3;
    const float lsel = (h & 1) ? oa[0][0] : oa[1][0];
    const float l = __uint_as_float((unsigned)__builtin_amdgcn_ds_bpermute((((h & 1) ? 0 : 48) + l15b) * 4, (int)__float_as_uint(lsel)));
    const float inv1 = __builtin_amdgcn_rcpf(l);
    f32x2 inv = {inv1, inv1};
    KEEP(inv);
    u32x4 ov;
    ov.x = cvtpk(lo2(oa[0]) * inv); ov.y = cvtpk(hi2(oa[0]) * inv); ov.z = cvtpk(lo2(oa[1]) * inv); ov.w = cvtpk(hi2(oa[1]) * inv);
    { const bool z0 = (h & 1) && gb == 0, z1 = !(h & 1) && gb == 3; ov.x = z0 ? 0u : ov.x; ov.y = z0 ? 0u : ov.y; ov.z = z1 ? 0u : ov.z; ov.w = z1 ? 0u : ov.w; }
    st128_sc1((unsigned)((16 * w + l15b) * 64 + ((gb ^ swb) << 4)), p.ws + WS_O + (size_t)(ca * 16 + h) * 8192, ov);
    FS(16);
    BAR();
    {
      const unsigned rb = (unsigned)(((lane >> 4) + 12 * w) * 896), va = rb + ((w & 1) ? wb1 : wb0), vb = rb + ((w & 1) ? wb0 : wb1), lb = (unsigned)(WI + 3 * w * 1024);
      dma<0, 0, false>(va, wob, lb); dma<1024, 4 * 896, false>(vb, wob, lb); dma<2048, 8 * 896, false>(va, wob, lb);
      const int ins = (w < 4) ? 108 + w : 3 * w + 2;
      dma<0, 0, false>((unsigned)(((lane >> 4) + 4 * ins) * 896) + ((ins & 1) ? wb1 : wb0), wob, (unsigned)(WI + ins * 1024));
    }
    WAITV(4);
    BAR();
    if (tid == 0) st32_sc1(ctl + FLAG_IDX(1, ca, h), epoch);
    FS(18);
    STAMP(5);
  }
  {
    LANEVARS();
    constexpr int OT = 0;
    const int tq = j >> 2;
    WAITV(0);
    BAR();
    if (w < 4) {
      const int rx = 4 * (g & 1) + q4;
      ldsp ab0 = lds + WI + (4 * g + q4) * 256 + (((2 * prc) ^ rx) << 5) + 8 * p4;
      ldsp ab1 = lds + WI + (4 * g + q4) * 256 + (((2 * prc + 1) ^ rx) << 5) + 8 * p4;
      ldsp ab0h = ab0 + 65536, ab1h = ab1 + 65536;
#pragma unroll
      for (int s = 0; s < 16; ++s) {
        const int rb = (28 * s - 4 * (s & 1)) * 256;
        if (rb + 4096 < 65536) { wr[s] = cat(ldtr(ab0 + rb), ldtr(ab0 + rb + 4096)); wr[16 + s] = cat(ldtr(ab1 + rb), ldtr(ab1 + rb + 4096)); }
        else { wr[s] = cat(ldtr(ab0h + (rb - 65536)), ldtr(ab0h + (rb - 65536 + 4096))); wr[16 + s] = cat(ldtr(ab1h + (rb - 65536)), ldtr(ab1h + (rb - 65536 + 4096))); }
      }
    } else {
      __builtin_amdgcn_s_setprio(3);
      poll_flags(ctl, (unsigned)(FLAG_IDX(1, c, l15) * 4), epoch);
      const char* ob_ = p.ws + WS_O + (size_t)(c * 16 + 4 * (w - 4)) * 8192 + tq * 2048;
      const unsigned lb = (unsigned)(OT + (w - 4) * 8192);
      dma2c_sc1<0>(lane16, ob_, lb); dma2c_sc1<2048>(lane16 + 8192, ob_, lb); dma2c_sc1<4096>(lane16 + 16384, ob_, lb); dma2c_sc1<6144>(lane16 + 24576, ob_, lb);
      __builtin_amdgcn_s_setprio(0);
    }
    FS(19);
    WAITV(0);
    BAR();
    FS(20);
    STAMP(6);
    STAMP(7);
    if (w < 4) {
      f32x4 a0[2], a1[2];
#pragma unroll
      for (int t = 0; t < 2; ++t) { a0[t] = f32x4{0, 0, 0, 0}; a1[t] = f32x4{0, 0, 0, 0}; }
      ldsp ob = lds + OT + l15 * 64 + ((g ^ ((l15 >> 1) & 3)) << 4);
      u32x2 xr[2][2];
#pragma unroll
      for (int t = 0; t < 2; ++t) { ldsp xp = lds + M_XT + (16 * t + l15) * 256 + (g & 1) * 8;
        xr[t][0] = *(LAS u32x2*)(xp + (((4 * prc + (g >> 1)) ^ l15) << 4)); xr[t][1] = *(LAS u32x2*)(xp + (((4 * prc + 2 + (g >> 1)) ^ l15) << 4)); }
#pragma unroll
      for (int t = 0; t < 2; ++t) { KEEP(a0[t]); KEEP(a1[t]); }
      bf16x8 of[4][2];
#pragma unroll
      for (int s = 0; s < 3; ++s) { of[s][0] = ld128(ob + s * 2048); of[s][1] = ld128(ob + s * 2048 + 1024); }
#pragma unroll
      for (int s = 0; s < 16; ++s) {
        if (s + 3 < 16) { of[(s + 3) & 3][0] = ld128(ob + (s + 3) * 2048); of[(s + 3) & 3][1] = ld128(ob + (s + 3) * 2048 + 1024); }
        SCHED();
#pragma unroll
        for (int t = 0; t < 2; ++t) { MACC(a0[t], wr[s], of[s & 3][t]); MACC(a1[t], wr[16 + s], of[s & 3][t]); }
        SCHED();
      }
      NOPS();
#pragma unroll
      for (int t = 0; t < 2; ++t) { KEEP(a0[t]); KEEP(a1[t]); }
      FS(21);
      const unsigned ho = (unsigned)((128 * c + 32 * tq + l15) * 896 + (col4 / 32 + pr) * 64 + g * 16);
#pragma unroll
      for (int t = 0; t < 2; ++t) {
        u32x4 hv;
        hv.x = cvtpk(lo2(a0[t]) + bf2(xr[t][0].x)); hv.y = cvtpk(hi2(a0[t]) + bf2(xr[t][0].y));
        hv.z = cvtpk(lo2(a1[t]) + bf2(xr[t][1].x)); hv.w = cvtpk(hi2(a1[t]) + bf2(xr[t][1].y));
        if (pr < ngr) st128_sc1(ho + t * 16 * 896, p.ws + WS_H, hv);
        float s1 = 0.f, s2 = 0.f;
#pragma unroll
        for (int i = 0; i < 4; ++i) { s1 = dot2(hv[i], ONES, s1); s2 = dot2(hv[i], hv[i], s2); }
        s1 = red_g_sum(s1); s2 = red_g_sum(s2);
        if (pr >= ngr) { s1 = 0.f; s2 = 0.f; }
        if (g == 0) ((LAS f32x2*)(lds + M_PART))[(16 * t + l15) * 4 + pr] = f32x2{s1, s2};
      }
    } else {
      pin<28>(wr);
#pragma unroll
      for (int i = 0; i < 28; ++i) *(LAS bf16x8*)(lds + 32768 + (w - 4) * 28672 + i * 1024 + lane * 16) = wr[i];
    }
    FS(22);
    BAR();
    if (tid < 32) {
      const f32x4 u0 = *(LAS f32x4*)(lds + M_PART + tid * 32), u1 = *(LAS f32x4*)(lds + M_PART + tid * 32 + 16);
      st64_sc1((unsigned)((128 * c + 32 * tq + tid) * 32 + ns * 8), p.ws + WS_ST,
               u32x2{__float_as_uint((u0[0] + u0[2]) + (u1[0] + u1[2])), __float_as_uint((u0[1] + u0[3]) + (u1[1] + u1[3]))});
    }
    if (w < 4) {
#pragma unroll
      for (int i = 0; i < 28; ++i) wr[i] = ld128(lds + 32768 + w * 28672 + i * 1024 + lane * 16);
    }
    FS(23);
    if (w < 4) WAITV(0);
    BAR();
    if (tid == 0) st32_sc1(ctl + FLAG_IDX(2, c, j), epoch);
    FS(25);
    STAMP(8);
  }
  {
    LANEVARS();
    const int f0_ = 448 * (j >> 2) + col4, th = wh;
    FS(26);
    f32x4 c0 = {0, 0, 0, 0}, c1 = {0, 0, 0, 0};
    pin<28>(wr);
    bf16x8 onesf = __builtin_bit_cast(bf16x8, (u32x4{ONES, ONES, ONES, ONES}));
    KEEP(onesf); KEEP(c0); KEEP(c1);
    asm volatile("s_nop 1");
#pragma unroll
    for (int s = 0; s < 14; ++s) { MACC(c0, wr[s], onesf); MACC(c1, wr[14 + s], onesf); }
    NOPS(); KEEP(c0); KEEP(c1);
    SCHED();
    if (w == 7) poll_flags(ctl, (unsigned)(FLAG_IDX(2, c, l15) * 4), epoch);
    FS(29);
    BAR();
    const unsigned vo = (unsigned)((16 * w + (lane >> 3)) * 896) + x8, wl2k = (unsigned)(w * 2048);
    const char* hb = p.ws + WS_H + (size_t)c * 128 * 896;
    auto issue4 = [&](auto KC) { constexpr int kc = decltype(KC)::value; dma<kc * 16384, kc * 128, true>(vo, hb, wl2k); dma<kc * 16384 + 1024, kc * 128 + 8 * 896, true>(vo, hb, wl2k); };
    if (lane < 32) dma<M_TILE, 0, true>(lane16, p.ws + WS_ST + (size_t)(128 * c + 16 * w) * 32, (unsigned)(w * 512));
    issue4(IC(0)); RAWBAR(); issue4(IC(1));
    FS(31);
    f32x4 acc[2][4];
#pragma unroll
    for (int t = 0; t < 4; ++t) { acc[0][t] = f32x4{0, 0, 0, 0}; acc[1][t] = f32x4{0, 0, 0, 0}; }
    const int xb = (64 * th + l15) * 128 + ((g ^ (l15 & 7)) << 4);
    bf16x8 xf[2][4];
    auto rd1 = [&](int st, int buf, int t) { xf[buf][t] = ld128(lds + (st >> 1) * 16384 + (xb ^ ((st & 1) << 6)) + t * 2048); };
    auto rdf = [&](int st, int buf) { rd1(st, buf, 0); rd1(st, buf, 1); rd1(st, buf, 2); rd1(st, buf, 3); };
#pragma unroll
    for (int t = 0; t < 4; ++t) { KEEP(acc[0][t]); KEEP(acc[1][t]); }
    bf16x8 bf[2][14];
    const unsigned w2o0 = WT_W2 + ((unsigned)(col0 / 16 + 2 * ng5) * 56u + 14u * kq) * 1024u, w2o1 = (2 * ng5 + 1 < nblk) ? w2o0 + 56u * 1024u : w2o0;
    WAITV(2); BAR();
    FS(32);
    STAMP(9);
    STAMP(10);
    rdf(0, 0);
    sfor<0, 14>([&](auto ST) {
      constexpr int st = decltype(ST)::value;
      if constexpr (st == 1) WAITV(2); if constexpr (st == 3) WAITV(4); if constexpr (st == 5) WAITV(6); if constexpr (st == 7) WAITV(8); if constexpr (st == 9) WAITV(10); if constexpr (st == 11) WAITV(12);
      if constexpr ((st & 1) && st < 13) BARN();
      if constexpr (st == 2) {
        if (tid < 128) {
          const f32x4 u0 = *(LAS f32x4*)(lds + M_TILE + tid * 32), u1 = *(LAS f32x4*)(lds + M_TILE + tid * 32 + 16);
          float mean = ((u0[0] + u0[2]) + (u1[0] + u1[2])) * (1.f / 448.f), sq = (u0[1] + u0[3]) + (u1[1] + u1[3]);
          KEEP(mean); KEEP(sq);
          const float rstd = rsqrtf(fmaxf(sq * (1.f / 448.f) - mean * mean, 0.f) + 1e-5f);
          *(LAS f32x2*)(lds + M_LN + tid * 8) = f32x2{rstd, -mean * rstd};
        }
      }
      SCHED();
#pragma unroll
      for (int t = 0; t < 2; ++t) { MACC(acc[0][t], wr[st], xf[st & 1][t]); if constexpr (st < 13) rd1(st + 1, (st + 1) & 1, t); MACC(acc[1][t], wr[14 + st], xf[st & 1][t]); }
      SCHED();
      if constexpr (st <= 4) issue4(IC(st + 2));
      if constexpr (st >= 5) { constexpr int s = st - 5; ldfrags<1>(&bf[0][s], lane16 + w2o0 + s * 1024, wt); ldfrags<1>(&bf[1][s], lane16 + w2o1 + s * 1024, wt); }
      SCHED();
#pragma unroll
      for (int t = 2; t < 4; ++t) { MACC(acc[0][t], wr[st], xf[st & 1][t]); if constexpr (st < 13) rd1(st + 1, (st + 1) & 1, t); MACC(acc[1][t], wr[14 + st], xf[st & 1][t]); }
      SCHED();
    });
    NOPS();
#pragma unroll
    for (int t = 0; t < 4; ++t) { KEEP(acc[0][t]); KEEP(acc[1][t]); }
    FS(33);
    STAMP(11);
    {
      const u32x2 bb0 = *(LAS u32x2*)(lds + M_B1 + (32 * prc + 4 * g) * 2), bb1 = *(LAS u32x2*)(lds + M_B1 + (32 * prc + 4 * g) * 2 + 32);
      const f32x2 b00 = bf2(bb0.x), b01 = bf2(bb0.y), b10 = bf2(bb1.x), b11 = bf2(bb1.y);
      const unsigned ho = (unsigned)((128 * c + 64 * th + l15) * 3584 + (f0_ / 32 + pr) * 64 + g * 16);
      f32x2 ln[4];
#pragma unroll
      for (int t = 0; t < 4; ++t) ln[t] = *(LAS f32x2*)(lds + M_LN + (64 * th + 16 * t + l15) * 8);
#pragma unroll
      for (int t = 0; t < 4; ++t) {
        const float a = ln[t][0], nm = ln[t][1];
        f32x2 aa = {a, a}, nn = {nm, nm};
        KEEP(aa); KEEP(nn);
        f32x2 z[4] = {lo2(acc[0][t]) * aa + (lo2(c0) * nn + b00), hi2(acc[0][t]) * aa + (hi2(c0) * nn + b01), lo2(acc[1][t]) * aa + (lo2(c1) * nn + b10), hi2(acc[1][t]) * aa + (hi2(c1) * nn + b11)};
        u32x4 hv;
#pragma unroll
        for (int i = 0; i < 4; ++i) {
          f32x2 r = {__builtin_elementwise_maximum(z[i][0], 0.f), __builtin_elementwise_maximum(z[i][1], 0.f)};
          hv[i] = cvtpk(r * r);
        }
        if (pr < ngr) st128_sc1(ho + t * 16 * 3584, p.ws + WS_H1, hv);
      }
    }
    FS(34);
    WAITV(0);
    BAR();
    if (tid == 0) st32_sc1(ctl + FLAG_IDX(3, c, j), epoch);
    FS(38);
    STAMP(12);
    if (w != 7) {
      ldfrags<5>(&bf[0][9], lane16 + w2o0 + 9 * 1024, wt); ldfrags<5>(&bf[1][9], lane16 + w2o1 + 9 * 1024, wt);
    }
    if (w >= 5 && w < 7) {
      const int m0_ = col0 >> 5;
      const char* hr = p.ws + WS_H + (size_t)(128 * c + 64 * th5) * 896;
      sfor<0, 6>([&](auto I) { constexpr int i = decltype(I)::value;
        const unsigned u = (unsigned)((w - 5) * 384 + i * 64 + lane), r0 = (u * 43691u) >> 19, c0_ = u - 12u * r0; unsigned cc0 = m0_ * 4 + c0_; if (cc0 > 55) cc0 = 55;
        dma<M_TILE + i * 1024, 0, true>(r0 * 896 + cc0 * 16, hr, (unsigned)((w - 5) * 6144)); });
    }
    const unsigned vo5 = (unsigned)((32 * ng5 + (lane >> 3)) * 3584 + kq * 896) + x8, wl4k = (unsigned)(w * 4096);
    const char* hb5 = p.ws + WS_H1 + (size_t)(128 * c + 64 * th5) * 3584;
    auto issue5 = [&](auto I) {
      constexpr int i = decltype(I)::value, sa = (i & 3) * 32768;
      sfor<0, 4>([&](auto II) { constexpr int ii = decltype(II)::value; dma<sa + ii * 1024, i * 128 + ii * 8 * 3584, true>(vo5, hb5, wl4k); });
    };
    FS(39);
    if (w == 7) {
      poll_flags(ctl, (unsigned)(FLAG_IDX(3, c, l15) * 4), epoch);
    }
    FS(40);
    BAR();
    FS(41);
    STAMP(13);
    issue5(IC(0));
    if (w == 7) {
      ldfrags<5>(&bf[0][9], lane16 + w2o0 + 9 * 1024, wt); ldfrags<5>(&bf[1][9], lane16 + w2o1 + 9 * 1024, wt);
    }
    FS(42);
    f32x4 ac5[2][4];
#pragma unroll
    for (int t = 0; t < 4; ++t) { ac5[0][t] = f32x4{0, 0, 0, 0}; ac5[1][t] = f32x4{0, 0, 0, 0}; }
    int yb[4];
#pragma unroll
    for (int t = 0; t < 4; ++t) yb[t] = kq * 8192 + (16 * ((t + kq) & 3) + l15) * 128 + ((g ^ (l15 & 7)) << 4);
    bf16x8 yf[2][4];
    auto rz1 = [&](int st, int buf, int t) { yf[buf][t] = ld128(lds + ((st >> 1) & 3) * 32768 + (yb[t] ^ ((st & 1) << 6))); };
    auto rdy = [&](int st, int buf) { rz1(st, buf, 0); rz1(st, buf, 1); rz1(st, buf, 2); rz1(st, buf, 3); };
#pragma unroll
    for (int t = 0; t < 4; ++t) { KEEP(ac5[0][t]); KEEP(ac5[1][t]); }
    WAITV(0);
    pin<14>(bf[0]); pin<14>(bf[1]);
    BAR();
    FS(43);
    STAMP(14);
    rdy(0, 0);
    sfor<0, 14>([&](auto ST) {
      constexpr int st = decltype(ST)::value;
      if constexpr ((st & 1) && st < 13) {
        if constexpr (st < 11) WAITV(4); else WAITV(0);
        BARN();
      }
      SCHED();
#pragma unroll
      for (int t = 0; t < 2; ++t) { MACC(ac5[0][t], bf[0][st], yf[st & 1][t]); if constexpr (st < 13) rz1(st + 1, (st + 1) & 1, t); MACC(ac5[1][t], bf[1][st], yf[st & 1][t]); }
      SCHED();
      if constexpr (st == 0) { issue5(IC(1)); issue5(IC(2)); }
      if constexpr ((st & 1) && st <= 7) issue5(IC((st + 5) / 2));
      SCHED();
#pragma unroll
      for (int t = 2; t < 4; ++t) { MACC(ac5[0][t], bf[0][st], yf[st & 1][t]); if constexpr (st < 13) rz1(st + 1, (st + 1) & 1, t); MACC(ac5[1][t], bf[1][st], yf[st & 1][t]); }
      SCHED();
    });
    NOPS();
#pragma unroll
    for (int t = 0; t < 4; ++t) { KEEP(ac5[0][t]); KEEP(ac5[1][t]); }
    FS(44);
    BAR();
#pragma unroll
    for (int t = 1; t < 4; ++t) {
      ldsp d = lds + ((((t + kq) & 3) * 3 + (t - 1)) * 2 + ng5) * 2048 + lane * 16;
      *(LAS f32x4*)d = ac5[0][t]; *(LAS f32x4*)(d + 1024) = ac5[1][t];
    }
    const int tokl = 16 * kq + l15;
    const int t0 = col0 / 16 + 2 * ng5, t1 = (2 * ng5 + 1 < nblk) ? t0 + 1 : t0, m0_ = col0 >> 5;
    const u32x2 hr0 = *(LAS u32x2*)(lds + M_TILE + tokl * 192 + ((t0 >> 1) - m0_) * 64 + g * 16 + (t0 & 1) * 8);
    const u32x2 hr1 = *(LAS u32x2*)(lds + M_TILE + tokl * 192 + ((t1 >> 1) - m0_) * 64 + g * 16 + (t1 & 1) * 8);
    const u32x2 b20 = *(LAS u32x2*)(lds + M_B2 + (32 * ng5 + 4 * g) * 2), b21 = *(LAS u32x2*)(lds + M_B2 + ((2 * ng5 + 1 < nblk) ? (32 * ng5 + 16) : 32 * ng5) * 2 + 8 * g);
    f32x2 r00 = lo2(ac5[0][0]) + (bf2(b20.x) + bf2(hr0.x)), r01 = hi2(ac5[0][0]) + (bf2(b20.y) + bf2(hr0.y));
    f32x2 r10 = lo2(ac5[1][0]) + (bf2(b21.x) + bf2(hr1.x)), r11 = hi2(ac5[1][0]) + (bf2(b21.y) + bf2(hr1.y));
    BAR();
    FS(45);
#pragma unroll
    for (int t = 1; t < 4; ++t) {
      ldsp d = lds + ((kq * 3 + (t - 1)) * 2 + ng5) * 2048 + lane * 16;
      const f32x4 u0 = *(LAS f32x4*)d, u1 = *(LAS f32x4*)(d + 1024);
      r00 += lo2(u0); r01 += hi2(u0); r10 += lo2(u1); r11 += hi2(u1);
    }
    GAS char* op = (GAS char*)p.out + (size_t)(128 * c + 64 * th5 + tokl) * 896 + (col0 + 32 * ng5 + 4 * g) * 2;
    *(GAS u32x2*)op = u32x2{cvtpk(r00), cvtpk(r01)};
    if (2 * ng5 + 1 < nblk) *(GAS u32x2*)(op + 32) = u32x2{cvtpk(r10), cvtpk(r11)};
    STAMP(15);
    FS(63);
  }
}

extern "C" void kernel_launch(void* const* d_in, const int* in_sizes, int n_in, void* d_out, int out_size, void* d_ws, size_t ws_size, hipStream_t stream) {
  Params p;
  p.x = (const char*)d_in[0]; p.wqkv = (const char*)d_in[1]; p.wo = (const char*)d_in[2];
  p.w1 = (const char*)d_in[4]; p.b1 = (const char*)d_in[5]; p.w2 = (const char*)d_in[6]; p.b2 = (const char*)d_in[7];
  p.out = (char*)d_out; p.ws = (char*)d_ws;
  (void)hipFuncSetAttribute((const void*)fused_block, hipFuncAttributeMaxDynamicSharedMemorySize, LDS_BYTES);
  hipLaunchKernelGGL(fused_block, dim3(256), dim3(512), LDS_BYTES, stream, p);
}
```

```cpp
#define E2_TLB
#define MAPA
#define E1_CODE
#include <hip/hip_runtime.h>
#include <hip/hip_bf16.h>
#include <stdint.h>
#include <type_traits>

#define LAS __attribute__((address_space(3)))
#define GAS __attribute__((address_space(1)))
typedef short bf16x8 __attribute__((ext_vector_type(8)));
typedef short s16x4 __attribute__((ext_vector_type(4)));
typedef short v4i16_t __attribute__((ext_vector_type(4)));
typedef float f32x4 __attribute__((ext_vector_type(4)));
typedef float f32x2 __attribute__((ext_vector_type(2)));
typedef unsigned u32x4 __attribute__((ext_vector_type(4)));
typedef unsigned u32x2 __attribute__((ext_vector_type(2)));
typedef __bf16 bf16x2_t __attribute__((ext_vector_type(2)));
typedef LAS char* ldsp;

constexpr int SEQ = 2048;
constexpr size_t WS_CTL = 0;
constexpr size_t WS_KV = 262144;
constexpr size_t WS_O = WS_KV + 256 * 16384;
constexpr size_t WS_H = WS_O + 256 * 8192;
constexpr size_t WS_H1 = WS_H + (size_t)SEQ * 896;
constexpr size_t WS_ST = WS_H1 + (size_t)SEQ * 3584;
constexpr size_t WS_WT = WS_ST + (size_t)SEQ * 32;
constexpr unsigned WT_W1 = 0, WT_W2 = WT_W1 + 1568 * 1024, WT_BYTES = WT_W2 + 1568 * 1024;
constexpr size_t WS_DBG = WS_WT + WT_BYTES;
constexpr int LDS_BYTES = 160 * 1024;
constexpr int M_PT = 143360;
constexpr int M_XT = 147456;
constexpr int M_TILE = 131072;
constexpr int MISC = 157696;
constexpr int M_LN = MISC, M_B1 = MISC + 1024, M_B2 = MISC + 1280, M_PF = MISC + 1536, M_PART = MISC + 2048, M_FILL = MISC + 3072;
constexpr unsigned SPIN_CAP = 1u << 16;
constexpr unsigned CODE_LINES = 208;

#define FLAG_IDX(ph, c, j) (64 + (((ph) * 16 + (c)) * 16 + (j)) * 32)

#define WAITV(n) asm volatile("s_waitcnt vmcnt(" #n ")" ::: "memory")
#define BAR() do { asm volatile("s_waitcnt lgkmcnt(0)" ::: "memory"); __builtin_amdgcn_s_barrier(); asm volatile("" ::: "memory"); } while (0)
#define BARN() do { asm volatile("" ::: "memory"); __builtin_amdgcn_s_barrier(); asm volatile("" ::: "memory"); } while (0)
#define SCHED() __builtin_amdgcn_sched_barrier(0)
#define RAWBAR() __builtin_amdgcn_s_barrier()
#define ILV(n) do { _Pragma("unroll") for (int i_ = 0; i_ < (n); ++i_) { __builtin_amdgcn_sched_group_barrier(0x008, 1, 0); __builtin_amdgcn_sched_group_barrier(0x100, 1, 0); } } while (0)
#define MFMA(a, b, c) __builtin_amdgcn_mfma_f32_16x16x32_bf16((a), (b), (c), 0, 0, 0)
#define MACC(acc, a, b) asm volatile("v_mfma_f32_16x16x32_bf16 %0, %1, %2, %0" : "+v"(acc) : "v"(a), "v"(b))
#define MACCN(acc, a, b) asm volatile("s_nop 1\n\tv_mfma_f32_16x16x32_bf16 %0, %1, %2, %0" : "+v"(acc) : "v"(a), "v"(b))
#define NOPS() asm volatile("s_nop 7\n\ts_nop 3")
#define KEEP(x) asm volatile("" : "+v"(x))
#define IC(x) std::integral_constant<int, (x)>{}

template <int I, int N, class F> __device__ __forceinline__ void sfor(F&& f) { if constexpr (I < N) { f(std::integral_constant<int, I>{}); sfor<I + 1, N>(f); } }

__device__ __forceinline__ unsigned cvtpk(float lo, float hi) { f32x2 v = {lo, hi}; bf16x2_t b = __builtin_convertvector(v, bf16x2_t); return __builtin_bit_cast(unsigned, b); }
__device__ __forceinline__ unsigned cvtpk(f32x2 v) { bf16x2_t b = __builtin_convertvector(v, bf16x2_t); return __builtin_bit_cast(unsigned, b); }
__device__ __forceinline__ float dot2(unsigned a, unsigned b, float c) { return __builtin_amdgcn_fdot2_f32_bf16(__builtin_bit_cast(bf16x2_t, a), __builtin_bit_cast(bf16x2_t, b), c, false); }
__device__ __forceinline__ float bflo(unsigned u) { return __uint_as_float(u << 16); }
__device__ __forceinline__ float bfhi(unsigned u) { return __uint_as_float(u & 0xffff0000u); }
__device__ __forceinline__ f32x2 bf2(unsigned u) { return f32x2{bflo(u), bfhi(u)}; }
__device__ __forceinline__ f32x2 lo2(f32x4 v) { return f32x2{v[0], v[1]}; }
__device__ __forceinline__ f32x2 hi2(f32x4 v) { return f32x2{v[2], v[3]}; }
__device__ __forceinline__ bf16x8 ld128(ldsp p) { return *(const LAS bf16x8*)p; }
__device__ __forceinline__ s16x4 ldtr(ldsp p) { return __builtin_bit_cast(s16x4, __builtin_amdgcn_ds_read_tr16_b64_v4i16((LAS v4i16_t*)p)); }
__device__ __forceinline__ bf16x8 cat(s16x4 lo, s16x4 hi) { return __builtin_shufflevector(lo, hi, 0, 1, 2, 3, 4, 5, 6, 7); }
__device__ __forceinline__ int rfl(int x) { return __builtin_amdgcn_readfirstlane(x); }
template <int LADD, int VADD, bool SC1>
__device__ __forceinline__ void dma(unsigned voff, const void* sbase, unsigned lbase) {
  const unsigned va = voff + (unsigned)VADD;
  if constexpr (SC1) asm volatile("s_add_i32 m0, %2, %3\n\ts_nop 0\n\tglobal_load_lds_dwordx4 %0, %1 sc1" ::"v"(va), "s"(sbase), "s"(lbase), "i"(LADD) : "memory", "scc");
  else asm volatile("s_add_i32 m0, %2, %3\n\ts_nop 0\n\tglobal_load_lds_dwordx4 %0, %1" ::"v"(va), "s"(sbase), "s"(lbase), "i"(LADD) : "memory", "scc");
}
template <int LADD>
__device__ __forceinline__ void dma2c_sc1(unsigned voff, const void* sbase, unsigned lbase) {
  asm volatile("s_add_i32 m0, %2, %3\n\ts_nop 0\n\tglobal_load_lds_dwordx4 %0, %1 sc1\n\tglobal_load_lds_dwordx4 %0, %1 offset:1024 sc1" ::"v"(voff), "s"(sbase), "s"(lbase), "i"(LADD) : "memory", "scc");
}
template <int OFF>
__device__ __forceinline__ void ldfrag1(bf16x8& f, unsigned voff, const void* sbase) {
  asm volatile("global_load_dwordx4 %0, %1, %2 offset:%3 sc1" : "=v"(f) : "v"(voff), "s"(sbase), "i"(OFF) : "memory");
}
__device__ __forceinline__ void pin1(bf16x8& f) { asm volatile("" : "+v"(f)); }
template <int N>
__device__ __forceinline__ void ldfrags(bf16x8* f, unsigned voff, const void* sbase) {
  sfor<0, N>([&](auto I) { constexpr int i = decltype(I)::value; ldfrag1<(i & 3) * 1024>(f[i], voff + (unsigned)((i >> 2) * 4096), sbase); });
}
template <int N>
__device__ __forceinline__ void pin(bf16x8* f) { sfor<0, N>([&](auto I) { pin1(f[decltype(I)::value]); }); }
__device__ __forceinline__ void st128_sc1(unsigned voff, const void* sbase, u32x4 v) {
  asm volatile("global_store_dwordx4 %0, %1, %2 sc1\n\ts_nop 1" ::"v"(voff), "v"(v), "s"(sbase) : "memory");
}
__device__ __forceinline__ void st64_sc1(unsigned voff, const void* sbase, u32x2 v) {
  asm volatile("global_store_dwordx2 %0, %1, %2 sc1\n\ts_nop 1" ::"v"(voff), "v"(v), "s"(sbase) : "memory");
}
__device__ __forceinline__ void st32_sc1(const void* sbase, unsigned v) {
  asm volatile("global_store_dword %0, %1, %2 sc1\n\ts_nop 1" ::"v"(0u), "v"(v), "s"(sbase) : "memory");
}
__device__ __forceinline__ float max3(float a, float b, float c) { return __builtin_elementwise_maximum(__builtin_elementwise_maximum(a, b), c); }
__device__ __forceinline__ float red_g_sum(float v) {
  auto a = __builtin_amdgcn_permlane16_swap(__float_as_uint(v), __float_as_uint(v), false, false);
  v = __uint_as_float(a[0]) + __uint_as_float(a[1]);
  auto b = __builtin_amdgcn_permlane32_swap(__float_as_uint(v), __float_as_uint(v), false, false);
  return __uint_as_float(b[0]) + __uint_as_float(b[1]);
}
__device__ __forceinline__ float red_g_max(float v) {
  auto a = __builtin_amdgcn_permlane16_swap(__float_as_uint(v), __float_as_uint(v), false, false);
  v = __builtin_elementwise_maximum(__uint_as_float(a[0]), __uint_as_float(a[1]));
  auto b = __builtin_amdgcn_permlane32_swap(__float_as_uint(v), __float_as_uint(v), false, false);
  return __builtin_elementwise_maximum(__uint_as_float(b[0]), __uint_as_float(b[1]));
}
__device__ __forceinline__ void poll_flags(const unsigned* base, unsigned voff, unsigned epoch) {
#pragma unroll 1
  for (unsigned spins = 0; spins < SPIN_CAP; ++spins) {
    unsigned v;
    asm volatile("global_load_dword %0, %1, %2 sc1\n\ts_waitcnt vmcnt(0)" : "=&v"(v) : "v"(voff), "s"(base) : "memory");
    if (__all(v == epoch)) break;
    __builtin_amdgcn_s_sleep(1);
  }
}

struct Params {
  const char* x; const char* wqkv; const char* wo; const char* w1; const char* b1; const char* w2; const char* b2;
  char* out; char* ws;
};

#define FS(i) do {} while (0)
#define STAMP(i) do {} while (0)

#define LANEVARS() int tz_ = tid; asm volatile("" : "+v"(tz_)); const int lane = tz_ & 63, l15 = lane & 15, g = lane >> 4, q4 = l15 >> 2, p4 = l15 & 3; \
  const unsigned lane16 = (unsigned)lane * 16u, x8 = (unsigned)(((lane & 7) ^ ((lane >> 3) & 7)) << 4); (void)q4; (void)p4; (void)g; (void)x8; (void)lane16

__global__ void __launch_bounds__(512, 2) fused_block(Params p) {
  extern __shared__ __attribute__((aligned(16))) char smem[];
  ldsp lds = (ldsp)smem;
  unsigned long long pc0; asm volatile("s_getpc_b64 %0" : "=s"(pc0));
  const int tid = threadIdx.x, lane = tid & 63, w = rfl(tid >> 6);
  const int l15 = lane & 15, g = lane >> 4, q4 = l15 >> 2, p4 = l15 & 3;
  const int b = blockIdx.x, r_ = b >> 3;
  const int c = 2 * (b & 7) + (r_ >> 4), j = r_ & 15;
  const int ca = 4 * ((b & 7) >> 1) + (r_ >> 3), ha = 8 * (b & 1) + (r_ & 7);
  const unsigned* ctl = (const unsigned*)(p.ws + WS_CTL);
  unsigned epoch = 0;
  FS(0);
  STAMP(0);
  const unsigned ONES = 0x3f803f80u;
  const unsigned lane16 = (unsigned)lane * 16u;
  const unsigned x8 = (unsigned)(((lane & 7) ^ ((lane >> 3) & 7)) << 4);

  constexpr int KIMG = 0, VIMG = 24576, QIMG = 49152;
  {
    const int h = ha;
    constexpr int SLOT = 28672, WOFF = 16384;
    const unsigned iw = (unsigned)(w >> 2);
    unsigned voff[4];
    {
      const unsigned xo = (unsigned)((32 * w + (lane >> 3)) * 896) + x8;
#pragma unroll
      for (int ii = 0; ii < 4; ++ii) {
        const unsigned u = (unsigned)(192 * (w & 3) + 64 * ii + lane), row = (u * 43691u) >> 19, rem = u - 12u * row;
        const unsigned piece = rem >> 2, c16 = rem & 3, hb = (c16 >> 1) ^ ((row >> 3) & 1);
        const unsigned wo_ = (ii < 3) ? row * 2688u + piece * 896u + hb * 32u + (c16 & 1) * 16u : 0u;
        voff[ii] = iw ? wo_ : xo + ii * 7168u;
      }
    }
    const unsigned long long mW = 0ull - (unsigned long long)iw;
    const unsigned long long bx_ = (unsigned long long)(uintptr_t)p.x + (unsigned)(ca * 128 * 896), bw_ = (unsigned long long)(uintptr_t)p.wqkv + (unsigned)(56 * h - 8 * (h & 1));
    const char* gb = (const char*)(uintptr_t)(bx_ + ((bw_ - bx_) & mW));
    const unsigned kstep = 128 + iw * (64 * 2688 - 128), ldst = w * 4096 + iw * (WOFF + (w - 4) * 3072 - w * 4096);
    const unsigned smask = iw - 1u, l3 = iw ? (unsigned)M_FILL : ldst + 3072u;
    auto issue1 = [&](auto SL, auto II) {
      constexpr int sl = decltype(SL)::value, ii = decltype(II)::value;
      if constexpr (ii < 3) dma<sl * SLOT + ii * 1024, 0, false>(voff[ii], gb, ldst);
      else { dma<0, 0, false>(voff[3], gb, l3 + (smask & (unsigned)(sl * SLOT))); gb += kstep; }
    };
    auto issue = [&](auto SL) { issue1(SL, IC(0)); issue1(SL, IC(1)); issue1(SL, IC(2)); issue1(SL, IC(3)); };
    FS(1);
    issue(IC(0));
    RAWBAR(); issue(IC(1));
    {
      unsigned ln = (unsigned)(r_ * 8 + w); if (ln >= CODE_LINES) ln = 0;
      const unsigned long long a = (pc0 & ~255ull) + (unsigned long long)(ln * 128u);
      if (lane == 0) asm volatile("s_mov_b32 m0, %1\n\ts_nop 0\n\tglobal_load_lds_dword %0, off" ::"v"(a), "i"(M_FILL) : "memory");
    }

    {
      const unsigned long long A0 = (unsigned long long)(uintptr_t)p.ws, A1 = (unsigned long long)(uintptr_t)p.w1, A2 = (unsigned long long)(uintptr_t)p.w2, A3 = (unsigned long long)(uintptr_t)p.out, A4 = (unsigned long long)(uintptr_t)p.wo;
      const int sl = r_ * 64 + lane;
      unsigned long long a = A0 + (unsigned long long)(sl < 1280 ? sl : 0) * 16384ull;
      a = (sl >= 1280) ? A1 + (unsigned long long)(sl - 1280) * 16384ull : a;
      a = (sl >= 1378) ? A2 + (unsigned long long)(sl - 1378) * 16384ull : a;
      a = (sl >= 1476) ? A3 + (unsigned long long)(sl - 1476) * 16384ull : a;
      a = (sl >= 1588) ? A4 + (unsigned long long)(sl - 1588) * 16384ull : a;
      a = (sl >= 1612) ? A0 : a;
      if (w == 3) asm volatile("s_mov_b32 m0, %1\n\ts_nop 0\n\tglobal_load_lds_dword %0, off" ::"v"(a), "i"(M_FILL) : "memory");
    }
    FS(2);
    const int ptq0 = (b < 224) ? b : 223, ptm = (ptq0 >= 112) ? 2 : 1, ptq = ptq0 - ((ptm == 2) ? 112 : 0);
    const int pts = (ptm == 1) ? ptq >> 3 : ptq >> 1, ptG = (ptm == 1) ? ptq & 7 : ptq & 1;
    auto pt_issue = [&]() {
      const unsigned stride = (ptm == 1) ? 3584u : 896u;
      const char* src = ((ptm == 1) ? p.w1 : p.w2) + (size_t)(32 * pts * stride + ptG * 448);
      const int wq = (w < 7) ? w : 6;
#pragma unroll
      for (int i = 0; i < 2; ++i) { const unsigned u = (unsigned)((2 * wq + i) * 64 + lane), row = (u * 9363u) >> 18, c16 = u - 28u * row;
        dma<M_PT, 0, false>(row * stride + c16 * 16u, src, (unsigned)((2 * wq + i) * 1024)); }
      if (lane == 63) asm volatile("s_mov_b32 m0, %2\n\ts_nop 0\n\tglobal_load_lds_dword %0, %1 sc1" ::"v"(0u), "s"(p.ws + WS_CTL), "i"(M_PF) : "memory");
    };
    const int tg = w >> 1, ng = w & 1;
    f32x4 acc[3][2], asum = {0, 0, 0, 0}, asq = {0, 0, 0, 0}, acs[3];
#pragma unroll
    for (int e = 0; e < 3; ++e) { acc[e][0] = f32x4{0, 0, 0, 0}; acc[e][1] = f32x4{0, 0, 0, 0}; acs[e] = f32x4{0, 0, 0, 0}; }
    const int xb = (32 * tg + 16 * ng + l15) * 128 + ((g ^ (l15 & 7)) << 4);
    int ab[3];
#pragma unroll
    for (int e = 0; e < 3; ++e) ab[e] = WOFF + (8 * g + q4) * 192 + ((((3 * ng + e) * 32) ^ ((g & 1) << 5))) + 8 * p4;
    bf16x8 xf[2][2];
    s16x4 al[2][3], ah[2][3];
    auto rdx = [&](int st, int buf, int t) { xf[buf][t] = ld128(lds + ((st >> 1) % 5) * SLOT + (xb ^ ((st & 1) << 6) ^ (t * 2048))); };
    auto rdl = [&](int st, int buf, int e) { al[buf][e] = ldtr(lds + ((st >> 1) % 5) * SLOT + ab[e] + (32 * (st & 1)) * 192); };
    auto rdh_ = [&](int st, int buf, int e) { ah[buf][e] = ldtr(lds + ((st >> 1) % 5) * SLOT + ab[e] + (32 * (st & 1) + 4) * 192); };
    auto rdf = [&](int st, int buf) { rdx(st, buf, 0); rdx(st, buf, 1);
#pragma unroll
      for (int e = 0; e < 3; ++e) { rdl(st, buf, e); rdh_(st, buf, e); } };
    bf16x8 onesf = __builtin_bit_cast(bf16x8, (u32x4{ONES, ONES, ONES, ONES}));
    KEEP(onesf); KEEP(asum); KEEP(asq);
#pragma unroll
    for (int e = 0; e < 3; ++e) { KEEP(acc[e][0]); KEEP(acc[e][1]); KEEP(acs[e]); }
    WAITV(5);
    BAR();
    FS(3);
    rdf(0, 0);
    sfor<0, 14>([&](auto ST) {
      constexpr int st = decltype(ST)::value, kc = st >> 1, cb = st & 1;
      if constexpr ((st & 1) && st < 13) {
        if constexpr (kc == 0) WAITV(5);
        if constexpr (kc == 0) {} else if constexpr (kc == 1) WAITV(9); else if constexpr (kc <= 3) WAITV(6); else if constexpr (kc == 4) WAITV(4); else WAITV(0);
        BARN();
      }
      if constexpr (st == 2) pt_issue();
      constexpr int nb = (st + 1) & 1; constexpr bool more = st < 13;
      const bf16x8 a0 = cat(al[cb][0], ah[cb][0]), a1 = cat(al[cb][1], ah[cb][1]), a2 = cat(al[cb][2], ah[cb][2]);
      SCHED();
      MACC(acc[0][0], a0, xf[cb][0]); if constexpr (more) rdx(st + 1, nb, 0);
      MACC(acc[0][1], a0, xf[cb][1]); if constexpr (more) rdl(st + 1, nb, 0);
      MACC(asum, onesf, xf[cb][0]); if constexpr (more) rdh_(st + 1, nb, 0);
      MACC(acs[0], a0, onesf);
      SCHED();
      if constexpr (st < 2) { issue1(IC(st + 2), IC(0)); issue1(IC(st + 2), IC(1)); }
      else if constexpr (kc + 3 <= 6) issue1(IC((kc + 3) % 5), IC(2 * (st & 1)));
      SCHED();
      MACC(acc[1][0], a1, xf[cb][0]); if constexpr (more) rdx(st + 1, nb, 1);
      MACC(acc[1][1], a1, xf[cb][1]); if constexpr (more) rdl(st + 1, nb, 1);
      MACC(asq, xf[cb][0], xf[cb][0]); if constexpr (more) rdh_(st + 1, nb, 1);
      MACC(acs[1], a1, onesf);
      SCHED();
      if constexpr (st < 2) { issue1(IC(st + 2), IC(2)); issue1(IC(st + 2), IC(3)); }
      else if constexpr (kc + 3 <= 6) issue1(IC((kc + 3) % 5), IC(2 * (st & 1) + 1));
      SCHED();
      MACC(acc[2][0], a2, xf[cb][0]); if constexpr (more) rdl(st + 1, nb, 2);
      MACC(acc[2][1], a2, xf[cb][1]); if constexpr (more) rdh_(st + 1, nb, 2);
      MACC(acs[2], a2, onesf);
      SCHED();
    });
    NOPS(); KEEP(asum); KEEP(asq);
#pragma unroll
    for (int e = 0; e < 3; ++e) { KEEP(acc[e][0]); KEEP(acc[e][1]); KEEP(acs[e]); }
    FS(4);
    STAMP(1);
    epoch = (unsigned)rfl((int)*(LAS unsigned*)(lds + M_PF + 252)) + 1u;
    {
      const unsigned ptb = (ptm == 1) ? WT_W1 : WT_W2, ptn = (ptm == 1) ? 14u : 56u;
      ldsp src = lds + M_PT + (4 * g + q4) * 448 + 8 * p4;
#pragma unroll
      for (int i = 0; i < 2; ++i) {
        const int t = (w + 8 * i < 14) ? w + 8 * i : w;
        const bf16x8 f = cat(ldtr(src + t * 32), ldtr(src + 16 * 448 + t * 32));
        st128_sc1(lane16 + ptb + ((unsigned)(14 * ptG + t) * ptn + (unsigned)pts) * 1024u, p.ws + WS_WT, __builtin_bit_cast(u32x4, f));
      }
    }
    {
      const float d01 = (p4 & 1) ? asq[1] : asq[0], d23 = (p4 & 1) ? asq[3] : asq[2], dq = (p4 & 2) ? d23 : d01;
      float mean = asum[0] * (1.f / 448.f), dq2 = dq;
      KEEP(mean); KEEP(dq2);
      if (g == q4) *(LAS f32x2*)(lds + M_LN + (32 * tg + 16 * ng + l15) * 8) = f32x2{mean, rsqrtf(fmaxf(dq2 * (1.f / 448.f) - mean * mean, 0.f) + 1e-5f)};
    }
    BAR();
    FS(5);
    float mu[2], rs[2];
#pragma unroll
    for (int t = 0; t < 2; ++t) { const f32x2 st_ = *(LAS f32x2*)(lds + M_LN + (32 * tg + 16 * (t ^ ng) + l15) * 8); mu[t] = st_[0]; rs[t] = st_[1]; }
    const bool pad0 = (h & 1) && g == 0, pad1 = !(h & 1) && g == 3;
    const int rsw = ((l15 >> 1) & 3) << 4, rowb = (32 * tg + l15) * 64;
    auto epi = [&](auto NG) {
      constexpr int ngc = decltype(NG)::value;
      sfor<0, 3>([&](auto E) {
        constexpr int e = decltype(E)::value, nt = 3 * ngc + e, which = nt >> 1, par = nt & 1;
        constexpr int ibase = (which == 0) ? QIMG : (which == 1) ? KIMG + 16384 : VIMG + 16384;
        const int off = (which == 2) ? ((((2 * par + (g >> 1)) << 4) ^ rsw) + (g & 1) * 8) : (((g << 4) ^ rsw) + par * 8);
        const bool pad = par ? pad1 : pad0;
#pragma unroll
        for (int t = 0; t < 2; ++t) {
          const float sc = (which == 0) ? rs[t] * 0.27264657f : rs[t], nm = -mu[t] * sc;
          f32x2 sc2 = {sc, sc}, nm2 = {nm, nm};
          KEEP(sc2); KEEP(nm2);
          const f32x2 v0 = lo2(acc[e][t]) * sc2 + lo2(acs[e]) * nm2, v1 = hi2(acc[e][t]) * sc2 + hi2(acs[e]) * nm2;
          u32x2 o; o.x = cvtpk(v0); o.y = cvtpk(v1);
          o.x = pad ? ((which == 2) ? 0x00003f80u : 0u) : o.x; o.y = pad ? 0u : o.y;
          *(LAS u32x2*)(lds + ibase + (t ^ ngc) * 1024 + rowb + off) = o;
        }
      });
    };
    if (ng) epi(IC(1)); else epi(IC(0));
    if (ca < 2) {
      const u32x4 z = {0, 0, 0, 0};
#pragma unroll
      for (int i = 0; i < 2; ++i) { *(LAS u32x4*)(lds + KIMG + tid * 16 + i * 8192) = z; *(LAS u32x4*)(lds + VIMG + tid * 16 + i * 8192) = z; }
    }
    BAR();
    FS(6);
    {
      const char* blob = p.ws + WS_KV + (size_t)(ca * 16 + h) * 16384;
      const u32x4 v0 = *(LAS u32x4*)(lds + KIMG + 16384 + tid * 16), v1 = *(LAS u32x4*)(lds + VIMG + 16384 + tid * 16);
      st128_sc1(tid * 16, blob, v0); st128_sc1(8192 + tid * 16, blob, v1);
    }
    FS(7);
    WAITV(0);
    BAR();
    if (tid == 0) { st32_sc1(ctl + FLAG_IDX(0, ca, h), epoch); st32_sc1(ctl + FLAG_IDX(4, b >> 4, b & 15), epoch); }
    FS(8);
    STAMP(2);
  }
  const int ns = j & 3, col4 = 128 * ns - (((ns >> 1) & ns) << 5), ngr = 4 - (ns >> 1);
  const int pr = w & 3, wh = w >> 2;
  const int prc = (pr < ngr) ? pr : 0;
  const int th5 = j >> 3, cs5 = j & 7, col0 = 64 * cs5 - 16 * ((cs5 >> 2) * (cs5 & 3)), nblk = 4 - (cs5 >> 2);
  const int ng5 = w & 1, kq = w >> 1;
  const char* wt = p.ws + WS_WT;
  bf16x8 wr[32];
  const int T1 = (448 * (j >> 2) + col4) / 16;
  const unsigned w1o = WT_W1 + (unsigned)(T1 + 2 * prc) * 14u * 1024u;
  const unsigned wro = lane16 + w1o;
  constexpr int WI = 32768;
  unsigned wb0, wb1;
  { const int rin = lane >> 4, pos = lane & 15;
    int blk = (pos >> 1) ^ rin; if (blk >= 2 * ngr) blk -= 2; wb0 = (unsigned)(blk * 32 + (pos & 1) * 16);
    blk = (pos >> 1) ^ (4 + rin); if (blk >= 2 * ngr) blk -= 2; wb1 = (unsigned)(blk * 32 + (pos & 1) * 16); }
  const char* wob = p.wo + col4 * 2;
  {
    if (w < 6) {
      const unsigned rb = (unsigned)(((lane >> 4) + 4 * (24 + w)) * 896) + ((w & 1) ? wb1 : wb0);
      sfor<0, 14>([&](auto I) { constexpr int i = decltype(I)::value; dma<i * 6144, i * 24 * 896, false>(rb, wob, (unsigned)(WI + (24 + w) * 1024)); });
    }
    int pos = lane & 15; if (pos >= 4 * ngr) pos = 4 * ngr - 1;
    int xch = (lane & 15) ^ (((lane >> 4) + 4 * w) & 15); if (xch >= 4 * ngr) xch = 0;
    dma<M_XT, 0, false>((unsigned)(((lane >> 4) + 4 * w) * 896 + xch * 16), p.x + (size_t)(128 * c + 32 * (j >> 2)) * 896 + col4 * 2, (unsigned)(w * 1024));
    if (w == 5 && lane < 16) dma<M_B1, 0, false>((unsigned)(pos * 16), p.b1 + (448 * (j >> 2) + col4) * 2, 0u);
    if (w == 4 && lane < 8) dma<M_B2, 0, false>((unsigned)((lane < 2 * nblk ? lane : 0) * 16), p.b2 + col0 * 2, 0u);
  }
  {
    const int h = ha;
    FS(9);
    const int sw = (l15 >> 1) & 3;
    if (w == 7 && ca >= 1) poll_flags(ctl, (unsigned)(FLAG_IDX(0, (ca >= 2 && (lane & 1)) ? ca - 2 : ca - 1, h) * 4), epoch);
    if (w == 6) {
      const unsigned tfo = (unsigned)(FLAG_IDX(4, 4 * g, l15) * 4);
#pragma unroll 1
      for (unsigned spins = 0; spins < SPIN_CAP; ++spins) {
        unsigned t0, t1, t2, t3;
        asm volatile("global_load_dword %0, %4, %5 sc1\n\tglobal_load_dword %1, %4, %5 offset:2048 sc1\n\tglobal_load_dword %2, %6, %5 sc1\n\tglobal_load_dword %3, %6, %5 offset:2048 sc1\n\ts_waitcnt vmcnt(0)"
                     : "=&v"(t0), "=&v"(t1), "=&v"(t2), "=&v"(t3) : "v"(tfo), "s"(ctl), "v"(tfo + 4096u) : "memory");
        if (__all(t0 == epoch && t1 == epoch && t2 == epoch && t3 == epoch)) break;
        __builtin_amdgcn_s_sleep(2);
      }
      if (b == 0 && lane == 0) st32_sc1(ctl, epoch);
    }
    BAR();
    FS(10);
    {
      const int tl = w >> 2, src = ca - 2 + tl, pc = (w & 3) * 2048;
      if (src >= 0) {
        const char* blob = p.ws + WS_KV + (size_t)(src * 16 + h) * 16384 + pc;
        dma2c_sc1<KIMG>(lane16, blob, (unsigned)(tl * 8192 + pc)); dma2c_sc1<VIMG>(lane16, blob + 8192, (unsigned)(tl * 8192 + pc));
      }
    }
    const bf16x8 qf = ld128(lds + QIMG + (16 * w + l15) * 64 + ((g ^ sw) << 4));
    WAITV(0);
    BAR();
    FS(11);
    STAMP(3);
    STAMP(4);
    if (w >= 4) ldfrags<7>(wr, wro, wt);
    f32x4 sc[17];
    const float NEG = -__builtin_inff();
    ldsp kb = lds + KIMG + (16 * w + l15) * 64 + ((g ^ sw) << 4);
    {
      bf16x8 kf[2][6];
#pragma unroll
      for (int u = 0; u < 6; ++u) kf[0][u] = ld128(kb + u * 1024);
#pragma unroll
      for (int bt = 0; bt < 3; ++bt) {
        if (bt < 2) {
#pragma unroll
          for (int u = 0; u < 6; ++u) if (6 * (bt + 1) + u < 17) kf[(bt + 1) & 1][u] = ld128(kb + (6 * (bt + 1) + u) * 1024);
        }
        SCHED();
#pragma unroll
        for (int u = 0; u < 6; ++u) if (6 * bt + u < 17) sc[6 * bt + u] = MFMA(kf[bt & 1][u], qf, (f32x4{0, 0, 0, 0}));
        SCHED();
      }
    }
    FS(12);
    if (w >= 4) ldfrags<7>(wr + 7, wro + 7 * 1024, wt);
    const int vsw = (2 * g + (q4 >> 1)) & 3;
    ldsp vb0 = lds + VIMG + (16 * w + 4 * g + q4) * 64 + (((p4 >> 1) ^ vsw) << 4) + (p4 & 1) * 8;
    ldsp vb1 = lds + VIMG + (16 * w + 4 * g + q4) * 64 + (((2 + (p4 >> 1)) ^ vsw) << 4) + (p4 & 1) * 8;
    bf16x8 vf[2][3][2];
    auto rdv = [&](int bt) {
#pragma unroll
      for (int i = 0; i < 3; ++i) { const int uu = 3 * bt + i, ua = 2 * uu, ub = (uu < 8) ? 2 * uu + 1 : 15;
        vf[bt & 1][i][0] = cat(ldtr(vb0 + ua * 1024), ldtr(vb0 + ub * 1024)); vf[bt & 1][i][1] = cat(ldtr(vb1 + ua * 1024), ldtr(vb1 + ub * 1024)); }
    };
    rdv(0);
    SCHED();
    if (ca < 2) {
      const int tmin = (2 - ca) * 8;
#pragma unroll
      for (int u = 0; u < 16; ++u) if (w + u < tmin) sc[u] = f32x4{NEG, NEG, NEG, NEG};
    }
#pragma unroll
    for (int r = 0; r < 4; ++r) { sc[0][r] = (4 * g + r <= l15) ? NEG : sc[0][r]; sc[16][r] = (4 * g + r > l15) ? NEG : sc[16][r]; }
    float m = max3(sc[0][0], sc[0][1], sc[0][2]), m2 = max3(sc[0][3], sc[1][0], sc[1][1]);
    m = max3(m, sc[1][2], sc[1][3]);
#pragma unroll
    for (int u = 2; u < 17; ++u) { if (u & 1) m = max3(max3(m, sc[u][0], sc[u][1]), sc[u][2], sc[u][3]); else m2 = max3(max3(m2, sc[u][0], sc[u][1]), sc[u][2], sc[u][3]); }
    m = max3(m, m2, m2);
    m = red_g_max(m);
    FS(13);
    SCHED();
    if (w >= 4) ldfrags<7>(wr + 14, wro + 14 * 1024, wt);
    unsigned pk[17][2];
    f32x2 mm = {-m, -m};
    KEEP(mm);
    f32x4 oa[2] = {f32x4{0, 0, 0, 0}, f32x4{0, 0, 0, 0}};
    KEEP(oa[0]); KEEP(oa[1]);
#pragma unroll
    for (int bt = 0; bt < 3; ++bt) {
#pragma unroll
      for (int u = 6 * bt; u < 6 * bt + 6 && u < 17; ++u) {
        const f32x2 d0 = lo2(sc[u]) + mm, d1 = hi2(sc[u]) + mm;
        pk[u][0] = cvtpk(__builtin_amdgcn_exp2f(d0[0]), __builtin_amdgcn_exp2f(d0[1]));
        pk[u][1] = cvtpk(__builtin_amdgcn_exp2f(d1[0]), __builtin_amdgcn_exp2f(d1[1]));
      }
      if (bt < 2) rdv(bt + 1);
      if (bt == 0 && w >= 4) ldfrags<7>(wr + 21, wro + 21 * 1024, wt);
      SCHED();
#pragma unroll
      for (int i = 0; i < 3; ++i) {
        const int uu = 3 * bt + i;
        u32x4 pu; pu.x = pk[2 * uu][0]; pu.y = pk[2 * uu][1];
        if (uu < 8) { pu.z = pk[2 * uu + 1][0]; pu.w = pk[2 * uu + 1][1]; } else { pu.z = 0; pu.w = 0; }
        const bf16x8 pf = __builtin_bit_cast(bf16x8, pu);
        MACCN(oa[0], vf[bt & 1][i][0], pf); MACC(oa[1], vf[bt & 1][i][1], pf);
      }
      SCHED();
    }
    NOPS(); KEEP(oa[0]); KEEP(oa[1]);
    FS(15);
    int tz2 = tid; asm volatile("" : "+v"(tz2));
    const int l15b = tz2 & 15, gb = (tz2 & 63) >> 4, swb = (l15b >> 1) & 3;
    const float lsel = (h & 1) ? oa[0][0] : oa[1][0];
    const float l = __uint_as_float((unsigned)__builtin_amdgcn_ds_bpermute((((h & 1) ? 0 : 48) + l15b) * 4, (int)__float_as_uint(lsel)));
    const float inv1 = __builtin_amdgcn_rcpf(l);
    f32x2 inv = {inv1, inv1};
    KEEP(inv);
    u32x4 ov;
    ov.x = cvtpk(lo2(oa[0]) * inv); ov.y = cvtpk(hi2(oa[0]) * inv); ov.z = cvtpk(lo2(oa[1]) * inv); ov.w = cvtpk(hi2(oa[1]) * inv);
    { const bool z0 = (h & 1) && gb == 0, z1 = !(h & 1) && gb == 3; ov.x = z0 ? 0u : ov.x; ov.y = z0 ? 0u : ov.y; ov.z = z1 ? 0u : ov.z; ov.w = z1 ? 0u : ov.w; }
    st128_sc1((unsigned)((16 * w + l15b) * 64 + ((gb ^ swb) << 4)), p.ws + WS_O + (size_t)(ca * 16 + h) * 8192, ov);
    FS(16);
    BAR();
    {
      const unsigned rb = (unsigned)(((lane >> 4) + 12 * w) * 896), va = rb + ((w & 1) ? wb1 : wb0), vb = rb + ((w & 1) ? wb0 : wb1), lb = (unsigned)(WI + 3 * w * 1024);
      dma<0, 0, false>(va, wob, lb); dma<1024, 4 * 896, false>(vb, wob, lb); dma<2048, 8 * 896, false>(va, wob, lb);
      const int ins = (w < 4) ? 108 + w : 3 * w + 2;
      dma<0, 0, false>((unsigned)(((lane >> 4) + 4 * ins) * 896) + ((ins & 1) ? wb1 : wb0), wob, (unsigned)(WI + ins * 1024));
    }
    WAITV(4);
    BAR();
    if (tid == 0) st32_sc1(ctl + FLAG_IDX(1, ca, h), epoch);
    FS(18);
    STAMP(5);
  }
  {
    LANEVARS();
    constexpr int OT = 0;
    const int tq = j >> 2;
    WAITV(0);
    BAR();
    if (w < 4) {
      const int rx = 4 * (g & 1) + q4;
      ldsp ab0 = lds + WI + (4 * g + q4) * 256 + (((2 * prc) ^ rx) << 5) + 8 * p4;
      ldsp ab1 = lds + WI + (4 * g + q4) * 256 + (((2 * prc + 1) ^ rx) << 5) + 8 * p4;
      ldsp ab0h = ab0 + 65536, ab1h = ab1 + 65536;
#pragma unroll
      for (int s = 0; s < 16; ++s) {
        const int rb = (28 * s - 4 * (s & 1)) * 256;
        if (rb + 4096 < 65536) { wr[s] = cat(ldtr(ab0 + rb), ldtr(ab0 + rb + 4096)); wr[16 + s] = cat(ldtr(ab1 + rb), ldtr(ab1 + rb + 4096)); }
        else { wr[s] = cat(ldtr(ab0h + (rb - 65536)), ldtr(ab0h + (rb - 65536 + 4096))); wr[16 + s] = cat(ldtr(ab1h + (rb - 65536)), ldtr(ab1h + (rb - 65536 + 4096))); }
      }
    } else {
      __builtin_amdgcn_s_setprio(3);
      poll_flags(ctl, (unsigned)(FLAG_IDX(1, c, l15) * 4), epoch);
      const char* ob_ = p.ws + WS_O + (size_t)(c * 16 + 4 * (w - 4)) * 8192 + tq * 2048;
      const unsigned lb = (unsigned)(OT + (w - 4) * 8192);
      dma2c_sc1<0>(lane16, ob_, lb); dma2c_sc1<2048>(lane16 + 8192, ob_, lb); dma2c_sc1<4096>(lane16 + 16384, ob_, lb); dma2c_sc1<6144>(lane16 + 24576, ob_, lb);
      __builtin_amdgcn_s_setprio(0);
    }
    FS(19);
    WAITV(0);
    BAR();
    FS(20);
    STAMP(6);
    STAMP(7);
    if (w < 4) {
      f32x4 a0[2], a1[2];
#pragma unroll
      for (int t = 0; t < 2; ++t) { a0[t] = f32x4{0, 0, 0, 0}; a1[t] = f32x4{0, 0, 0, 0}; }
      ldsp ob = lds + OT + l15 * 64 + ((g ^ ((l15 >> 1) & 3)) << 4);
      u32x2 xr[2][2];
#pragma unroll
      for (int t = 0; t < 2; ++t) { ldsp xp = lds + M_XT + (16 * t + l15) * 256 + (g & 1) * 8;
        xr[t][0] = *(LAS u32x2*)(xp + (((4 * prc + (g >> 1)) ^ l15) << 4)); xr[t][1] = *(LAS u32x2*)(xp + (((4 * prc + 2 + (g >> 1)) ^ l15) << 4)); }
#pragma unroll
      for (int t = 0; t < 2; ++t) { KEEP(a0[t]); KEEP(a1[t]); }
      bf16x8 of[4][2];
#pragma unroll
      for (int s = 0; s < 3; ++s) { of[s][0] = ld128(ob + s * 2048); of[s][1] = ld128(ob + s * 2048 + 1024); }
#pragma unroll
      for (int s = 0; s < 16; ++s) {
        if (s + 3 < 16) { of[(s + 3) & 3][0] = ld128(ob + (s + 3) * 2048); of[(s + 3) & 3][1] = ld128(ob + (s + 3) * 2048 + 1024); }
        SCHED();
#pragma unroll
        for (int t = 0; t < 2; ++t) { MACC(a0[t], wr[s], of[s & 3][t]); MACC(a1[t], wr[16 + s], of[s & 3][t]); }
        SCHED();
      }
      NOPS();
#pragma unroll
      for (int t = 0; t < 2; ++t) { KEEP(a0[t]); KEEP(a1[t]); }
      FS(21);
      const unsigned ho = (unsigned)((128 * c + 32 * tq + l15) * 896 + (col4 / 32 + pr) * 64 + g * 16);
#pragma unroll
      for (int t = 0; t < 2; ++t) {
        u32x4 hv;
        hv.x = cvtpk(lo2(a0[t]) + bf2(xr[t][0].x)); hv.y = cvtpk(hi2(a0[t]) + bf2(xr[t][0].y));
        hv.z = cvtpk(lo2(a1[t]) + bf2(xr[t][1].x)); hv.w = cvtpk(hi2(a1[t]) + bf2(xr[t][1].y));
        if (pr < ngr) st128_sc1(ho + t * 16 * 896, p.ws + WS_H, hv);
        float s1 = 0.f, s2 = 0.f;
#pragma unroll
        for (int i = 0; i < 4; ++i) { s1 = dot2(hv[i], ONES, s1); s2 = dot2(hv[i], hv[i], s2); }
        s1 = red_g_sum(s1); s2 = red_g_sum(s2);
        if (pr >= ngr) { s1 = 0.f; s2 = 0.f; }
        if (g == 0) ((LAS f32x2*)(lds + M_PART))[(16 * t + l15) * 4 + pr] = f32x2{s1, s2};
      }
    } else {
      pin<28>(wr);
#pragma unroll
      for (int i = 0; i < 28; ++i) *(LAS bf16x8*)(lds + 32768 + (w - 4) * 28672 + i * 1024 + lane * 16) = wr[i];
    }
    FS(22);
    BAR();
    if (tid < 32) {
      const f32x4 u0 = *(LAS f32x4*)(lds + M_PART + tid * 32), u1 = *(LAS f32x4*)(lds + M_PART + tid * 32 + 16);
      st64_sc1((unsigned)((128 * c + 32 * tq + tid) * 32 + ns * 8), p.ws + WS_ST,
               u32x2{__float_as_uint((u0[0] + u0[2]) + (u1[0] + u1[2])), __float_as_uint((u0[1] + u0[3]) + (u1[1] + u1[3]))});
    }
    if (w < 4) {
#pragma unroll
      for (int i = 0; i < 28; ++i) wr[i] = ld128(lds + 32768 + w * 28672 + i * 1024 + lane * 16);
    }
    FS(23);
    if (w < 4) WAITV(0);
    BAR();
    if (tid == 0) st32_sc1(ctl + FLAG_IDX(2, c, j), epoch);
    FS(25);
    STAMP(8);
  }
  {
    LANEVARS();
    const int f0_ = 448 * (j >> 2) + col4, th = wh;
    FS(26);
    f32x4 c0 = {0, 0, 0, 0}, c1 = {0, 0, 0, 0};
    pin<28>(wr);
    bf16x8 onesf = __builtin_bit_cast(bf16x8, (u32x4{ONES, ONES, ONES, ONES}));
    KEEP(onesf); KEEP(c0); KEEP(c1);
    asm volatile("s_nop 1");
#pragma unroll
    for (int s = 0; s < 14; ++s) { MACC(c0, wr[s], onesf); MACC(c1, wr[14 + s], onesf); }
    NOPS(); KEEP(c0); KEEP(c1);
    SCHED();
    if (w == 7) poll_flags(ctl, (unsigned)(FLAG_IDX(2, c, l15) * 4), epoch);
    FS(29);
    BAR();
    const unsigned vo = (unsigned)((16 * w + (lane >> 3)) * 896) + x8, wl2k = (unsigned)(w * 2048);
    const char* hb = p.ws + WS_H + (size_t)c * 128 * 896;
    auto issue4 = [&](auto KC) { constexpr int kc = decltype(KC)::value; dma<kc * 16384, kc * 128, true>(vo, hb, wl2k); dma<kc * 16384 + 1024, kc * 128 + 8 * 896, true>(vo, hb, wl2k); };
    if (lane < 32) dma<M_TILE, 0, true>(lane16, p.ws + WS_ST + (size_t)(128 * c + 16 * w) * 32, (unsigned)(w * 512));
    issue4(IC(0)); RAWBAR(); issue4(IC(1));
    FS(31);
    f32x4 acc[2][4];
#pragma unroll
    for (int t = 0; t < 4; ++t) { acc[0][t] = f32x4{0, 0, 0, 0}; acc[1][t] = f32x4{0, 0, 0, 0}; }
    const int xb = (64 * th + l15) * 128 + ((g ^ (l15 & 7)) << 4);
    bf16x8 xf[2][4];
    auto rd1 = [&](int st, int buf, int t) { xf[buf][t] = ld128(lds + (st >> 1) * 16384 + (xb ^ ((st & 1) << 6)) + t * 2048); };
    auto rdf = [&](int st, int buf) { rd1(st, buf, 0); rd1(st, buf, 1); rd1(st, buf, 2); rd1(st, buf, 3); };
#pragma unroll
    for (int t = 0; t < 4; ++t) { KEEP(acc[0][t]); KEEP(acc[1][t]); }
    bf16x8 bf[2][14];
    const unsigned w2o0 = WT_W2 + ((unsigned)(col0 / 16 + 2 * ng5) * 56u + 14u * kq) * 1024u, w2o1 = (2 * ng5 + 1 < nblk) ? w2o0 + 56u * 1024u : w2o0;
    WAITV(2); BAR();
    FS(32);
    STAMP(9);
    STAMP(10);
    rdf(0, 0);
    sfor<0, 14>([&](auto ST) {
      constexpr int st = decltype(ST)::value;
      if constexpr (st == 1) WAITV(2); if constexpr (st == 3) WAITV(4); if constexpr (st == 5) WAITV(6); if constexpr (st == 7) WAITV(8); if constexpr (st == 9) WAITV(10); if constexpr (st == 11) WAITV(12);
      if constexpr ((st & 1) && st < 13) BARN();
      if constexpr (st == 2) {
        if (tid < 128) {
          const f32x4 u0 = *(LAS f32x4*)(lds + M_TILE + tid * 32), u1 = *(LAS f32x4*)(lds + M_TILE + tid * 32 + 16);
          float mean = ((u0[0] + u0[2]) + (u1[0] + u1[2])) * (1.f / 448.f), sq = (u0[1] + u0[3]) + (u1[1] + u1[3]);
          KEEP(mean); KEEP(sq);
          const float rstd = rsqrtf(fmaxf(sq * (1.f / 448.f) - mean * mean, 0.f) + 1e-5f);
          *(LAS f32x2*)(lds + M_LN + tid * 8) = f32x2{rstd, -mean * rstd};
        }
      }
      SCHED();
#pragma unroll
      for (int t = 0; t < 2; ++t) { MACC(acc[0][t], wr[st], xf[st & 1][t]); if constexpr (st < 13) rd1(st + 1, (st + 1) & 1, t); MACC(acc[1][t], wr[14 + st], xf[st & 1][t]); }
      SCHED();
      if constexpr (st <= 4) issue4(IC(st + 2));
      if constexpr (st >= 5) { constexpr int s = st - 5; ldfrags<1>(&bf[0][s], lane16 + w2o0 + s * 1024, wt); ldfrags<1>(&bf[1][s], lane16 + w2o1 + s * 1024, wt); }
      SCHED();
#pragma unroll
      for (int t = 2; t < 4; ++t) { MACC(acc[0][t], wr[st], xf[st & 1][t]); if constexpr (st < 13) rd1(st + 1, (st + 1) & 1, t); MACC(acc[1][t], wr[14 + st], xf[st & 1][t]); }
      SCHED();
    });
    NOPS();
#pragma unroll
    for (int t = 0; t < 4; ++t) { KEEP(acc[0][t]); KEEP(acc[1][t]); }
    FS(33);
    STAMP(11);
    {
      const u32x2 bb0 = *(LAS u32x2*)(lds + M_B1 + (32 * prc + 4 * g) * 2), bb1 = *(LAS u32x2*)(lds + M_B1 + (32 * prc + 4 * g) * 2 + 32);
      const f32x2 b00 = bf2(bb0.x), b01 = bf2(bb0.y), b10 = bf2(bb1.x), b11 = bf2(bb1.y);
      const unsigned ho = (unsigned)((128 * c + 64 * th + l15) * 3584 + (f0_ / 32 + pr) * 64 + g * 16);
      f32x2 ln[4];
#pragma unroll
      for (int t = 0; t < 4; ++t) ln[t] = *(LAS f32x2*)(lds + M_LN + (64 * th + 16 * t + l15) * 8);
#pragma unroll
      for (int t = 0; t < 4; ++t) {
        const float a = ln[t][0], nm = ln[t][1];
        f32x2 aa = {a, a}, nn = {nm, nm};
        KEEP(aa); KEEP(nn);
        f32x2 z[4] = {lo2(acc[0][t]) * aa + (lo2(c0) * nn + b00), hi2(acc[0][t]) * aa + (hi2(c0) * nn + b01), lo2(acc[1][t]) * aa + (lo2(c1) * nn + b10), hi2(acc[1][t]) * aa + (hi2(c1) * nn + b11)};
        u32x4 hv;
#pragma unroll
        for (int i = 0; i < 4; ++i) {
          f32x2 r = {__builtin_elementwise_maximum(z[i][0], 0.f), __builtin_elementwise_maximum(z[i][1], 0.f)};
          hv[i] = cvtpk(r * r);
        }
        if (pr < ngr) st128_sc1(ho + t * 16 * 3584, p.ws + WS_H1, hv);
      }
    }
    FS(34);
    WAITV(0);
    BAR();
    if (tid == 0) st32_sc1(ctl + FLAG_IDX(3, c, j), epoch);
    FS(38);
    STAMP(12);
    if (w != 7) {
      ldfrags<5>(&bf[0][9], lane16 + w2o0 + 9 * 1024, wt); ldfrags<5>(&bf[1][9], lane16 + w2o1 + 9 * 1024, wt);
    }
    if (w >= 5 && w < 7) {
      const int m0_ = col0 >> 5;
      const char* hr = p.ws + WS_H + (size_t)(128 * c + 64 * th5) * 896;
      sfor<0, 6>([&](auto I) { constexpr int i = decltype(I)::value;
        const unsigned u = (unsigned)((w - 5) * 384 + i * 64 + lane), r0 = (u * 43691u) >> 19, c0_ = u - 12u * r0; unsigned cc0 = m0_ * 4 + c0_; if (cc0 > 55) cc0 = 55;
        dma<M_TILE + i * 1024, 0, true>(r0 * 896 + cc0 * 16, hr, (unsigned)((w - 5) * 6144)); });
    }
    const unsigned vo5 = (unsigned)((32 * ng5 + (lane >> 3)) * 3584 + kq * 896) + x8, wl4k = (unsigned)(w * 4096);
    const char* hb5 = p.ws + WS_H1 + (size_t)(128 * c + 64 * th5) * 3584;
    auto issue5 = [&](auto I) {
      constexpr int i = decltype(I)::value, sa = (i & 3) * 32768;
      sfor<0, 4>([&](auto II) { constexpr int ii = decltype(II)::value; dma<sa + ii * 1024, i * 128 + ii * 8 * 3584, true>(vo5, hb5, wl4k); });
    };
    FS(39);
    if (w == 7) {
      poll_flags(ctl, (unsigned)(FLAG_IDX(3, c, l15) * 4), epoch);
    }
    FS(40);
    BAR();
    FS(41);
    STAMP(13);
    issue5(IC(0));
    if (w == 7) {
      ldfrags<5>(&bf[0][9], lane16 + w2o0 + 9 * 1024, wt); ldfrags<5>(&bf[1][9], lane16 + w2o1 + 9 * 1024, wt);
    }
    FS(42);
    f32x4 ac5[2][4];
#pragma unroll
    for (int t = 0; t < 4; ++t) { ac5[0][t] = f32x4{0, 0, 0, 0}; ac5[1][t] = f32x4{0, 0, 0, 0}; }
    int yb[4];
#pragma unroll
    for (int t = 0; t < 4; ++t) yb[t] = kq * 8192 + (16 * ((t + kq) & 3) + l15) * 128 + ((g ^ (l15 & 7)) << 4);
    bf16x8 yf[2][4];
    auto rz1 = [&](int st, int buf, int t) { yf[buf][t] = ld128(lds + ((st >> 1) & 3) * 32768 + (yb[t] ^ ((st & 1) << 6))); };
    auto rdy = [&](int st, int buf) { rz1(st, buf, 0); rz1(st, buf, 1); rz1(st, buf, 2); rz1(st, buf, 3); };
#pragma unroll
    for (int t = 0; t < 4; ++t) { KEEP(ac5[0][t]); KEEP(ac5[1][t]); }
    WAITV(0);
    pin<14>(bf[0]); pin<14>(bf[1]);
    BAR();
    FS(43);
    STAMP(14);
    rdy(0, 0);
    sfor<0, 14>([&](auto ST) {
      constexpr int st = decltype(ST)::value;
      if constexpr ((st & 1) && st < 13) {
        if constexpr (st < 11) WAITV(4); else WAITV(0);
        BARN();
      }
      SCHED();
#pragma unroll
      for (int t = 0; t < 2; ++t) { MACC(ac5[0][t], bf[0][st], yf[st & 1][t]); if constexpr (st < 13) rz1(st + 1, (st + 1) & 1, t); MACC(ac5[1][t], bf[1][st], yf[st & 1][t]); }
      SCHED();
      if constexpr (st == 0) { issue5(IC(1)); issue5(IC(2)); }
      if constexpr ((st & 1) && st <= 7) issue5(IC((st + 5) / 2));
      SCHED();
#pragma unroll
      for (int t = 2; t < 4; ++t) { MACC(ac5[0][t], bf[0][st], yf[st & 1][t]); if constexpr (st < 13) rz1(st + 1, (st + 1) & 1, t); MACC(ac5[1][t], bf[1][st], yf[st & 1][t]); }
      SCHED();
    });
    NOPS();
#pragma unroll
    for (int t = 0; t < 4; ++t) { KEEP(ac5[0][t]); KEEP(ac5[1][t]); }
    FS(44);
    BAR();
#pragma unroll
    for (int t = 1; t < 4; ++t) {
      ldsp d = lds + ((((t + kq) & 3) * 3 + (t - 1)) * 2 + ng5) * 2048 + lane * 16;
      *(LAS f32x4*)d = ac5[0][t]; *(LAS f32x4*)(d + 1024) = ac5[1][t];
    }
    const int tokl = 16 * kq + l15;
    const int t0 = col0 / 16 + 2 * ng5, t1 = (2 * ng5 + 1 < nblk) ? t0 + 1 : t0, m0_ = col0 >> 5;
    const u32x2 hr0 = *(LAS u32x2*)(lds + M_TILE + tokl * 192 + ((t0 >> 1) - m0_) * 64 + g * 16 + (t0 & 1) * 8);
    const u32x2 hr1 = *(LAS u32x2*)(lds + M_TILE + tokl * 192 + ((t1 >> 1) - m0_) * 64 + g * 16 + (t1 & 1) * 8);
    const u32x2 b20 = *(LAS u32x2*)(lds + M_B2 + (32 * ng5 + 4 * g) * 2), b21 = *(LAS u32x2*)(lds + M_B2 + ((2 * ng5 + 1 < nblk) ? (32 * ng5 + 16) : 32 * ng5) * 2 + 8 * g);
    f32x2 r00 = lo2(ac5[0][0]) + (bf2(b20.x) + bf2(hr0.x)), r01 = hi2(ac5[0][0]) + (bf2(b20.y) + bf2(hr0.y));
    f32x2 r10 = lo2(ac5[1][0]) + (bf2(b21.x) + bf2(hr1.x)), r11 = hi2(ac5[1][0]) + (bf2(b21.y) + bf2(hr1.y));
    BAR();
    FS(45);
#pragma unroll
    for (int t = 1; t < 4; ++t) {
      ldsp d = lds + ((kq * 3 + (t - 1)) * 2 + ng5) * 2048 + lane * 16;
      const f32x4 u0 = *(LAS f32x4*)d, u1 = *(LAS f32x4*)(d + 1024);
      r00 += lo2(u0); r01 += hi2(u0); r10 += lo2(u1); r11 += hi2(u1);
    }
    GAS char* op = (GAS char*)p.out + (size_t)(128 * c + 64 * th5 + tokl) * 896 + (col0 + 32 * ng5 + 4 * g) * 2;
    *(GAS u32x2*)op = u32x2{cvtpk(r00), cvtpk(r01)};
    if (2 * ng5 + 1 < nblk) *(GAS u32x2*)(op + 32) = u32x2{cvtpk(r10), cvtpk(r11)};
    STAMP(15);
    FS(63);
  }
}

extern "C" void kernel_launch(void* const* d_in, const int* in_sizes, int n_in, void* d_out, int out_size, void* d_ws, size_t ws_size, hipStream_t stream) {
  Params p;
  p.x = (const char*)d_in[0]; p.wqkv = (const char*)d_in[1]; p.wo = (const char*)d_in[2];
  p.w1 = (const char*)d_in[4]; p.b1 = (const char*)d_in[5]; p.w2 = (const char*)d_in[6]; p.b2 = (const char*)d_in[7];
  p.out = (char*)d_out; p.ws = (char*)d_ws;
  (void)hipFuncSetAttribute((const void*)fused_block, hipFuncAttributeMaxDynamicSharedMemorySize, LDS_BYTES);
  hipLaunchKernelGGL(fused_block, dim3(256), dim3(512), LDS_BYTES, stream, p);
}
```

```cpp
#include <hip/hip_runtime.h>
#include <hip/hip_bf16.h>
#include <stdint.h>
#include <type_traits>

#define LAS __attribute__((address_space(3)))
#define GAS __attribute__((address_space(1)))
typedef short bf16x8 __attribute__((ext_vector_type(8)));
typedef short s16x4 __attribute__((ext_vector_type(4)));
typedef short v4i16_t __attribute__((ext_vector_type(4)));
typedef float f32x4 __attribute__((ext_vector_type(4)));
typedef float f32x2 __attribute__((ext_vector_type(2)));
typedef unsigned u32x4 __attribute__((ext_vector_type(4)));
typedef unsigned u32x2 __attribute__((ext_vector_type(2)));
typedef __bf16 bf16x2_t __attribute__((ext_vector_type(2)));
typedef LAS char* ldsp;

constexpr int SEQ = 2048;
constexpr size_t WS_CTL = 0;
constexpr size_t WS_KV = 262144;
constexpr size_t WS_O = WS_KV + 256 * 16384;
constexpr size_t WS_H = WS_O + 256 * 8192;
constexpr size_t WS_H1 = WS_H + (size_t)SEQ * 896;
constexpr size_t WS_ST = WS_H1 + (size_t)SEQ * 3584;
constexpr size_t WS_WT = WS_ST + (size_t)SEQ * 32;
constexpr unsigned WT_W1 = 0, WT_W2 = WT_W1 + 1568 * 1024, WT_BYTES = WT_W2 + 1568 * 1024;
constexpr int LDS_BYTES = 160 * 1024;
constexpr int M_PT = 143360;
constexpr int M_XT = 147456;
constexpr int M_TILE = 131072;
constexpr int MISC = 157696;
constexpr int M_LN = MISC, M_B1 = MISC + 1024, M_B2 = MISC + 1280, M_PF = MISC + 1536, M_PART = MISC + 2048, M_FILL = MISC + 3072;
constexpr unsigned SPIN_CAP = 1u << 16;
constexpr unsigned CODE_LINES = 192;

#define FLAG_IDX(ph, c, j) (64 + (((ph) * 16 + (c)) * 16 + (j)) * 32)

#define WAITV(n) asm volatile("s_waitcnt vmcnt(" #n ")" ::: "memory")
#define BAR() do { asm volatile("s_waitcnt lgkmcnt(0)" ::: "memory"); __builtin_amdgcn_s_barrier(); asm volatile("" ::: "memory"); } while (0)
#define BARN() do { asm volatile("" ::: "memory"); __builtin_amdgcn_s_barrier(); asm volatile("" ::: "memory"); } while (0)
#define SCHED() __builtin_amdgcn_sched_barrier(0)
#define RAWBAR() __builtin_amdgcn_s_barrier()
#define MFMA(a, b, c) __builtin_amdgcn_mfma_f32_16x16x32_bf16((a), (b), (c), 0, 0, 0)
#define MACC(acc, a, b) asm volatile("v_mfma_f32_16x16x32_bf16 %0, %1, %2, %0" : "+v"(acc) : "v"(a), "v"(b))
#define MACCN(acc, a, b) asm volatile("s_nop 1\n\tv_mfma_f32_16x16x32_bf16 %0, %1, %2, %0" : "+v"(acc) : "v"(a), "v"(b))
#define NOPS() asm volatile("s_nop 7\n\ts_nop 3")
#define KEEP(x) asm volatile("" : "+v"(x))
#define IC(x) std::integral_constant<int, (x)>{}

template <int I, int N, class F> __device__ __forceinline__ void sfor(F&& f) { if constexpr (I < N) { f(std::integral_constant<int, I>{}); sfor<I + 1, N>(f); } }

__device__ __forceinline__ unsigned cvtpk(float lo, float hi) { f32x2 v = {lo, hi}; bf16x2_t b = __builtin_convertvector(v, bf16x2_t); return __builtin_bit_cast(unsigned, b); }
__device__ __forceinline__ unsigned cvtpk(f32x2 v) { bf16x2_t b = __builtin_convertvector(v, bf16x2_t); return __builtin_bit_cast(unsigned, b); }
__device__ __forceinline__ float dot2(unsigned a, unsigned b, float c) { return __builtin_amdgcn_fdot2_f32_bf16(__builtin_bit_cast(bf16x2_t, a), __builtin_bit_cast(bf16x2_t, b), c, false); }
__device__ __forceinline__ float bflo(unsigned u) { return __uint_as_float(u << 16); }
__device__ __forceinline__ float bfhi(unsigned u) { return __uint_as_float(u & 0xffff0000u); }
__device__ __forceinline__ f32x2 bf2(unsigned u) { return f32x2{bflo(u), bfhi(u)}; }
__device__ __forceinline__ f32x2 lo2(f32x4 v) { return f32x2{v[0], v[1]}; }
__device__ __forceinline__ f32x2 hi2(f32x4 v) { return f32x2{v[2], v[3]}; }
__device__ __forceinline__ bf16x8 ld128(ldsp p) { return *(const LAS bf16x8*)p; }
__device__ __forceinline__ s16x4 ldtr(ldsp p) { return __builtin_bit_cast(s16x4, __builtin_amdgcn_ds_read_tr16_b64_v4i16((LAS v4i16_t*)p)); }
__device__ __forceinline__ bf16x8 cat(s16x4 lo, s16x4 hi) { return __builtin_shufflevector(lo, hi, 0, 1, 2, 3, 4, 5, 6, 7); }
__device__ __forceinline__ int rfl(int x) { return __builtin_amdgcn_readfirstlane(x); }
template <int LADD, int VADD, bool SC1>
__device__ __forceinline__ void dma(unsigned voff, const void* sbase, unsigned lbase) {
  const unsigned va = voff + (unsigned)VADD;
  if constexpr (SC1) asm volatile("s_add_i32 m0, %2, %3\n\ts_nop 0\n\tglobal_load_lds_dwordx4 %0, %1 sc1" ::"v"(va), "s"(sbase), "s"(lbase), "i"(LADD) : "memory", "scc");
  else asm volatile("s_add_i32 m0, %2, %3\n\ts_nop 0\n\tglobal_load_lds_dwordx4 %0, %1" ::"v"(va), "s"(sbase), "s"(lbase), "i"(LADD) : "memory", "scc");
}
template <int LADD>
__device__ __forceinline__ void dma2c_sc1(unsigned voff, const void* sbase, unsigned lbase) {
  asm volatile("s_add_i32 m0, %2, %3\n\ts_nop 0\n\tglobal_load_lds_dwordx4 %0, %1 sc1\n\tglobal_load_lds_dwordx4 %0, %1 offset:1024 sc1" ::"v"(voff), "s"(sbase), "s"(lbase), "i"(LADD) : "memory", "scc");
}
template <int OFF>
__device__ __forceinline__ void ldfrag1(bf16x8& f, unsigned voff, const void* sbase) {
  asm volatile("global_load_dwordx4 %0, %1, %2 offset:%3 sc1" : "=v"(f) : "v"(voff), "s"(sbase), "i"(OFF) : "memory");
}
__device__ __forceinline__ void pin1(bf16x8& f) { asm volatile("" : "+v"(f)); }
template <int N>
__device__ __forceinline__ void ldfrags(bf16x8* f, unsigned voff, const void* sbase) {
  sfor<0, N>([&](auto I) { constexpr int i = decltype(I)::value; ldfrag1<(i & 3) * 1024>(f[i], voff + (unsigned)((i >> 2) * 4096), sbase); });
}
template <int N>
__device__ __forceinline__ void pin(bf16x8* f) { sfor<0, N>([&](auto I) { pin1(f[decltype(I)::value]); }); }
__device__ __forceinline__ void st128_sc1(unsigned voff, const void* sbase, u32x4 v) {
  asm volatile("global_store_dwordx4 %0, %1, %2 sc1\n\ts_nop 1" ::"v"(voff), "v"(v), "s"(sbase) : "memory");
}
__device__ __forceinline__ void st64_sc1(unsigned voff, const void* sbase, u32x2 v) {
  asm volatile("global_store_dwordx2 %0, %1, %2 sc1\n\ts_nop 1" ::"v"(voff), "v"(v), "s"(sbase) : "memory");
}
__device__ __forceinline__ void st32_sc1(const void* sbase, unsigned v) {
  asm volatile("global_store_dword %0, %1, %2 sc1\n\ts_nop 1" ::"v"(0u), "v"(v), "s"(sbase) : "memory");
}
__device__ __forceinline__ float max3(float a, float b, float c) { return __builtin_elementwise_maximum(__builtin_elementwise_maximum(a, b), c); }
__device__ __forceinline__ float red_g_sum(float v) {
  auto a = __builtin_amdgcn_permlane16_swap(__float_as_uint(v), __float_as_uint(v), false, false);
  v = __uint_as_float(a[0]) + __uint_as_float(a[1]);
  auto b = __builtin_amdgcn_permlane32_swap(__float_as_uint(v), __float_as_uint(v), false, false);
  return __uint_as_float(b[0]) + __uint_as_float(b[1]);
}
__device__ __forceinline__ float red_g_max(float v) {
  auto a = __builtin_amdgcn_permlane16_swap(__float_as_uint(v), __float_as_uint(v), false, false);
  v = __builtin_elementwise_maximum(__uint_as_float(a[0]), __uint_as_float(a[1]));
  auto b = __builtin_amdgcn_permlane32_swap(__float_as_uint(v), __float_as_uint(v), false, false);
  return __builtin_elementwise_maximum(__uint_as_float(b[0]), __uint_as_float(b[1]));
}
__device__ __forceinline__ void poll_flags(const unsigned* base, unsigned voff, unsigned epoch) {
#pragma unroll 1
  for (unsigned spins = 0; spins < SPIN_CAP; ++spins) {
    unsigned v;
    asm volatile("global_load_dword %0, %1, %2 sc1\n\ts_waitcnt vmcnt(0)" : "=&v"(v) : "v"(voff), "s"(base) : "memory");
    if (__all(v == epoch)) break;
    __builtin_amdgcn_s_sleep(1);
  }
}

struct Params {
  const char* x; const char* wqkv; const char* wo; const char* w1; const char* b1; const char* w2; const char* b2;
  char* out; char* ws;
};

#define LANEVARS() int tz_ = tid; asm volatile("" : "+v"(tz_)); const int lane = tz_ & 63, l15 = lane & 15, g = lane >> 4, q4 = l15 >> 2, p4 = l15 & 3; \
  const unsigned lane16 = (unsigned)lane * 16u, x8 = (unsigned)(((lane & 7) ^ ((lane >> 3) & 7)) << 4); (void)q4; (void)p4; (void)g; (void)x8; (void)lane16

__global__ void __launch_bounds__(512, 2) fused_block(Params p) {
  extern __shared__ __attribute__((aligned(16))) char smem[];
  ldsp lds = (ldsp)smem;
  unsigned long long pc0; asm volatile("s_getpc_b64 %0" : "=s"(pc0));
  const int tid = threadIdx.x, lane = tid & 63, w = rfl(tid >> 6);
  const int l15 = lane & 15, g = lane >> 4, q4 = l15 >> 2, p4 = l15 & 3;
  const int b = blockIdx.x, r_ = b >> 3;
  const int c = 2 * (b & 7) + (r_ >> 4), j = r_ & 15;
  const int ca = 4 * ((b & 7) >> 1) + (r_ >> 3), ha = 8 * (b & 1) + (r_ & 7);
  const unsigned* ctl = (const unsigned*)(p.ws + WS_CTL);
  unsigned epoch = 0;
  const unsigned ONES = 0x3f803f80u;
  const unsigned lane16 = (unsigned)lane * 16u;
  const unsigned x8 = (unsigned)(((lane & 7) ^ ((lane >> 3) & 7)) << 4);

  constexpr int KIMG = 0, VIMG = 24576, QIMG = 49152;
  {
    const int h = ha;
    constexpr int SLOT = 28672, WOFF = 16384;
    const unsigned iw = (unsigned)(w >> 2);
    unsigned voff[4];
    {
      const unsigned xo = (unsigned)((32 * w + (lane >> 3)) * 896) + x8;
#pragma unroll
      for (int ii = 0; ii < 4; ++ii) {
        const unsigned u = (unsigned)(192 * (w & 3) + 64 * ii + lane), row = (u * 43691u) >> 19, rem = u - 12u * row;
        const unsigned piece = rem >> 2, c16 = rem & 3, hb = (c16 >> 1) ^ ((row >> 3) & 1);
        const unsigned wo_ = (ii < 3) ? row * 2688u + piece * 896u + hb * 32u + (c16 & 1) * 16u : 0u;
        voff[ii] = iw ? wo_ : xo + ii * 7168u;
      }
    }
    const unsigned long long mW = 0ull - (unsigned long long)iw;
    const unsigned long long bx_ = (unsigned long long)(uintptr_t)p.x + (unsigned)(ca * 128 * 896), bw_ = (unsigned long long)(uintptr_t)p.wqkv + (unsigned)(56 * h - 8 * (h & 1));
    const char* gb = (const char*)(uintptr_t)(bx_ + ((bw_ - bx_) & mW));
    const unsigned kstep = 128 + iw * (64 * 2688 - 128), ldst = w * 4096 + iw * (WOFF + (w - 4) * 3072 - w * 4096);
    const unsigned smask = iw - 1u, l3 = iw ? (unsigned)M_FILL : ldst + 3072u;
    auto issue1 = [&](auto SL, auto II) {
      constexpr int sl = decltype(SL)::value, ii = decltype(II)::value;
      if constexpr (ii < 3) dma<sl * SLOT + ii * 1024, 0, false>(voff[ii], gb, ldst);
      else { dma<0, 0, false>(voff[3], gb, l3 + (smask & (unsigned)(sl * SLOT))); gb += kstep; }
    };
    auto issue = [&](auto SL) { issue1(SL, IC(0)); issue1(SL, IC(1)); issue1(SL, IC(2)); issue1(SL, IC(3)); };
    issue(IC(0));
    RAWBAR(); issue(IC(1));
    {
      unsigned ln = (unsigned)(r_ * 8 + w); if (ln >= CODE_LINES) ln = 0;
      const unsigned long long a = (pc0 & ~255ull) + (unsigned long long)(ln * 128u);
      if (lane == 0) asm volatile("s_mov_b32 m0, %1\n\ts_nop 0\n\tglobal_load_lds_dword %0, off" ::"v"(a), "i"(M_FILL) : "memory");
    }

    const int ptq0 = (b < 224) ? b : 223, ptm = (ptq0 >= 112) ? 2 : 1, ptq = ptq0 - ((ptm == 2) ? 112 : 0);
    const int pts = (ptm == 1) ? ptq >> 3 : ptq >> 1, ptG = (ptm == 1) ? ptq & 7 : ptq & 1;
    auto pt_issue = [&]() {
      const unsigned stride = (ptm == 1) ? 3584u : 896u;
      const char* src = ((ptm == 1) ? p.w1 : p.w2) + (size_t)(32 * pts * stride + ptG * 448);
      const int wq = (w < 7) ? w : 6;
#pragma unroll
      for (int i = 0; i < 2; ++i) { const unsigned u = (unsigned)((2 * wq + i) * 64 + lane), row = (u * 9363u) >> 18, c16 = u - 28u * row;
        dma<M_PT, 0, false>(row * stride + c16 * 16u, src, (unsigned)((2 * wq + i) * 1024)); }
      if (lane == 63) asm volatile("s_mov_b32 m0, %2\n\ts_nop 0\n\tglobal_load_lds_dword %0, %1 sc1" ::"v"(0u), "s"(p.ws + WS_CTL), "i"(M_PF) : "memory");
    };
    const int tg = w >> 1, ng = w & 1;
    f32x4 acc[3][2], asum = {0, 0, 0, 0}, asq = {0, 0, 0, 0}, acs[3];
#pragma unroll
    for (int e = 0; e < 3; ++e) { acc[e][0] = f32x4{0, 0, 0, 0}; acc[e][1] = f32x4{0, 0, 0, 0}; acs[e] = f32x4{0, 0, 0, 0}; }
    const int xb = (32 * tg + 16 * ng + l15) * 128 + ((g ^ (l15 & 7)) << 4);
    int ab[3];
#pragma unroll
    for (int e = 0; e < 3; ++e) ab[e] = WOFF + (8 * g + q4) * 192 + ((((3 * ng + e) * 32) ^ ((g & 1) << 5))) + 8 * p4;
    bf16x8 xf[2][2];
    s16x4 al[2][3], ah[2][3];
    auto rdx = [&](int st, int buf, int t) { xf[buf][t] = ld128(lds + ((st >> 1) % 5) * SLOT + (xb ^ ((st & 1) << 6) ^ (t * 2048))); };
    auto rdl = [&](int st, int buf, int e) { al[buf][e] = ldtr(lds + ((st >> 1) % 5) * SLOT + ab[e] + (32 * (st & 1)) * 192); };
    auto rdh_ = [&](int st, int buf, int e) { ah[buf][e] = ldtr(lds + ((st >> 1) % 5) * SLOT + ab[e] + (32 * (st & 1) + 4) * 192); };
    auto rdf = [&](int st, int buf) { rdx(st, buf, 0); rdx(st, buf, 1);
#pragma unroll
      for (int e = 0; e < 3; ++e) { rdl(st, buf, e); rdh_(st, buf, e); } };
    bf16x8 onesf = __builtin_bit_cast(bf16x8, (u32x4{ONES, ONES, ONES, ONES}));
    KEEP(onesf); KEEP(asum); KEEP(asq);
#pragma unroll
    for (int e = 0; e < 3; ++e) { KEEP(acc[e][0]); KEEP(acc[e][1]); KEEP(acs[e]); }
    WAITV(5);
    BAR();
    rdf(0, 0);
    sfor<0, 14>([&](auto ST) {
      constexpr int st = decltype(ST)::value, kc = st >> 1, cb = st & 1;
      if constexpr ((st & 1) && st < 13) {
        if constexpr (kc == 0) WAITV(5); else if constexpr (kc == 1) WAITV(9); else if constexpr (kc <= 3) WAITV(6); else if constexpr (kc == 4) WAITV(4); else WAITV(0);
        BARN();
      }
      if constexpr (st == 2) pt_issue();
      constexpr int nb = (st + 1) & 1; constexpr bool more = st < 13;
      const bf16x8 a0 = cat(al[cb][0], ah[cb][0]), a1 = cat(al[cb][1], ah[cb][1]), a2 = cat(al[cb][2], ah[cb][2]);
      SCHED();
      MACC(acc[0][0], a0, xf[cb][0]); if constexpr (more) rdx(st + 1, nb, 0);
      MACC(acc[0][1], a0, xf[cb][1]); if constexpr (more) rdl(st + 1, nb, 0);
      MACC(asum, onesf, xf[cb][0]); if constexpr (more) rdh_(st + 1, nb, 0);
      MACC(acs[0], a0, onesf);
      SCHED();
      if constexpr (st < 2) { issue1(IC(st + 2), IC(0)); issue1(IC(st + 2), IC(1)); }
      else if constexpr (kc + 3 <= 6) issue1(IC((kc + 3) % 5), IC(2 * (st & 1)));
      SCHED();
      MACC(acc[1][0], a1, xf[cb][0]); if constexpr (more) rdx(st + 1, nb, 1);
      MACC(acc[1][1], a1, xf[cb][1]); if constexpr (more) rdl(st + 1, nb, 1);
      MACC(asq, xf[cb][0], xf[cb][0]); if constexpr (more) rdh_(st + 1, nb, 1);
      MACC(acs[1], a1, onesf);
      SCHED();
      if constexpr (st < 2) { issue1(IC(st + 2), IC(2)); issue1(IC(st + 2), IC(3)); }
      else if constexpr (kc + 3 <= 6) issue1(IC((kc + 3) % 5), IC(2 * (st & 1) + 1));
      SCHED();
      MACC(acc[2][0], a2, xf[cb][0]); if constexpr (more) rdl(st + 1, nb, 2);
      MACC(acc[2][1], a2, xf[cb][1]); if constexpr (more) rdh_(st + 1, nb, 2);
      MACC(acs[2], a2, onesf);
      SCHED();
    });
    NOPS(); KEEP(asum); KEEP(asq);
#pragma unroll
    for (int e = 0; e < 3; ++e) { KEEP(acc[e][0]); KEEP(acc[e][1]); KEEP(acs[e]); }
    epoch = (unsigned)rfl((int)*(LAS unsigned*)(lds + M_PF + 252)) + 1u;
    {
      const unsigned ptb = (ptm == 1) ? WT_W1 : WT_W2, ptn = (ptm == 1) ? 14u : 56u;
      ldsp src = lds + M_PT + (4 * g + q4) * 448 + 8 * p4;
#pragma unroll
      for (int i = 0; i < 2; ++i) {
        const int t = (w + 8 * i < 14) ? w + 8 * i : w;
        const bf16x8 f = cat(ldtr(src + t * 32), ldtr(src + 16 * 448 + t * 32));
        st128_sc1(lane16 + ptb + ((unsigned)(14 * ptG + t) * ptn + (unsigned)pts) * 1024u, p.ws + WS_WT, __builtin_bit_cast(u32x4, f));
      }
    }
    {
      const float d01 = (p4 & 1) ? asq[1] : asq[0], d23 = (p4 & 1) ? asq[3] : asq[2], dq = (p4 & 2) ? d23 : d01;
      float mean = asum[0] * (1.f / 448.f), dq2 = dq;
      KEEP(mean); KEEP(dq2);
      if (g == q4) *(LAS f32x2*)(lds + M_LN + (32 * tg + 16 * ng + l15) * 8) = f32x2{mean, rsqrtf(fmaxf(dq2 * (1.f / 448.f) - mean * mean, 0.f) + 1e-5f)};
    }
    BAR();
    float mu[2], rs[2];
#pragma unroll
    for (int t = 0; t < 2; ++t) { const f32x2 st_ = *(LAS f32x2*)(lds + M_LN + (32 * tg + 16 * (t ^ ng) + l15) * 8); mu[t] = st_[0]; rs[t] = st_[1]; }
    const bool pad0 = (h & 1) && g == 0, pad1 = !(h & 1) && g == 3;
    const int rsw = ((l15 >> 1) & 3) << 4, rowb = (32 * tg + l15) * 64;
    auto epi = [&](auto NG) {
      constexpr int ngc = decltype(NG)::value;
      sfor<0, 3>([&](auto E) {
        constexpr int e = decltype(E)::value, nt = 3 * ngc + e, which = nt >> 1, par = nt & 1;
        constexpr int ibase = (which == 0) ? QIMG : (which == 1) ? KIMG + 16384 : VIMG + 16384;
        const int off = (which == 2) ? ((((2 * par + (g >> 1)) << 4) ^ rsw) + (g & 1) * 8) : (((g << 4) ^ rsw) + par * 8);
        const bool pad = par ? pad1 : pad0;
#pragma unroll
        for (int t = 0; t < 2; ++t) {
          const float sc = (which == 0) ? rs[t] * 0.27264657f : rs[t], nm = -mu[t] * sc;
          f32x2 sc2 = {sc, sc}, nm2 = {nm, nm};
          KEEP(sc2); KEEP(nm2);
          const f32x2 v0 = lo2(acc[e][t]) * sc2 + lo2(acs[e]) * nm2, v1 = hi2(acc[e][t]) * sc2 + hi2(acs[e]) * nm2;
          u32x2 o; o.x = cvtpk(v0); o.y = cvtpk(v1);
          o.x = pad ? ((which == 2) ? 0x00003f80u : 0u) : o.x; o.y = pad ? 0u : o.y;
          *(LAS u32x2*)(lds + ibase + (t ^ ngc) * 1024 + rowb + off) = o;
        }
      });
    };
    if (ng) epi(IC(1)); else epi(IC(0));
    if (ca < 2) {
      const u32x4 z = {0, 0, 0, 0};
#pragma unroll
      for (int i = 0; i < 2; ++i) { *(LAS u32x4*)(lds + KIMG + tid * 16 + i * 8192) = z; *(LAS u32x4*)(lds + VIMG + tid * 16 + i * 8192) = z; }
    }
    BAR();
    {
      const char* blob = p.ws + WS_KV + (size_t)(ca * 16 + h) * 16384;
      const u32x4 v0 = *(LAS u32x4*)(lds + KIMG + 16384 + tid * 16), v1 = *(LAS u32x4*)(lds + VIMG + 16384 + tid * 16);
      st128_sc1(tid * 16, blob, v0); st128_sc1(8192 + tid * 16, blob, v1);
    }
    WAITV(0);
    BAR();
    if (tid == 0) { st32_sc1(ctl + FLAG_IDX(0, ca, h), epoch); st32_sc1(ctl + FLAG_IDX(4, b >> 4, b & 15), epoch); }
  }
  const int ns = j & 3, col4 = 128 * ns - (((ns >> 1) & ns) << 5), ngr = 4 - (ns >> 1);
  const int pr = w & 3, wh = w >> 2;
  const int prc = (pr < ngr) ? pr : 0;
  const int th5 = j >> 3, cs5 = j & 7, col0 = 64 * cs5 - 16 * ((cs5 >> 2) * (cs5 & 3)), nblk = 4 - (cs5 >> 2);
  const int ng5 = w & 1, kq = w >> 1;
  const char* wt = p.ws + WS_WT;
  bf16x8 wr[32];
  const int T1 = (448 * (j >> 2) + col4) / 16;
  const unsigned w1o = WT_W1 + (unsigned)(T1 + 2 * prc) * 14u * 1024u;
  const unsigned wro = lane16 + w1o;
  constexpr int WI = 32768;
  unsigned wb0, wb1;
  { const int rin = lane >> 4, pos = lane & 15;
    int blk = (pos >> 1) ^ rin; if (blk >= 2 * ngr) blk -= 2; wb0 = (unsigned)(blk * 32 + (pos & 1) * 16);
    blk = (pos >> 1) ^ (4 + rin); if (blk >= 2 * ngr) blk -= 2; wb1 = (unsigned)(blk * 32 + (pos & 1) * 16); }
  const char* wob = p.wo + col4 * 2;
  {
    if (w < 6) {
      const unsigned rb = (unsigned)(((lane >> 4) + 4 * (24 + w)) * 896) + ((w & 1) ? wb1 : wb0);
      sfor<0, 14>([&](auto I) { constexpr int i = decltype(I)::value; dma<i * 6144, i * 24 * 896, false>(rb, wob, (unsigned)(WI + (24 + w) * 1024)); });
    }
    int pos = lane & 15; if (pos >= 4 * ngr) pos = 4 * ngr - 1;
    int xch = (lane & 15) ^ (((lane >> 4) + 4 * w) & 15); if (xch >= 4 * ngr) xch = 0;
    dma<M_XT, 0, false>((unsigned)(((lane >> 4) + 4 * w) * 896 + xch * 16), p.x + (size_t)(128 * c + 32 * (j >> 2)) * 896 + col4 * 2, (unsigned)(w * 1024));
    if (w == 5 && lane < 16) dma<M_B1, 0, false>((unsigned)(pos * 16), p.b1 + (448 * (j >> 2) + col4) * 2, 0u);
    if (w == 4 && lane < 8) dma<M_B2, 0, false>((unsigned)((lane < 2 * nblk ? lane : 0) * 16), p.b2 + col0 * 2, 0u);
  }
  {
    const int h = ha;
    const int sw = (l15 >> 1) & 3;
    if (w == 7 && ca >= 1) poll_flags(ctl, (unsigned)(FLAG_IDX(0, (ca >= 2 && (lane & 1)) ? ca - 2 : ca - 1, h) * 4), epoch);
    if (w == 6) {
      const unsigned tfo = (unsigned)(FLAG_IDX(4, 4 * g, l15) * 4);
#pragma unroll 1
      for (unsigned spins = 0; spins < SPIN_CAP; ++spins) {
        unsigned t0, t1, t2, t3;
        asm volatile("global_load_dword %0, %4, %5 sc1\n\tglobal_load_dword %1, %4, %5 offset:2048 sc1\n\tglobal_load_dword %2, %6, %5 sc1\n\tglobal_load_dword %3, %6, %5 offset:2048 sc1\n\ts_waitcnt vmcnt(0)"
                     : "=&v"(t0), "=&v"(t1), "=&v"(t2), "=&v"(t3) : "v"(tfo), "s"(ctl), "v"(tfo + 4096u) : "memory");
        if (__all(t0 == epoch && t1 == epoch && t2 == epoch && t3 == epoch)) break;
        __builtin_amdgcn_s_sleep(2);
      }
      if (b == 0 && lane == 0) st32_sc1(ctl, epoch);
    }
    BAR();
    {
      const int tl = w >> 2, src = ca - 2 + tl, pc = (w & 3) * 2048;
      if (src >= 0) {
        const char* blob = p.ws + WS_KV + (size_t)(src * 16 + h) * 16384 + pc;
        dma2c_sc1<KIMG>(lane16, blob, (unsigned)(tl * 8192 + pc)); dma2c_sc1<VIMG>(lane16, blob + 8192, (unsigned)(tl * 8192 + pc));
      }
    }
    const bf16x8 qf = ld128(lds + QIMG + (16 * w + l15) * 64 + ((g ^ sw) << 4));
    WAITV(0);
    BAR();
    if (w >= 4) ldfrags<7>(wr, wro, wt);
    f32x4 sc[17];
    const float NEG = -__builtin_inff();
    ldsp kb = lds + KIMG + (16 * w + l15) * 64 + ((g ^ sw) << 4);
    {
      bf16x8 kf[2][6];
#pragma unroll
      for (int u = 0; u < 6; ++u) kf[0][u] = ld128(kb + u * 1024);
#pragma unroll
      for (int bt = 0; bt < 3; ++bt) {
        if (bt < 2) {
#pragma unroll
          for (int u = 0; u < 6; ++u) if (6 * (bt + 1) + u < 17) kf[(bt + 1) & 1][u] = ld128(kb + (6 * (bt + 1) + u) * 1024);
        }
        SCHED();
#pragma unroll
        for (int u = 0; u < 6; ++u) if (6 * bt + u < 17) sc[6 * bt + u] = MFMA(kf[bt & 1][u], qf, (f32x4{0, 0, 0, 0}));
        SCHED();
      }
    }
    if (w >= 4) ldfrags<7>(wr + 7, wro + 7 * 1024, wt);
    const int vsw = (2 * g + (q4 >> 1)) & 3;
    ldsp vb0 = lds + VIMG + (16 * w + 4 * g + q4) * 64 + (((p4 >> 1) ^ vsw) << 4) + (p4 & 1) * 8;
    ldsp vb1 = lds + VIMG + (16 * w + 4 * g + q4) * 64 + (((2 + (p4 >> 1)) ^ vsw) << 4) + (p4 & 1) * 8;
    bf16x8 vf[2][3][2];
    auto rdv = [&](int bt) {
#pragma unroll
      for (int i = 0; i < 3; ++i) { const int uu = 3 * bt + i, ua = 2 * uu, ub = (uu < 8) ? 2 * uu + 1 : 15;
        vf[bt & 1][i][0] = cat(ldtr(vb0 + ua * 1024), ldtr(vb0 + ub * 1024)); vf[bt & 1][i][1] = cat(ldtr(vb1 + ua * 1024), ldtr(vb1 + ub * 1024)); }
    };
    rdv(0);
    SCHED();
    if (ca < 2) {
      const int tmin = (2 - ca) * 8;
#pragma unroll
      for (int u = 0; u < 16; ++u) if (w + u < tmin) sc[u] = f32x4{NEG, NEG, NEG, NEG};
    }
#pragma unroll
    for (int r = 0; r < 4; ++r) { sc[0][r] = (4 * g + r <= l15) ? NEG : sc[0][r]; sc[16][r] = (4 * g + r > l15) ? NEG : sc[16][r]; }
    float m = max3(sc[0][0], sc[0][1], sc[0][2]), m2 = max3(sc[0][3], sc[1][0], sc[1][1]);
    m = max3(m, sc[1][2], sc[1][3]);
#pragma unroll
    for (int u = 2; u < 17; ++u) { if (u & 1) m = max3(max3(m, sc[u][0], sc[u][1]), sc[u][2], sc[u][3]); else m2 = max3(max3(m2, sc[u][0], sc[u][1]), sc[u][2], sc[u][3]); }
    m = max3(m, m2, m2);
    m = red_g_max(m);
    SCHED();
    if (w >= 4) ldfrags<7>(wr + 14, wro + 14 * 1024, wt);
    unsigned pk[17][2];
    f32x2 mm = {-m, -m};
    KEEP(mm);
    f32x4 oa[2] = {f32x4{0, 0, 0, 0}, f32x4{0, 0, 0, 0}};
    KEEP(oa[0]); KEEP(oa[1]);
#pragma unroll
    for (int bt = 0; bt < 3; ++bt) {
#pragma unroll
      for (int u = 6 * bt; u < 6 * bt + 6 && u < 17; ++u) {
        const f32x2 d0 = lo2(sc[u]) + mm, d1 = hi2(sc[u]) + mm;
        pk[u][0] = cvtpk(__builtin_amdgcn_exp2f(d0[0]), __builtin_amdgcn_exp2f(d0[1]));
        pk[u][1] = cvtpk(__builtin_amdgcn_exp2f(d1[0]), __builtin_amdgcn_exp2f(d1[1]));
      }
      if (bt < 2) rdv(bt + 1);
      if (bt == 0 && w >= 4) ldfrags<7>(wr + 21, wro + 21 * 1024, wt);
      SCHED();
#pragma unroll
      for (int i = 0; i < 3; ++i) {
        const int uu = 3 * bt + i;
        u32x4 pu; pu.x = pk[2 * uu][0]; pu.y = pk[2 * uu][1];
        if (uu < 8) { pu.z = pk[2 * uu + 1][0]; pu.w = pk[2 * uu + 1][1]; } else { pu.z = 0; pu.w = 0; }
        const bf16x8 pf = __builtin_bit_cast(bf16x8, pu);
        MACCN(oa[0], vf[bt & 1][i][0], pf); MACC(oa[1], vf[bt & 1][i][1], pf);
      }
      SCHED();
    }
    NOPS(); KEEP(oa[0]); KEEP(oa[1]);
    int tz2 = tid; asm volatile("" : "+v"(tz2));
    const int l15b = tz2 & 15, gb = (tz2 & 63) >> 4, swb = (l15b >> 1) & 3;
    const float lsel = (h & 1) ? oa[0][0] : oa[1][0];
    const float l = __uint_as_float((unsigned)__builtin_amdgcn_ds_bpermute((((h & 1) ? 0 : 48) + l15b) * 4, (int)__float_as_uint(lsel)));
    const float inv1 = __builtin_amdgcn_rcpf(l);
    f32x2 inv = {inv1, inv1};
    KEEP(inv);
    u32x4 ov;
    ov.x = cvtpk(lo2(oa[0]) * inv); ov.y = cvtpk(hi2(oa[0]) * inv); ov.z = cvtpk(lo2(oa[1]) * inv); ov.w = cvtpk(hi2(oa[1]) * inv);
    { const bool z0 = (h & 1) && gb == 0, z1 = !(h & 1) && gb == 3; ov.x = z0 ? 0u : ov.x; ov.y = z0 ? 0u : ov.y; ov.z = z1 ? 0u : ov.z; ov.w = z1 ? 0u : ov.w; }
    st128_sc1((unsigned)((16 * w + l15b) * 64 + ((gb ^ swb) << 4)), p.ws + WS_O + (size_t)(ca * 16 + h) * 8192, ov);
    BAR();
    {
      const unsigned rb = (unsigned)(((lane >> 4) + 12 * w) * 896), va = rb + ((w & 1) ? wb1 : wb0), vb = rb + ((w & 1) ? wb0 : wb1), lb = (unsigned)(WI + 3 * w * 1024);
      dma<0, 0, false>(va, wob, lb); dma<1024, 4 * 896, false>(vb, wob, lb); dma<2048, 8 * 896, false>(va, wob, lb);
      const int ins = (w < 4) ? 108 + w : 3 * w + 2;
      dma<0, 0, false>((unsigned)(((lane >> 4) + 4 * ins) * 896) + ((ins & 1) ? wb1 : wb0), wob, (unsigned)(WI + ins * 1024));
    }
    WAITV(4);
    BAR();
    if (tid == 0) st32_sc1(ctl + FLAG_IDX(1, ca, h), epoch);
  }
  {
    LANEVARS();
    constexpr int OT = 0;
    const int tq = j >> 2;
    WAITV(0);
    BAR();
    if (w < 4) {
      const int rx = 4 * (g & 1) + q4;
      ldsp ab0 = lds + WI + (4 * g + q4) * 256 + (((2 * prc) ^ rx) << 5) + 8 * p4;
      ldsp ab1 = lds + WI + (4 * g + q4) * 256 + (((2 * prc + 1) ^ rx) << 5) + 8 * p4;
      ldsp ab0h = ab0 + 65536, ab1h = ab1 + 65536;
#pragma unroll
      for (int s = 0; s < 16; ++s) {
        const int rb = (28 * s - 4 * (s & 1)) * 256;
        if (rb + 4096 < 65536) { wr[s] = cat(ldtr(ab0 + rb), ldtr(ab0 + rb + 4096)); wr[16 + s] = cat(ldtr(ab1 + rb), ldtr(ab1 + rb + 4096)); }
        else { wr[s] = cat(ldtr(ab0h + (rb - 65536)), ldtr(ab0h + (rb - 65536 + 4096))); wr[16 + s] = cat(ldtr(ab1h + (rb - 65536)), ldtr(ab1h + (rb - 65536 + 4096))); }
      }
    } else {
      __builtin_amdgcn_s_setprio(3);
      poll_flags(ctl, (unsigned)(FLAG_IDX(1, c, l15) * 4), epoch);
      const char* ob_ = p.ws + WS_O + (size_t)(c * 16 + 4 * (w - 4)) * 8192 + tq * 2048;
      const unsigned lb = (unsigned)(OT + (w - 4) * 8192);
      dma2c_sc1<0>(lane16, ob_, lb); dma2c_sc1<2048>(lane16 + 8192, ob_, lb); dma2c_sc1<4096>(lane16 + 16384, ob_, lb); dma2c_sc1<6144>(lane16 + 24576, ob_, lb);
      __builtin_amdgcn_s_setprio(0);
    }
    WAITV(0);
    BAR();
    if (w < 4) {
      f32x4 a0[2], a1[2];
#pragma unroll
      for (int t = 0; t < 2; ++t) { a0[t] = f32x4{0, 0, 0, 0}; a1[t] = f32x4{0, 0, 0, 0}; }
      ldsp ob = lds + OT + l15 * 64 + ((g ^ ((l15 >> 1) & 3)) << 4);
      u32x2 xr[2][2];
#pragma unroll
      for (int t = 0; t < 2; ++t) { ldsp xp = lds + M_XT + (16 * t + l15) * 256 + (g & 1) * 8;
        xr[t][0] = *(LAS u32x2*)(xp + (((4 * prc + (g >> 1)) ^ l15) << 4)); xr[t][1] = *(LAS u32x2*)(xp + (((4 * prc + 2 + (g >> 1)) ^ l15) << 4)); }
#pragma unroll
      for (int t = 0; t < 2; ++t) { KEEP(a0[t]); KEEP(a1[t]); }
      bf16x8 of[4][2];
#pragma unroll
      for (int s = 0; s < 3; ++s) { of[s][0] = ld128(ob + s * 2048); of[s][1] = ld128(ob + s * 2048 + 1024); }
#pragma unroll
      for (int s = 0; s < 16; ++s) {
        if (s + 3 < 16) { of[(s + 3) & 3][0] = ld128(ob + (s + 3) * 2048); of[(s + 3) & 3][1] = ld128(ob + (s + 3) * 2048 + 1024); }
        SCHED();
#pragma unroll
        for (int t = 0; t < 2; ++t) { MACC(a0[t], wr[s], of[s & 3][t]); MACC(a1[t], wr[16 + s], of[s & 3][t]); }
        SCHED();
      }
      NOPS();
#pragma unroll
      for (int t = 0; t < 2; ++t) { KEEP(a0[t]); KEEP(a1[t]); }
      const unsigned ho = (unsigned)((128 * c + 32 * tq + l15) * 896 + (col4 / 32 + pr) * 64 + g * 16);
#pragma unroll
      for (int t = 0; t < 2; ++t) {
        u32x4 hv;
        hv.x = cvtpk(lo2(a0[t]) + bf2(xr[t][0].x)); hv.y = cvtpk(hi2(a0[t]) + bf2(xr[t][0].y));
        hv.z = cvtpk(lo2(a1[t]) + bf2(xr[t][1].x)); hv.w = cvtpk(hi2(a1[t]) + bf2(xr[t][1].y));
        if (pr < ngr) st128_sc1(ho + t * 16 * 896, p.ws + WS_H, hv);
        float s1 = 0.f, s2 = 0.f;
#pragma unroll
        for (int i = 0; i < 4; ++i) { s1 = dot2(hv[i], ONES, s1); s2 = dot2(hv[i], hv[i], s2); }
        s1 = red_g_sum(s1); s2 = red_g_sum(s2);
        if (pr >= ngr) { s1 = 0.f; s2 = 0.f; }
        if (g == 0) ((LAS f32x2*)(lds + M_PART))[(16 * t + l15) * 4 + pr] = f32x2{s1, s2};
      }
    } else {
      pin<28>(wr);
#pragma unroll
      for (int i = 0; i < 28; ++i) *(LAS bf16x8*)(lds + 32768 + (w - 4) * 28672 + i * 1024 + lane * 16) = wr[i];
    }
    BAR();
    if (tid < 32) {
      const f32x4 u0 = *(LAS f32x4*)(lds + M_PART + tid * 32), u1 = *(LAS f32x4*)(lds + M_PART + tid * 32 + 16);
      st64_sc1((unsigned)((128 * c + 32 * tq + tid) * 32 + ns * 8), p.ws + WS_ST,
               u32x2{__float_as_uint((u0[0] + u0[2]) + (u1[0] + u1[2])), __float_as_uint((u0[1] + u0[3]) + (u1[1] + u1[3]))});
    }
    if (w < 4) {
#pragma unroll
      for (int i = 0; i < 28; ++i) wr[i] = ld128(lds + 32768 + w * 28672 + i * 1024 + lane * 16);
    }
    if (w < 4) WAITV(0);
    BAR();
    if (tid == 0) st32_sc1(ctl + FLAG_IDX(2, c, j), epoch);
  }
  {
    LANEVARS();
    const int f0_ = 448 * (j >> 2) + col4, th = wh;
    f32x4 c0 = {0, 0, 0, 0}, c1 = {0, 0, 0, 0};
    pin<28>(wr);
    bf16x8 onesf = __builtin_bit_cast(bf16x8, (u32x4{ONES, ONES, ONES, ONES}));
    KEEP(onesf); KEEP(c0); KEEP(c1);
    asm volatile("s_nop 1");
#pragma unroll
    for (int s = 0; s < 14; ++s) { MACC(c0, wr[s], onesf); MACC(c1, wr[14 + s], onesf); }
    NOPS(); KEEP(c0); KEEP(c1);
    SCHED();
    if (w == 7) poll_flags(ctl, (unsigned)(FLAG_IDX(2, c, l15) * 4), epoch);
    BAR();
    const unsigned vo = (unsigned)((16 * w + (lane >> 3)) * 896) + x8, wl2k = (unsigned)(w * 2048);
    const char* hb = p.ws + WS_H + (size_t)c * 128 * 896;
    auto issue4 = [&](auto KC) { constexpr int kc = decltype(KC)::value; dma<kc * 16384, kc * 128, true>(vo, hb, wl2k); dma<kc * 16384 + 1024, kc * 128 + 8 * 896, true>(vo, hb, wl2k); };
    if (lane < 32) dma<M_TILE, 0, true>(lane16, p.ws + WS_ST + (size_t)(128 * c + 16 * w) * 32, (unsigned)(w * 512));
    issue4(IC(0)); RAWBAR(); issue4(IC(1));
    f32x4 acc[2][4];
#pragma unroll
    for (int t = 0; t < 4; ++t) { acc[0][t] = f32x4{0, 0, 0, 0}; acc[1][t] = f32x4{0, 0, 0, 0}; }
    const int xb = (64 * th + l15) * 128 + ((g ^ (l15 & 7)) << 4);
    bf16x8 xf[2][4];
    auto rd1 = [&](int st, int buf, int t) { xf[buf][t] = ld128(lds + (st >> 1) * 16384 + (xb ^ ((st & 1) << 6)) + t * 2048); };
    auto rdf = [&](int st, int buf) { rd1(st, buf, 0); rd1(st, buf, 1); rd1(st, buf, 2); rd1(st, buf, 3); };
#pragma unroll
    for (int t = 0; t < 4; ++t) { KEEP(acc[0][t]); KEEP(acc[1][t]); }
    bf16x8 bf[2][14];
    const unsigned w2o0 = WT_W2 + ((unsigned)(col0 / 16 + 2 * ng5) * 56u + 14u * kq) * 1024u, w2o1 = (2 * ng5 + 1 < nblk) ? w2o0 + 56u * 1024u : w2o0;
    WAITV(2); BAR();
    rdf(0, 0);
    sfor<0, 14>([&](auto ST) {
      constexpr int st = decltype(ST)::value;
      if constexpr (st == 1) WAITV(2); if constexpr (st == 3) WAITV(4); if constexpr (st == 5) WAITV(6); if constexpr (st == 7) WAITV(8); if constexpr (st == 9) WAITV(10); if constexpr (st == 11) WAITV(12);
      if constexpr ((st & 1) && st < 13) BARN();
      if constexpr (st == 2) {
        if (tid < 128) {
          const f32x4 u0 = *(LAS f32x4*)(lds + M_TILE + tid * 32), u1 = *(LAS f32x4*)(lds + M_TILE + tid * 32 + 16);
          float mean = ((u0[0] + u0[2]) + (u1[0] + u1[2])) * (1.f / 448.f), sq = (u0[1] + u0[3]) + (u1[1] + u1[3]);
          KEEP(mean); KEEP(sq);
          const float rstd = rsqrtf(fmaxf(sq * (1.f / 448.f) - mean * mean, 0.f) + 1e-5f);
          *(LAS f32x2*)(lds + M_LN + tid * 8) = f32x2{rstd, -mean * rstd};
        }
      }
      SCHED();
#pragma unroll
      for (int t = 0; t < 2; ++t) { MACC(acc[0][t], wr[st], xf[st & 1][t]); if constexpr (st < 13) rd1(st + 1, (st + 1) & 1, t); MACC(acc[1][t], wr[14 + st], xf[st & 1][t]); }
      SCHED();
      if constexpr (st <= 4) issue4(IC(st + 2));
      if constexpr (st >= 5) { constexpr int s = st - 5; ldfrags<1>(&bf[0][s], lane16 + w2o0 + s * 1024, wt); ldfrags<1>(&bf[1][s], lane16 + w2o1 + s * 1024, wt); }
      SCHED();
#pragma unroll
      for (int t = 2; t < 4; ++t) { MACC(acc[0][t], wr[st], xf[st & 1][t]); if constexpr (st < 13) rd1(st + 1, (st + 1) & 1, t); MACC(acc[1][t], wr[14 + st], xf[st & 1][t]); }
      SCHED();
    });
    NOPS();
#pragma unroll
    for (int t = 0; t < 4; ++t) { KEEP(acc[0][t]); KEEP(acc[1][t]); }
    {
      const u32x2 bb0 = *(LAS u32x2*)(lds + M_B1 + (32 * prc + 4 * g) * 2), bb1 = *(LAS u32x2*)(lds + M_B1 + (32 * prc + 4 * g) * 2 + 32);
      const f32x2 b00 = bf2(bb0.x), b01 = bf2(bb0.y), b10 = bf2(bb1.x), b11 = bf2(bb1.y);
      const unsigned ho = (unsigned)((128 * c + 64 * th + l15) * 3584 + (f0_ / 32 + pr) * 64 + g * 16);
      f32x2 ln[4];
#pragma unroll
      for (int t = 0; t < 4; ++t) ln[t] = *(LAS f32x2*)(lds + M_LN + (64 * th + 16 * t + l15) * 8);
#pragma unroll
      for (int t = 0; t < 4; ++t) {
        const float a = ln[t][0], nm = ln[t][1];
        f32x2 aa = {a, a}, nn = {nm, nm};
        KEEP(aa); KEEP(nn);
        f32x2 z[4] = {lo2(acc[0][t]) * aa + (lo2(c0) * nn + b00), hi2(acc[0][t]) * aa + (hi2(c0) * nn + b01), lo2(acc[1][t]) * aa + (lo2(c1) * nn + b10), hi2(acc[1][t]) * aa + (hi2(c1) * nn + b11)};
        u32x4 hv;
#pragma unroll
        for (int i = 0; i < 4; ++i) {
          f32x2 r = {__builtin_elementwise_maximum(z[i][0], 0.f), __builtin_elementwise_maximum(z[i][1], 0.f)};
          hv[i] = cvtpk(r * r);
        }
        if (pr < ngr) st128_sc1(ho + t * 16 * 3584, p.ws + WS_H1, hv);
      }
    }
    WAITV(0);
    BAR();
    if (tid == 0) st32_sc1(ctl + FLAG_IDX(3, c, j), epoch);
    if (w != 7) {
      ldfrags<5>(&bf[0][9], lane16 + w2o0 + 9 * 1024, wt); ldfrags<5>(&bf[1][9], lane16 + w2o1 + 9 * 1024, wt);
    }
    if (w >= 5 && w < 7) {
      const int m0_ = col0 >> 5;
      const char* hr = p.ws + WS_H + (size_t)(128 * c + 64 * th5) * 896;
      sfor<0, 6>([&](auto I) { constexpr int i = decltype(I)::value;
        const unsigned u = (unsigned)((w - 5) * 384 + i * 64 + lane), r0 = (u * 43691u) >> 19, c0_ = u - 12u * r0; unsigned cc0 = m0_ * 4 + c0_; if (cc0 > 55) cc0 = 55;
        dma<M_TILE + i * 1024, 0, true>(r0 * 896 + cc0 * 16, hr, (unsigned)((w - 5) * 6144)); });
    }
    const unsigned vo5 = (unsigned)((32 * ng5 + (lane >> 3)) * 3584 + kq * 896) + x8, wl4k = (unsigned)(w * 4096);
    const char* hb5 = p.ws + WS_H1 + (size_t)(128 * c + 64 * th5) * 3584;
    auto issue5 = [&](auto I) {
      constexpr int i = decltype(I)::value, sa = (i & 3) * 32768;
      sfor<0, 4>([&](auto II) { constexpr int ii = decltype(II)::value; dma<sa + ii * 1024, i * 128 + ii * 8 * 3584, true>(vo5, hb5, wl4k); });
    };
    if (w == 7) {
      poll_flags(ctl, (unsigned)(FLAG_IDX(3, c, l15) * 4), epoch);
    }
    BAR();
    issue5(IC(0));
    if (w == 7) {
      ldfrags<5>(&bf[0][9], lane16 + w2o0 + 9 * 1024, wt); ldfrags<5>(&bf[1][9], lane16 + w2o1 + 9 * 1024, wt);
    }
    f32x4 ac5[2][4];
#pragma unroll
    for (int t = 0; t < 4; ++t) { ac5[0][t] = f32x4{0, 0, 0, 0}; ac5[1][t] = f32x4{0, 0, 0, 0}; }
    int yb[4];
#pragma unroll
    for (int t = 0; t < 4; ++t) yb[t] = kq * 8192 + (16 * ((t + kq) & 3) + l15) * 128 + ((g ^ (l15 & 7)) << 4);
    bf16x8 yf[2][4];
    auto rz1 = [&](int st, int buf, int t) { yf[buf][t] = ld128(lds + ((st >> 1) & 3) * 32768 + (yb[t] ^ ((st & 1) << 6))); };
    auto rdy = [&](int st, int buf) { rz1(st, buf, 0); rz1(st, buf, 1); rz1(st, buf, 2); rz1(st, buf, 3); };
#pragma unroll
    for (int t = 0; t < 4; ++t) { KEEP(ac5[0][t]); KEEP(ac5[1][t]); }
    WAITV(0);
    pin<14>(bf[0]); pin<14>(bf[1]);
    BAR();
    rdy(0, 0);
    sfor<0, 14>([&](auto ST) {
      constexpr int st = decltype(ST)::value;
      if constexpr ((st & 1) && st < 13) {
        if constexpr (st < 11) WAITV(4); else WAITV(0);
        BARN();
      }
      SCHED();
#pragma unroll
      for (int t = 0; t < 2; ++t) { MACC(ac5[0][t], bf[0][st], yf[st & 1][t]); if constexpr (st < 13) rz1(st + 1, (st + 1) & 1, t); MACC(ac5[1][t], bf[1][st], yf[st & 1][t]); }
      SCHED();
      if constexpr (st == 0) { issue5(IC(1)); issue5(IC(2)); }
      if constexpr ((st & 1) && st <= 7) issue5(IC((st + 5) / 2));
      SCHED();
#pragma unroll
      for (int t = 2; t < 4; ++t) { MACC(ac5[0][t], bf[0][st], yf[st & 1][t]); if constexpr (st < 13) rz1(st + 1, (st + 1) & 1, t); MACC(ac5[1][t], bf[1][st], yf[st & 1][t]); }
      SCHED();
    });
    NOPS();
#pragma unroll
    for (int t = 0; t < 4; ++t) { KEEP(ac5[0][t]); KEEP(ac5[1][t]); }
    auto paddr = [&](int blk) { return (blk < 16) ? blk * 2048 : 98304 + (blk - 16) * 2048; };
#pragma unroll
    for (int t = 1; t < 4; ++t) {
      ldsp d = lds + paddr((((t + kq) & 3) * 3 + (t - 1)) * 2 + ng5) + lane * 16;
      *(LAS f32x4*)d = ac5[0][t]; *(LAS f32x4*)(d + 1024) = ac5[1][t];
    }
    const int tokl = 16 * kq + l15;
    const int t0 = col0 / 16 + 2 * ng5, t1 = (2 * ng5 + 1 < nblk) ? t0 + 1 : t0, m0_ = col0 >> 5;
    const u32x2 hr0 = *(LAS u32x2*)(lds + M_TILE + tokl * 192 + ((t0 >> 1) - m0_) * 64 + g * 16 + (t0 & 1) * 8);
    const u32x2 hr1 = *(LAS u32x2*)(lds + M_TILE + tokl * 192 + ((t1 >> 1) - m0_) * 64 + g * 16 + (t1 & 1) * 8);
    const u32x2 b20 = *(LAS u32x2*)(lds + M_B2 + (32 * ng5 + 4 * g) * 2), b21 = *(LAS u32x2*)(lds + M_B2 + ((2 * ng5 + 1 < nblk) ? (32 * ng5 + 16) : 32 * ng5) * 2 + 8 * g);
    f32x2 r00 = lo2(ac5[0][0]) + (bf2(b20.x) + bf2(hr0.x)), r01 = hi2(ac5[0][0]) + (bf2(b20.y) + bf2(hr0.y));
    f32x2 r10 = lo2(ac5[1][0]) + (bf2(b21.x) + bf2(hr1.x)), r11 = hi2(ac5[1][0]) + (bf2(b21.y) + bf2(hr1.y));
    BAR();
#pragma unroll
    for (int t = 1; t < 4; ++t) {
      ldsp d = lds + paddr((kq * 3 + (t - 1)) * 2 + ng5) + lane * 16;
      const f32x4 u0 = *(LAS f32x4*)d, u1 = *(LAS f32x4*)(d + 1024);
      r00 += lo2(u0); r01 += hi2(u0); r10 += lo2(u1); r11 += hi2(u1);
    }
    GAS char* op = (GAS char*)p.out + (size_t)(128 * c + 64 * th5 + tokl) * 896 + (col0 + 32 * ng5 + 4 * g) * 2;
    *(GAS u32x2*)op = u32x2{cvtpk(r00), cvtpk(r01)};
    if (2 * ng5 + 1 < nblk) *(GAS u32x2*)(op + 32) = u32x2{cvtpk(r10), cvtpk(r11)};
  }
}

extern "C" void kernel_launch(void* const* d_in, const int* in_sizes, int n_in, void* d_out, int out_size, void* d_ws, size_t ws_size, hipStream_t stream) {
  Params p;
  p.x = (const char*)d_in[0]; p.wqkv = (const char*)d_in[1]; p.wo = (const char*)d_in[2];
  p.w1 = (const char*)d_in[4]; p.b1 = (const char*)d_in[5]; p.w2 = (const char*)d_in[6]; p.b2 = (const char*)d_in[7];
  p.out = (char*)d_out; p.ws = (char*)d_ws;
  (void)hipFuncSetAttribute((const void*)fused_block, hipFuncAttributeMaxDynamicSharedMemorySize, LDS_BYTES);
  hipLaunchKernelGGL(fused_block, dim3(256), dim3(512), LDS_BYTES, stream, p);
}
```

```cpp
#include <hip/hip_runtime.h>
#include <hip/hip_bf16.h>
#include <stdint.h>
#include <type_traits>

#define LAS __attribute__((address_space(3)))
#define GAS __attribute__((address_space(1)))
typedef short bf16x8 __attribute__((ext_vector_type(8)));
typedef short s16x4 __attribute__((ext_vector_type(4)));
typedef short v4i16_t __attribute__((ext_vector_type(4)));
typedef float f32x4 __attribute__((ext_vector_type(4)));
typedef float f32x2 __attribute__((ext_vector_type(2)));
typedef unsigned u32x4 __attribute__((ext_vector_type(4)));
typedef unsigned u32x2 __attribute__((ext_vector_type(2)));
typedef __bf16 bf16x2_t __attribute__((ext_vector_type(2)));
typedef LAS char* ldsp;

constexpr int SEQ = 2048;
constexpr size_t WS_CTL = 0;
constexpr size_t WS_KV = 262144;
constexpr size_t WS_O = WS_KV + 256 * 16384;
constexpr size_t WS_H = WS_O + 256 * 8192;
constexpr size_t WS_H1 = WS_H + (size_t)SEQ * 896;
constexpr size_t WS_ST = WS_H1 + (size_t)SEQ * 3584;
constexpr size_t WS_WT = WS_ST + (size_t)SEQ * 32;
constexpr unsigned WT_W1 = 0, WT_W2 = WT_W1 + 1568 * 1024, WT_BYTES = WT_W2 + 1568 * 1024;
constexpr int LDS_BYTES = 160 * 1024;
constexpr int M_PT = 143360;
constexpr int M_XT = 147456;
constexpr int M_TILE = 131072;
constexpr int MISC = 157696;
constexpr int M_LN = MISC, M_B1 = MISC + 1024, M_B2 = MISC + 1280, M_PF = MISC + 1536, M_PART = MISC + 2048, M_FILL = MISC + 3072;
constexpr unsigned SPIN_CAP = 1u << 16;
constexpr unsigned CODE_LINES = 192;

#define FLAG_IDX(ph, c, j) (64 + (((ph) * 16 + (c)) * 16 + (j)) * 32)

#define WAITV(n) asm volatile("s_waitcnt vmcnt(" #n ")" ::: "memory")
#define BAR() do { asm volatile("s_waitcnt lgkmcnt(0)" ::: "memory"); __builtin_amdgcn_s_barrier(); asm volatile("" ::: "memory"); } while (0)
#define BARN() do { asm volatile("" ::: "memory"); __builtin_amdgcn_s_barrier(); asm volatile("" ::: "memory"); } while (0)
#define SCHED() __builtin_amdgcn_sched_barrier(0)
#define RAWBAR() __builtin_amdgcn_s_barrier()
#define MFMA(a, b, c) __builtin_amdgcn_mfma_f32_16x16x32_bf16((a), (b), (c), 0, 0, 0)
#define MACC(acc, a, b) asm volatile("v_mfma_f32_16x16x32_bf16 %0, %1, %2, %0" : "+v"(acc) : "v"(a), "v"(b))
#define MACCN(acc, a, b) asm volatile("s_nop 1\n\tv_mfma_f32_16x16x32_bf16 %0, %1, %2, %0" : "+v"(acc) : "v"(a), "v"(b))
#define NOPS() asm volatile("s_nop 7\n\ts_nop 3")
#define KEEP(x) asm volatile("" : "+v"(x))
#define IC(x) std::integral_constant<int, (x)>{}

template <int I, int N, class F> __device__ __forceinline__ void sfor(F&& f) { if constexpr (I < N) { f(std::integral_constant<int, I>{}); sfor<I + 1, N>(f); } }

__device__ __forceinline__ unsigned cvtpk(float lo, float hi) { f32x2 v = {lo, hi}; bf16x2_t b = __builtin_convertvector(v, bf16x2_t); return __builtin_bit_cast(unsigned, b); }
__device__ __forceinline__ unsigned cvtpk(f32x2 v) { bf16x2_t b = __builtin_convertvector(v, bf16x2_t); return __builtin_bit_cast(unsigned, b); }
__device__ __forceinline__ float dot2(unsigned a, unsigned b, float c) { return __builtin_amdgcn_fdot2_f32_bf16(__builtin_bit_cast(bf16x2_t, a), __builtin_bit_cast(bf16x2_t, b), c, false); }
__device__ __forceinline__ float bflo(unsigned u) { return __uint_as_float(u << 16); }
__device__ __forceinline__ float bfhi(unsigned u) { return __uint_as_float(u & 0xffff0000u); }
__device__ __forceinline__ f32x2 bf2(unsigned u) { return f32x2{bflo(u), bfhi(u)}; }
__device__ __forceinline__ f32x2 lo2(f32x4 v) { return f32x2{v[0], v[1]}; }
__device__ __forceinline__ f32x2 hi2(f32x4 v) { return f32x2{v[2], v[3]}; }
__device__ __forceinline__ bf16x8 ld128(ldsp p) { return *(const LAS bf16x8*)p; }
__device__ __forceinline__ s16x4 ldtr(ldsp p) { return __builtin_bit_cast(s16x4, __builtin_amdgcn_ds_read_tr16_b64_v4i16((LAS v4i16_t*)p)); }
__device__ __forceinline__ bf16x8 cat(s16x4 lo, s16x4 hi) { return __builtin_shufflevector(lo, hi, 0, 1, 2, 3, 4, 5, 6, 7); }
__device__ __forceinline__ int rfl(int x) { return __builtin_amdgcn_readfirstlane(x); }
template <int LADD, int VADD, bool SC1>
__device__ __forceinline__ void dma(unsigned voff, const void* sbase, unsigned lbase) {
  const unsigned va = voff + (unsigned)VADD;
  if constexpr (SC1) asm volatile("s_add_i32 m0, %2, %3\n\ts_nop 0\n\tglobal_load_lds_dwordx4 %0, %1 sc1" ::"v"(va), "s"(sbase), "s"(lbase), "i"(LADD) : "memory", "scc");
  else asm volatile("s_add_i32 m0, %2, %3\n\ts_nop 0\n\tglobal_load_lds_dwordx4 %0, %1" ::"v"(va), "s"(sbase), "s"(lbase), "i"(LADD) : "memory", "scc");
}
template <int LADD>
__device__ __forceinline__ void dma2c_sc1(unsigned voff, const void* sbase, unsigned lbase) {
  asm volatile("s_add_i32 m0, %2, %3\n\ts_nop 0\n\tglobal_load_lds_dwordx4 %0, %1 sc1\n\tglobal_load_lds_dwordx4 %0, %1 offset:1024 sc1" ::"v"(voff), "s"(sbase), "s"(lbase), "i"(LADD) : "memory", "scc");
}
template <int OFF>
__device__ __forceinline__ void ldfrag1(bf16x8& f, unsigned voff, const void* sbase) {
  asm volatile("global_load_dwordx4 %0, %1, %2 offset:%3 sc1" : "=v"(f) : "v"(voff), "s"(sbase), "i"(OFF) : "memory");
}
__device__ __forceinline__ void pin1(bf16x8& f) { asm volatile("" : "+v"(f)); }
template <int N>
__device__ __forceinline__ void ldfrags(bf16x8* f, unsigned voff, const void* sbase) {
  sfor<0, N>([&](auto I) { constexpr int i = decltype(I)::value; ldfrag1<(i & 3) * 1024>(f[i], voff + (unsigned)((i >> 2) * 4096), sbase); });
}
template <int N>
__device__ __forceinline__ void pin(bf16x8* f) { sfor<0, N>([&](auto I) { pin1(f[decltype(I)::value]); }); }
__device__ __forceinline__ void st128_sc1(unsigned voff, const void* sbase, u32x4 v) {
  asm volatile("global_store_dwordx4 %0, %1, %2 sc1\n\ts_nop 1" ::"v"(voff), "v"(v), "s"(sbase) : "memory");
}
__device__ __forceinline__ void st64_sc1(unsigned voff, const void* sbase, u32x2 v) {
  asm volatile("global_store_dwordx2 %0, %1, %2 sc1\n\ts_nop 1" ::"v"(voff), "v"(v), "s"(sbase) : "memory");
}
__device__ __forceinline__ void st32_sc1(const void* sbase, unsigned v) {
  asm volatile("global_store_dword %0, %1, %2 sc1\n\ts_nop 1" ::"v"(0u), "v"(v), "s"(sbase) : "memory");
}
__device__ __forceinline__ float max3(float a, float b, float c) { return __builtin_elementwise_maximum(__builtin_elementwise_maximum(a, b), c); }
__device__ __forceinline__ float red_g_sum(float v) {
  auto a = __builtin_amdgcn_permlane16_swap(__float_as_uint(v), __float_as_uint(v), false, false);
  v = __uint_as_float(a[0]) + __uint_as_float(a[1]);
  auto b = __builtin_amdgcn_permlane32_swap(__float_as_uint(v), __float_as_uint(v), false, false);
  return __uint_as_float(b[0]) + __uint_as_float(b[1]);
}
__device__ __forceinline__ float red_g_max(float v) {
  auto a = __builtin_amdgcn_permlane16_swap(__float_as_uint(v), __float_as_uint(v), false, false);
  v = __builtin_elementwise_maximum(__uint_as_float(a[0]), __uint_as_float(a[1]));
  auto b = __builtin_amdgcn_permlane32_swap(__float_as_uint(v), __float_as_uint(v), false, false);
  return __builtin_elementwise_maximum(__uint_as_float(b[0]), __uint_as_float(b[1]));
}
__device__ __forceinline__ void poll_flags(const unsigned* base, unsigned voff, unsigned epoch) {
#pragma unroll 1
  for (unsigned spins = 0; spins < SPIN_CAP; ++spins) {
    unsigned v;
    asm volatile("global_load_dword %0, %1, %2 sc1\n\ts_waitcnt vmcnt(0)" : "=&v"(v) : "v"(voff), "s"(base) : "memory");
    if (__all(v == epoch)) break;
    __builtin_amdgcn_s_sleep(1);
  }
}

struct Params {
  const char* x; const char* wqkv; const char* wo; const char* w1; const char* b1; const char* w2; const char* b2;
  char* out; char* ws;
};

#define LANEVARS() int tz_ = tid; asm volatile("" : "+v"(tz_)); const int lane = tz_ & 63, l15 = lane & 15, g = lane >> 4, q4 = l15 >> 2, p4 = l15 & 3; \
  const unsigned lane16 = (unsigned)lane * 16u, x8 = (unsigned)(((lane & 7) ^ ((lane >> 3) & 7)) << 4); (void)q4; (void)p4; (void)g; (void)x8; (void)lane16

__global__ void __launch_bounds__(512, 2) fused_block(Params p) {
  extern __shared__ __attribute__((aligned(16))) char smem[];
  ldsp lds = (ldsp)smem;
  unsigned long long pc0; asm volatile("s_getpc_b64 %0" : "=s"(pc0));
  const int tid = threadIdx.x, lane = tid & 63, w = rfl(tid >> 6);
  const int l15 = lane & 15, g = lane >> 4, q4 = l15 >> 2, p4 = l15 & 3;
  const int b = blockIdx.x, r_ = b >> 3;
  const int c = 2 * (b & 7) + (r_ >> 4), j = r_ & 15;
  const int ca = 4 * ((b & 7) >> 1) + (r_ >> 3), ha = 8 * (b & 1) + (r_ & 7);
  const unsigned* ctl = (const unsigned*)(p.ws + WS_CTL);
  unsigned epoch = 0;
  const unsigned ONES = 0x3f803f80u;
  const unsigned lane16 = (unsigned)lane * 16u;
  const unsigned x8 = (unsigned)(((lane & 7) ^ ((lane >> 3) & 7)) << 4);

  constexpr int KIMG = 0, VIMG = 24576, QIMG = 49152;
  {
    const int h = ha;
    constexpr int SLOT = 28672, WOFF = 16384;
    const unsigned iw = (unsigned)(w >> 2);
    unsigned voff[4];
    {
      const unsigned xo = (unsigned)((32 * w + (lane >> 3)) * 896) + x8;
#pragma unroll
      for (int ii = 0; ii < 4; ++ii) {
        const unsigned u = (unsigned)(192 * (w & 3) + 64 * ii + lane), row = (u * 43691u) >> 19, rem = u - 12u * row;
        const unsigned piece = rem >> 2, c16 = rem & 3, hb = (c16 >> 1) ^ ((row >> 3) & 1);
        const unsigned wo_ = (ii < 3) ? row * 2688u + piece * 896u + hb * 32u + (c16 & 1) * 16u : 0u;
        voff[ii] = iw ? wo_ : xo + ii * 7168u;
      }
    }
    const unsigned long long mW = 0ull - (unsigned long long)iw;
    const unsigned long long bx_ = (unsigned long long)(uintptr_t)p.x + (unsigned)(ca * 128 * 896), bw_ = (unsigned long long)(uintptr_t)p.wqkv + (unsigned)(56 * h - 8 * (h & 1));
    const char* gb = (const char*)(uintptr_t)(bx_ + ((bw_ - bx_) & mW));
    const unsigned kstep = 128 + iw * (64 * 2688 - 128), ldst = w * 4096 + iw * (WOFF + (w - 4) * 3072 - w * 4096);
    const unsigned smask = iw - 1u, l3 = iw ? (unsigned)M_FILL : ldst + 3072u;
    auto issue1 = [&](auto SL, auto II) {
      constexpr int sl = decltype(SL)::value, ii = decltype(II)::value;
      if constexpr (ii < 3) dma<sl * SLOT + ii * 1024, 0, false>(voff[ii], gb, ldst);
      else { dma<0, 0, false>(voff[3], gb, l3 + (smask & (unsigned)(sl * SLOT))); gb += kstep; }
    };
    auto issue = [&](auto SL) { issue1(SL, IC(0)); issue1(SL, IC(1)); issue1(SL, IC(2)); issue1(SL, IC(3)); };
    issue(IC(0));
    RAWBAR(); issue(IC(1));
    {
      unsigned ln = (unsigned)(r_ * 8 + w); if (ln >= CODE_LINES) ln = 0;
      const unsigned long long a = (pc0 & ~255ull) + (unsigned long long)(ln * 128u);
      if (lane == 0) asm volatile("s_mov_b32 m0, %1\n\ts_nop 0\n\tglobal_load_lds_dword %0, off" ::"v"(a), "i"(M_FILL) : "memory");
    }

    const int ptq0 = (b < 224) ? b : 223, ptm = (ptq0 >= 112) ? 2 : 1, ptq = ptq0 - ((ptm == 2) ? 112 : 0);
    const int pts = (ptm == 1) ? ptq >> 3 : ptq >> 1, ptG = (ptm == 1) ? ptq & 7 : ptq & 1;
    auto pt_issue = [&]() {
      const unsigned stride = (ptm == 1) ? 3584u : 896u;
      const char* src = ((ptm == 1) ? p.w1 : p.w2) + (size_t)(32 * pts * stride + ptG * 448);
      const int wq = (w < 7) ? w : 6;
#pragma unroll
      for (int i = 0; i < 2; ++i) { const unsigned u = (unsigned)((2 * wq + i) * 64 + lane), row = (u * 9363u) >> 18, c16 = u - 28u * row;
        dma<M_PT, 0, false>(row * stride + c16 * 16u, src, (unsigned)((2 * wq + i) * 1024)); }
      if (lane == 63) asm volatile("s_mov_b32 m0, %2\n\ts_nop 0\n\tglobal_load_lds_dword %0, %1 sc1" ::"v"(0u), "s"(p.ws + WS_CTL), "i"(M_PF) : "memory");
      if (w == 7) {
        unsigned ln = (unsigned)(b * 13 + (lane < 13 ? lane : 12)); if (ln > 3135u) ln = 3135u;
        asm volatile("s_mov_b32 m0, %2\n\ts_nop 0\n\tglobal_load_lds_dword %0, %1" ::"v"(ln * 128u), "s"(p.wo), "i"(M_FILL) : "memory");
      }
    };
    const int tg = w >> 1, ng = w & 1;
    f32x4 acc[3][2], asum = {0, 0, 0, 0}, asq = {0, 0, 0, 0}, acs[3];
#pragma unroll
    for (int e = 0; e < 3; ++e) { acc[e][0] = f32x4{0, 0, 0, 0}; acc[e][1] = f32x4{0, 0, 0, 0}; acs[e] = f32x4{0, 0, 0, 0}; }
    const int xb = (32 * tg + 16 * ng + l15) * 128 + ((g ^ (l15 & 7)) << 4);
    int ab[3];
#pragma unroll
    for (int e = 0; e < 3; ++e) ab[e] = WOFF + (8 * g + q4) * 192 + ((((3 * ng + e) * 32) ^ ((g & 1) << 5))) + 8 * p4;
    bf16x8 xf[2][2];
    s16x4 al[2][3], ah[2][3];
    auto rdx = [&](int st, int buf, int t) { xf[buf][t] = ld128(lds + ((st >> 1) % 5) * SLOT + (xb ^ ((st & 1) << 6) ^ (t * 2048))); };
    auto rdl = [&](int st, int buf, int e) { al[buf][e] = ldtr(lds + ((st >> 1) % 5) * SLOT + ab[e] + (32 * (st & 1)) * 192); };
    auto rdh_ = [&](int st, int buf, int e) { ah[buf][e] = ldtr(lds + ((st >> 1) % 5) * SLOT + ab[e] + (32 * (st & 1) + 4) * 192); };
    auto rdf = [&](int st, int buf) { rdx(st, buf, 0); rdx(st, buf, 1);
#pragma unroll
      for (int e = 0; e < 3; ++e) { rdl(st, buf, e); rdh_(st, buf, e); } };
    bf16x8 onesf = __builtin_bit_cast(bf16x8, (u32x4{ONES, ONES, ONES, ONES}));
    KEEP(onesf); KEEP(asum); KEEP(asq);
#pragma unroll
    for (int e = 0; e < 3; ++e) { KEEP(acc[e][0]); KEEP(acc[e][1]); KEEP(acs[e]); }
    WAITV(5);
    BAR();
    rdf(0, 0);
    sfor<0, 14>([&](auto ST) {
      constexpr int st = decltype(ST)::value, kc = st >> 1, cb = st & 1;
      if constexpr ((st & 1) && st < 13) {
        if constexpr (kc == 0) WAITV(5); else if constexpr (kc == 1) WAITV(9); else if constexpr (kc <= 3) WAITV(6); else if constexpr (kc == 4) WAITV(4); else WAITV(0);
        BARN();
      }
      if constexpr (st == 2) pt_issue();
      constexpr int nb = (st + 1) & 1; constexpr bool more = st < 13;
      const bf16x8 a0 = cat(al[cb][0], ah[cb][0]), a1 = cat(al[cb][1], ah[cb][1]), a2 = cat(al[cb][2], ah[cb][2]);
      SCHED();
      MACC(acc[0][0], a0, xf[cb][0]); if constexpr (more) rdx(st + 1, nb, 0);
      MACC(acc[0][1], a0, xf[cb][1]); if constexpr (more) rdl(st + 1, nb, 0);
      MACC(asum, onesf, xf[cb][0]); if constexpr (more) rdh_(st + 1, nb, 0);
      MACC(acs[0], a0, onesf);
      SCHED();
      if constexpr (st < 2) { issue1(IC(st + 2), IC(0)); issue1(IC(st + 2), IC(1)); }
      else if constexpr (kc + 3 <= 6) issue1(IC((kc + 3) % 5), IC(2 * (st & 1)));
      SCHED();
      MACC(acc[1][0], a1, xf[cb][0]); if constexpr (more) rdx(st + 1, nb, 1);
      MACC(acc[1][1], a1, xf[cb][1]); if constexpr (more) rdl(st + 1, nb, 1);
      MACC(asq, xf[cb][0], xf[cb][0]); if constexpr (more) rdh_(st + 1, nb, 1);
      MACC(acs[1], a1, onesf);
      SCHED();
      if constexpr (st < 2) { issue1(IC(st + 2), IC(2)); issue1(IC(st + 2), IC(3)); }
      else if constexpr (kc + 3 <= 6) issue1(IC((kc + 3) % 5), IC(2 * (st & 1) + 1));
      SCHED();
      MACC(acc[2][0], a2, xf[cb][0]); if constexpr (more) rdl(st + 1, nb, 2);
      MACC(acc[2][1], a2, xf[cb][1]); if constexpr (more) rdh_(st + 1, nb, 2);
      MACC(acs[2], a2, onesf);
      SCHED();
    });
    NOPS(); KEEP(asum); KEEP(asq);
#pragma unroll
    for (int e = 0; e < 3; ++e) { KEEP(acc[e][0]); KEEP(acc[e][1]); KEEP(acs[e]); }
    epoch = (unsigned)rfl((int)*(LAS unsigned*)(lds + M_PF + 252)) + 1u;
    {
      const unsigned ptb = (ptm == 1) ? WT_W1 : WT_W2, ptn = (ptm == 1) ? 14u : 56u;
      ldsp src = lds + M_PT + (4 * g + q4) * 448 + 8 * p4;
#pragma unroll
      for (int i = 0; i < 2; ++i) {
        const int t = (w + 8 * i < 14) ? w + 8 * i : w;
        const bf16x8 f = cat(ldtr(src + t * 32), ldtr(src + 16 * 448 + t * 32));
        st128_sc1(lane16 + ptb + ((unsigned)(14 * ptG + t) * ptn + (unsigned)pts) * 1024u, p.ws + WS_WT, __builtin_bit_cast(u32x4, f));
      }
    }
    {
      const float d01 = (p4 & 1) ? asq[1] : asq[0], d23 = (p4 & 1) ? asq[3] : asq[2], dq = (p4 & 2) ? d23 : d01;
      float mean = asum[0] * (1.f / 448.f), dq2 = dq;
      KEEP(mean); KEEP(dq2);
      if (g == q4) *(LAS f32x2*)(lds + M_LN + (32 * tg + 16 * ng + l15) * 8) = f32x2{mean, rsqrtf(fmaxf(dq2 * (1.f / 448.f) - mean * mean, 0.f) + 1e-5f)};
    }
    BAR();
    float mu[2], rs[2];
#pragma unroll
    for (int t = 0; t < 2; ++t) { const f32x2 st_ = *(LAS f32x2*)(lds + M_LN + (32 * tg + 16 * (t ^ ng) + l15) * 8); mu[t] = st_[0]; rs[t] = st_[1]; }
    const bool pad0 = (h & 1) && g == 0, pad1 = !(h & 1) && g == 3;
    const int rsw = ((l15 >> 1) & 3) << 4, rowb = (32 * tg + l15) * 64;
    auto epi = [&](auto NG) {
      constexpr int ngc = decltype(NG)::value;
      sfor<0, 3>([&](auto E) {
        constexpr int e = decltype(E)::value, nt = 3 * ngc + e, which = nt >> 1, par = nt & 1;
        constexpr int ibase = (which == 0) ? QIMG : (which == 1) ? KIMG + 16384 : VIMG + 16384;
        const int off = (which == 2) ? ((((2 * par + (g >> 1)) << 4) ^ rsw) + (g & 1) * 8) : (((g << 4) ^ rsw) + par * 8);
        const bool pad = par ? pad1 : pad0;
#pragma unroll
        for (int t = 0; t < 2; ++t) {
          const float sc = (which == 0) ? rs[t] * 0.27264657f : rs[t], nm = -mu[t] * sc;
          f32x2 sc2 = {sc, sc}, nm2 = {nm, nm};
          KEEP(sc2); KEEP(nm2);
          const f32x2 v0 = lo2(acc[e][t]) * sc2 + lo2(acs[e]) * nm2, v1 = hi2(acc[e][t]) * sc2 + hi2(acs[e]) * nm2;
          u32x2 o; o.x = cvtpk(v0); o.y = cvtpk(v1);
          o.x = pad ? ((which == 2) ? 0x00003f80u : 0u) : o.x; o.y = pad ? 0u : o.y;
          *(LAS u32x2*)(lds + ibase + (t ^ ngc) * 1024 + rowb + off) = o;
        }
      });
    };
    if (ng) epi(IC(1)); else epi(IC(0));
    if (ca < 2) {
      const u32x4 z = {0, 0, 0, 0};
#pragma unroll
      for (int i = 0; i < 2; ++i) { *(LAS u32x4*)(lds + KIMG + tid * 16 + i * 8192) = z; *(LAS u32x4*)(lds + VIMG + tid * 16 + i * 8192) = z; }
    }
    BAR();
    {
      const char* blob = p.ws + WS_KV + (size_t)(ca * 16 + h) * 16384;
      const u32x4 v0 = *(LAS u32x4*)(lds + KIMG + 16384 + tid * 16), v1 = *(LAS u32x4*)(lds + VIMG + 16384 + tid * 16);
      st128_sc1(tid * 16, blob, v0); st128_sc1(8192 + tid * 16, blob, v1);
    }
    WAITV(0);
    BAR();
    if (tid == 0) { st32_sc1(ctl + FLAG_IDX(0, ca, h), epoch); st32_sc1(ctl + FLAG_IDX(4, b >> 4, b & 15), epoch); }
  }
  const int ns = j & 3, col4 = 128 * ns - (((ns >> 1) & ns) << 5), ngr = 4 - (ns >> 1);
  const int pr = w & 3, wh = w >> 2;
  const int prc = (pr < ngr) ? pr : 0;
  const int th5 = j >> 3, cs5 = j & 7, col0 = 64 * cs5 - 16 * ((cs5 >> 2) * (cs5 & 3)), nblk = 4 - (cs5 >> 2);
  const int ng5 = w & 1, kq = w >> 1;
  const char* wt = p.ws + WS_WT;
  bf16x8 wr[32];
  const int T1 = (448 * (j >> 2) + col4) / 16;
  const unsigned w1o = WT_W1 + (unsigned)(T1 + 2 * prc) * 14u * 1024u;
  const unsigned wro = lane16 + w1o;
  constexpr int WI = 32768;
  unsigned wb0, wb1;
  { const int rin = lane >> 4, pos = lane & 15;
    int blk = (pos >> 1) ^ rin; if (blk >= 2 * ngr) blk -= 2; wb0 = (unsigned)(blk * 32 + (pos & 1) * 16);
    blk = (pos >> 1) ^ (4 + rin); if (blk >= 2 * ngr) blk -= 2; wb1 = (unsigned)(blk * 32 + (pos & 1) * 16); }
  const char* wob = p.wo + col4 * 2;
  {
    if (w < 6) {
      const unsigned rb = (unsigned)(((lane >> 4) + 4 * (24 + w)) * 896) + ((w & 1) ? wb1 : wb0);
      sfor<0, 14>([&](auto I) { constexpr int i = decltype(I)::value; dma<i * 6144, i * 24 * 896, false>(rb, wob, (unsigned)(WI + (24 + w) * 1024)); });
    }
    int pos = lane & 15; if (pos >= 4 * ngr) pos = 4 * ngr - 1;
    int xch = (lane & 15) ^ (((lane >> 4) + 4 * w) & 15); if (xch >= 4 * ngr) xch = 0;
    dma<M_XT, 0, false>((unsigned)(((lane >> 4) + 4 * w) * 896 + xch * 16), p.x + (size_t)(128 * c + 32 * (j >> 2)) * 896 + col4 * 2, (unsigned)(w * 1024));
    if (w == 5 && lane < 16) dma<M_B1, 0, false>((unsigned)(pos * 16), p.b1 + (448 * (j >> 2) + col4) * 2, 0u);
    if (w == 4 && lane < 8) dma<M_B2, 0, false>((unsigned)((lane < 2 * nblk ? lane : 0) * 16), p.b2 + col0 * 2, 0u);
  }
  {
    const int h = ha;
    const int sw = (l15 >> 1) & 3;
    if (w == 7 && ca >= 1) poll_flags(ctl, (unsigned)(FLAG_IDX(0, (ca >= 2 && (lane & 1)) ? ca - 2 : ca - 1, h) * 4), epoch);
    BAR();
    {
      const int tl = w >> 2, src = ca - 2 + tl, pc = (w & 3) * 2048;
      if (src >= 0) {
        const char* blob = p.ws + WS_KV + (size_t)(src * 16 + h) * 16384 + pc;
        dma2c_sc1<KIMG>(lane16, blob, (unsigned)(tl * 8192 + pc)); dma2c_sc1<VIMG>(lane16, blob + 8192, (unsigned)(tl * 8192 + pc));
      }
    }
    const bf16x8 qf = ld128(lds + QIMG + (16 * w + l15) * 64 + ((g ^ sw) << 4));
    WAITV(0);
    BAR();
    unsigned tfo, tfl;
    { const int sl = (l15 < 14) ? l15 : 13, hi = (lane >> 4) & 1;
      const int g1 = 8 * sl + (T1 + (hi ? 2 * ngr - 1 : 0)) / 14, g2 = 112 + 2 * (14 * kq + sl) + (col0 / 16 + (hi ? nblk - 1 : 0)) / 14, gi = (lane >= 32) ? g2 : g1;
      tfo = (unsigned)(FLAG_IDX(4, gi >> 4, gi & 15) * 4); }
    asm volatile("global_load_dword %0, %1, %2 sc1" : "=v"(tfl) : "v"(tfo), "s"(ctl) : "memory");
    f32x4 sc[17];
    const float NEG = -__builtin_inff();
    ldsp kb = lds + KIMG + (16 * w + l15) * 64 + ((g ^ sw) << 4);
    {
      bf16x8 kf[2][6];
#pragma unroll
      for (int u = 0; u < 6; ++u) kf[0][u] = ld128(kb + u * 1024);
#pragma unroll
      for (int bt = 0; bt < 3; ++bt) {
        if (bt < 2) {
#pragma unroll
          for (int u = 0; u < 6; ++u) if (6 * (bt + 1) + u < 17) kf[(bt + 1) & 1][u] = ld128(kb + (6 * (bt + 1) + u) * 1024);
        }
        SCHED();
#pragma unroll
        for (int u = 0; u < 6; ++u) if (6 * bt + u < 17) sc[6 * bt + u] = MFMA(kf[bt & 1][u], qf, (f32x4{0, 0, 0, 0}));
        SCHED();
      }
    }
    asm volatile("s_waitcnt vmcnt(0)" : "+v"(tfl)::"memory");
    if (!__all(tfl == epoch)) poll_flags(ctl, tfo, epoch);
    if (w >= 4) ldfrags<7>(wr, wro, wt);
    const int vsw = (2 * g + (q4 >> 1)) & 3;
    ldsp vb0 = lds + VIMG + (16 * w + 4 * g + q4) * 64 + (((p4 >> 1) ^ vsw) << 4) + (p4 & 1) * 8;
    ldsp vb1 = lds + VIMG + (16 * w + 4 * g + q4) * 64 + (((2 + (p4 >> 1)) ^ vsw) << 4) + (p4 & 1) * 8;
    bf16x8 vf[2][3][2];
    auto rdv = [&](int bt) {
#pragma unroll
      for (int i = 0; i < 3; ++i) { const int uu = 3 * bt + i, ua = 2 * uu, ub = (uu < 8) ? 2 * uu + 1 : 15;
        vf[bt & 1][i][0] = cat(ldtr(vb0 + ua * 1024), ldtr(vb0 + ub * 1024)); vf[bt & 1][i][1] = cat(ldtr(vb1 + ua * 1024), ldtr(vb1 + ub * 1024)); }
    };
    rdv(0);
    SCHED();
    if (ca < 2) {
      const int tmin = (2 - ca) * 8;
#pragma unroll
      for (int u = 0; u < 16; ++u) if (w + u < tmin) sc[u] = f32x4{NEG, NEG, NEG, NEG};
    }
#pragma unroll
    for (int r = 0; r < 4; ++r) { sc[0][r] = (4 * g + r <= l15) ? NEG : sc[0][r]; sc[16][r] = (4 * g + r > l15) ? NEG : sc[16][r]; }
    float m = max3(sc[0][0], sc[0][1], sc[0][2]), m2 = max3(sc[0][3], sc[1][0], sc[1][1]);
    m = max3(m, sc[1][2], sc[1][3]);
#pragma unroll
    for (int u = 2; u < 17; ++u) { if (u & 1) m = max3(max3(m, sc[u][0], sc[u][1]), sc[u][2], sc[u][3]); else m2 = max3(max3(m2, sc[u][0], sc[u][1]), sc[u][2], sc[u][3]); }
    m = max3(m, m2, m2);
    m = red_g_max(m);
    SCHED();
    if (w >= 4) ldfrags<7>(wr + 7, wro + 7 * 1024, wt);
    unsigned pk[17][2];
    f32x2 mm = {-m, -m};
    KEEP(mm);
    f32x4 oa[2] = {f32x4{0, 0, 0, 0}, f32x4{0, 0, 0, 0}};
    KEEP(oa[0]); KEEP(oa[1]);
#pragma unroll
    for (int bt = 0; bt < 3; ++bt) {
#pragma unroll
      for (int u = 6 * bt; u < 6 * bt + 6 && u < 17; ++u) {
        const f32x2 d0 = lo2(sc[u]) + mm, d1 = hi2(sc[u]) + mm;
        pk[u][0] = cvtpk(__builtin_amdgcn_exp2f(d0[0]), __builtin_amdgcn_exp2f(d0[1]));
        pk[u][1] = cvtpk(__builtin_amdgcn_exp2f(d1[0]), __builtin_amdgcn_exp2f(d1[1]));
      }
      if (bt < 2) rdv(bt + 1);
      if (bt < 2 && w >= 4) ldfrags<7>(wr + 14 + 7 * bt, wro + (14 + 7 * bt) * 1024, wt);
      SCHED();
#pragma unroll
      for (int i = 0; i < 3; ++i) {
        const int uu = 3 * bt + i;
        u32x4 pu; pu.x = pk[2 * uu][0]; pu.y = pk[2 * uu][1];
        if (uu < 8) { pu.z = pk[2 * uu + 1][0]; pu.w = pk[2 * uu + 1][1]; } else { pu.z = 0; pu.w = 0; }
        const bf16x8 pf = __builtin_bit_cast(bf16x8, pu);
        MACCN(oa[0], vf[bt & 1][i][0], pf); MACC(oa[1], vf[bt & 1][i][1], pf);
      }
      SCHED();
    }
    NOPS(); KEEP(oa[0]); KEEP(oa[1]);
    int tz2 = tid; asm volatile("" : "+v"(tz2));
    const int l15b = tz2 & 15, gb = (tz2 & 63) >> 4, swb = (l15b >> 1) & 3;
    const float lsel = (h & 1) ? oa[0][0] : oa[1][0];
    const float l = __uint_as_float((unsigned)__builtin_amdgcn_ds_bpermute((((h & 1) ? 0 : 48) + l15b) * 4, (int)__float_as_uint(lsel)));
    const float inv1 = __builtin_amdgcn_rcpf(l);
    f32x2 inv = {inv1, inv1};
    KEEP(inv);
    u32x4 ov;
    ov.x = cvtpk(lo2(oa[0]) * inv); ov.y = cvtpk(hi2(oa[0]) * inv); ov.z = cvtpk(lo2(oa[1]) * inv); ov.w = cvtpk(hi2(oa[1]) * inv);
    { const bool z0 = (h & 1) && gb == 0, z1 = !(h & 1) && gb == 3; ov.x = z0 ? 0u : ov.x; ov.y = z0 ? 0u : ov.y; ov.z = z1 ? 0u : ov.z; ov.w = z1 ? 0u : ov.w; }
    st128_sc1((unsigned)((16 * w + l15b) * 64 + ((gb ^ swb) << 4)), p.ws + WS_O + (size_t)(ca * 16 + h) * 8192, ov);
    BAR();
    {
      const unsigned rb = (unsigned)(((lane >> 4) + 12 * w) * 896), va = rb + ((w & 1) ? wb1 : wb0), vb = rb + ((w & 1) ? wb0 : wb1), lb = (unsigned)(WI + 3 * w * 1024);
      dma<0, 0, false>(va, wob, lb); dma<1024, 4 * 896, false>(vb, wob, lb); dma<2048, 8 * 896, false>(va, wob, lb);
      const int ins = (w < 4) ? 108 + w : 3 * w + 2;
      dma<0, 0, false>((unsigned)(((lane >> 4) + 4 * ins) * 896) + ((ins & 1) ? wb1 : wb0), wob, (unsigned)(WI + ins * 1024));
    }
    WAITV(4);
    BAR();
    if (tid == 0) st32_sc1(ctl + FLAG_IDX(1, ca, h), epoch);
  }
  {
    LANEVARS();
    constexpr int OT = 0;
    const int tq = j >> 2;
    WAITV(0);
    BAR();
    if (w < 4) {
      const int rx = 4 * (g & 1) + q4;
      ldsp ab0 = lds + WI + (4 * g + q4) * 256 + (((2 * prc) ^ rx) << 5) + 8 * p4;
      ldsp ab1 = lds + WI + (4 * g + q4) * 256 + (((2 * prc + 1) ^ rx) << 5) + 8 * p4;
      ldsp ab0h = ab0 + 65536, ab1h = ab1 + 65536;
#pragma unroll
      for (int s = 0; s < 16; ++s) {
        const int rb = (28 * s - 4 * (s & 1)) * 256;
        if (rb + 4096 < 65536) { wr[s] = cat(ldtr(ab0 + rb), ldtr(ab0 + rb + 4096)); wr[16 + s] = cat(ldtr(ab1 + rb), ldtr(ab1 + rb + 4096)); }
        else { wr[s] = cat(ldtr(ab0h + (rb - 65536)), ldtr(ab0h + (rb - 65536 + 4096))); wr[16 + s] = cat(ldtr(ab1h + (rb - 65536)), ldtr(ab1h + (rb - 65536 + 4096))); }
      }
    } else {
      __builtin_amdgcn_s_setprio(3);
      poll_flags(ctl, (unsigned)(FLAG_IDX(1, c, l15) * 4), epoch);
      const char* ob_ = p.ws + WS_O + (size_t)(c * 16 + 4 * (w - 4)) * 8192 + tq * 2048;
      const unsigned lb = (unsigned)(OT + (w - 4) * 8192);
      dma2c_sc1<0>(lane16, ob_, lb); dma2c_sc1<2048>(lane16 + 8192, ob_, lb); dma2c_sc1<4096>(lane16 + 16384, ob_, lb); dma2c_sc1<6144>(lane16 + 24576, ob_, lb);
      __builtin_amdgcn_s_setprio(0);
    }
    WAITV(0);
    BAR();
    if (w < 4) {
      f32x4 a0[2], a1[2];
#pragma unroll
      for (int t = 0; t < 2; ++t) { a0[t] = f32x4{0, 0, 0, 0}; a1[t] = f32x4{0, 0, 0, 0}; }
      ldsp ob = lds + OT + l15 * 64 + ((g ^ ((l15 >> 1) & 3)) << 4);
      u32x2 xr[2][2];
#pragma unroll
      for (int t = 0; t < 2; ++t) { ldsp xp = lds + M_XT + (16 * t + l15) * 256 + (g & 1) * 8;
        xr[t][0] = *(LAS u32x2*)(xp + (((4 * prc + (g >> 1)) ^ l15) << 4)); xr[t][1] = *(LAS u32x2*)(xp + (((4 * prc + 2 + (g >> 1)) ^ l15) << 4)); }
#pragma unroll
      for (int t = 0; t < 2; ++t) { KEEP(a0[t]); KEEP(a1[t]); }
      bf16x8 of[4][2];
#pragma unroll
      for (int s = 0; s < 3; ++s) { of[s][0] = ld128(ob + s * 2048); of[s][1] = ld128(ob + s * 2048 + 1024); }
#pragma unroll
      for (int s = 0; s < 16; ++s) {
        if (s + 3 < 16) { of[(s + 3) & 3][0] = ld128(ob + (s + 3) * 2048); of[(s + 3) & 3][1] = ld128(ob + (s + 3) * 2048 + 1024); }
        SCHED();
#pragma unroll
        for (int t = 0; t < 2; ++t) { MACC(a0[t], wr[s], of[s & 3][t]); MACC(a1[t], wr[16 + s], of[s & 3][t]); }
        SCHED();
      }
      NOPS();
#pragma unroll
      for (int t = 0; t < 2; ++t) { KEEP(a0[t]); KEEP(a1[t]); }
      const unsigned ho = (unsigned)((128 * c + 32 * tq + l15) * 896 + (col4 / 32 + pr) * 64 + g * 16);
#pragma unroll
      for (int t = 0; t < 2; ++t) {
        u32x4 hv;
        hv.x = cvtpk(lo2(a0[t]) + bf2(xr[t][0].x)); hv.y = cvtpk(hi2(a0[t]) + bf2(xr[t][0].y));
        hv.z = cvtpk(lo2(a1[t]) + bf2(xr[t][1].x)); hv.w = cvtpk(hi2(a1[t]) + bf2(xr[t][1].y));
        if (pr < ngr) st128_sc1(ho + t * 16 * 896, p.ws + WS_H, hv);
        float s1 = 0.f, s2 = 0.f;
#pragma unroll
        for (int i = 0; i < 4; ++i) { s1 = dot2(hv[i], ONES, s1); s2 = dot2(hv[i], hv[i], s2); }
        s1 = red_g_sum(s1); s2 = red_g_sum(s2);
        if (pr >= ngr) { s1 = 0.f; s2 = 0.f; }
        if (g == 0) ((LAS f32x2*)(lds + M_PART))[(16 * t + l15) * 4 + pr] = f32x2{s1, s2};
      }
    } else {
      pin<28>(wr);
#pragma unroll
      for (int i = 0; i < 28; ++i) *(LAS bf16x8*)(lds + 32768 + (w - 4) * 28672 + i * 1024 + lane * 16) = wr[i];
      if (b == 0 && w == 5) {
#pragma unroll 1
        for (int i = 0; i < 4; ++i) poll_flags(ctl, (unsigned)(FLAG_IDX(4, 4 * i + g, l15) * 4), epoch);
        if (lane == 0) st32_sc1(ctl, epoch);
      }
    }
    BAR();
    if (tid < 32) {
      const f32x4 u0 = *(LAS f32x4*)(lds + M_PART + tid * 32), u1 = *(LAS f32x4*)(lds + M_PART + tid * 32 + 16);
      st64_sc1((unsigned)((128 * c + 32 * tq + tid) * 32 + ns * 8), p.ws + WS_ST,
               u32x2{__float_as_uint((u0[0] + u0[2]) + (u1[0] + u1[2])), __float_as_uint((u0[1] + u0[3]) + (u1[1] + u1[3]))});
    }
    if (w < 4) {
#pragma unroll
      for (int i = 0; i < 28; ++i) wr[i] = ld128(lds + 32768 + w * 28672 + i * 1024 + lane * 16);
    }
    if (w < 4) WAITV(0);
    BAR();
    if (tid == 0) st32_sc1(ctl + FLAG_IDX(2, c, j), epoch);
  }
  {
    LANEVARS();
    const int f0_ = 448 * (j >> 2) + col4, th = wh;
    f32x4 c0 = {0, 0, 0, 0}, c1 = {0, 0, 0, 0};
    pin<28>(wr);
    bf16x8 onesf = __builtin_bit_cast(bf16x8, (u32x4{ONES, ONES, ONES, ONES}));
    KEEP(onesf); KEEP(c0); KEEP(c1);
    asm volatile("s_nop 1");
#pragma unroll
    for (int s = 0; s < 14; ++s) { MACC(c0, wr[s], onesf); MACC(c1, wr[14 + s], onesf); }
    NOPS(); KEEP(c0); KEEP(c1);
    SCHED();
    if (w == 7) poll_flags(ctl, (unsigned)(FLAG_IDX(2, c, l15) * 4), epoch);
    BAR();
    const unsigned vo = (unsigned)((16 * w + (lane >> 3)) * 896) + x8, wl2k = (unsigned)(w * 2048);
    const char* hb = p.ws + WS_H + (size_t)c * 128 * 896;
    auto issue4 = [&](auto KC) { constexpr int kc = decltype(KC)::value; dma<kc * 16384, kc * 128, true>(vo, hb, wl2k); dma<kc * 16384 + 1024, kc * 128 + 8 * 896, true>(vo, hb, wl2k); };
    if (lane < 32) dma<M_TILE, 0, true>(lane16, p.ws + WS_ST + (size_t)(128 * c + 16 * w) * 32, (unsigned)(w * 512));
    issue4(IC(0)); RAWBAR(); issue4(IC(1));
    f32x4 acc[2][4];
#pragma unroll
    for (int t = 0; t < 4; ++t) { acc[0][t] = f32x4{0, 0, 0, 0}; acc[1][t] = f32x4{0, 0, 0, 0}; }
    const int xb = (64 * th + l15) * 128 + ((g ^ (l15 & 7)) << 4);
    bf16x8 xf[2][4];
    auto rd1 = [&](int st, int buf, int t) { xf[buf][t] = ld128(lds + (st >> 1) * 16384 + (xb ^ ((st & 1) << 6)) + t * 2048); };
    auto rdf = [&](int st, int buf) { rd1(st, buf, 0); rd1(st, buf, 1); rd1(st, buf, 2); rd1(st, buf, 3); };
#pragma unroll
    for (int t = 0; t < 4; ++t) { KEEP(acc[0][t]); KEEP(acc[1][t]); }
    bf16x8 bf[2][14];
    const unsigned w2o0 = WT_W2 + ((unsigned)(col0 / 16 + 2 * ng5) * 56u + 14u * kq) * 1024u, w2o1 = (2 * ng5 + 1 < nblk) ? w2o0 + 56u * 1024u : w2o0;
    WAITV(2); BAR();
    rdf(0, 0);
    sfor<0, 14>([&](auto ST) {
      constexpr int st = decltype(ST)::value;
      if constexpr (st == 1) WAITV(2); if constexpr (st == 3) WAITV(4); if constexpr (st == 5) WAITV(6); if constexpr (st == 7) WAITV(8); if constexpr (st == 9) WAITV(10); if constexpr (st == 11) WAITV(12);
      if constexpr ((st & 1) && st < 13) BARN();
      if constexpr (st == 2) {
        if (tid < 128) {
          const f32x4 u0 = *(LAS f32x4*)(lds + M_TILE + tid * 32), u1 = *(LAS f32x4*)(lds + M_TILE + tid * 32 + 16);
          float mean = ((u0[0] + u0[2]) + (u1[0] + u1[2])) * (1.f / 448.f), sq = (u0[1] + u0[3]) + (u1[1] + u1[3]);
          KEEP(mean); KEEP(sq);
          const float rstd = rsqrtf(fmaxf(sq * (1.f / 448.f) - mean * mean, 0.f) + 1e-5f);
          *(LAS f32x2*)(lds + M_LN + tid * 8) = f32x2{rstd, -mean * rstd};
        }
      }
      SCHED();
#pragma unroll
      for (int t = 0; t < 2; ++t) { MACC(acc[0][t], wr[st], xf[st & 1][t]); if constexpr (st < 13) rd1(st + 1, (st + 1) & 1, t); MACC(acc[1][t], wr[14 + st], xf[st & 1][t]); }
      SCHED();
      if constexpr (st <= 4) issue4(IC(st + 2));
      if constexpr (st >= 5) { constexpr int s = st - 5; ldfrags<1>(&bf[0][s], lane16 + w2o0 + s * 1024, wt); ldfrags<1>(&bf[1][s], lane16 + w2o1 + s * 1024, wt); }
      SCHED();
#pragma unroll
      for (int t = 2; t < 4; ++t) { MACC(acc[0][t], wr[st], xf[st & 1][t]); if constexpr (st < 13) rd1(st + 1, (st + 1) & 1, t); MACC(acc[1][t], wr[14 + st], xf[st & 1][t]); }
      SCHED();
    });
    NOPS();
#pragma unroll
    for (int t = 0; t < 4; ++t) { KEEP(acc[0][t]); KEEP(acc[1][t]); }
    {
      const u32x2 bb0 = *(LAS u32x2*)(lds + M_B1 + (32 * prc + 4 * g) * 2), bb1 = *(LAS u32x2*)(lds + M_B1 + (32 * prc + 4 * g) * 2 + 32);
      const f32x2 b00 = bf2(bb0.x), b01 = bf2(bb0.y), b10 = bf2(bb1.x), b11 = bf2(bb1.y);
      const unsigned ho = (unsigned)((128 * c + 64 * th + l15) * 3584 + (f0_ / 32 + pr) * 64 + g * 16);
      f32x2 ln[4];
#pragma unroll
      for (int t = 0; t < 4; ++t) ln[t] = *(LAS f32x2*)(lds + M_LN + (64 * th + 16 * t + l15) * 8);
#pragma unroll
      for (int t = 0; t < 4; ++t) {
        const float a = ln[t][0], nm = ln[t][1];
        f32x2 aa = {a, a}, nn = {nm, nm};
        KEEP(aa); KEEP(nn);
        f32x2 z[4] = {lo2(acc[0][t]) * aa + (lo2(c0) * nn + b00), hi2(acc[0][t]) * aa + (hi2(c0) * nn + b01), lo2(acc[1][t]) * aa + (lo2(c1) * nn + b10), hi2(acc[1][t]) * aa + (hi2(c1) * nn + b11)};
        u32x4 hv;
#pragma unroll
        for (int i = 0; i < 4; ++i) {
          f32x2 r = {__builtin_elementwise_maximum(z[i][0], 0.f), __builtin_elementwise_maximum(z[i][1], 0.f)};
          hv[i] = cvtpk(r * r);
        }
        if (pr < ngr) st128_sc1(ho + t * 16 * 3584, p.ws + WS_H1, hv);
      }
    }
    WAITV(0);
    BAR();
    if (tid == 0) st32_sc1(ctl + FLAG_IDX(3, c, j), epoch);
    if (w != 7) {
      ldfrags<5>(&bf[0][9], lane16 + w2o0 + 9 * 1024, wt); ldfrags<5>(&bf[1][9], lane16 + w2o1 + 9 * 1024, wt);
    }
    if (w >= 5 && w < 7) {
      const int m0_ = col0 >> 5;
      const char* hr = p.ws + WS_H + (size_t)(128 * c + 64 * th5) * 896;
      sfor<0, 6>([&](auto I) { constexpr int i = decltype(I)::value;
        const unsigned u = (unsigned)((w - 5) * 384 + i * 64 + lane), r0 = (u * 43691u) >> 19, c0_ = u - 12u * r0; unsigned cc0 = m0_ * 4 + c0_; if (cc0 > 55) cc0 = 55;
        dma<M_TILE + i * 1024, 0, true>(r0 * 896 + cc0 * 16, hr, (unsigned)((w - 5) * 6144)); });
    }
    const unsigned vo5 = (unsigned)((32 * ng5 + (lane >> 3)) * 3584 + kq * 896) + x8, wl4k = (unsigned)(w * 4096);
    const char* hb5 = p.ws + WS_H1 + (size_t)(128 * c + 64 * th5) * 3584;
    auto issue5 = [&](auto I) {
      constexpr int i = decltype(I)::value, sa = (i & 3) * 32768;
      sfor<0, 4>([&](auto II) { constexpr int ii = decltype(II)::value; dma<sa + ii * 1024, i * 128 + ii * 8 * 3584, true>(vo5, hb5, wl4k); });
    };
    if (w == 7) {
      poll_flags(ctl, (unsigned)(FLAG_IDX(3, c, l15) * 4), epoch);
    }
    BAR();
    issue5(IC(0));
    if (w == 7) {
      ldfrags<5>(&bf[0][9], lane16 + w2o0 + 9 * 1024, wt); ldfrags<5>(&bf[1][9], lane16 + w2o1 + 9 * 1024, wt);
    }
    f32x4 ac5[2][4];
#pragma unroll
    for (int t = 0; t < 4; ++t) { ac5[0][t] = f32x4{0, 0, 0, 0}; ac5[1][t] = f32x4{0, 0, 0, 0}; }
    int yb[4];
#pragma unroll
    for (int t = 0; t < 4; ++t) yb[t] = kq * 8192 + (16 * ((t + kq) & 3) + l15) * 128 + ((g ^ (l15 & 7)) << 4);
    bf16x8 yf[2][4];
    auto rz1 = [&](int st, int buf, int t) { yf[buf][t] = ld128(lds + ((st >> 1) & 3) * 32768 + (yb[t] ^ ((st & 1) << 6))); };
    auto rdy = [&](int st, int buf) { rz1(st, buf, 0); rz1(st, buf, 1); rz1(st, buf, 2); rz1(st, buf, 3); };
#pragma unroll
    for (int t = 0; t < 4; ++t) { KEEP(ac5[0][t]); KEEP(ac5[1][t]); }
    WAITV(0);
    pin<14>(bf[0]); pin<14>(bf[1]);
    BAR();
    rdy(0, 0);
    sfor<0, 14>([&](auto ST) {
      constexpr int st = decltype(ST)::value;
      if constexpr ((st & 1) && st < 13) {
        if constexpr (st < 11) WAITV(4); else WAITV(0);
        BARN();
      }
      SCHED();
#pragma unroll
      for (int t = 0; t < 2; ++t) { MACC(ac5[0][t], bf[0][st], yf[st & 1][t]); if constexpr (st < 13) rz1(st + 1, (st + 1) & 1, t); MACC(ac5[1][t], bf[1][st], yf[st & 1][t]); }
      SCHED();
      if constexpr (st == 0) { issue5(IC(1)); issue5(IC(2)); }
      if constexpr ((st & 1) && st <= 7) issue5(IC((st + 5) / 2));
      SCHED();
#pragma unroll
      for (int t = 2; t < 4; ++t) { MACC(ac5[0][t], bf[0][st], yf[st & 1][t]); if constexpr (st < 13) rz1(st + 1, (st + 1) & 1, t); MACC(ac5[1][t], bf[1][st], yf[st & 1][t]); }
      SCHED();
    });
    NOPS();
#pragma unroll
    for (int t = 0; t < 4; ++t) { KEEP(ac5[0][t]); KEEP(ac5[1][t]); }
    auto paddr = [&](int blk) { return (blk < 16) ? blk * 2048 : 98304 + (blk - 16) * 2048; };
#pragma unroll
    for (int t = 1; t < 4; ++t) {
      ldsp d = lds + paddr((((t + kq) & 3) * 3 + (t - 1)) * 2 + ng5) + lane * 16;
      *(LAS f32x4*)d = ac5[0][t]; *(LAS f32x4*)(d + 1024) = ac5[1][t];
    }
    const int tokl = 16 * kq + l15;
    const int t0 = col0 / 16 + 2 * ng5, t1 = (2 * ng5 + 1 < nblk) ? t0 + 1 : t0, m0_ = col0 >> 5;
    const u32x2 hr0 = *(LAS u32x2*)(lds + M_TILE + tokl * 192 + ((t0 >> 1) - m0_) * 64 + g * 16 + (t0 & 1) * 8);
    const u32x2 hr1 = *(LAS u32x2*)(lds + M_TILE + tokl * 192 + ((t1 >> 1) - m0_) * 64 + g * 16 + (t1 & 1) * 8);
    const u32x2 b20 = *(LAS u32x2*)(lds + M_B2 + (32 * ng5 + 4 * g) * 2), b21 = *(LAS u32x2*)(lds + M_B2 + ((2 * ng5 + 1 < nblk) ? (32 * ng5 + 16) : 32 * ng5) * 2 + 8 * g);
    f32x2 r00 = lo2(ac5[0][0]) + (bf2(b20.x) + bf2(hr0.x)), r01 = hi2(ac5[0][0]) + (bf2(b20.y) + bf2(hr0.y));
    f32x2 r10 = lo2(ac5[1][0]) + (bf2(b21.x) + bf2(hr1.x)), r11 = hi2(ac5[1][0]) + (bf2(b21.y) + bf2(hr1.y));
    BAR();
#pragma unroll
    for (int t = 1; t < 4; ++t) {
      ldsp d = lds + paddr((kq * 3 + (t - 1)) * 2 + ng5) + lane * 16;
      const f32x4 u0 = *(LAS f32x4*)d, u1 = *(LAS f32x4*)(d + 1024);
      r00 += lo2(u0); r01 += hi2(u0); r10 += lo2(u1); r11 += hi2(u1);
    }
    GAS char* op = (GAS char*)p.out + (size_t)(128 * c + 64 * th5 + tokl) * 896 + (col0 + 32 * ng5 + 4 * g) * 2;
    *(GAS u32x2*)op = u32x2{cvtpk(r00), cvtpk(r01)};
    if (2 * ng5 + 1 < nblk) *(GAS u32x2*)(op + 32) = u32x2{cvtpk(r10), cvtpk(r11)};
  }
}

extern "C" void kernel_launch(void* const* d_in, const int* in_sizes, int n_in, void* d_out, int out_size, void* d_ws, size_t ws_size, hipStream_t stream) {
  Params p;
  p.x = (const char*)d_in[0]; p.wqkv = (const char*)d_in[1]; p.wo = (const char*)d_in[2];
  p.w1 = (const char*)d_in[4]; p.b1 = (const char*)d_in[5]; p.w2 = (const char*)d_in[6]; p.b2 = (const char*)d_in[7];
  p.out = (char*)d_out; p.ws = (char*)d_ws;
  (void)hipFuncSetAttribute((const void*)fused_block, hipFuncAttributeMaxDynamicSharedMemorySize, LDS_BYTES);
  hipLaunchKernelGGL(fused_block, dim3(256), dim3(512), LDS_BYTES, stream, p);
}
```

```cpp
#include <hip/hip_runtime.h>
#include <hip/hip_bf16.h>
#include <stdint.h>
#include <type_traits>

#define LAS __attribute__((address_space(3)))
#define GAS __attribute__((address_space(1)))
typedef short bf16x8 __attribute__((ext_vector_type(8)));
typedef short s16x4 __attribute__((ext_vector_type(4)));
typedef short v4i16_t __attribute__((ext_vector_type(4)));
typedef float f32x4 __attribute__((ext_vector_type(4)));
typedef float f32x2 __attribute__((ext_vector_type(2)));
typedef unsigned u32x4 __attribute__((ext_vector_type(4)));
typedef unsigned u32x2 __attribute__((ext_vector_type(2)));
typedef __bf16 bf16x2_t __attribute__((ext_vector_type(2)));
typedef LAS char* ldsp;

constexpr int SEQ = 2048;
constexpr size_t WS_CTL = 0;
constexpr size_t WS_KV = 262144;
constexpr size_t WS_O = WS_KV + 256 * 16384;
constexpr size_t WS_H = WS_O + 256 * 8192;
constexpr size_t WS_H1 = WS_H + (size_t)SEQ * 896;
constexpr size_t WS_ST = WS_H1 + (size_t)SEQ * 3584;
constexpr size_t WS_WT = WS_ST + (size_t)SEQ * 32;
constexpr unsigned WT_W1 = 0, WT_W2 = WT_W1 + 1568 * 1024, WT_BYTES = WT_W2 + 1568 * 1024;
constexpr int LDS_BYTES = 160 * 1024;
constexpr int M_PT = 143360;
constexpr int M_XT = 147456;
constexpr int M_TILE = 131072;
constexpr int MISC = 157696;
constexpr int M_LN = MISC, M_B1 = MISC + 1024, M_B2 = MISC + 1280, M_PF = MISC + 1536, M_PART = MISC + 2048, M_FILL = MISC + 3072;
constexpr unsigned SPIN_CAP = 1u << 16;
constexpr unsigned CODE_LINES = 192;

#define FLAG_IDX(ph, c, j) (64 + (((ph) * 16 + (c)) * 16 + (j)) * 32)

#define WAITV(n) asm volatile("s_waitcnt vmcnt(" #n ")" ::: "memory")
#define BAR() do { asm volatile("s_waitcnt lgkmcnt(0)" ::: "memory"); __builtin_amdgcn_s_barrier(); asm volatile("" ::: "memory"); } while (0)
#define BARN() do { asm volatile("" ::: "memory"); __builtin_amdgcn_s_barrier(); asm volatile("" ::: "memory"); } while (0)
#define SCHED() __builtin_amdgcn_sched_barrier(0)
#define RAWBAR() __builtin_amdgcn_s_barrier()
#define MFMA(a, b, c) __builtin_amdgcn_mfma_f32_16x16x32_bf16((a), (b), (c), 0, 0, 0)
#define MACC(acc, a, b) asm volatile("v_mfma_f32_16x16x32_bf16 %0, %1, %2, %0" : "+v"(acc) : "v"(a), "v"(b))
#define MACCN(acc, a, b) asm volatile("s_nop 1\n\tv_mfma_f32_16x16x32_bf16 %0, %1, %2, %0" : "+v"(acc) : "v"(a), "v"(b))
#define NOPS() asm volatile("s_nop 7\n\ts_nop 3")
#define KEEP(x) asm volatile("" : "+v"(x))
#define IC(x) std::integral_constant<int, (x)>{}

template <int I, int N, class F> __device__ __forceinline__ void sfor(F&& f) { if constexpr (I < N) { f(std::integral_constant<int, I>{}); sfor<I + 1, N>(f); } }

__device__ __forceinline__ unsigned cvtpk(float lo, float hi) { f32x2 v = {lo, hi}; bf16x2_t b = __builtin_convertvector(v, bf16x2_t); return __builtin_bit_cast(unsigned, b); }
__device__ __forceinline__ unsigned cvtpk(f32x2 v) { bf16x2_t b = __builtin_convertvector(v, bf16x2_t); return __builtin_bit_cast(unsigned, b); }
__device__ __forceinline__ float dot2(unsigned a, unsigned b, float c) { return __builtin_amdgcn_fdot2_f32_bf16(__builtin_bit_cast(bf16x2_t, a), __builtin_bit_cast(bf16x2_t, b), c, false); }
__device__ __forceinline__ float bflo(unsigned u) { return __uint_as_float(u << 16); }
__device__ __forceinline__ float bfhi(unsigned u) { return __uint_as_float(u & 0xffff0000u); }
__device__ __forceinline__ f32x2 bf2(unsigned u) { return f32x2{bflo(u), bfhi(u)}; }
__device__ __forceinline__ f32x2 lo2(f32x4 v) { return f32x2{v[0], v[1]}; }
__device__ __forceinline__ f32x2 hi2(f32x4 v) { return f32x2{v[2], v[3]}; }
__device__ __forceinline__ bf16x8 ld128(ldsp p) { return *(const LAS bf16x8*)p; }
__device__ __forceinline__ s16x4 ldtr(ldsp p) { return __builtin_bit_cast(s16x4, __builtin_amdgcn_ds_read_tr16_b64_v4i16((LAS v4i16_t*)p)); }
__device__ __forceinline__ bf16x8 cat(s16x4 lo, s16x4 hi) { return __builtin_shufflevector(lo, hi, 0, 1, 2, 3, 4, 5, 6, 7); }
__device__ __forceinline__ int rfl(int x) { return __builtin_amdgcn_readfirstlane(x); }
template <int LADD, int VADD, bool SC1>
__device__ __forceinline__ void dma(unsigned voff, const void* sbase, unsigned lbase) {
  const unsigned va = voff + (unsigned)VADD;
  if constexpr (SC1) asm volatile("s_add_i32 m0, %2, %3\n\ts_nop 0\n\tglobal_load_lds_dwordx4 %0, %1 sc1" ::"v"(va), "s"(sbase), "s"(lbase), "i"(LADD) : "memory", "scc");
  else asm volatile("s_add_i32 m0, %2, %3\n\ts_nop 0\n\tglobal_load_lds_dwordx4 %0, %1" ::"v"(va), "s"(sbase), "s"(lbase), "i"(LADD) : "memory", "scc");
}
template <int LADD>
__device__ __forceinline__ void dma2c_sc1(unsigned voff, const void* sbase, unsigned lbase) {
  asm volatile("s_add_i32 m0, %2, %3\n\ts_nop 0\n\tglobal_load_lds_dwordx4 %0, %1 sc1\n\tglobal_load_lds_dwordx4 %0, %1 offset:1024 sc1" ::"v"(voff), "s"(sbase), "s"(lbase), "i"(LADD) : "memory", "scc");
}
template <int OFF>
__device__ __forceinline__ void ldfrag1(bf16x8& f, unsigned voff, const void* sbase) {
  asm volatile("global_load_dwordx4 %0, %1, %2 offset:%3 sc1" : "=v"(f) : "v"(voff), "s"(sbase), "i"(OFF) : "memory");
}
__device__ __forceinline__ void pin1(bf16x8& f) { asm volatile("" : "+v"(f)); }
template <int N>
__device__ __forceinline__ void ldfrags(bf16x8* f, unsigned voff, const void* sbase) {
  sfor<0, N>([&](auto I) { constexpr int i = decltype(I)::value; ldfrag1<(i & 3) * 1024>(f[i], voff + (unsigned)((i >> 2) * 4096), sbase); });
}
template <int N>
__device__ __forceinline__ void pin(bf16x8* f) { sfor<0, N>([&](auto I) { pin1(f[decltype(I)::value]); }); }
__device__ __forceinline__ void st128_sc1(unsigned voff, const void* sbase, u32x4 v) {
  asm volatile("global_store_dwordx4 %0, %1, %2 sc1\n\ts_nop 1" ::"v"(voff), "v"(v), "s"(sbase) : "memory");
}
__device__ __forceinline__ void st64_sc1(unsigned voff, const void* sbase, u32x2 v) {
  asm volatile("global_store_dwordx2 %0, %1, %2 sc1\n\ts_nop 1" ::"v"(voff), "v"(v), "s"(sbase) : "memory");
}
__device__ __forceinline__ void st32_sc1(const void* sbase, unsigned v) {
  asm volatile("global_store_dword %0, %1, %2 sc1\n\ts_nop 1" ::"v"(0u), "v"(v), "s"(sbase) : "memory");
}
__device__ __forceinline__ float max3(float a, float b, float c) { return __builtin_elementwise_maximum(__builtin_elementwise_maximum(a, b), c); }
__device__ __forceinline__ float red_g_sum(float v) {
  auto a = __builtin_amdgcn_permlane16_swap(__float_as_uint(v), __float_as_uint(v), false, false);
  v = __uint_as_float(a[0]) + __uint_as_float(a[1]);
  auto b = __builtin_amdgcn_permlane32_swap(__float_as_uint(v), __float_as_uint(v), false, false);
  return __uint_as_float(b[0]) + __uint_as_float(b[1]);
}
__device__ __forceinline__ float red_g_max(float v) {
  auto a = __builtin_amdgcn_permlane16_swap(__float_as_uint(v), __float_as_uint(v), false, false);
  v = __builtin_elementwise_maximum(__uint_as_float(a[0]), __uint_as_float(a[1]));
  auto b = __builtin_amdgcn_permlane32_swap(__float_as_uint(v), __float_as_uint(v), false, false);
  return __builtin_elementwise_maximum(__uint_as_float(b[0]), __uint_as_float(b[1]));
}
__device__ __forceinline__ void poll_flags(const unsigned* base, unsigned voff, unsigned epoch) {
#pragma unroll 1
  for (unsigned spins = 0; spins < SPIN_CAP; ++spins) {
    unsigned v;
    asm volatile("global_load_dword %0, %1, %2 sc1\n\ts_waitcnt vmcnt(0)" : "=&v"(v) : "v"(voff), "s"(base) : "memory");
    if (__all(v == epoch)) break;
    __builtin_amdgcn_s_sleep(1);
  }
}

struct Params {
  const char* x; const char* wqkv; const char* wo; const char* w1; const char* b1; const char* w2; const char* b2;
  char* out; char* ws;
};

#define LANEVARS() int tz_ = tid; asm volatile("" : "+v"(tz_)); const int lane = tz_ & 63, l15 = lane & 15, g = lane >> 4, q4 = l15 >> 2, p4 = l15 & 3; \
  const unsigned lane16 = (unsigned)lane * 16u, x8 = (unsigned)(((lane & 7) ^ ((lane >> 3) & 7)) << 4); (void)q4; (void)p4; (void)g; (void)x8; (void)lane16

__global__ void __launch_bounds__(512, 2) fused_block(Params p) {
  extern __shared__ __attribute__((aligned(16))) char smem[];
  ldsp lds = (ldsp)smem;
  unsigned long long pc0; asm volatile("s_getpc_b64 %0" : "=s"(pc0));
  const int tid = threadIdx.x, lane = tid & 63, w = rfl(tid >> 6);
  const int l15 = lane & 15, g = lane >> 4, q4 = l15 >> 2, p4 = l15 & 3;
  const int b = blockIdx.x, r_ = b >> 3;
  const int c = 2 * (b & 7) + (r_ >> 4), j = r_ & 15;
  const int ca = 4 * ((b & 7) >> 1) + (r_ >> 3), ha = 8 * (b & 1) + (r_ & 7);
  const unsigned* ctl = (const unsigned*)(p.ws + WS_CTL);
  unsigned epoch = 0;
  const unsigned ONES = 0x3f803f80u;
  const unsigned lane16 = (unsigned)lane * 16u;
  const unsigned x8 = (unsigned)(((lane & 7) ^ ((lane >> 3) & 7)) << 4);

  constexpr int KIMG = 0, VIMG = 24576, QIMG = 49152;
  {
    const int h = ha;
    constexpr int SLOT = 28672, WOFF = 16384;
    const unsigned iw = (unsigned)(w >> 2);
    unsigned voff[4];
    {
      const unsigned xo = (unsigned)((32 * w + (lane >> 3)) * 896) + x8;
#pragma unroll
      for (int ii = 0; ii < 4; ++ii) {
        const unsigned u = (unsigned)(192 * (w & 3) + 64 * ii + lane), row = (u * 43691u) >> 19, rem = u - 12u * row;
        const unsigned piece = rem >> 2, c16 = rem & 3, hb = (c16 >> 1) ^ ((row >> 3) & 1);
        const unsigned wo_ = (ii < 3) ? row * 2688u + piece * 896u + hb * 32u + (c16 & 1) * 16u : 0u;
        voff[ii] = iw ? wo_ : xo + ii * 7168u;
      }
    }
    const unsigned long long mW = 0ull - (unsigned long long)iw;
    const unsigned long long bx_ = (unsigned long long)(uintptr_t)p.x + (unsigned)(ca * 128 * 896), bw_ = (unsigned long long)(uintptr_t)p.wqkv + (unsigned)(56 * h - 8 * (h & 1));
    const char* gb = (const char*)(uintptr_t)(bx_ + ((bw_ - bx_) & mW));
    const unsigned kstep = 128 + iw * (64 * 2688 - 128), ldst = w * 4096 + iw * (WOFF + (w - 4) * 3072 - w * 4096);
    const unsigned smask = iw - 1u, l3 = iw ? (unsigned)M_FILL : ldst + 3072u;
    auto issue1 = [&](auto SL, auto II) {
      constexpr int sl = decltype(SL)::value, ii = decltype(II)::value;
      if constexpr (ii < 3) dma<sl * SLOT + ii * 1024, 0, false>(voff[ii], gb, ldst);
      else { dma<0, 0, false>(voff[3], gb, l3 + (smask & (unsigned)(sl * SLOT))); gb += kstep; }
    };
    auto issue = [&](auto SL) { issue1(SL, IC(0)); issue1(SL, IC(1)); issue1(SL, IC(2)); issue1(SL, IC(3)); };
    issue(IC(0));
    RAWBAR(); issue(IC(1));
    {
      unsigned ln = (unsigned)(r_ * 8 + w); if (ln >= CODE_LINES) ln = 0;
      const unsigned long long a = (pc0 & ~255ull) + (unsigned long long)(ln * 128u);
      if (lane == 0) asm volatile("s_mov_b32 m0, %1\n\ts_nop 0\n\tglobal_load_lds_dword %0, off" ::"v"(a), "i"(M_FILL) : "memory");
    }

    {
      const unsigned idx = (unsigned)((r_ * 8 + w) * 25 + (lane < 25 ? lane : 24));
      const unsigned wrow = (idx * 43691u) >> 19, wrem = idx - 12u * wrow;
      const unsigned i2 = idx - 3840u, xrow = (i2 * 52429u) >> 18, xln = i2 - 5u * xrow;
      const unsigned long long aw = (unsigned long long)(uintptr_t)p.wqkv + ((128u + wrow) * 2688u + (wrem >> 2) * 896u + (b & 1) * 384u + (wrem & 3) * 128u);
      const unsigned long long ax = (unsigned long long)(uintptr_t)p.x + ((unsigned)(512 * ((b & 7) >> 1)) + xrow) * 896ull + (2u + xln) * 128u;
      const unsigned long long a = (idx < 3840u) ? aw : ax;
      asm volatile("s_mov_b32 m0, %1\n\ts_nop 0\n\tglobal_load_lds_dword %0, off" ::"v"(a), "i"(M_FILL) : "memory");
    }
    const int ptq0 = (b < 224) ? b : 223, ptm = (ptq0 >= 112) ? 2 : 1, ptq = ptq0 - ((ptm == 2) ? 112 : 0);
    const int pts = (ptm == 1) ? ptq >> 3 : ptq >> 1, ptG = (ptm == 1) ? ptq & 7 : ptq & 1;
    auto pt_issue = [&]() {
      const unsigned stride = (ptm == 1) ? 3584u : 896u;
      const char* src = ((ptm == 1) ? p.w1 : p.w2) + (size_t)(32 * pts * stride + ptG * 448);
      const int wq = (w < 7) ? w : 6;
#pragma unroll
      for (int i = 0; i < 2; ++i) { const unsigned u = (unsigned)((2 * wq + i) * 64 + lane), row = (u * 9363u) >> 18, c16 = u - 28u * row;
        dma<M_PT, 0, false>(row * stride + c16 * 16u, src, (unsigned)((2 * wq + i) * 1024)); }
      if (lane == 63) asm volatile("s_mov_b32 m0, %2\n\ts_nop 0\n\tglobal_load_lds_dword %0, %1 sc1" ::"v"(0u), "s"(p.ws + WS_CTL), "i"(M_PF) : "memory");
      if (w == 7) {
        unsigned ln = (unsigned)(b * 13 + (lane < 13 ? lane : 12)); if (ln > 3135u) ln = 3135u;
        unsigned long long a = (unsigned long long)(uintptr_t)p.wo + ln * 128u;
        const unsigned long long ab1 = (unsigned long long)(uintptr_t)p.b1, ab2 = (unsigned long long)(uintptr_t)p.b2;
        a = (lane >= 16 && lane < 44) ? ab1 + (unsigned)(lane - 16) * 128u : a;
        a = (lane >= 44 && lane < 51) ? ab2 + (unsigned)(lane - 44) * 128u : a;
        asm volatile("s_mov_b32 m0, %1\n\ts_nop 0\n\tglobal_load_lds_dword %0, off" ::"v"(a), "i"(M_FILL) : "memory");
      }
    };
    const int tg = w >> 1, ng = w & 1;
    f32x4 acc[3][2], asum = {0, 0, 0, 0}, asq = {0, 0, 0, 0}, acs[3];
#pragma unroll
    for (int e = 0; e < 3; ++e) { acc[e][0] = f32x4{0, 0, 0, 0}; acc[e][1] = f32x4{0, 0, 0, 0}; acs[e] = f32x4{0, 0, 0, 0}; }
    const int xb = (32 * tg + 16 * ng + l15) * 128 + ((g ^ (l15 & 7)) << 4);
    int ab[3];
#pragma unroll
    for (int e = 0; e < 3; ++e) ab[e] = WOFF + (8 * g + q4) * 192 + ((((3 * ng + e) * 32) ^ ((g & 1) << 5))) + 8 * p4;
    bf16x8 xf[2][2];
    s16x4 al[2][3], ah[2][3];
    auto rdx = [&](int st, int buf, int t) { xf[buf][t] = ld128(lds + ((st >> 1) % 5) * SLOT + (xb ^ ((st & 1) << 6) ^ (t * 2048))); };
    auto rdl = [&](int st, int buf, int e) { al[buf][e] = ldtr(lds + ((st >> 1) % 5) * SLOT + ab[e] + (32 * (st & 1)) * 192); };
    auto rdh_ = [&](int st, int buf, int e) { ah[buf][e] = ldtr(lds + ((st >> 1) % 5) * SLOT + ab[e] + (32 * (st & 1) + 4) * 192); };
    auto rdf = [&](int st, int buf) { rdx(st, buf, 0); rdx(st, buf, 1);
#pragma unroll
      for (int e = 0; e < 3; ++e) { rdl(st, buf, e); rdh_(st, buf, e); } };
    bf16x8 onesf = __builtin_bit_cast(bf16x8, (u32x4{ONES, ONES, ONES, ONES}));
    KEEP(onesf); KEEP(asum); KEEP(asq);
#pragma unroll
    for (int e = 0; e < 3; ++e) { KEEP(acc[e][0]); KEEP(acc[e][1]); KEEP(acs[e]); }
    WAITV(6);
    BAR();
    rdf(0, 0);
    sfor<0, 14>([&](auto ST) {
      constexpr int st = decltype(ST)::value, kc = st >> 1, cb = st & 1;
      if constexpr ((st & 1) && st < 13) {
        if constexpr (kc == 0) WAITV(6); else if constexpr (kc == 1) WAITV(9); else if constexpr (kc <= 3) WAITV(6); else if constexpr (kc == 4) WAITV(4); else WAITV(0);
        BARN();
      }
      if constexpr (st == 2) pt_issue();
      constexpr int nb = (st + 1) & 1; constexpr bool more = st < 13;
      const bf16x8 a0 = cat(al[cb][0], ah[cb][0]), a1 = cat(al[cb][1], ah[cb][1]), a2 = cat(al[cb][2], ah[cb][2]);
      SCHED();
      MACC(acc[0][0], a0, xf[cb][0]); if constexpr (more) rdx(st + 1, nb, 0);
      MACC(acc[0][1], a0, xf[cb][1]); if constexpr (more) rdl(st + 1, nb, 0);
      MACC(asum, onesf, xf[cb][0]); if constexpr (more) rdh_(st + 1, nb, 0);
      MACC(acs[0], a0, onesf);
      SCHED();
      if constexpr (st < 2) { issue1(IC(st + 2), IC(0)); issue1(IC(st + 2), IC(1)); }
      else if constexpr (kc + 3 <= 6) issue1(IC((kc + 3) % 5), IC(2 * (st & 1)));
      SCHED();
      MACC(acc[1][0], a1, xf[cb][0]); if constexpr (more) rdx(st + 1, nb, 1);
      MACC(acc[1][1], a1, xf[cb][1]); if constexpr (more) rdl(st + 1, nb, 1);
      MACC(asq, xf[cb][0], xf[cb][0]); if constexpr (more) rdh_(st + 1, nb, 1);
      MACC(acs[1], a1, onesf);
      SCHED();
      if constexpr (st < 2) { issue1(IC(st + 2), IC(2)); issue1(IC(st + 2), IC(3)); }
      else if constexpr (kc + 3 <= 6) issue1(IC((kc + 3) % 5), IC(2 * (st & 1) + 1));
      SCHED();
      MACC(acc[2][0], a2, xf[cb][0]); if constexpr (more) rdl(st + 1, nb, 2);
      MACC(acc[2][1], a2, xf[cb][1]); if constexpr (more) rdh_(st + 1, nb, 2);
      MACC(acs[2], a2, onesf);
      SCHED();
    });
    NOPS(); KEEP(asum); KEEP(asq);
#pragma unroll
    for (int e = 0; e < 3; ++e) { KEEP(acc[e][0]); KEEP(acc[e][1]); KEEP(acs[e]); }
    epoch = (unsigned)rfl((int)*(LAS unsigned*)(lds + M_PF + 252)) + 1u;
    {
      const unsigned ptb = (ptm == 1) ? WT_W1 : WT_W2, ptn = (ptm == 1) ? 14u : 56u;
      ldsp src = lds + M_PT + (4 * g + q4) * 448 + 8 * p4;
#pragma unroll
      for (int i = 0; i < 2; ++i) {
        const int t = (w + 8 * i < 14) ? w + 8 * i : w;
        const bf16x8 f = cat(ldtr(src + t * 32), ldtr(src + 16 * 448 + t * 32));
        st128_sc1(lane16 + ptb + ((unsigned)(14 * ptG + t) * ptn + (unsigned)pts) * 1024u, p.ws + WS_WT, __builtin_bit_cast(u32x4, f));
      }
    }
    {
      const float d01 = (p4 & 1) ? asq[1] : asq[0], d23 = (p4 & 1) ? asq[3] : asq[2], dq = (p4 & 2) ? d23 : d01;
      float mean = asum[0] * (1.f / 448.f), dq2 = dq;
      KEEP(mean); KEEP(dq2);
      if (g == q4) *(LAS f32x2*)(lds + M_LN + (32 * tg + 16 * ng + l15) * 8) = f32x2{mean, rsqrtf(fmaxf(dq2 * (1.f / 448.f) - mean * mean, 0.f) + 1e-5f)};
    }
    BAR();
    float mu[2], rs[2];
#pragma unroll
    for (int t = 0; t < 2; ++t) { const f32x2 st_ = *(LAS f32x2*)(lds + M_LN + (32 * tg + 16 * (t ^ ng) + l15) * 8); mu[t] = st_[0]; rs[t] = st_[1]; }
    const bool pad0 = (h & 1) && g == 0, pad1 = !(h & 1) && g == 3;
    const int rsw = ((l15 >> 1) & 3) << 4, rowb = (32 * tg + l15) * 64;
    auto epi = [&](auto NG) {
      constexpr int ngc = decltype(NG)::value;
      sfor<0, 3>([&](auto E) {
        constexpr int e = decltype(E)::value, nt = 3 * ngc + e, which = nt >> 1, par = nt & 1;
        constexpr int ibase = (which == 0) ? QIMG : (which == 1) ? KIMG + 16384 : VIMG + 16384;
        const int off = (which == 2) ? ((((2 * par + (g >> 1)) << 4) ^ rsw) + (g & 1) * 8) : (((g << 4) ^ rsw) + par * 8);
        const bool pad = par ? pad1 : pad0;
#pragma unroll
        for (int t = 0; t < 2; ++t) {
          const float sc = (which == 0) ? rs[t] * 0.27264657f : rs[t], nm = -mu[t] * sc;
          f32x2 sc2 = {sc, sc}, nm2 = {nm, nm};
          KEEP(sc2); KEEP(nm2);
          const f32x2 v0 = lo2(acc[e][t]) * sc2 + lo2(acs[e]) * nm2, v1 = hi2(acc[e][t]) * sc2 + hi2(acs[e]) * nm2;
          u32x2 o; o.x = cvtpk(v0); o.y = cvtpk(v1);
          o.x = pad ? ((which == 2) ? 0x00003f80u : 0u) : o.x; o.y = pad ? 0u : o.y;
          *(LAS u32x2*)(lds + ibase + (t ^ ngc) * 1024 + rowb + off) = o;
        }
      });
    };
    if (ng) epi(IC(1)); else epi(IC(0));
    if (ca < 2) {
      const u32x4 z = {0, 0, 0, 0};
#pragma unroll
      for (int i = 0; i < 2; ++i) { *(LAS u32x4*)(lds + KIMG + tid * 16 + i * 8192) = z; *(LAS u32x4*)(lds + VIMG + tid * 16 + i * 8192) = z; }
    }
    BAR();
    {
      const char* blob = p.ws + WS_KV + (size_t)(ca * 16 + h) * 16384;
      const u32x4 v0 = *(LAS u32x4*)(lds + KIMG + 16384 + tid * 16), v1 = *(LAS u32x4*)(lds + VIMG + 16384 + tid * 16);
      st128_sc1(tid * 16, blob, v0); st128_sc1(8192 + tid * 16, blob, v1);
    }
    WAITV(0);
    BAR();
    if (tid == 0) { st32_sc1(ctl + FLAG_IDX(0, ca, h), epoch); st32_sc1(ctl + FLAG_IDX(4, b >> 4, b & 15), epoch); }
  }
  const int ns = j & 3, col4 = 128 * ns - (((ns >> 1) & ns) << 5), ngr = 4 - (ns >> 1);
  const int pr = w & 3, wh = w >> 2;
  const int prc = (pr < ngr) ? pr : 0;
  const int th5 = j >> 3, cs5 = j & 7, col0 = 64 * cs5 - 16 * ((cs5 >> 2) * (cs5 & 3)), nblk = 4 - (cs5 >> 2);
  const int ng5 = w & 1, kq = w >> 1;
  const char* wt = p.ws + WS_WT;
  bf16x8 wr[32];
  const int T1 = (448 * (j >> 2) + col4) / 16;
  const unsigned w1o = WT_W1 + (unsigned)(T1 + 2 * prc) * 14u * 1024u;
  const unsigned wro = lane16 + w1o;
  constexpr int WI = 32768;
  unsigned wb0, wb1;
  { const int rin = lane >> 4, pos = lane & 15;
    int blk = (pos >> 1) ^ rin; if (blk >= 2 * ngr) blk -= 2; wb0 = (unsigned)(blk * 32 + (pos & 1) * 16);
    blk = (pos >> 1) ^ (4 + rin); if (blk >= 2 * ngr) blk -= 2; wb1 = (unsigned)(blk * 32 + (pos & 1) * 16); }
  const char* wob = p.wo + col4 * 2;
  {
    if (w < 6) {
      const unsigned rb = (unsigned)(((lane >> 4) + 4 * (24 + w)) * 896) + ((w & 1) ? wb1 : wb0);
      sfor<0, 14>([&](auto I) { constexpr int i = decltype(I)::value; dma<i * 6144, i * 24 * 896, false>(rb, wob, (unsigned)(WI + (24 + w) * 1024)); });
    }
    int pos = lane & 15; if (pos >= 4 * ngr) pos = 4 * ngr - 1;
    int xch = (lane & 15) ^ (((lane >> 4) + 4 * w) & 15); if (xch >= 4 * ngr) xch = 0;
    dma<M_XT, 0, false>((unsigned)(((lane >> 4) + 4 * w) * 896 + xch * 16), p.x + (size_t)(128 * c + 32 * (j >> 2)) * 896 + col4 * 2, (unsigned)(w * 1024));
    if (w == 5 && lane < 16) dma<M_B1, 0, false>((unsigned)(pos * 16), p.b1 + (448 * (j >> 2) + col4) * 2, 0u);
    if (w == 4 && lane < 8) dma<M_B2, 0, false>((unsigned)((lane < 2 * nblk ? lane : 0) * 16), p.b2 + col0 * 2, 0u);
  }
  {
    const int h = ha;
    const int sw = (l15 >> 1) & 3;
    if (w == 7 && ca >= 1) poll_flags(ctl, (unsigned)(FLAG_IDX(0, (ca >= 2 && (lane & 1)) ? ca - 2 : ca - 1, h) * 4), epoch);
    BAR();
    {
      const int tl = w >> 2, src = ca - 2 + tl, pc = (w & 3) * 2048;
      if (src >= 0) {
        const char* blob = p.ws + WS_KV + (size_t)(src * 16 + h) * 16384 + pc;
        dma2c_sc1<KIMG>(lane16, blob, (unsigned)(tl * 8192 + pc)); dma2c_sc1<VIMG>(lane16, blob + 8192, (unsigned)(tl * 8192 + pc));
      }
    }
    const bf16x8 qf = ld128(lds + QIMG + (16 * w + l15) * 64 + ((g ^ sw) << 4));
    WAITV(0);
    BAR();
    unsigned tfo, tfl;
    { const int sl = (l15 < 14) ? l15 : 13, hi = (lane >> 4) & 1;
      const int g1 = 8 * sl + (T1 + (hi ? 2 * ngr - 1 : 0)) / 14, g2 = 112 + 2 * (14 * kq + sl) + (col0 / 16 + (hi ? nblk - 1 : 0)) / 14, gi = (lane >= 32) ? g2 : g1;
      tfo = (unsigned)(FLAG_IDX(4, gi >> 4, gi & 15) * 4); }
    asm volatile("global_load_dword %0, %1, %2 sc1" : "=v"(tfl) : "v"(tfo), "s"(ctl) : "memory");
    f32x4 sc[17];
    const float NEG = -__builtin_inff();
    ldsp kb = lds + KIMG + (16 * w + l15) * 64 + ((g ^ sw) << 4);
    {
      bf16x8 kf[2][6];
#pragma unroll
      for (int u = 0; u < 6; ++u) kf[0][u] = ld128(kb + u * 1024);
#pragma unroll
      for (int bt = 0; bt < 3; ++bt) {
        if (bt < 2) {
#pragma unroll
          for (int u = 0; u < 6; ++u) if (6 * (bt + 1) + u < 17) kf[(bt + 1) & 1][u] = ld128(kb + (6 * (bt + 1) + u) * 1024);
        }
        SCHED();
#pragma unroll
        for (int u = 0; u < 6; ++u) if (6 * bt + u < 17) sc[6 * bt + u] = MFMA(kf[bt & 1][u], qf, (f32x4{0, 0, 0, 0}));
        SCHED();
      }
    }
    asm volatile("s_waitcnt vmcnt(0)" : "+v"(tfl)::"memory");
    if (!__all(tfl == epoch)) poll_flags(ctl, tfo, epoch);
    if (w >= 4) ldfrags<7>(wr, wro, wt);
    const int vsw = (2 * g + (q4 >> 1)) & 3;
    ldsp vb0 = lds + VIMG + (16 * w + 4 * g + q4) * 64 + (((p4 >> 1) ^ vsw) << 4) + (p4 & 1) * 8;
    ldsp vb1 = lds + VIMG + (16 * w + 4 * g + q4) * 64 + (((2 + (p4 >> 1)) ^ vsw) << 4) + (p4 & 1) * 8;
    bf16x8 vf[2][3][2];
    auto rdv = [&](int bt) {
#pragma unroll
      for (int i = 0; i < 3; ++i) { const int uu = 3 * bt + i, ua = 2 * uu, ub = (uu < 8) ? 2 * uu + 1 : 15;
        vf[bt & 1][i][0] = cat(ldtr(vb0 + ua * 1024), ldtr(vb0 + ub * 1024)); vf[bt & 1][i][1] = cat(ldtr(vb1 + ua * 1024), ldtr(vb1 + ub * 1024)); }
    };
    rdv(0);
    SCHED();
    if (ca < 2) {
      const int tmin = (2 - ca) * 8;
#pragma unroll
      for (int u = 0; u < 16; ++u) if (w + u < tmin) sc[u] = f32x4{NEG, NEG, NEG, NEG};
    }
#pragma unroll
    for (int r = 0; r < 4; ++r) { sc[0][r] = (4 * g + r <= l15) ? NEG : sc[0][r]; sc[16][r] = (4 * g + r > l15) ? NEG : sc[16][r]; }
    float m = max3(sc[0][0], sc[0][1], sc[0][2]), m2 = max3(sc[0][3], sc[1][0], sc[1][1]);
    m = max3(m, sc[1][2], sc[1][3]);
#pragma unroll
    for (int u = 2; u < 17; ++u) { if (u & 1) m = max3(max3(m, sc[u][0], sc[u][1]), sc[u][2], sc[u][3]); else m2 = max3(max3(m2, sc[u][0], sc[u][1]), sc[u][2], sc[u][3]); }
    m = max3(m, m2, m2);
    m = red_g_max(m);
    SCHED();
    if (w >= 4) ldfrags<7>(wr + 7, wro + 7 * 1024, wt);
    unsigned pk[17][2];
    f32x2 mm = {-m, -m};
    KEEP(mm);
    f32x4 oa[2] = {f32x4{0, 0, 0, 0}, f32x4{0, 0, 0, 0}};
    KEEP(oa[0]); KEEP(oa[1]);
#pragma unroll
    for (int bt = 0; bt < 3; ++bt) {
#pragma unroll
      for (int u = 6 * bt; u < 6 * bt + 6 && u < 17; ++u) {
        const f32x2 d0 = lo2(sc[u]) + mm, d1 = hi2(sc[u]) + mm;
        pk[u][0] = cvtpk(__builtin_amdgcn_exp2f(d0[0]), __builtin_amdgcn_exp2f(d0[1]));
        pk[u][1] = cvtpk(__builtin_amdgcn_exp2f(d1[0]), __builtin_amdgcn_exp2f(d1[1]));
      }
      if (bt < 2) rdv(bt + 1);
      if (bt < 2 && w >= 4) ldfrags<7>(wr + 14 + 7 * bt, wro + (14 + 7 * bt) * 1024, wt);
      SCHED();
#pragma unroll
      for (int i = 0; i < 3; ++i) {
        const int uu = 3 * bt + i;
        u32x4 pu; pu.x = pk[2 * uu][0]; pu.y = pk[2 * uu][1];
        if (uu < 8) { pu.z = pk[2 * uu + 1][0]; pu.w = pk[2 * uu + 1][1]; } else { pu.z = 0; pu.w = 0; }
        const bf16x8 pf = __builtin_bit_cast(bf16x8, pu);
        MACCN(oa[0], vf[bt & 1][i][0], pf); MACC(oa[1], vf[bt & 1][i][1], pf);
      }
      SCHED();
    }
    NOPS(); KEEP(oa[0]); KEEP(oa[1]);
    int tz2 = tid; asm volatile("" : "+v"(tz2));
    const int l15b = tz2 & 15, gb = (tz2 & 63) >> 4, swb = (l15b >> 1) & 3;
    const float lsel = (h & 1) ? oa[0][0] : oa[1][0];
    const float l = __uint_as_float((unsigned)__builtin_amdgcn_ds_bpermute((((h & 1) ? 0 : 48) + l15b) * 4, (int)__float_as_uint(lsel)));
    const float inv1 = __builtin_amdgcn_rcpf(l);
    f32x2 inv = {inv1, inv1};
    KEEP(inv);
    u32x4 ov;
    ov.x = cvtpk(lo2(oa[0]) * inv); ov.y = cvtpk(hi2(oa[0]) * inv); ov.z = cvtpk(lo2(oa[1]) * inv); ov.w = cvtpk(hi2(oa[1]) * inv);
    { const bool z0 = (h & 1) && gb == 0, z1 = !(h & 1) && gb == 3; ov.x = z0 ? 0u : ov.x; ov.y = z0 ? 0u : ov.y; ov.z = z1 ? 0u : ov.z; ov.w = z1 ? 0u : ov.w; }
    st128_sc1((unsigned)((16 * w + l15b) * 64 + ((gb ^ swb) << 4)), p.ws + WS_O + (size_t)(ca * 16 + h) * 8192, ov);
    BAR();
    {
      const unsigned rb = (unsigned)(((lane >> 4) + 12 * w) * 896), va = rb + ((w & 1) ? wb1 : wb0), vb = rb + ((w & 1) ? wb0 : wb1), lb = (unsigned)(WI + 3 * w * 1024);
      dma<0, 0, false>(va, wob, lb); dma<1024, 4 * 896, false>(vb, wob, lb); dma<2048, 8 * 896, false>(va, wob, lb);
      const int ins = (w < 4) ? 108 + w : 3 * w + 2;
      dma<0, 0, false>((unsigned)(((lane >> 4) + 4 * ins) * 896) + ((ins & 1) ? wb1 : wb0), wob, (unsigned)(WI + ins * 1024));
    }
    WAITV(4);
    BAR();
    if (tid == 0) st32_sc1(ctl + FLAG_IDX(1, ca, h), epoch);
  }
  {
    LANEVARS();
    constexpr int OT = 0;
    const int tq = j >> 2;
    WAITV(0);
    BAR();
    if (w < 4) {
      const int rx = 4 * (g & 1) + q4;
      ldsp ab0 = lds + WI + (4 * g + q4) * 256 + (((2 * prc) ^ rx) << 5) + 8 * p4;
      ldsp ab1 = lds + WI + (4 * g + q4) * 256 + (((2 * prc + 1) ^ rx) << 5) + 8 * p4;
      ldsp ab0h = ab0 + 65536, ab1h = ab1 + 65536;
#pragma unroll
      for (int s = 0; s < 16; ++s) {
        const int rb = (28 * s - 4 * (s & 1)) * 256;
        if (rb + 4096 < 65536) { wr[s] = cat(ldtr(ab0 + rb), ldtr(ab0 + rb + 4096)); wr[16 + s] = cat(ldtr(ab1 + rb), ldtr(ab1 + rb + 4096)); }
        else { wr[s] = cat(ldtr(ab0h + (rb - 65536)), ldtr(ab0h + (rb - 65536 + 4096))); wr[16 + s] = cat(ldtr(ab1h + (rb - 65536)), ldtr(ab1h + (rb - 65536 + 4096))); }
      }
    } else {
      __builtin_amdgcn_s_setprio(3);
      poll_flags(ctl, (unsigned)(FLAG_IDX(1, c, l15) * 4), epoch);
      const char* ob_ = p.ws + WS_O + (size_t)(c * 16 + 4 * (w - 4)) * 8192 + tq * 2048;
      const unsigned lb = (unsigned)(OT + (w - 4) * 8192);
      dma2c_sc1<0>(lane16, ob_, lb); dma2c_sc1<2048>(lane16 + 8192, ob_, lb); dma2c_sc1<4096>(lane16 + 16384, ob_, lb); dma2c_sc1<6144>(lane16 + 24576, ob_, lb);
      __builtin_amdgcn_s_setprio(0);
    }
    WAITV(0);
    BAR();
    if (w < 4) {
      f32x4 a0[2], a1[2];
#pragma unroll
      for (int t = 0; t < 2; ++t) { a0[t] = f32x4{0, 0, 0, 0}; a1[t] = f32x4{0, 0, 0, 0}; }
      ldsp ob = lds + OT + l15 * 64 + ((g ^ ((l15 >> 1) & 3)) << 4);
      u32x2 xr[2][2];
#pragma unroll
      for (int t = 0; t < 2; ++t) { ldsp xp = lds + M_XT + (16 * t + l15) * 256 + (g & 1) * 8;
        xr[t][0] = *(LAS u32x2*)(xp + (((4 * prc + (g >> 1)) ^ l15) << 4)); xr[t][1] = *(LAS u32x2*)(xp + (((4 * prc + 2 + (g >> 1)) ^ l15) << 4)); }
#pragma unroll
      for (int t = 0; t < 2; ++t) { KEEP(a0[t]); KEEP(a1[t]); }
      bf16x8 of[4][2];
#pragma unroll
      for (int s = 0; s < 3; ++s) { of[s][0] = ld128(ob + s * 2048); of[s][1] = ld128(ob + s * 2048 + 1024); }
#pragma unroll
      for (int s = 0; s < 16; ++s) {
        if (s + 3 < 16) { of[(s + 3) & 3][0] = ld128(ob + (s + 3) * 2048); of[(s + 3) & 3][1] = ld128(ob + (s + 3) * 2048 + 1024); }
        SCHED();
#pragma unroll
        for (int t = 0; t < 2; ++t) { MACC(a0[t], wr[s], of[s & 3][t]); MACC(a1[t], wr[16 + s], of[s & 3][t]); }
        SCHED();
      }
      NOPS();
#pragma unroll
      for (int t = 0; t < 2; ++t) { KEEP(a0[t]); KEEP(a1[t]); }
      const unsigned ho = (unsigned)((128 * c + 32 * tq + l15) * 896 + (col4 / 32 + pr) * 64 + g * 16);
#pragma unroll
      for (int t = 0; t < 2; ++t) {
        u32x4 hv;
        hv.x = cvtpk(lo2(a0[t]) + bf2(xr[t][0].x)); hv.y = cvtpk(hi2(a0[t]) + bf2(xr[t][0].y));
        hv.z = cvtpk(lo2(a1[t]) + bf2(xr[t][1].x)); hv.w = cvtpk(hi2(a1[t]) + bf2(xr[t][1].y));
        if (pr < ngr) st128_sc1(ho + t * 16 * 896, p.ws + WS_H, hv);
        float s1 = 0.f, s2 = 0.f;
#pragma unroll
        for (int i = 0; i < 4; ++i) { s1 = dot2(hv[i], ONES, s1); s2 = dot2(hv[i], hv[i], s2); }
        s1 = red_g_sum(s1); s2 = red_g_sum(s2);
        if (pr >= ngr) { s1 = 0.f; s2 = 0.f; }
        if (g == 0) ((LAS f32x2*)(lds + M_PART))[(16 * t + l15) * 4 + pr] = f32x2{s1, s2};
      }
    } else {
      pin<28>(wr);
#pragma unroll
      for (int i = 0; i < 28; ++i) *(LAS bf16x8*)(lds + 32768 + (w - 4) * 28672 + i * 1024 + lane * 16) = wr[i];
      if (b == 0 && w == 5) {
#pragma unroll 1
        for (int i = 0; i < 4; ++i) poll_flags(ctl, (unsigned)(FLAG_IDX(4, 4 * i + g, l15) * 4), epoch);
        if (lane == 0) st32_sc1(ctl, epoch);
      }
    }
    BAR();
    if (tid < 32) {
      const f32x4 u0 = *(LAS f32x4*)(lds + M_PART + tid * 32), u1 = *(LAS f32x4*)(lds + M_PART + tid * 32 + 16);
      st64_sc1((unsigned)((128 * c + 32 * tq + tid) * 32 + ns * 8), p.ws + WS_ST,
               u32x2{__float_as_uint((u0[0] + u0[2]) + (u1[0] + u1[2])), __float_as_uint((u0[1] + u0[3]) + (u1[1] + u1[3]))});
    }
    if (w < 4) {
#pragma unroll
      for (int i = 0; i < 28; ++i) wr[i] = ld128(lds + 32768 + w * 28672 + i * 1024 + lane * 16);
    }
    if (w < 4) WAITV(0);
    BAR();
    if (tid == 0) st32_sc1(ctl + FLAG_IDX(2, c, j), epoch);
  }
  {
    LANEVARS();
    const int f0_ = 448 * (j >> 2) + col4, th = wh;
    f32x4 c0 = {0, 0, 0, 0}, c1 = {0, 0, 0, 0};
    pin<28>(wr);
    bf16x8 onesf = __builtin_bit_cast(bf16x8, (u32x4{ONES, ONES, ONES, ONES}));
    KEEP(onesf); KEEP(c0); KEEP(c1);
    asm volatile("s_nop 1");
#pragma unroll
    for (int s = 0; s < 14; ++s) { MACC(c0, wr[s], onesf); MACC(c1, wr[14 + s], onesf); }
    NOPS(); KEEP(c0); KEEP(c1);
    SCHED();
    if (w == 7) poll_flags(ctl, (unsigned)(FLAG_IDX(2, c, l15) * 4), epoch);
    BAR();
    const unsigned vo = (unsigned)((16 * w + (lane >> 3)) * 896) + x8, wl2k = (unsigned)(w * 2048);
    const char* hb = p.ws + WS_H + (size_t)c * 128 * 896;
    auto issue4 = [&](auto KC) { constexpr int kc = decltype(KC)::value; dma<kc * 16384, kc * 128, true>(vo, hb, wl2k); dma<kc * 16384 + 1024, kc * 128 + 8 * 896, true>(vo, hb, wl2k); };
    if (lane < 32) dma<M_TILE, 0, true>(lane16, p.ws + WS_ST + (size_t)(128 * c + 16 * w) * 32, (unsigned)(w * 512));
    issue4(IC(0)); RAWBAR(); issue4(IC(1));
    f32x4 acc[2][4];
#pragma unroll
    for (int t = 0; t < 4; ++t) { acc[0][t] = f32x4{0, 0, 0, 0}; acc[1][t] = f32x4{0, 0, 0, 0}; }
    const int xb = (64 * th + l15) * 128 + ((g ^ (l15 & 7)) << 4);
    bf16x8 xf[2][4];
    auto rd1 = [&](int st, int buf, int t) { xf[buf][t] = ld128(lds + (st >> 1) * 16384 + (xb ^ ((st & 1) << 6)) + t * 2048); };
    auto rdf = [&](int st, int buf) { rd1(st, buf, 0); rd1(st, buf, 1); rd1(st, buf, 2); rd1(st, buf, 3); };
#pragma unroll
    for (int t = 0; t < 4; ++t) { KEEP(acc[0][t]); KEEP(acc[1][t]); }
    bf16x8 bf[2][14];
    const unsigned w2o0 = WT_W2 + ((unsigned)(col0 / 16 + 2 * ng5) * 56u + 14u * kq) * 1024u, w2o1 = (2 * ng5 + 1 < nblk) ? w2o0 + 56u * 1024u : w2o0;
    WAITV(2); BAR();
    rdf(0, 0);
    sfor<0, 14>([&](auto ST) {
      constexpr int st = decltype(ST)::value;
      if constexpr (st == 1) WAITV(2); if constexpr (st == 3) WAITV(4); if constexpr (st == 5) WAITV(6); if constexpr (st == 7) WAITV(8); if constexpr (st == 9) WAITV(10); if constexpr (st == 11) WAITV(12);
      if constexpr ((st & 1) && st < 13) BARN();
      if constexpr (st == 2) {
        if (tid < 128) {
          const f32x4 u0 = *(LAS f32x4*)(lds + M_TILE + tid * 32), u1 = *(LAS f32x4*)(lds + M_TILE + tid * 32 + 16);
          float mean = ((u0[0] + u0[2]) + (u1[0] + u1[2])) * (1.f / 448.f), sq = (u0[1] + u0[3]) + (u1[1] + u1[3]);
          KEEP(mean); KEEP(sq);
          const float rstd = rsqrtf(fmaxf(sq * (1.f / 448.f) - mean * mean, 0.f) + 1e-5f);
          *(LAS f32x2*)(lds + M_LN + tid * 8) = f32x2{rstd, -mean * rstd};
        }
      }
      SCHED();
#pragma unroll
      for (int t = 0; t < 2; ++t) { MACC(acc[0][t], wr[st], xf[st & 1][t]); if constexpr (st < 13) rd1(st + 1, (st + 1) & 1, t); MACC(acc[1][t], wr[14 + st], xf[st & 1][t]); }
      SCHED();
      if constexpr (st <= 4) issue4(IC(st + 2));
      if constexpr (st >= 5) { constexpr int s = st - 5; ldfrags<1>(&bf[0][s], lane16 + w2o0 + s * 1024, wt); ldfrags<1>(&bf[1][s], lane16 + w2o1 + s * 1024, wt); }
      SCHED();
#pragma unroll
      for (int t = 2; t < 4; ++t) { MACC(acc[0][t], wr[st], xf[st & 1][t]); if constexpr (st < 13) rd1(st + 1, (st + 1) & 1, t); MACC(acc[1][t], wr[14 + st], xf[st & 1][t]); }
      SCHED();
    });
    NOPS();
#pragma unroll
    for (int t = 0; t < 4; ++t) { KEEP(acc[0][t]); KEEP(acc[1][t]); }
    {
      const u32x2 bb0 = *(LAS u32x2*)(lds + M_B1 + (32 * prc + 4 * g) * 2), bb1 = *(LAS u32x2*)(lds + M_B1 + (32 * prc + 4 * g) * 2 + 32);
      const f32x2 b00 = bf2(bb0.x), b01 = bf2(bb0.y), b10 = bf2(bb1.x), b11 = bf2(bb1.y);
      const unsigned ho = (unsigned)((128 * c + 64 * th + l15) * 3584 + (f0_ / 32 + pr) * 64 + g * 16);
      f32x2 ln[4];
#pragma unroll
      for (int t = 0; t < 4; ++t) ln[t] = *(LAS f32x2*)(lds + M_LN + (64 * th + 16 * t + l15) * 8);
#pragma unroll
      for (int t = 0; t < 4; ++t) {
        const float a = ln[t][0], nm = ln[t][1];
        f32x2 aa = {a, a}, nn = {nm, nm};
        KEEP(aa); KEEP(nn);
        f32x2 z[4] = {lo2(acc[0][t]) * aa + (lo2(c0) * nn + b00), hi2(acc[0][t]) * aa + (hi2(c0) * nn + b01), lo2(acc[1][t]) * aa + (lo2(c1) * nn + b10), hi2(acc[1][t]) * aa + (hi2(c1) * nn + b11)};
        u32x4 hv;
#pragma unroll
        for (int i = 0; i < 4; ++i) {
          f32x2 r = {__builtin_elementwise_maximum(z[i][0], 0.f), __builtin_elementwise_maximum(z[i][1], 0.f)};
          hv[i] = cvtpk(r * r);
        }
        if (pr < ngr) st128_sc1(ho + t * 16 * 3584, p.ws + WS_H1, hv);
      }
    }
    WAITV(0);
    BAR();
    if (tid == 0) st32_sc1(ctl + FLAG_IDX(3, c, j), epoch);
    if (w != 7) {
      ldfrags<5>(&bf[0][9], lane16 + w2o0 + 9 * 1024, wt); ldfrags<5>(&bf[1][9], lane16 + w2o1 + 9 * 1024, wt);
    }
    if (w >= 5 && w < 7) {
      const int m0_ = col0 >> 5;
      const char* hr = p.ws + WS_H + (size_t)(128 * c + 64 * th5) * 896;
      sfor<0, 6>([&](auto I) { constexpr int i = decltype(I)::value;
        const unsigned u = (unsigned)((w - 5) * 384 + i * 64 + lane), r0 = (u * 43691u) >> 19, c0_ = u - 12u * r0; unsigned cc0 = m0_ * 4 + c0_; if (cc0 > 55) cc0 = 55;
        dma<M_TILE + i * 1024, 0, true>(r0 * 896 + cc0 * 16, hr, (unsigned)((w - 5) * 6144)); });
    }
    const unsigned vo5 = (unsigned)((32 * ng5 + (lane >> 3)) * 3584 + kq * 896) + x8, wl4k = (unsigned)(w * 4096);
    const char* hb5 = p.ws + WS_H1 + (size_t)(128 * c + 64 * th5) * 3584;
    auto issue5 = [&](auto I) {
      constexpr int i = decltype(I)::value, sa = (i & 3) * 32768;
      sfor<0, 4>([&](auto II) { constexpr int ii = decltype(II)::value; dma<sa + ii * 1024, i * 128 + ii * 8 * 3584, true>(vo5, hb5, wl4k); });
    };
    if (w == 7) {
      poll_flags(ctl, (unsigned)(FLAG_IDX(3, c, l15) * 4), epoch);
    }
    BAR();
    issue5(IC(0));
    if (w == 7) {
      ldfrags<5>(&bf[0][9], lane16 + w2o0 + 9 * 1024, wt); ldfrags<5>(&bf[1][9], lane16 + w2o1 + 9 * 1024, wt);
    }
    f32x4 ac5[2][4];
#pragma unroll
    for (int t = 0; t < 4; ++t) { ac5[0][t] = f32x4{0, 0, 0, 0}; ac5[1][t] = f32x4{0, 0, 0, 0}; }
    int yb[4];
#pragma unroll
    for (int t = 0; t < 4; ++t) yb[t] = kq * 8192 + (16 * ((t + kq) & 3) + l15) * 128 + ((g ^ (l15 & 7)) << 4);
    bf16x8 yf[2][4];
    auto rz1 = [&](int st, int buf, int t) { yf[buf][t] = ld128(lds + ((st >> 1) & 3) * 32768 + (yb[t] ^ ((st & 1) << 6))); };
    auto rdy = [&](int st, int buf) { rz1(st, buf, 0); rz1(st, buf, 1); rz1(st, buf, 2); rz1(st, buf, 3); };
#pragma unroll
    for (int t = 0; t < 4; ++t) { KEEP(ac5[0][t]); KEEP(ac5[1][t]); }
    WAITV(0);
    pin<14>(bf[0]); pin<14>(bf[1]);
    BAR();
    rdy(0, 0);
    sfor<0, 14>([&](auto ST) {
      constexpr int st = decltype(ST)::value;
      if constexpr ((st & 1) && st < 13) {
        if constexpr (st < 11) WAITV(4); else WAITV(0);
        BARN();
      }
      SCHED();
#pragma unroll
      for (int t = 0; t < 2; ++t) { MACC(ac5[0][t], bf[0][st], yf[st & 1][t]); if constexpr (st < 13) rz1(st + 1, (st + 1) & 1, t); MACC(ac5[1][t], bf[1][st], yf[st & 1][t]); }
      SCHED();
      if constexpr (st == 0) { issue5(IC(1)); issue5(IC(2)); }
      if constexpr ((st & 1) && st <= 7) issue5(IC((st + 5) / 2));
      SCHED();
#pragma unroll
      for (int t = 2; t < 4; ++t) { MACC(ac5[0][t], bf[0][st], yf[st & 1][t]); if constexpr (st < 13) rz1(st + 1, (st + 1) & 1, t); MACC(ac5[1][t], bf[1][st], yf[st & 1][t]); }
      SCHED();
    });
    NOPS();
#pragma unroll
    for (int t = 0; t < 4; ++t) { KEEP(ac5[0][t]); KEEP(ac5[1][t]); }
    auto paddr = [&](int blk) { return (blk < 16) ? blk * 2048 : 98304 + (blk - 16) * 2048; };
#pragma unroll
    for (int t = 1; t < 4; ++t) {
      ldsp d = lds + paddr((((t + kq) & 3) * 3 + (t - 1)) * 2 + ng5) + lane * 16;
      *(LAS f32x4*)d = ac5[0][t]; *(LAS f32x4*)(d + 1024) = ac5[1][t];
    }
    const int tokl = 16 * kq + l15;
    const int t0 = col0 / 16 + 2 * ng5, t1 = (2 * ng5 + 1 < nblk) ? t0 + 1 : t0, m0_ = col0 >> 5;
    const u32x2 hr0 = *(LAS u32x2*)(lds + M_TILE + tokl * 192 + ((t0 >> 1) - m0_) * 64 + g * 16 + (t0 & 1) * 8);
    const u32x2 hr1 = *(LAS u32x2*)(lds + M_TILE + tokl * 192 + ((t1 >> 1) - m0_) * 64 + g * 16 + (t1 & 1) * 8);
    const u32x2 b20 = *(LAS u32x2*)(lds + M_B2 + (32 * ng5 + 4 * g) * 2), b21 = *(LAS u32x2*)(lds + M_B2 + ((2 * ng5 + 1 < nblk) ? (32 * ng5 + 16) : 32 * ng5) * 2 + 8 * g);
    f32x2 r00 = lo2(ac5[0][0]) + (bf2(b20.x) + bf2(hr0.x)), r01 = hi2(ac5[0][0]) + (bf2(b20.y) + bf2(hr0.y));
    f32x2 r10 = lo2(ac5[1][0]) + (bf2(b21.x) + bf2(hr1.x)), r11 = hi2(ac5[1][0]) + (bf2(b21.y) + bf2(hr1.y));
    BAR();
#pragma unroll
    for (int t = 1; t < 4; ++t) {
      ldsp d = lds + paddr((kq * 3 + (t - 1)) * 2 + ng5) + lane * 16;
      const f32x4 u0 = *(LAS f32x4*)d, u1 = *(LAS f32x4*)(d + 1024);
      r00 += lo2(u0); r01 += hi2(u0); r10 += lo2(u1); r11 += hi2(u1);
    }
    GAS char* op = (GAS char*)p.out + (size_t)(128 * c + 64 * th5 + tokl) * 896 + (col0 + 32 * ng5 + 4 * g) * 2;
    *(GAS u32x2*)op = u32x2{cvtpk(r00), cvtpk(r01)};
    if (2 * ng5 + 1 < nblk) *(GAS u32x2*)(op + 32) = u32x2{cvtpk(r10), cvtpk(r11)};
  }
}

extern "C" void kernel_launch(void* const* d_in, const int* in_sizes, int n_in, void* d_out, int out_size, void* d_ws, size_t ws_size, hipStream_t stream) {
  Params p;
  p.x = (const char*)d_in[0]; p.wqkv = (const char*)d_in[1]; p.wo = (const char*)d_in[2];
  p.w1 = (const char*)d_in[4]; p.b1 = (const char*)d_in[5]; p.w2 = (const char*)d_in[6]; p.b2 = (const char*)d_in[7];
  p.out = (char*)d_out; p.ws = (char*)d_ws;
  (void)hipFuncSetAttribute((const void*)fused_block, hipFuncAttributeMaxDynamicSharedMemorySize, LDS_BYTES);
  hipLaunchKernelGGL(fused_block, dim3(256), dim3(512), LDS_BYTES, stream, p);
}
```

```cpp
#include <hip/hip_runtime.h>
#include <hip/hip_bf16.h>
#include <stdint.h>
#include <type_traits>

#define LAS __attribute__((address_space(3)))
#define GAS __attribute__((address_space(1)))
typedef short bf16x8 __attribute__((ext_vector_type(8)));
typedef short s16x4 __attribute__((ext_vector_type(4)));
typedef short v4i16_t __attribute__((ext_vector_type(4)));
typedef float f32x4 __attribute__((ext_vector_type(4)));
typedef float f32x2 __attribute__((ext_vector_type(2)));
typedef unsigned u32x4 __attribute__((ext_vector_type(4)));
typedef unsigned u32x2 __attribute__((ext_vector_type(2)));
typedef __bf16 bf16x2_t __attribute__((ext_vector_type(2)));
typedef LAS char* ldsp;

constexpr int SEQ = 2048;
constexpr size_t WS_CTL = 0;
constexpr size_t WS_KV = 262144;
constexpr size_t WS_O = WS_KV + 256 * 16384;
constexpr size_t WS_H = WS_O + 256 * 8192;
constexpr size_t WS_H1 = WS_H + (size_t)SEQ * 896;
constexpr size_t WS_ST = WS_H1 + (size_t)SEQ * 3584;
constexpr size_t WS_WT = WS_ST + (size_t)SEQ * 32;
constexpr unsigned WT_W1 = 0, WT_W2 = WT_W1 + 1568 * 1024, WT_BYTES = WT_W2 + 1568 * 1024;
constexpr int LDS_BYTES = 160 * 1024;
constexpr int M_PT = 143360;
constexpr int M_XT = 147456;
constexpr int M_TILE = 131072;
constexpr int MISC = 157696;
constexpr int M_LN = MISC, M_B1 = MISC + 1024, M_B2 = MISC + 1280, M_PF = MISC + 1536, M_PART = MISC + 2048, M_FILL = MISC + 3072;
constexpr unsigned SPIN_CAP = 1u << 16;
constexpr unsigned CODE_LINES = 192;

#define FLAG_IDX(ph, c, j) (64 + (((ph) * 16 + (c)) * 16 + (j)) * 32)

#define WAITV(n) asm volatile("s_waitcnt vmcnt(" #n ")" ::: "memory")
#define BAR() do { asm volatile("s_waitcnt lgkmcnt(0)" ::: "memory"); __builtin_amdgcn_s_barrier(); asm volatile("" ::: "memory"); } while (0)
#define BARN() do { asm volatile("" ::: "memory"); __builtin_amdgcn_s_barrier(); asm volatile("" ::: "memory"); } while (0)
#define SCHED() __builtin_amdgcn_sched_barrier(0)
#define RAWBAR() __builtin_amdgcn_s_barrier()
#define MFMA(a, b, c) __builtin_amdgcn_mfma_f32_16x16x32_bf16((a), (b), (c), 0, 0, 0)
#define MACC(acc, a, b) asm volatile("v_mfma_f32_16x16x32_bf16 %0, %1, %2, %0" : "+v"(acc) : "v"(a), "v"(b))
#define MACCN(acc, a, b) asm volatile("s_nop 1\n\tv_mfma_f32_16x16x32_bf16 %0, %1, %2, %0" : "+v"(acc) : "v"(a), "v"(b))
#define NOPS() asm volatile("s_nop 7\n\ts_nop 3")
#define KEEP(x) asm volatile("" : "+v"(x))
#define IC(x) std::integral_constant<int, (x)>{}

template <int I, int N, class F> __device__ __forceinline__ void sfor(F&& f) { if constexpr (I < N) { f(std::integral_constant<int, I>{}); sfor<I + 1, N>(f); } }

__device__ __forceinline__ unsigned cvtpk(float lo, float hi) { f32x2 v = {lo, hi}; bf16x2_t b = __builtin_convertvector(v, bf16x2_t); return __builtin_bit_cast(unsigned, b); }
__device__ __forceinline__ unsigned cvtpk(f32x2 v) { bf16x2_t b = __builtin_convertvector(v, bf16x2_t); return __builtin_bit_cast(unsigned, b); }
__device__ __forceinline__ float dot2(unsigned a, unsigned b, float c) { return __builtin_amdgcn_fdot2_f32_bf16(__builtin_bit_cast(bf16x2_t, a), __builtin_bit_cast(bf16x2_t, b), c, false); }
__device__ __forceinline__ float bflo(unsigned u) { return __uint_as_float(u << 16); }
__device__ __forceinline__ float bfhi(unsigned u) { return __uint_as_float(u & 0xffff0000u); }
__device__ __forceinline__ f32x2 bf2(unsigned u) { return f32x2{bflo(u), bfhi(u)}; }
__device__ __forceinline__ f32x2 lo2(f32x4 v) { return f32x2{v[0], v[1]}; }
__device__ __forceinline__ f32x2 hi2(f32x4 v) { return f32x2{v[2], v[3]}; }
__device__ __forceinline__ bf16x8 ld128(ldsp p) { return *(const LAS bf16x8*)p; }
__device__ __forceinline__ s16x4 ldtr(ldsp p) { return __builtin_bit_cast(s16x4, __builtin_amdgcn_ds_read_tr16_b64_v4i16((LAS v4i16_t*)p)); }
__device__ __forceinline__ bf16x8 cat(s16x4 lo, s16x4 hi) { return __builtin_shufflevector(lo, hi, 0, 1, 2, 3, 4, 5, 6, 7); }
__device__ __forceinline__ int rfl(int x) { return __builtin_amdgcn_readfirstlane(x); }
template <int LADD, int VADD, bool SC1>
__device__ __forceinline__ void dma(unsigned voff, const void* sbase, unsigned lbase) {
  const unsigned va = voff + (unsigned)VADD;
  if constexpr (SC1) asm volatile("s_add_i32 m0, %2, %3\n\ts_nop 0\n\tglobal_load_lds_dwordx4 %0, %1 sc1" ::"v"(va), "s"(sbase), "s"(lbase), "i"(LADD) : "memory", "scc");
  else asm volatile("s_add_i32 m0, %2, %3\n\ts_nop 0\n\tglobal_load_lds_dwordx4 %0, %1" ::"v"(va), "s"(sbase), "s"(lbase), "i"(LADD) : "memory", "scc");
}
template <int LADD>
__device__ __forceinline__ void dma2c_sc1(unsigned voff, const void* sbase, unsigned lbase) {
  asm volatile("s_add_i32 m0, %2, %3\n\ts_nop 0\n\tglobal_load_lds_dwordx4 %0, %1 sc1\n\tglobal_load_lds_dwordx4 %0, %1 offset:1024 sc1" ::"v"(voff), "s"(sbase), "s"(lbase), "i"(LADD) : "memory", "scc");
}
template <int OFF>
__device__ __forceinline__ void ldfrag1(bf16x8& f, unsigned voff, const void* sbase) {
  asm volatile("global_load_dwordx4 %0, %1, %2 offset:%3 sc1" : "=v"(f) : "v"(voff), "s"(sbase), "i"(OFF) : "memory");
}
__device__ __forceinline__ void pin1(bf16x8& f) { asm volatile("" : "+v"(f)); }
template <int N>
__device__ __forceinline__ void ldfrags(bf16x8* f, unsigned voff, const void* sbase) {
  sfor<0, N>([&](auto I) { constexpr int i = decltype(I)::value; ldfrag1<(i & 3) * 1024>(f[i], voff + (unsigned)((i >> 2) * 4096), sbase); });
}
template <int N>
__device__ __forceinline__ void pin(bf16x8* f) { sfor<0, N>([&](auto I) { pin1(f[decltype(I)::value]); }); }
__device__ __forceinline__ void st128_sc1(unsigned voff, const void* sbase, u32x4 v) {
  asm volatile("global_store_dwordx4 %0, %1, %2 sc1\n\ts_nop 1" ::"v"(voff), "v"(v), "s"(sbase) : "memory");
}
__device__ __forceinline__ void st64_sc1(unsigned voff, const void* sbase, u32x2 v) {
  asm volatile("global_store_dwordx2 %0, %1, %2 sc1\n\ts_nop 1" ::"v"(voff), "v"(v), "s"(sbase) : "memory");
}
__device__ __forceinline__ void st32_sc1(const void* sbase, unsigned v) {
  asm volatile("global_store_dword %0, %1, %2 sc1\n\ts_nop 1" ::"v"(0u), "v"(v), "s"(sbase) : "memory");
}
__device__ __forceinline__ float max3(float a, float b, float c) { return __builtin_elementwise_maximum(__builtin_elementwise_maximum(a, b), c); }
__device__ __forceinline__ float red_g_sum(float v) {
  auto a = __builtin_amdgcn_permlane16_swap(__float_as_uint(v), __float_as_uint(v), false, false);
  v = __uint_as_float(a[0]) + __uint_as_float(a[1]);
  auto b = __builtin_amdgcn_permlane32_swap(__float_as_uint(v), __float_as_uint(v), false, false);
  return __uint_as_float(b[0]) + __uint_as_float(b[1]);
}
__device__ __forceinline__ float red_g_max(float v) {
  auto a = __builtin_amdgcn_permlane16_swap(__float_as_uint(v), __float_as_uint(v), false, false);
  v = __builtin_elementwise_maximum(__uint_as_float(a[0]), __uint_as_float(a[1]));
  auto b = __builtin_amdgcn_permlane32_swap(__float_as_uint(v), __float_as_uint(v), false, false);
  return __builtin_elementwise_maximum(__uint_as_float(b[0]), __uint_as_float(b[1]));
}
__device__ __forceinline__ void poll_flags(const unsigned* base, unsigned voff, unsigned epoch) {
#pragma unroll 1
  for (unsigned spins = 0; spins < SPIN_CAP; ++spins) {
    unsigned v;
    asm volatile("global_load_dword %0, %1, %2 sc1\n\ts_waitcnt vmcnt(0)" : "=&v"(v) : "v"(voff), "s"(base) : "memory");
    if (__all(v == epoch)) break;
    __builtin_amdgcn_s_sleep(1);
  }
}

struct Params {
  const char* x; const char* wqkv; const char* wo; const char* w1; const char* b1; const char* w2; const char* b2;
  char* out; char* ws;
};

#define LANEVARS() int tz_ = tid; asm volatile("" : "+v"(tz_)); const int lane = tz_ & 63, l15 = lane & 15, g = lane >> 4, q4 = l15 >> 2, p4 = l15 & 3; \
  const unsigned lane16 = (unsigned)lane * 16u, x8 = (unsigned)(((lane & 7) ^ ((lane >> 3) & 7)) << 4); (void)q4; (void)p4; (void)g; (void)x8; (void)lane16

__global__ void __launch_bounds__(512, 2) fused_block(Params p) {
  extern __shared__ __attribute__((aligned(16))) char smem[];
  ldsp lds = (ldsp)smem;
  unsigned long long pc0; asm volatile("s_getpc_b64 %0" : "=s"(pc0));
  const int tid = threadIdx.x, lane = tid & 63, w = rfl(tid >> 6);
  const int l15 = lane & 15, g = lane >> 4, q4 = l15 >> 2, p4 = l15 & 3;
  const int b = blockIdx.x, r_ = b >> 3;
  const int c = 2 * (b & 7) + (r_ >> 4), j = r_ & 15;
  const int ca = 4 * ((b & 7) >> 1) + (r_ >> 3), ha = 8 * (b & 1) + (r_ & 7);
  const unsigned* ctl = (const unsigned*)(p.ws + WS_CTL);
  unsigned epoch = 0;
  const unsigned ONES = 0x3f803f80u;
  const unsigned lane16 = (unsigned)lane * 16u;
  const unsigned x8 = (unsigned)(((lane & 7) ^ ((lane >> 3) & 7)) << 4);

  constexpr int KIMG = 0, VIMG = 24576, QIMG = 49152;
  {
    const int h = ha;
    constexpr int SLOT = 28672, WOFF = 16384;
    const unsigned iw = (unsigned)(w >> 2);
    unsigned voff[4];
    {
      const unsigned xo = (unsigned)((32 * w + (lane >> 3)) * 896) + x8;
#pragma unroll
      for (int ii = 0; ii < 4; ++ii) {
        const unsigned u = (unsigned)(192 * (w & 3) + 64 * ii + lane), row = (u * 43691u) >> 19, rem = u - 12u * row;
        const unsigned piece = rem >> 2, c16 = rem & 3, hb = (c16 >> 1) ^ ((row >> 3) & 1);
        const unsigned wo_ = (ii < 3) ? row * 2688u + piece * 896u + hb * 32u + (c16 & 1) * 16u : 0u;
        voff[ii] = iw ? wo_ : xo + ii * 7168u;
      }
    }
    const unsigned long long mW = 0ull - (unsigned long long)iw;
    const unsigned long long bx_ = (unsigned long long)(uintptr_t)p.x + (unsigned)(ca * 128 * 896), bw_ = (unsigned long long)(uintptr_t)p.wqkv + (unsigned)(56 * h - 8 * (h & 1));
    const char* gb = (const char*)(uintptr_t)(bx_ + ((bw_ - bx_) & mW));
    const unsigned kstep = 128 + iw * (64 * 2688 - 128), ldst = w * 4096 + iw * (WOFF + (w - 4) * 3072 - w * 4096);
    const unsigned smask = iw - 1u, l3 = iw ? (unsigned)M_FILL : ldst + 3072u;
    auto issue1 = [&](auto SL, auto II) {
      constexpr int sl = decltype(SL)::value, ii = decltype(II)::value;
      if constexpr (ii < 3) dma<sl * SLOT + ii * 1024, 0, false>(voff[ii], gb, ldst);
      else { dma<0, 0, false>(voff[3], gb, l3 + (smask & (unsigned)(sl * SLOT))); gb += kstep; }
    };
    auto issue = [&](auto SL) { issue1(SL, IC(0)); issue1(SL, IC(1)); issue1(SL, IC(2)); issue1(SL, IC(3)); };
    issue(IC(0));
    RAWBAR(); issue(IC(1));
    {
      unsigned ln = (unsigned)(r_ * 8 + w); if (ln >= CODE_LINES) ln = 0;
      const unsigned long long a = (pc0 & ~255ull) + (unsigned long long)(ln * 128u);
      if (lane == 0) asm volatile("s_mov_b32 m0, %1\n\ts_nop 0\n\tglobal_load_lds_dword %0, off" ::"v"(a), "i"(M_FILL) : "memory");
    }

    const int ptq0 = (b < 224) ? b : 223, ptm = (ptq0 >= 112) ? 2 : 1, ptq = ptq0 - ((ptm == 2) ? 112 : 0);
    const int pts = (ptm == 1) ? ptq >> 3 : ptq >> 1, ptG = (ptm == 1) ? ptq & 7 : ptq & 1;
    auto pt_issue = [&]() {
      const unsigned stride = (ptm == 1) ? 3584u : 896u;
      const char* src = ((ptm == 1) ? p.w1 : p.w2) + (size_t)(32 * pts * stride + ptG * 448);
      const int wq = (w < 7) ? w : 6;
#pragma unroll
      for (int i = 0; i < 2; ++i) { const unsigned u = (unsigned)((2 * wq + i) * 64 + lane), row = (u * 9363u) >> 18, c16 = u - 28u * row;
        dma<M_PT, 0, false>(row * stride + c16 * 16u, src, (unsigned)((2 * wq + i) * 1024)); }
      if (lane == 63) asm volatile("s_mov_b32 m0, %2\n\ts_nop 0\n\tglobal_load_lds_dword %0, %1 sc1" ::"v"(0u), "s"(p.ws + WS_CTL), "i"(M_PF) : "memory");
      if (w == 7) {
        unsigned ln = (unsigned)(b * 13 + (lane < 13 ? lane : 12)); if (ln > 3135u) ln = 3135u;
        unsigned long long a = (unsigned long long)(uintptr_t)p.wo + ln * 128u;
        const unsigned long long ab1 = (unsigned long long)(uintptr_t)p.b1, ab2 = (unsigned long long)(uintptr_t)p.b2;
        a = (lane >= 16 && lane < 44) ? ab1 + (unsigned)(lane - 16) * 128u : a;
        a = (lane >= 44 && lane < 51) ? ab2 + (unsigned)(lane - 44) * 128u : a;
        asm volatile("s_mov_b32 m0, %1\n\ts_nop 0\n\tglobal_load_lds_dword %0, off" ::"v"(a), "i"(M_FILL) : "memory");
      }
    };
    const int tg = w >> 1, ng = w & 1;
    f32x4 acc[3][2], asum = {0, 0, 0, 0}, asq = {0, 0, 0, 0}, acs[3];
#pragma unroll
    for (int e = 0; e < 3; ++e) { acc[e][0] = f32x4{0, 0, 0, 0}; acc[e][1] = f32x4{0, 0, 0, 0}; acs[e] = f32x4{0, 0, 0, 0}; }
    const int xb = (32 * tg + 16 * ng + l15) * 128 + ((g ^ (l15 & 7)) << 4);
    int ab[3];
#pragma unroll
    for (int e = 0; e < 3; ++e) ab[e] = WOFF + (8 * g + q4) * 192 + ((((3 * ng + e) * 32) ^ ((g & 1) << 5))) + 8 * p4;
    bf16x8 xf[2][2];
    s16x4 al[2][3], ah[2][3];
    auto rdx = [&](int st, int buf, int t) { xf[buf][t] = ld128(lds + ((st >> 1) % 5) * SLOT + (xb ^ ((st & 1) << 6) ^ (t * 2048))); };
    auto rdl = [&](int st, int buf, int e) { al[buf][e] = ldtr(lds + ((st >> 1) % 5) * SLOT + ab[e] + (32 * (st & 1)) * 192); };
    auto rdh_ = [&](int st, int buf, int e) { ah[buf][e] = ldtr(lds + ((st >> 1) % 5) * SLOT + ab[e] + (32 * (st & 1) + 4) * 192); };
    auto rdf = [&](int st, int buf) { rdx(st, buf, 0); rdx(st, buf, 1);
#pragma unroll
      for (int e = 0; e < 3; ++e) { rdl(st, buf, e); rdh_(st, buf, e); } };
    bf16x8 onesf = __builtin_bit_cast(bf16x8, (u32x4{ONES, ONES, ONES, ONES}));
    KEEP(onesf); KEEP(asum); KEEP(asq);
#pragma unroll
    for (int e = 0; e < 3; ++e) { KEEP(acc[e][0]); KEEP(acc[e][1]); KEEP(acs[e]); }
    WAITV(5);
    BAR();
    rdf(0, 0);
    sfor<0, 14>([&](auto ST) {
      constexpr int st = decltype(ST)::value, kc = st >> 1, cb = st & 1;
      if constexpr ((st & 1) && st < 13) {
        if constexpr (kc == 0) WAITV(5); else if constexpr (kc == 1) WAITV(9); else if constexpr (kc <= 3) WAITV(6); else if constexpr (kc == 4) WAITV(4); else WAITV(0);
        BARN();
      }
      if constexpr (st == 2) pt_issue();
      constexpr int nb = (st + 1) & 1; constexpr bool more = st < 13;
      const bf16x8 a0 = cat(al[cb][0], ah[cb][0]), a1 = cat(al[cb][1], ah[cb][1]), a2 = cat(al[cb][2], ah[cb][2]);
      SCHED();
      MACC(acc[0][0], a0, xf[cb][0]); if constexpr (more) rdx(st + 1, nb, 0);
      MACC(acc[0][1], a0, xf[cb][1]); if constexpr (more) rdl(st + 1, nb, 0);
      MACC(asum, onesf, xf[cb][0]); if constexpr (more) rdh_(st + 1, nb, 0);
      MACC(acs[0], a0, onesf);
      SCHED();
      if constexpr (st < 2) { issue1(IC(st + 2), IC(0)); issue1(IC(st + 2), IC(1)); }
      else if constexpr (kc + 3 <= 6) issue1(IC((kc + 3) % 5), IC(2 * (st & 1)));
      SCHED();
      MACC(acc[1][0], a1, xf[cb][0]); if constexpr (more) rdx(st + 1, nb, 1);
      MACC(acc[1][1], a1, xf[cb][1]); if constexpr (more) rdl(st + 1, nb, 1);
      MACC(asq, xf[cb][0], xf[cb][0]); if constexpr (more) rdh_(st + 1, nb, 1);
      MACC(acs[1], a1, onesf);
      SCHED();
      if constexpr (st < 2) { issue1(IC(st + 2), IC(2)); issue1(IC(st + 2), IC(3)); }
      else if constexpr (kc + 3 <= 6) issue1(IC((kc + 3) % 5), IC(2 * (st & 1) + 1));
      SCHED();
      MACC(acc[2][0], a2, xf[cb][0]); if constexpr (more) rdl(st + 1, nb, 2);
      MACC(acc[2][1], a2, xf[cb][1]); if constexpr (more) rdh_(st + 1, nb, 2);
      MACC(acs[2], a2, onesf);
      SCHED();
    });
    NOPS(); KEEP(asum); KEEP(asq);
#pragma unroll
    for (int e = 0; e < 3; ++e) { KEEP(acc[e][0]); KEEP(acc[e][1]); KEEP(acs[e]); }
    epoch = (unsigned)rfl((int)*(LAS unsigned*)(lds + M_PF + 252)) + 1u;
    {
      const unsigned ptb = (ptm == 1) ? WT_W1 : WT_W2, ptn = (ptm == 1) ? 14u : 56u;
      ldsp src = lds + M_PT + (4 * g + q4) * 448 + 8 * p4;
#pragma unroll
      for (int i = 0; i < 2; ++i) {
        const int t = (w + 8 * i < 14) ? w + 8 * i : w;
        const bf16x8 f = cat(ldtr(src + t * 32), ldtr(src + 16 * 448 + t * 32));
        st128_sc1(lane16 + ptb + ((unsigned)(14 * ptG + t) * ptn + (unsigned)pts) * 1024u, p.ws + WS_WT, __builtin_bit_cast(u32x4, f));
      }
    }
    {
      const float d01 = (p4 & 1) ? asq[1] : asq[0], d23 = (p4 & 1) ? asq[3] : asq[2], dq = (p4 & 2) ? d23 : d01;
      float mean = asum[0] * (1.f / 448.f), dq2 = dq;
      KEEP(mean); KEEP(dq2);
      if (g == q4) *(LAS f32x2*)(lds + M_LN + (32 * tg + 16 * ng + l15) * 8) = f32x2{mean, rsqrtf(fmaxf(dq2 * (1.f / 448.f) - mean * mean, 0.f) + 1e-5f)};
    }
    BAR();
    float mu[2], rs[2];
#pragma unroll
    for (int t = 0; t < 2; ++t) { const f32x2 st_ = *(LAS f32x2*)(lds + M_LN + (32 * tg + 16 * (t ^ ng) + l15) * 8); mu[t] = st_[0]; rs[t] = st_[1]; }
    const bool pad0 = (h & 1) && g == 0, pad1 = !(h & 1) && g == 3;
    const int rsw = ((l15 >> 1) & 3) << 4, rowb = (32 * tg + l15) * 64;
    auto epi = [&](auto NG) {
      constexpr int ngc = decltype(NG)::value;
      sfor<0, 3>([&](auto E) {
        constexpr int e = decltype(E)::value, nt = 3 * ngc + e, which = nt >> 1, par = nt & 1;
        constexpr int ibase = (which == 0) ? QIMG : (which == 1) ? KIMG + 16384 : VIMG + 16384;
        const int off = (which == 2) ? ((((2 * par + (g >> 1)) << 4) ^ rsw) + (g & 1) * 8) : (((g << 4) ^ rsw) + par * 8);
        const bool pad = par ? pad1 : pad0;
#pragma unroll
        for (int t = 0; t < 2; ++t) {
          const float sc = (which == 0) ? rs[t] * 0.27264657f : rs[t], nm = -mu[t] * sc;
          f32x2 sc2 = {sc, sc}, nm2 = {nm, nm};
          KEEP(sc2); KEEP(nm2);
          const f32x2 v0 = lo2(acc[e][t]) * sc2 + lo2(acs[e]) * nm2, v1 = hi2(acc[e][t]) * sc2 + hi2(acs[e]) * nm2;
          u32x2 o; o.x = cvtpk(v0); o.y = cvtpk(v1);
          o.x = pad ? ((which == 2) ? 0x00003f80u : 0u) : o.x; o.y = pad ? 0u : o.y;
          *(LAS u32x2*)(lds + ibase + (t ^ ngc) * 1024 + rowb + off) = o;
        }
      });
    };
    if (ng) epi(IC(1)); else epi(IC(0));
    if (ca < 2) {
      const u32x4 z = {0, 0, 0, 0};
#pragma unroll
      for (int i = 0; i < 2; ++i) { *(LAS u32x4*)(lds + KIMG + tid * 16 + i * 8192) = z; *(LAS u32x4*)(lds + VIMG + tid * 16 + i * 8192) = z; }
    }
    BAR();
    {
      const char* blob = p.ws + WS_KV + (size_t)(ca * 16 + h) * 16384;
      const u32x4 v0 = *(LAS u32x4*)(lds + KIMG + 16384 + tid * 16), v1 = *(LAS u32x4*)(lds + VIMG + 16384 + tid * 16);
      st128_sc1(tid * 16, blob, v0); st128_sc1(8192 + tid * 16, blob, v1);
    }
    WAITV(0);
    BAR();
    if (tid == 0) { st32_sc1(ctl + FLAG_IDX(0, ca, h), epoch); st32_sc1(ctl + FLAG_IDX(4, b >> 4, b & 15), epoch); }
  }
  const int ns = j & 3, col4 = 128 * ns - (((ns >> 1) & ns) << 5), ngr = 4 - (ns >> 1);
  const int pr = w & 3, wh = w >> 2;
  const int prc = (pr < ngr) ? pr : 0;
  const int th5 = j >> 3, cs5 = j & 7, col0 = 64 * cs5 - 16 * ((cs5 >> 2) * (cs5 & 3)), nblk = 4 - (cs5 >> 2);
  const int ng5 = w & 1, kq = w >> 1;
  const char* wt = p.ws + WS_WT;
  bf16x8 wr[32];
  const int T1 = (448 * (j >> 2) + col4) / 16;
  const unsigned w1o = WT_W1 + (unsigned)(T1 + 2 * prc) * 14u * 1024u;
  const unsigned wro = lane16 + w1o;
  constexpr int WI = 32768;
  unsigned wb0, wb1;
  { const int rin = lane >> 4, pos = lane & 15;
    int blk = (pos >> 1) ^ rin; if (blk >= 2 * ngr) blk -= 2; wb0 = (unsigned)(blk * 32 + (pos & 1) * 16);
    blk = (pos >> 1) ^ (4 + rin); if (blk >= 2 * ngr) blk -= 2; wb1 = (unsigned)(blk * 32 + (pos & 1) * 16); }
  const char* wob = p.wo + col4 * 2;
  {
    if (w < 6) {
      const unsigned rb = (unsigned)(((lane >> 4) + 4 * (24 + w)) * 896) + ((w & 1) ? wb1 : wb0);
      sfor<0, 14>([&](auto I) { constexpr int i = decltype(I)::value; dma<i * 6144, i * 24 * 896, false>(rb, wob, (unsigned)(WI + (24 + w) * 1024)); });
    }
    int pos = lane & 15; if (pos >= 4 * ngr) pos = 4 * ngr - 1;
    int xch = (lane & 15) ^ (((lane >> 4) + 4 * w) & 15); if (xch >= 4 * ngr) xch = 0;
    dma<M_XT, 0, false>((unsigned)(((lane >> 4) + 4 * w) * 896 + xch * 16), p.x + (size_t)(128 * c + 32 * (j >> 2)) * 896 + col4 * 2, (unsigned)(w * 1024));
    if (w == 5 && lane < 16) dma<M_B1, 0, false>((unsigned)(pos * 16), p.b1 + (448 * (j >> 2) + col4) * 2, 0u);
    if (w == 4 && lane < 8) dma<M_B2, 0, false>((unsigned)((lane < 2 * nblk ? lane : 0) * 16), p.b2 + col0 * 2, 0u);
  }
  {
    const int h = ha;
    const int sw = (l15 >> 1) & 3;
    if (w == 7 && ca >= 1) poll_flags(ctl, (unsigned)(FLAG_IDX(0, (ca >= 2 && (lane & 1)) ? ca - 2 : ca - 1, h) * 4), epoch);
    BAR();
    {
      const int tl = w >> 2, src = ca - 2 + tl, pc = (w & 3) * 2048;
      if (src >= 0) {
        const char* blob = p.ws + WS_KV + (size_t)(src * 16 + h) * 16384 + pc;
        dma2c_sc1<KIMG>(lane16, blob, (unsigned)(tl * 8192 + pc)); dma2c_sc1<VIMG>(lane16, blob + 8192, (unsigned)(tl * 8192 + pc));
      }
    }
    const bf16x8 qf = ld128(lds + QIMG + (16 * w + l15) * 64 + ((g ^ sw) << 4));
    WAITV(0);
    BAR();
    unsigned tfo, tfl;
    { const int sl = (l15 < 14) ? l15 : 13, hi = (lane >> 4) & 1;
      const int g1 = 8 * sl + (T1 + (hi ? 2 * ngr - 1 : 0)) / 14, g2 = 112 + 2 * (14 * kq + sl) + (col0 / 16 + (hi ? nblk - 1 : 0)) / 14, gi = (lane >= 32) ? g2 : g1;
      tfo = (unsigned)(FLAG_IDX(4, gi >> 4, gi & 15) * 4); }
    asm volatile("global_load_dword %0, %1, %2 sc1" : "=v"(tfl) : "v"(tfo), "s"(ctl) : "memory");
    f32x4 sc[17];
    const float NEG = -__builtin_inff();
    ldsp kb = lds + KIMG + (16 * w + l15) * 64 + ((g ^ sw) << 4);
    {
      bf16x8 kf[2][6];
#pragma unroll
      for (int u = 0; u < 6; ++u) kf[0][u] = ld128(kb + u * 1024);
#pragma unroll
      for (int bt = 0; bt < 3; ++bt) {
        if (bt < 2) {
#pragma unroll
          for (int u = 0; u < 6; ++u) if (6 * (bt + 1) + u < 17) kf[(bt + 1) & 1][u] = ld128(kb + (6 * (bt + 1) + u) * 1024);
        }
        SCHED();
#pragma unroll
        for (int u = 0; u < 6; ++u) if (6 * bt + u < 17) sc[6 * bt + u] = MFMA(kf[bt & 1][u], qf, (f32x4{0, 0, 0, 0}));
        SCHED();
      }
    }
    asm volatile("s_waitcnt vmcnt(0)" : "+v"(tfl)::"memory");
    if (!__all(tfl == epoch)) poll_flags(ctl, tfo, epoch);
    if (w >= 4) ldfrags<7>(wr, wro, wt);
    const int vsw = (2 * g + (q4 >> 1)) & 3;
    ldsp vb0 = lds + VIMG + (16 * w + 4 * g + q4) * 64 + (((p4 >> 1) ^ vsw) << 4) + (p4 & 1) * 8;
    ldsp vb1 = lds + VIMG + (16 * w + 4 * g + q4) * 64 + (((2 + (p4 >> 1)) ^ vsw) << 4) + (p4 & 1) * 8;
    bf16x8 vf[2][3][2];
    auto rdv = [&](int bt) {
#pragma unroll
      for (int i = 0; i < 3; ++i) { const int uu = 3 * bt + i, ua = 2 * uu, ub = (uu < 8) ? 2 * uu + 1 : 15;
        vf[bt & 1][i][0] = cat(ldtr(vb0 + ua * 1024), ldtr(vb0 + ub * 1024)); vf[bt & 1][i][1] = cat(ldtr(vb1 + ua * 1024), ldtr(vb1 + ub * 1024)); }
    };
    rdv(0);
    SCHED();
    if (ca < 2) {
      const int tmin = (2 - ca) * 8;
#pragma unroll
      for (int u = 0; u < 16; ++u) if (w + u < tmin) sc[u] = f32x4{NEG, NEG, NEG, NEG};
    }
#pragma unroll
    for (int r = 0; r < 4; ++r) { sc[0][r] = (4 * g + r <= l15) ? NEG : sc[0][r]; sc[16][r] = (4 * g + r > l15) ? NEG : sc[16][r]; }
    float m = max3(sc[0][0], sc[0][1], sc[0][2]), m2 = max3(sc[0][3], sc[1][0], sc[1][1]);
    m = max3(m, sc[1][2], sc[1][3]);
#pragma unroll
    for (int u = 2; u < 17; ++u) { if (u & 1) m = max3(max3(m, sc[u][0], sc[u][1]), sc[u][2], sc[u][3]); else m2 = max3(max3(m2, sc[u][0], sc[u][1]), sc[u][2], sc[u][3]); }
    m = max3(m, m2, m2);
    m = red_g_max(m);
    SCHED();
    if (w >= 4) ldfrags<7>(wr + 7, wro + 7 * 1024, wt);
    unsigned pk[17][2];
    f32x2 mm = {-m, -m};
    KEEP(mm);
    f32x4 oa[2] = {f32x4{0, 0, 0, 0}, f32x4{0, 0, 0, 0}};
    KEEP(oa[0]); KEEP(oa[1]);
#pragma unroll
    for (int bt = 0; bt < 3; ++bt) {
#pragma unroll
      for (int u = 6 * bt; u < 6 * bt + 6 && u < 17; ++u) {
        const f32x2 d0 = lo2(sc[u]) + mm, d1 = hi2(sc[u]) + mm;
        pk[u][0] = cvtpk(__builtin_amdgcn_exp2f(d0[0]), __builtin_amdgcn_exp2f(d0[1]));
        pk[u][1] = cvtpk(__builtin_amdgcn_exp2f(d1[0]), __builtin_amdgcn_exp2f(d1[1]));
      }
      if (bt < 2) rdv(bt + 1);
      if (bt < 2 && w >= 4) ldfrags<7>(wr + 14 + 7 * bt, wro + (14 + 7 * bt) * 1024, wt);
      SCHED();
#pragma unroll
      for (int i = 0; i < 3; ++i) {
        const int uu = 3 * bt + i;
        u32x4 pu; pu.x = pk[2 * uu][0]; pu.y = pk[2 * uu][1];
        if (uu < 8) { pu.z = pk[2 * uu + 1][0]; pu.w = pk[2 * uu + 1][1]; } else { pu.z = 0; pu.w = 0; }
        const bf16x8 pf = __builtin_bit_cast(bf16x8, pu);
        MACCN(oa[0], vf[bt & 1][i][0], pf); MACC(oa[1], vf[bt & 1][i][1], pf);
      }
      SCHED();
    }
    NOPS(); KEEP(oa[0]); KEEP(oa[1]);
    int tz2 = tid; asm volatile("" : "+v"(tz2));
    const int l15b = tz2 & 15, gb = (tz2 & 63) >> 4, swb = (l15b >> 1) & 3;
    const float lsel = (h & 1) ? oa[0][0] : oa[1][0];
    const float l = __uint_as_float((unsigned)__builtin_amdgcn_ds_bpermute((((h & 1) ? 0 : 48) + l15b) * 4, (int)__float_as_uint(lsel)));
    const float inv1 = __builtin_amdgcn_rcpf(l);
    f32x2 inv = {inv1, inv1};
    KEEP(inv);
    u32x4 ov;
    ov.x = cvtpk(lo2(oa[0]) * inv); ov.y = cvtpk(hi2(oa[0]) * inv); ov.z = cvtpk(lo2(oa[1]) * inv); ov.w = cvtpk(hi2(oa[1]) * inv);
    { const bool z0 = (h & 1) && gb == 0, z1 = !(h & 1) && gb == 3; ov.x = z0 ? 0u : ov.x; ov.y = z0 ? 0u : ov.y; ov.z = z1 ? 0u : ov.z; ov.w = z1 ? 0u : ov.w; }
    st128_sc1((unsigned)((16 * w + l15b) * 64 + ((gb ^ swb) << 4)), p.ws + WS_O + (size_t)(ca * 16 + h) * 8192, ov);
    BAR();
    {
      const unsigned rb = (unsigned)(((lane >> 4) + 12 * w) * 896), va = rb + ((w & 1) ? wb1 : wb0), vb = rb + ((w & 1) ? wb0 : wb1), lb = (unsigned)(WI + 3 * w * 1024);
      dma<0, 0, false>(va, wob, lb); dma<1024, 4 * 896, false>(vb, wob, lb); dma<2048, 8 * 896, false>(va, wob, lb);
      const int ins = (w < 4) ? 108 + w : 3 * w + 2;
      dma<0, 0, false>((unsigned)(((lane >> 4) + 4 * ins) * 896) + ((ins & 1) ? wb1 : wb0), wob, (unsigned)(WI + ins * 1024));
    }
    WAITV(4);
    BAR();
    if (tid == 0) st32_sc1(ctl + FLAG_IDX(1, ca, h), epoch);
  }
  {
    LANEVARS();
    constexpr int OT = 0;
    const int tq = j >> 2;
    WAITV(0);
    BAR();
    if (w < 4) {
      const int rx = 4 * (g & 1) + q4;
      ldsp ab0 = lds + WI + (4 * g + q4) * 256 + (((2 * prc) ^ rx) << 5) + 8 * p4;
      ldsp ab1 = lds + WI + (4 * g + q4) * 256 + (((2 * prc + 1) ^ rx) << 5) + 8 * p4;
      ldsp ab0h = ab0 + 65536, ab1h = ab1 + 65536;
#pragma unroll
      for (int s = 0; s < 16; ++s) {
        const int rb = (28 * s - 4 * (s & 1)) * 256;
        if (rb + 4096 < 65536) { wr[s] = cat(ldtr(ab0 + rb), ldtr(ab0 + rb + 4096)); wr[16 + s] = cat(ldtr(ab1 + rb), ldtr(ab1 + rb + 4096)); }
        else { wr[s] = cat(ldtr(ab0h + (rb - 65536)), ldtr(ab0h + (rb - 65536 + 4096))); wr[16 + s] = cat(ldtr(ab1h + (rb - 65536)), ldtr(ab1h + (rb - 65536 + 4096))); }
      }
    } else {
      __builtin_amdgcn_s_setprio(3);
      poll_flags(ctl, (unsigned)(FLAG_IDX(1, c, l15) * 4), epoch);
      const char* ob_ = p.ws + WS_O + (size_t)(c * 16 + 4 * (w - 4)) * 8192 + tq * 2048;
      const unsigned lb = (unsigned)(OT + (w - 4) * 8192);
      dma2c_sc1<0>(lane16, ob_, lb); dma2c_sc1<2048>(lane16 + 8192, ob_, lb); dma2c_sc1<4096>(lane16 + 16384, ob_, lb); dma2c_sc1<6144>(lane16 + 24576, ob_, lb);
      __builtin_amdgcn_s_setprio(0);
    }
    WAITV(0);
    BAR();
    if (w < 4) {
      f32x4 a0[2], a1[2];
#pragma unroll
      for (int t = 0; t < 2; ++t) { a0[t] = f32x4{0, 0, 0, 0}; a1[t] = f32x4{0, 0, 0, 0}; }
      ldsp ob = lds + OT + l15 * 64 + ((g ^ ((l15 >> 1) & 3)) << 4);
      u32x2 xr[2][2];
#pragma unroll
      for (int t = 0; t < 2; ++t) { ldsp xp = lds + M_XT + (16 * t + l15) * 256 + (g & 1) * 8;
        xr[t][0] = *(LAS u32x2*)(xp + (((4 * prc + (g >> 1)) ^ l15) << 4)); xr[t][1] = *(LAS u32x2*)(xp + (((4 * prc + 2 + (g >> 1)) ^ l15) << 4)); }
#pragma unroll
      for (int t = 0; t < 2; ++t) { KEEP(a0[t]); KEEP(a1[t]); }
      bf16x8 of[4][2];
#pragma unroll
      for (int s = 0; s < 3; ++s) { of[s][0] = ld128(ob + s * 2048); of[s][1] = ld128(ob + s * 2048 + 1024); }
#pragma unroll
      for (int s = 0; s < 16; ++s) {
        if (s + 3 < 16) { of[(s + 3) & 3][0] = ld128(ob + (s + 3) * 2048); of[(s + 3) & 3][1] = ld128(ob + (s + 3) * 2048 + 1024); }
        SCHED();
#pragma unroll
        for (int t = 0; t < 2; ++t) { MACC(a0[t], wr[s], of[s & 3][t]); MACC(a1[t], wr[16 + s], of[s & 3][t]); }
        SCHED();
      }
      NOPS();
#pragma unroll
      for (int t = 0; t < 2; ++t) { KEEP(a0[t]); KEEP(a1[t]); }
      const unsigned ho = (unsigned)((128 * c + 32 * tq + l15) * 896 + (col4 / 32 + pr) * 64 + g * 16);
#pragma unroll
      for (int t = 0; t < 2; ++t) {
        u32x4 hv;
        hv.x = cvtpk(lo2(a0[t]) + bf2(xr[t][0].x)); hv.y = cvtpk(hi2(a0[t]) + bf2(xr[t][0].y));
        hv.z = cvtpk(lo2(a1[t]) + bf2(xr[t][1].x)); hv.w = cvtpk(hi2(a1[t]) + bf2(xr[t][1].y));
        if (pr < ngr) st128_sc1(ho + t * 16 * 896, p.ws + WS_H, hv);
        float s1 = 0.f, s2 = 0.f;
#pragma unroll
        for (int i = 0; i < 4; ++i) { s1 = dot2(hv[i], ONES, s1); s2 = dot2(hv[i], hv[i], s2); }
        s1 = red_g_sum(s1); s2 = red_g_sum(s2);
        if (pr >= ngr) { s1 = 0.f; s2 = 0.f; }
        if (g == 0) ((LAS f32x2*)(lds + M_PART))[(16 * t + l15) * 4 + pr] = f32x2{s1, s2};
      }
    } else {
      pin<28>(wr);
#pragma unroll
      for (int i = 0; i < 28; ++i) *(LAS bf16x8*)(lds + 32768 + (w - 4) * 28672 + i * 1024 + lane * 16) = wr[i];
      if (b == 0 && w == 5) {
#pragma unroll 1
        for (int i = 0; i < 4; ++i) poll_flags(ctl, (unsigned)(FLAG_IDX(4, 4 * i + g, l15) * 4), epoch);
        if (lane == 0) st32_sc1(ctl, epoch);
      }
    }
    BAR();
    if (tid < 32) {
      const f32x4 u0 = *(LAS f32x4*)(lds + M_PART + tid * 32), u1 = *(LAS f32x4*)(lds + M_PART + tid * 32 + 16);
      st64_sc1((unsigned)((128 * c + 32 * tq + tid) * 32 + ns * 8), p.ws + WS_ST,
               u32x2{__float_as_uint((u0[0] + u0[2]) + (u1[0] + u1[2])), __float_as_uint((u0[1] + u0[3]) + (u1[1] + u1[3]))});
    }
    if (w < 4) {
#pragma unroll
      for (int i = 0; i < 28; ++i) wr[i] = ld128(lds + 32768 + w * 28672 + i * 1024 + lane * 16);
    }
    if (w < 4) WAITV(0);
    BAR();
    if (tid == 0) st32_sc1(ctl + FLAG_IDX(2, c, j), epoch);
  }
  {
    LANEVARS();
    const int f0_ = 448 * (j >> 2) + col4, th = wh;
    f32x4 c0 = {0, 0, 0, 0}, c1 = {0, 0, 0, 0};
    pin<28>(wr);
    bf16x8 onesf = __builtin_bit_cast(bf16x8, (u32x4{ONES, ONES, ONES, ONES}));
    KEEP(onesf); KEEP(c0); KEEP(c1);
    asm volatile("s_nop 1");
#pragma unroll
    for (int s = 0; s < 14; ++s) { MACC(c0, wr[s], onesf); MACC(c1, wr[14 + s], onesf); }
    NOPS(); KEEP(c0); KEEP(c1);
    SCHED();
    if (w == 7) poll_flags(ctl, (unsigned)(FLAG_IDX(2, c, l15) * 4), epoch);
    BAR();
    const unsigned vo = (unsigned)((16 * w + (lane >> 3)) * 896) + x8, wl2k = (unsigned)(w * 2048);
    const char* hb = p.ws + WS_H + (size_t)c * 128 * 896;
    auto issue4 = [&](auto KC) { constexpr int kc = decltype(KC)::value; dma<kc * 16384, kc * 128, true>(vo, hb, wl2k); dma<kc * 16384 + 1024, kc * 128 + 8 * 896, true>(vo, hb, wl2k); };
    if (lane < 32) dma<M_TILE, 0, true>(lane16, p.ws + WS_ST + (size_t)(128 * c + 16 * w) * 32, (unsigned)(w * 512));
    issue4(IC(0)); RAWBAR(); issue4(IC(1));
    f32x4 acc[2][4];
#pragma unroll
    for (int t = 0; t < 4; ++t) { acc[0][t] = f32x4{0, 0, 0, 0}; acc[1][t] = f32x4{0, 0, 0, 0}; }
    const int xb = (64 * th + l15) * 128 + ((g ^ (l15 & 7)) << 4);
    bf16x8 xf[2][4];
    auto rd1 = [&](int st, int buf, int t) { xf[buf][t] = ld128(lds + (st >> 1) * 16384 + (xb ^ ((st & 1) << 6)) + t * 2048); };
    auto rdf = [&](int st, int buf) { rd1(st, buf, 0); rd1(st, buf, 1); rd1(st, buf, 2); rd1(st, buf, 3); };
#pragma unroll
    for (int t = 0; t < 4; ++t) { KEEP(acc[0][t]); KEEP(acc[1][t]); }
    bf16x8 bf[2][14];
    const unsigned w2o0 = WT_W2 + ((unsigned)(col0 / 16 + 2 * ng5) * 56u + 14u * kq) * 1024u, w2o1 = (2 * ng5 + 1 < nblk) ? w2o0 + 56u * 1024u : w2o0;
    WAITV(2); BAR();
    rdf(0, 0);
    sfor<0, 14>([&](auto ST) {
      constexpr int st = decltype(ST)::value;
      if constexpr (st == 1) WAITV(2); if constexpr (st == 3) WAITV(4); if constexpr (st == 5) WAITV(6); if constexpr (st == 7) WAITV(8); if constexpr (st == 9) WAITV(10); if constexpr (st == 11) WAITV(12);
      if constexpr ((st & 1) && st < 13) BARN();
      if constexpr (st == 2) {
        if (tid < 128) {
          const f32x4 u0 = *(LAS f32x4*)(lds + M_TILE + tid * 32), u1 = *(LAS f32x4*)(lds + M_TILE + tid * 32 + 16);
          float mean = ((u0[0] + u0[2]) + (u1[0] + u1[2])) * (1.f / 448.f), sq = (u0[1] + u0[3]) + (u1[1] + u1[3]);
          KEEP(mean); KEEP(sq);
          const float rstd = rsqrtf(fmaxf(sq * (1.f / 448.f) - mean * mean, 0.f) + 1e-5f);
          *(LAS f32x2*)(lds + M_LN + tid * 8) = f32x2{rstd, -mean * rstd};
        }
      }
      SCHED();
#pragma unroll
      for (int t = 0; t < 2; ++t) { MACC(acc[0][t], wr[st], xf[st & 1][t]); if constexpr (st < 13) rd1(st + 1, (st + 1) & 1, t); MACC(acc[1][t], wr[14 + st], xf[st & 1][t]); }
      SCHED();
      if constexpr (st <= 4) issue4(IC(st + 2));
      if constexpr (st >= 5) { constexpr int s = st - 5; ldfrags<1>(&bf[0][s], lane16 + w2o0 + s * 1024, wt); ldfrags<1>(&bf[1][s], lane16 + w2o1 + s * 1024, wt); }
      SCHED();
#pragma unroll
      for (int t = 2; t < 4; ++t) { MACC(acc[0][t], wr[st], xf[st & 1][t]); if constexpr (st < 13) rd1(st + 1, (st + 1) & 1, t); MACC(acc[1][t], wr[14 + st], xf[st & 1][t]); }
      SCHED();
    });
    NOPS();
#pragma unroll
    for (int t = 0; t < 4; ++t) { KEEP(acc[0][t]); KEEP(acc[1][t]); }
    {
      const u32x2 bb0 = *(LAS u32x2*)(lds + M_B1 + (32 * prc + 4 * g) * 2), bb1 = *(LAS u32x2*)(lds + M_B1 + (32 * prc + 4 * g) * 2 + 32);
      const f32x2 b00 = bf2(bb0.x), b01 = bf2(bb0.y), b10 = bf2(bb1.x), b11 = bf2(bb1.y);
      const unsigned ho = (unsigned)((128 * c + 64 * th + l15) * 3584 + (f0_ / 32 + pr) * 64 + g * 16);
      f32x2 ln[4];
#pragma unroll
      for (int t = 0; t < 4; ++t) ln[t] = *(LAS f32x2*)(lds + M_LN + (64 * th + 16 * t + l15) * 8);
#pragma unroll
      for (int t = 0; t < 4; ++t) {
        const float a = ln[t][0], nm = ln[t][1];
        f32x2 aa = {a, a}, nn = {nm, nm};
        KEEP(aa); KEEP(nn);
        f32x2 z[4] = {lo2(acc[0][t]) * aa + (lo2(c0) * nn + b00), hi2(acc[0][t]) * aa + (hi2(c0) * nn + b01), lo2(acc[1][t]) * aa + (lo2(c1) * nn + b10), hi2(acc[1][t]) * aa + (hi2(c1) * nn + b11)};
        u32x4 hv;
#pragma unroll
        for (int i = 0; i < 4; ++i) {
          f32x2 r = {__builtin_elementwise_maximum(z[i][0], 0.f), __builtin_elementwise_maximum(z[i][1], 0.f)};
          hv[i] = cvtpk(r * r);
        }
        if (pr < ngr) st128_sc1(ho + t * 16 * 3584, p.ws + WS_H1, hv);
      }
    }
    WAITV(0);
    BAR();
    if (tid == 0) st32_sc1(ctl + FLAG_IDX(3, c, j), epoch);
    if (w != 7) {
      ldfrags<5>(&bf[0][9], lane16 + w2o0 + 9 * 1024, wt); ldfrags<5>(&bf[1][9], lane16 + w2o1 + 9 * 1024, wt);
    }
    if (w >= 5 && w < 7) {
      const int m0_ = col0 >> 5;
      const char* hr = p.ws + WS_H + (size_t)(128 * c + 64 * th5) * 896;
      sfor<0, 6>([&](auto I) { constexpr int i = decltype(I)::value;
        const unsigned u = (unsigned)((w - 5) * 384 + i * 64 + lane), r0 = (u * 43691u) >> 19, c0_ = u - 12u * r0; unsigned cc0 = m0_ * 4 + c0_; if (cc0 > 55) cc0 = 55;
        dma<M_TILE + i * 1024, 0, true>(r0 * 896 + cc0 * 16, hr, (unsigned)((w - 5) * 6144)); });
    }
    const unsigned vo5 = (unsigned)((32 * ng5 + (lane >> 3)) * 3584 + kq * 896) + x8, wl4k = (unsigned)(w * 4096);
    const char* hb5 = p.ws + WS_H1 + (size_t)(128 * c + 64 * th5) * 3584;
    auto issue5 = [&](auto I) {
      constexpr int i = decltype(I)::value, sa = (i & 3) * 32768;
      sfor<0, 4>([&](auto II) { constexpr int ii = decltype(II)::value; dma<sa + ii * 1024, i * 128 + ii * 8 * 3584, true>(vo5, hb5, wl4k); });
    };
    if (w == 7) {
      poll_flags(ctl, (unsigned)(FLAG_IDX(3, c, l15) * 4), epoch);
    }
    BAR();
    issue5(IC(0));
    if (w == 7) {
      ldfrags<5>(&bf[0][9], lane16 + w2o0 + 9 * 1024, wt); ldfrags<5>(&bf[1][9], lane16 + w2o1 + 9 * 1024, wt);
    }
    f32x4 ac5[2][4];
#pragma unroll
    for (int t = 0; t < 4; ++t) { ac5[0][t] = f32x4{0, 0, 0, 0}; ac5[1][t] = f32x4{0, 0, 0, 0}; }
    int yb[4];
#pragma unroll
    for (int t = 0; t < 4; ++t) yb[t] = kq * 8192 + (16 * ((t + kq) & 3) + l15) * 128 + ((g ^ (l15 & 7)) << 4);
    bf16x8 yf[2][4];
    auto rz1 = [&](int st, int buf, int t) { yf[buf][t] = ld128(lds + ((st >> 1) & 3) * 32768 + (yb[t] ^ ((st & 1) << 6))); };
    auto rdy = [&](int st, int buf) { rz1(st, buf, 0); rz1(st, buf, 1); rz1(st, buf, 2); rz1(st, buf, 3); };
#pragma unroll
    for (int t = 0; t < 4; ++t) { KEEP(ac5[0][t]); KEEP(ac5[1][t]); }
    WAITV(0);
    pin<14>(bf[0]); pin<14>(bf[1]);
    BAR();
    rdy(0, 0);
    sfor<0, 14>([&](auto ST) {
      constexpr int st = decltype(ST)::value;
      if constexpr ((st & 1) && st < 13) {
        if constexpr (st < 11) WAITV(4); else WAITV(0);
        BARN();
      }
      SCHED();
#pragma unroll
      for (int t = 0; t < 2; ++t) { MACC(ac5[0][t], bf[0][st], yf[st & 1][t]); if constexpr (st < 13) rz1(st + 1, (st + 1) & 1, t); MACC(ac5[1][t], bf[1][st], yf[st & 1][t]); }
      SCHED();
      if constexpr (st == 0) { issue5(IC(1)); issue5(IC(2)); }
      if constexpr ((st & 1) && st <= 7) issue5(IC((st + 5) / 2));
      SCHED();
#pragma unroll
      for (int t = 2; t < 4; ++t) { MACC(ac5[0][t], bf[0][st], yf[st & 1][t]); if constexpr (st < 13) rz1(st + 1, (st + 1) & 1, t); MACC(ac5[1][t], bf[1][st], yf[st & 1][t]); }
      SCHED();
    });
    NOPS();
#pragma unroll
    for (int t = 0; t < 4; ++t) { KEEP(ac5[0][t]); KEEP(ac5[1][t]); }
    auto paddr = [&](int blk) { return (blk < 16) ? blk * 2048 : 98304 + (blk - 16) * 2048; };
#pragma unroll
    for (int t = 1; t < 4; ++t) {
      ldsp d = lds + paddr((((t + kq) & 3) * 3 + (t - 1)) * 2 + ng5) + lane * 16;
      *(LAS f32x4*)d = ac5[0][t]; *(LAS f32x4*)(d + 1024) = ac5[1][t];
    }
    const int tokl = 16 * kq + l15;
    const int t0 = col0 / 16 + 2 * ng5, t1 = (2 * ng5 + 1 < nblk) ? t0 + 1 : t0, m0_ = col0 >> 5;
    const u32x2 hr0 = *(LAS u32x2*)(lds + M_TILE + tokl * 192 + ((t0 >> 1) - m0_) * 64 + g * 16 + (t0 & 1) * 8);
    const u32x2 hr1 = *(LAS u32x2*)(lds + M_TILE + tokl * 192 + ((t1 >> 1) - m0_) * 64 + g * 16 + (t1 & 1) * 8);
    const u32x2 b20 = *(LAS u32x2*)(lds + M_B2 + (32 * ng5 + 4 * g) * 2), b21 = *(LAS u32x2*)(lds + M_B2 + ((2 * ng5 + 1 < nblk) ? (32 * ng5 + 16) : 32 * ng5) * 2 + 8 * g);
    f32x2 r00 = lo2(ac5[0][0]) + (bf2(b20.x) + bf2(hr0.x)), r01 = hi2(ac5[0][0]) + (bf2(b20.y) + bf2(hr0.y));
    f32x2 r10 = lo2(ac5[1][0]) + (bf2(b21.x) + bf2(hr1.x)), r11 = hi2(ac5[1][0]) + (bf2(b21.y) + bf2(hr1.y));
    BAR();
#pragma unroll
    for (int t = 1; t < 4; ++t) {
      ldsp d = lds + paddr((kq * 3 + (t - 1)) * 2 + ng5) + lane * 16;
      const f32x4 u0 = *(LAS f32x4*)d, u1 = *(LAS f32x4*)(d + 1024);
      r00 += lo2(u0); r01 += hi2(u0); r10 += lo2(u1); r11 += hi2(u1);
    }
    GAS char* op = (GAS char*)p.out + (size_t)(128 * c + 64 * th5 + tokl) * 896 + (col0 + 32 * ng5 + 4 * g) * 2;
    *(GAS u32x2*)op = u32x2{cvtpk(r00), cvtpk(r01)};
    if (2 * ng5 + 1 < nblk) *(GAS u32x2*)(op + 32) = u32x2{cvtpk(r10), cvtpk(r11)};
  }
}

extern "C" void kernel_launch(void* const* d_in, const int* in_sizes, int n_in, void* d_out, int out_size, void* d_ws, size_t ws_size, hipStream_t stream) {
  Params p;
  p.x = (const char*)d_in[0]; p.wqkv = (const char*)d_in[1]; p.wo = (const char*)d_in[2];
  p.w1 = (const char*)d_in[4]; p.b1 = (const char*)d_in[5]; p.w2 = (const char*)d_in[6]; p.b2 = (const char*)d_in[7];
  p.out = (char*)d_out; p.ws = (char*)d_ws;
  (void)hipFuncSetAttribute((const void*)fused_block, hipFuncAttributeMaxDynamicSharedMemorySize, LDS_BYTES);
  hipLaunchKernelGGL(fused_block, dim3(256), dim3(512), LDS_BYTES, stream, p);
}
```
